```python
import torch

def _softmax(x, axis=-1):
    return torch.softmax(x, dim=axis)

def _gelu(x):
    return torch.nn.functional.gelu(x, approximate='tanh')

def _silu(x):
    return torch.nn.functional.silu(x)

def _sigmoid(x):
    return torch.sigmoid(x)

def _layernorm(x, axis=-1, eps=1e-05):
    m = x.mean(dim=axis, keepdim=True)
    v = x.var(dim=axis, keepdim=True, unbiased=False)
    return (x - m) / torch.sqrt(v + eps)

def _scatter_add(tgt, idx, src, axis=0):
    return tgt.clone().index_add_(axis, idx, src)

def _segment_sum(src, seg, n):
    out = torch.zeros((n,) + src.shape[1:], dtype=src.dtype)
    return out.index_add_(0, seg, src)

def _pad(x, pw):
    flat = tuple((v for pair in reversed(pw) for v in pair))
    return torch.nn.functional.pad(x, flat)
kernel_name = 'adaLN_cond__sliding_block_cached__glu_gated_swiglu__decode'

def setup_inputs():

    _w = lambda a: (lambda t: t.to(torch.bfloat16) if t.is_floating_point() else t)(torch.as_tensor(a))

    def _A_setup():
        B, Sq, D = (2, 1, 512)
        Hq, Hkv, Sk, Dh = (32, 32, 16384, 128)
        assert Hq % Hkv == 0, (Hq, Hkv)
        return {'x': _w((lambda *s: torch.randn(*s, dtype=torch.bfloat16) * 0.05)(B, Sq, D)), 'k_cache': _w((lambda *s: torch.randn(*s, dtype=torch.bfloat16) * 3.2)(B, Hkv, Sk, Dh)), 'v_cache': _w((lambda *s: torch.randn(*s, dtype=torch.bfloat16) * 3.2)(B, Hkv, Sk, Dh)), 'wq': _w((lambda *s: torch.randn(*s, dtype=torch.bfloat16) * 3.2)(D, Hq * Dh) * D ** (-0.5)), 'wo': _w((lambda *s: torch.randn(*s, dtype=torch.bfloat16) * 3.2)(Hq * Dh, D) * (Hq * Dh) ** (-0.5))}
    _A_in = _A_setup()

    def _B_setup():
        B, S, D, F = (2, 1, 512, 1376)
        return {'x': _w((lambda *s: torch.randn(*s, dtype=torch.bfloat16) * 0.05)(B, S, D)), 'w1': _w((lambda *s: torch.randn(*s, dtype=torch.bfloat16) * 1.6)(D, F) * (2.0 / D) ** 0.5), 'w3': _w((lambda *s: torch.randn(*s, dtype=torch.bfloat16) * 1.6)(D, F) * (2.0 / D) ** 0.5), 'w2': _w((lambda *s: torch.randn(*s, dtype=torch.bfloat16) * 1.6)(F, D) * (2.0 / F) ** 0.5)}
    _B_in = _B_setup()
    return {'A_x': _A_in['x'], 'A_k_cache': _A_in['k_cache'], 'A_v_cache': _A_in['v_cache'], 'A_wq': _A_in['wq'], 'A_wo': _A_in['wo'], 'B_x': _B_in['x'], 'B_w1': _B_in['w1'], 'B_w3': _B_in['w3'], 'B_w2': _B_in['w2'], 'c': _w((lambda *s: torch.randn(*s, dtype=torch.bfloat16) * 0.2)(2, 512) * 8.0), 'w_mod': _w((lambda *s: torch.randn(*s, dtype=torch.bfloat16) * 0.2)(512, 6 * 512) * 512 ** (-0.5) * 8.0)}

def reference(A_x, A_k_cache, A_v_cache, A_wq, A_wo, B_x, B_w1, B_w3, B_w2, c, w_mod):

    def _A_ref(x, k_cache, v_cache, wq, wo):
        B, Sq, D = x.shape
        Hq, Dh = (32, 128)
        Hkv, Sk = (k_cache.shape[1], k_cache.shape[2])
        W = min(4096, max(256, Sk // 4))
        rep = Hq // Hkv
        q = (lambda x, *d: x.permute(*d))((x @ wq).reshape(B, Sq, Hq, Dh), 0, 2, 1, 3)
        k = (lambda x, n, axis: torch.repeat_interleave(x, n, dim=axis))(k_cache[:, :, Sk - W:, :], rep, axis=1)
        v = (lambda x, n, axis: torch.repeat_interleave(x, n, dim=axis))(v_cache[:, :, Sk - W:, :], rep, axis=1)
        scores = torch.einsum('bhqd,bhkd->bhqk', q, k) * Dh ** (-0.5)
        p = _softmax(scores, axis=-1)
        o = torch.einsum('bhqk,bhkd->bhqd', p, v)
        return (lambda x, *d: x.permute(*d))(o, 0, 2, 1, 3).reshape(B, Sq, Hq * Dh) @ wo

    def _B_ref(x, w1, w3, w2):
        gate = torch.einsum('bsd,df->bsf', x, w1)
        up = torch.einsum('bsd,df->bsf', x, w3)
        h = _silu(gate) * up
        out = torch.einsum('bsf,fd->bsd', h, w2)
        return out
    x = A_x
    mod = (_silu(c) @ w_mod).reshape(c.shape[0], 6, 1, -1)
    sa, ca, ga, sb, cb, gb = (mod[:, 0].clone(), mod[:, 1].clone(), mod[:, 2].clone(), mod[:, 3].clone(), mod[:, 4].clone(), mod[:, 5].clone())
    h = _layernorm(x, axis=-1) * (1 + ca) + sa
    x = x + ga * _A_ref(h, A_k_cache, A_v_cache, A_wq, A_wo)
    h = _layernorm(x, axis=-1) * (1 + cb) + sb
    x = x + gb * _B_ref(h, B_w1, B_w3, B_w2)
    return x
```

```cpp
#define PROBE_MUL 10
#include <hip/hip_runtime.h>
#include <stdint.h>

typedef unsigned int u32;
typedef unsigned short u16;
typedef unsigned long long u64;
typedef __attribute__((ext_vector_type(4))) unsigned u32x4;
typedef __attribute__((ext_vector_type(2))) unsigned u32x2;
typedef __attribute__((ext_vector_type(2))) __bf16 bf16x2;
typedef __attribute__((ext_vector_type(4))) float f32x4;
typedef __attribute__((ext_vector_type(2))) float f32x2;
typedef __amdgpu_buffer_rsrc_t rsrc_t;

constexpr int NH = 32, SK = 16384, DH = 128, WIN = 4096, FF = 1376;
struct Prm { int E0, ES, E1, E2, E2C, EU, TEND, DT_UP, L0, L1, X1, X2, X3, X4, NE, FE, M0, M1, M2, M3, M4, GS, GX, N1, N2, GA, WD, DT_WO, LW, LU, EW, PM; };
constexpr Prm PRM0 = {32, 24, 32, 32, 56, 64, 2030, 60, 30, 25, 92, 86, 131, 88, 4, 12, 5, 8, 21, 4, 32, 30, 56, 28, 70, 12, 4, 100, 45, 15, 8, 0x24310};
#define PRM_ARG
constexpr int NL = 37;
constexpr int NCMIN = 192;
constexpr int WK = 8, WV = 5;
constexpr int WUSE = 12, LNS = 6;
constexpr int QP = 14, QD = 4;
constexpr int VQ = 6;
constexpr int NSL = (NL + 7) / 8;
constexpr int VS = VQ + NSL;
constexpr int MLS = VS + 2;
constexpr int VSIDE = MLS - 5, UPREP = MLS + 8;
constexpr int SIDE = NCMIN / 8 - 4;
constexpr int CPF = 18;
constexpr int TBP = 3, TBL = 10, TBU = 15;
constexpr int cons_end(int v) {
  const int a = v - WV + 1, c = (v - MLS) * 4;
  const int m = a < c ? a : c;
  return m < 0 ? 0 : (m > NL ? NL : m);
}
struct Tab { u32 e[512]; };
constexpr Tab make_tab(const Prm& P) {
  Tab T{};
  for (int bid = 0; bid < 256; ++bid) {
    const int xcd = bid & 7, idx = bid >> 3, part = idx & 3, i8 = idx >> 2, wi = i8 * 8 + xcd;
    const bool has_mod = wi < 48, has_up = wi >= 21;
    const int cb0 = (i8 + 5) & 7, cb1 = (i8 + 4) & 7, ms = wi - 20;
    const int mi = wi < 8 ? wi : (wi < 16 ? 8 + ((xcd + 4) & 7) : (wi < 20 ? (wi - 14) * 8 + 3 : 16 + ms / 7 * 8 + ms % 7 + (ms % 7 >= 3 ? 1 : 0)));
    const int ui = xcd * 5 + (xcd > 5 ? xcd - 5 : 0) + (i8 >= 3 ? (xcd >= 5 ? 1 : 0) + i8 - 3 : 0);
    const int wd = cb0 == 3 ? 0 : P.WD;
    const int e[4] = {P.E0 - 4 * wd + (cb0 == 3 ? P.ES : 0), P.E1 + (cb1 == 3 ? P.ES : 0), has_mod ? (wi < 16 ? P.E2C : P.E2) + ((mi & 7) == 3 ? P.ES : 0) : 0, has_up ? P.EU : 0};
    const int ew = wd ? P.EW : 0, pa = (1024 + e[0] + ew + e[1] + e[2] + e[3]) / 4;
    int c[5] = {0, 0, 0, 0, 128};
    for (int k = 0; k < 3; ++k) c[k + 1] = c[k] + (pa - e[k] - (k == 0 ? ew : 0) + 4) / 8;
    const int nl = c[part + 1] - c[part], wst = e[part] / 4;
    const bool has_w = part < 2 || (part == 2 ? has_mod : has_up);
    const int blk = !has_w ? 0 : (part == 0 ? cb0 : (part == 1 ? xcd * 8 + cb1 : (part == 2 ? mi : ui)));
    const int tau = (part == 3 && has_up ? P.TEND - P.DT_UP : (part == 0 && wd ? P.TEND - P.DT_WO : P.TEND)) * 256 / (2 * nl + wst);
    T.e[2 * bid] = (u32)(c[part] | nl << 7 | blk << 13 | (has_w ? 1 : 0) << 19 | (part == 0 ? wd : 0) << 20);
    T.e[2 * bid + 1] = (u32)(tau | (tau * wst / (part == 0 ? 8 - wd : 8)) << 16);
  }
  return T;
}
constexpr int step_range(const Prm& P, bool mx_) {
  int mx = 0, mn = 1 << 20;
  const Tab T = make_tab(P);
  for (int bid = 0; bid < 256; ++bid) { const int n = T.e[2 * bid] >> 7 & 63; mx = n > mx ? n : mx; mn = n < mn ? n : mn; }
  return mx_ ? mx : mn;
}
__constant__ const Tab TAB = make_tab(PRM0);
static_assert(step_range(PRM0, true) <= NL && step_range(PRM0, false) >= NCMIN / 8, "a wave's key range does not fit its load steps");
static_assert(cons_end(SIDE + WV + 1) <= NCMIN / 8 && cons_end(UPREP) <= NCMIN / 8 && SIDE > MLS + 3, "steps every wave runs");
constexpr int NUP = FF / 32;
constexpr int SPIN_MAX = 1 << 17;

constexpr u32 WS_EPOCH = 0;
constexpr u32 G_TB = 1024;
constexpr u32 G_NT = 2048;
constexpr u32 G_MOD = 4096;
constexpr u32 G_Q = G_MOD + 3072 * 8;
constexpr u32 G_ML = G_Q + 4096 * 8;
constexpr u32 G_P = G_ML + 256 * 16;
constexpr u32 G_XP = G_P + 256 * 256;
constexpr u32 G_OP = G_XP + 8 * 512 * 4;
constexpr u32 WS_END = G_OP + 43 * 512 * 8;
constexpr u32 SENT = 0xffffffffu;
constexpr u32 OOB = 0x40000000u;
#define WST 0

constexpr int AUX_NT = 2, AUX_SC1 = 16;

__device__ __forceinline__ float lo_f(u32 v) { return __uint_as_float(v << 16); }
__device__ __forceinline__ float hi_f(u32 v) { return __uint_as_float(v & 0xffff0000u); }
__device__ __forceinline__ float rbf(float x) { return (float)(__bf16)x; }
__device__ __forceinline__ u32 pack_bf(float lo, float hi) {
  bf16x2 r; r[0] = (__bf16)lo; r[1] = (__bf16)hi; return __builtin_bit_cast(u32, r);
}
__device__ __forceinline__ float dot2(u32 k, u32 q, float acc) {
  return __builtin_amdgcn_fdot2_f32_bf16(__builtin_bit_cast(bf16x2, k), __builtin_bit_cast(bf16x2, q), acc, false);
}
template <int CTRL> __device__ __forceinline__ float dpp(float x) {
  return __builtin_bit_cast(float, __builtin_amdgcn_mov_dpp(__builtin_bit_cast(int, x), CTRL, 0xf, 0xf, true));
}
constexpr int XOR1 = 0xB1, XOR2 = 0x4E, XOR7 = 0x141, XOR8 = 0x128;
__device__ __forceinline__ float swap32_add(float a, float b) {
  auto t = __builtin_amdgcn_permlane32_swap(__float_as_uint(a), __float_as_uint(b), false, false);
  return __uint_as_float(t[0]) + __uint_as_float(t[1]);
}
__device__ __forceinline__ float swap16_add(float a, float b) {
  auto t = __builtin_amdgcn_permlane16_swap(__float_as_uint(a), __float_as_uint(b), false, false);
  return __uint_as_float(t[0]) + __uint_as_float(t[1]);
}
__device__ __forceinline__ float wave_sum(float x) {
  x += dpp<XOR1>(x); x += dpp<XOR2>(x); x += dpp<XOR7>(x); x += dpp<XOR8>(x);
  x = swap16_add(x, x);
  return swap32_add(x, x);
}
__device__ __forceinline__ float wave_max(float x) {
  x = fmaxf(x, dpp<XOR1>(x)); x = fmaxf(x, dpp<XOR2>(x)); x = fmaxf(x, dpp<XOR7>(x)); x = fmaxf(x, dpp<XOR8>(x));
  auto s = __builtin_amdgcn_permlane16_swap(__float_as_uint(x), __float_as_uint(x), false, false);
  x = fmaxf(__uint_as_float(s[0]), __uint_as_float(s[1]));
  auto t = __builtin_amdgcn_permlane32_swap(__float_as_uint(x), __float_as_uint(x), false, false);
  return fmaxf(__uint_as_float(t[0]), __uint_as_float(t[1]));
}
template <int CTRL> __device__ __forceinline__ float sel_add(float lo, float hi, bool up) {
  const float t1 = lo + dpp<CTRL>(lo), t2 = hi + dpp<CTRL>(hi);
  return up ? t2 : t1;
}
template <int I> __device__ __forceinline__ float bc16(float x) {
  return __builtin_bit_cast(float, __builtin_amdgcn_update_dpp(0, __builtin_bit_cast(int, x), 0x150 + I, 0xf, 0xf, false));
}
__device__ __forceinline__ float bcast16(float x, int i) {
  switch (i) {
    case 0: return bc16<0>(x); case 1: return bc16<1>(x); case 2: return bc16<2>(x); case 3: return bc16<3>(x);
    case 4: return bc16<4>(x); case 5: return bc16<5>(x); case 6: return bc16<6>(x); case 7: return bc16<7>(x);
    case 8: return bc16<8>(x); case 9: return bc16<9>(x); case 10: return bc16<10>(x); case 11: return bc16<11>(x);
    case 12: return bc16<12>(x); case 13: return bc16<13>(x); case 14: return bc16<14>(x); default: return bc16<15>(x);
  }
}
__device__ __forceinline__ float silu_f(float x) { return x * __builtin_amdgcn_rcpf(1.f + __expf(-x)); }

template <int AUX> __device__ __forceinline__ u32x4 ld16(rsrc_t r, u32 voff, u32 soff) {
  return __builtin_amdgcn_raw_buffer_load_b128(r, voff, soff, AUX);
}
template <int AUX> __device__ __forceinline__ u32x2 ld8(rsrc_t r, u32 voff, u32 soff) {
  return __builtin_amdgcn_raw_buffer_load_b64(r, voff, soff, AUX);
}
__device__ __forceinline__ void st8_sc1(rsrc_t r, u32 voff, u32 v, u32 tag) {
  __builtin_amdgcn_raw_buffer_store_b64(u32x2{v, tag}, r, voff, 0, AUX_SC1);
}
__device__ __forceinline__ void st16_sc1(rsrc_t r, u32 voff, u32 v0, u32 v1, u32 tag) {
  __builtin_amdgcn_raw_buffer_store_b128(u32x4{v0, tag, v1, tag}, r, voff, 0, AUX_SC1);
}
__device__ __forceinline__ rsrc_t mk_rsrc(const void* p, u32 bytes) {
  return __builtin_amdgcn_make_buffer_rsrc(const_cast<void*>(p), 0, bytes, 0x00020000);
}
#define WAIT_VM(n) __builtin_amdgcn_s_waitcnt(((n) & 15) | (((n) >> 4) << 14) | (7 << 4) | (15 << 8))
#define SB() __builtin_amdgcn_sched_barrier(0)
#define CFENCE() asm volatile("" ::: "memory")
__device__ __forceinline__ bool all_ok(bool ok) { return __builtin_amdgcn_ballot_w64(!ok) == 0ull; }

template <int N> __device__ __forceinline__ void gather16(rsrc_t ws, u32 voff, u32 stride, u32 tag, u32x4 (&g)[N]) {
#pragma nounroll
  for (int spin = 0; spin < SPIN_MAX; ++spin) {
    CFENCE();
    bool ok = true;
#pragma unroll
    for (int i = 0; i < N; ++i) g[i] = ld16<AUX_SC1>(ws, voff + i * stride, 0);
#pragma unroll
    for (int i = 0; i < N; ++i) ok &= (g[i][1] == tag) & (g[i][3] == tag);
    if (all_ok(ok)) break;
    __builtin_amdgcn_s_sleep(8);
  }
}
template <int N> __device__ __forceinline__ void gather8(rsrc_t ws, u32 voff, u32 stride, u32 tag, u32 (&g)[N], bool act = true) {
#pragma nounroll
  for (int spin = 0; spin < SPIN_MAX; ++spin) {
    CFENCE();
    bool ok = true;
    u32x2 t[N];
#pragma unroll
    for (int i = 0; i < N; ++i) t[i] = ld8<AUX_SC1>(ws, voff + i * stride, 0);
#pragma unroll
    for (int i = 0; i < N; ++i) { ok &= (t[i][1] == tag); g[i] = t[i][0]; }
    if (all_ok(ok | !act)) break;
    __builtin_amdgcn_s_sleep(8);
  }
}

struct Smem {
  float red[8][128];
  float ml[8][2];
  float r2[64 * 20];
  u16 hb[3 * 1024];
  float stat[8][64];
  float red2[8][128];
  u32 dummy[64];
  u32x2 bc;
  u32x4 zero[4];
  u32 mflag[8];
  u32x4 wbuf[8][8][64];
  __attribute__((aligned(256))) u16 w2b[32][512];
  u16 actl[2][32];
};

#define PROBE_K 102
#define PROBE_OP 2
#define PROBE_PART -1
#define STAMP(k) do { if ((k) == PROBE_K && tid == 0) pst = wall_clock64(); } while (0)
#define WSTAMP(k) do {} while (0)
#define FINISH() do { STAMP(8); if (tid == 0) { __hip_atomic_store(reinterpret_cast<u64*>(ws + (1u << 20)) + bid * 2, pt0, __ATOMIC_RELAXED, __HIP_MEMORY_SCOPE_AGENT); \
   __hip_atomic_store(reinterpret_cast<u64*>(ws + (1u << 20)) + bid * 2 + 1, (PROBE_PART < 0 || part == PROBE_PART) ? pst : 0ull, __ATOMIC_RELAXED, __HIP_MEMORY_SCOPE_AGENT); } \
   if (bid == 8 && wave == 0) probe_end(ws, pt0, lane); return; } while (0)
#define PIN(x) asm volatile("" : "+v"(x))
#define LDS_BARRIER() do { __builtin_amdgcn_s_waitcnt(0xc07f); __builtin_amdgcn_s_barrier(); CFENCE(); } while (0)
#define WAVE_SYNC() do { __builtin_amdgcn_fence(__ATOMIC_RELEASE, "wavefront"); __builtin_amdgcn_wave_barrier(); __builtin_amdgcn_fence(__ATOMIC_ACQUIRE, "wavefront"); } while (0)

__device__ __forceinline__ void dma16(rsrc_t r, u32 voff, u32 soff, u32 lds_dst) {
  u32 keep;
  asm volatile("s_mov_b32 %0, m0\n\ts_mov_b32 m0, %4\n\ts_nop 4\n\tbuffer_load_dwordx4 %1, %2, %3 offen nt lds\n\ts_mov_b32 m0, %0"
               : "=&s"(keep) : "v"(voff), "s"(r), "s"(soff), "s"(lds_dst) : "memory");
}
__device__ __forceinline__ u32 perm_lo(u32 a, u32 b) { return __builtin_amdgcn_perm(b, a, 0x05040100u); }
__device__ __forceinline__ u32 perm_hi(u32 a, u32 b) { return __builtin_amdgcn_perm(b, a, 0x07060302u); }
__device__ __forceinline__ float rdl(float x, int l) { return __int_as_float(__builtin_amdgcn_readlane(__float_as_int(x), l)); }

typedef __attribute__((ext_vector_type(8))) __bf16 bf16x8;
__device__ __forceinline__ f32x4 mfma16(u32x4 a, u32x4 b, f32x4 c) {
  return __builtin_amdgcn_mfma_f32_16x16x32_bf16(__builtin_bit_cast(bf16x8, a), __builtin_bit_cast(bf16x8, b), c, 0, 0, 0);
}
__device__ __forceinline__ float sum8(const float* p) {
  const f32x4 a = *reinterpret_cast<const f32x4*>(p), b = *reinterpret_cast<const f32x4*>(p + 4);
  return ((a[0] + b[0]) + (a[1] + b[1])) + ((a[2] + b[2]) + (a[3] + b[3]));
}
__device__ __forceinline__ float ln_ri(float var) {
  return __builtin_amdgcn_rcpf(rbf(__builtin_amdgcn_sqrtf(rbf(rbf(fmaxf(var, 0.f)) + 1e-5f))));
}
__device__ __forceinline__ void ln_stats2(const float (&x0)[8], const float (&x1)[8], float& mb0, float& ri0, float& mb1, float& ri1) {
  const float k0 = rdl(x0[0], 0), k1 = rdl(x1[0], 0);
  float s0 = 0.f, s1 = 0.f, q0 = 0.f, q1 = 0.f;
#pragma unroll
  for (int k = 0; k < 8; ++k) {
    const float d0 = x0[k] - k0, d1 = x1[k] - k1;
    s0 += d0; s1 += d1; q0 = fmaf(d0, d0, q0); q1 = fmaf(d1, d1, q1);
  }
  float t = swap16_add(swap32_add(s0, s1), swap32_add(q0, q1));
  t += dpp<XOR1>(t); t += dpp<XOR2>(t); t += dpp<XOR7>(t); t += dpp<XOR8>(t);
  auto u = __builtin_amdgcn_permlane16_swap(__float_as_uint(t), __float_as_uint(t), false, false);
  const float ms = __uint_as_float(u[0]) * (1.f / 512.f), mq = __uint_as_float(u[1]) * (1.f / 512.f);
  const float ri = ln_ri(fmaf(-ms, ms, mq));
  mb0 = rbf(k0 + rdl(ms, 0)); mb1 = rbf(k1 + rdl(ms, 32));
  ri0 = rdl(ri, 0); ri1 = rdl(ri, 32);
}
template <int I> struct IC { static constexpr int v = I; };
template <int I, int N, class F> __device__ __forceinline__ void sfor(F&& f) {
  if constexpr (I < N) { if (f(IC<I>{})) sfor<I + 1, N>(f); }
}
typedef __attribute__((ext_vector_type(4))) short s16x4;
typedef __attribute__((address_space(3))) s16x4* lds_s16x4p;
__device__ __forceinline__ u32x2 tr_read(u32 lds_addr) {
  return __builtin_bit_cast(u32x2, __builtin_amdgcn_ds_read_tr16_b64_v4i16((lds_s16x4p)lds_addr));
}
__device__ __forceinline__ u32 next_tag(u32 e) { return ((e | 0x40004000u) + 1u) & ~0x40004000u; }
__device__ __forceinline__ float u2f(u32 v) { return __uint_as_float(v); }
#define LDSP(T, a) ((__attribute__((address_space(3))) T*)(a))
template <class T> __device__ __forceinline__ u32 lds_addr(T* p) { u32 a = (u32)(uintptr_t)p; PIN(a); return a; }

__device__ __forceinline__ void probe_end(unsigned char* ws, u64 pt0, int lane) {
  while (wall_clock64() < pt0 + 5500ull) __builtin_amdgcn_s_sleep(8);
  u64 a[4], c2[4];
  for (int i = 0; i < 4; ++i) {
    a[i] = __hip_atomic_load(reinterpret_cast<u64*>(ws + (1u << 20)) + (i * 64 + lane) * 2, __ATOMIC_RELAXED, __HIP_MEMORY_SCOPE_AGENT);
    c2[i] = __hip_atomic_load(reinterpret_cast<u64*>(ws + (1u << 20)) + (i * 64 + lane) * 2 + 1, __ATOMIC_RELAXED, __HIP_MEMORY_SCOPE_AGENT);
  }
  int mn0 = 1 << 30, mx1 = -(1 << 30), mn1 = 1 << 30, sum = 0, cnt = 0;
  for (int i = 0; i < 4; ++i) {
    const int d0 = (int)(a[i] - pt0);
    mn0 = min(mn0, d0);
    if (c2[i] != 0ull) { const int d1 = (PROBE_K >= 29 && PROBE_K <= 31) ? (int)(c2[i] - a[i]) : (int)(c2[i] - pt0); mx1 = max(mx1, d1); mn1 = min(mn1, d1); sum += d1; cnt += 1; }
  }
  for (int o = 1; o < 64; o <<= 1) { mn0 = min(mn0, __shfl_xor(mn0, o)); mx1 = max(mx1, __shfl_xor(mx1, o)); mn1 = min(mn1, __shfl_xor(mn1, o)); sum += __shfl_xor(sum, o); cnt += __shfl_xor(cnt, o); }
  int X = PROBE_OP == 0 ? mx1 : (PROBE_OP == 1 ? sum / max(cnt, 1) : mn1);
  if (PROBE_K == 102) X = mn0;
  X = (PROBE_K >= 29 && PROBE_K <= 31) ? mn0 + X * PROBE_MUL : mn0 + (X - mn0) * PROBE_MUL;
  const u64 tg = pt0 + (u64)(long long)(X + 8000);
  for (int i = 0; i < 400000 && wall_clock64() < tg; ++i) __builtin_amdgcn_s_sleep(1);
}
__global__ void __launch_bounds__(512, 2) fused_kernel(const u16* __restrict__ x, const u16* __restrict__ kc, const u16* __restrict__ vc,
                                                       const u16* __restrict__ wq, const u16* __restrict__ wo, const u16* __restrict__ w1,
                                                       const u16* __restrict__ w3, const u16* __restrict__ w2, const u16* __restrict__ cnd,
                                                       const u16* __restrict__ wmod, unsigned char* ws, u16* __restrict__ out PRM_ARG) {
  __shared__ __attribute__((aligned(256))) Smem sm;
  constexpr Prm P = PRM0;
  const int TEND = P.TEND;
  const u64 pt0 = wall_clock64(); u64 pst = (PROBE_K == 100 || PROBE_K == 102) ? pt0 : 0; const u64 pmin = pt0;
  const u32 t0 = (u32)pt0;
  const int bid = blockIdx.x, xcd = bid & 7, idx = bid >> 3, part = idx & 3;
  const int tid = threadIdx.x, lane = tid & 63, wave = __builtin_amdgcn_readfirstlane(tid >> 6);
  u32 tch2 = 0;
  if ((idx | 4) == 7) {
    u64 pc; u32 len;
    asm volatile("s_getpc_b64 %0\n.Lcpf_first:\n\ts_mov_b32 %1, .Lend_a-.Lcpf_first\n\ts_max_i32 %1, %1, .Lend_b-.Lcpf_first\n\ts_max_i32 %1, %1, .Lend_c-.Lcpf_first\n\ts_max_i32 %1, %1, .Lend_d-.Lcpf_first\n\ts_max_i32 %1, %1, .Lend_e-.Lcpf_first" : "=s"(pc), "=s"(len) :: "scc");
    tch2 = __builtin_amdgcn_raw_buffer_load_b32(mk_rsrc((const void*)pc, len), tid * 64, 0, 0);
  }
  SB();
  const bool p3 = part == 3;
  typedef __attribute__((address_space(4))) const u64* kap_t;
  const u64 wa = ((kap_t)__builtin_amdgcn_kernarg_segment_ptr())[((0x5934 >> (part * 4)) & 15) + (p3 ? wave & 1 : 0)];
  const u32 te0 = TAB.e[2 * bid], te1 = TAB.e[2 * bid + 1];
  u32 pitch = (u32)(0x005600c001000020ull >> (part * 16) & 0xffff) * 32u;
  const int sh = p3 ? 2 : 3;
  u32 wv0 = (u32)((p3 ? (wave >> 1) * 128 : wave * 64) + (part == 0 ? xcd * 512 : 0) + (lane >> sh)) * pitch + ((lane & (p3 ? 3 : 7)) ^ (lane >> (p3 ? 5 : 3))) * 16u;
  const u32 wimg = (u32)(uintptr_t)(&sm.wbuf[0][0][0]);
  u32 wsz = (part == 0 ? 4096u : 512u) * pitch, wld = wimg + (p3 ? (wave & 1) * 32768 + (wave >> 1) * 8192 : wave * 8192);
  asm volatile("" : "+v"(wv0), "+s"(wsz), "+s"(wld), "+s"(pitch));
  asm volatile("" :: "s"(x), "s"(kc), "s"(vc), "s"(w2), "s"(cnd), "s"(ws), "s"(out), "s"(wa), "s"(te0), "s"(te1));
  const int bl0 = te0 & 127, nl = te0 >> 7 & 63, blk = te0 >> 13 & 63;
  const bool has_w = te0 >> 19 & 1;
  const rsrc_t rw = mk_rsrc((const void*)wa, wsz);
  const u32 wstep = pitch << (6 - sh), wflip = p3 ? 32u : 0u;
  u32 wso = blk << (sh + 4);
  int wleft = 8;
  auto piece = [&]() {
    dma16(rw, wv0, wso, wld);
    wso += wstep; wld += 1024;
    wv0 ^= (wleft & 1) ? wflip : 0u;
    --wleft;
  };
  const u32 tka = (u32)wall_clock64();
  if (has_w) piece();
  SB();
  STAMP(23);
  const int b = idx >> 2 & 1, head = xcd * 4 + (((idx >> 3) & 1) << 1 | idx >> 4);
  const rsrc_t wsr = mk_rsrc(ws, WS_END);
  const int g = lane >> 4, c = lane & 15;
  const int i8 = idx >> 2, wi = i8 * 8 + xcd;
  const bool is_up = p3 && has_w, modc = part == 2 && wi < 16;
  const size_t base = ((size_t)(b * NH + head) * SK + (SK - WIN)) * DH;
  const rsrc_t rk = mk_rsrc(kc + base, WIN * DH * 2), rv = mk_rsrc(vc + base, WIN * DH * 2);
  u32 sot;
  {
    const u32 s0 = bl0 + lane;
    u32 sb = s0 & 96u;
#pragma unroll
    for (int k = 0; k < 5; ++k) sb |= (s0 >> k & 1u) << (P.PM >> (4 * k) & 7);
    const u32 src = (sb >> 5 & 3u) | (u32)(b * 32 + head) << 2;
    sb ^= (__builtin_popcount(src & P.M0) & 1) | (__builtin_popcount(src & P.M1) & 1) << 1 | (__builtin_popcount(src & P.M2) & 1) << 2 | (__builtin_popcount(src & P.M3) & 1) << 3 | (__builtin_popcount(src & P.M4) & 1) << 4;
    sot = (sb & 7u) << 10 | (u32)wave << 13 | (sb >> 3) << 16;
    sot = lane < nl ? sot : OOB;
    PIN(sot);
  }
  u32x4 kr[NL];
  int tau = te1 & 0xffff;
  const int tauw = te1 >> 16;
  int due = 0, now = 0;
  auto gate = [&]() {
    while (now < due) { now = (int)(((u32)wall_clock64() - t0) << 8); if (now < due) __builtin_amdgcn_s_sleep(1); }
    due += tau;
  };
  auto until = [&](int t) { while ((int)(((u32)wall_clock64() - t0) << 8) < t); };
  const u32 pslot = G_P + ((b * 32 + head) * 4 + part) * 256;
  const rsrc_t rx = mk_rsrc(part == 2 ? cnd : x, (part == 1 || (part == 2 && has_w)) ? 2048u : 0u);
  u32x2 ep; u32 xs0, xs1, tch1; u32x4 xr0, xr1;
  auto small_ops = [&]() {
    ep = ld8<AUX_SC1>(wsr, wave == 7 ? WS_EPOCH : OOB, 0);
    xs0 = __builtin_amdgcn_raw_buffer_load_b16(rx, tid * 2, 0, 0); xs1 = __builtin_amdgcn_raw_buffer_load_b16(rx, tid * 2, 1024, 0);
    xr0 = ld16<0>(rx, part == 1 ? lane * 16 : OOB, 0); xr1 = ld16<0>(rx, part == 1 ? lane * 16 : OOB, 1024);
    tch1 = __builtin_amdgcn_raw_buffer_load_b32(mk_rsrc(w2 + (size_t)blk * 32 * 512, is_up ? 64u : 0u), tid == 384 ? 0 : OOB, 0, AUX_NT);
  };
  if (modc) __builtin_amdgcn_raw_buffer_store_b128(u32x4{SENT, SENT, SENT, SENT}, wsr, tid < 320 ? G_P + wi * 5120 + tid * 16 : OOB, 0, AUX_SC1);
  if (has_w) {
    if (modc) now = 1 << 30;
#pragma nounroll
    while (wleft > (int)(te0 >> 20)) { due += tauw; gate(); due -= tau; piece(); }
    due += tauw; now = 0;
  }
  SB();
  u32 tag = 0;
  u32x4 bf[8];
  auto wo_prep = [&](int s0 = 0, int s1 = 2) {
    const u32 wi_ = wimg + wave * 8192;
    const int q = (lane >> 2) & 3, pp = lane & 3;
#pragma unroll
    for (int s = s0; s < s1; ++s) {
#pragma unroll
      for (int t = 0; t < 4; ++t) {
        const int r0 = s * 32 + g * 8 + q, r1 = r0 + 4, ch = t * 2 + (pp >> 1);
        const u32x2 lo = tr_read(wi_ + r0 * 128 + ((ch ^ (r0 & 7)) * 16) + (pp & 1) * 8), hi = tr_read(wi_ + r1 * 128 + ((ch ^ (r1 & 7)) * 16) + (pp & 1) * 8);
        bf[s * 4 + t] = u32x4{lo[0], lo[1], hi[0], hi[1]};
      }
    }
  };
  auto gemv_go = [&](u32 hh) {
    sm.hb[tid] = (u16)hh; sm.hb[512 + tid] = (u16)(hh >> 16);
    WAVE_SYNC();
    const u32x4* a = (c & 11) == 0 ? reinterpret_cast<const u32x4*>(&sm.hb[(c >> 2) * 512 + wave * 64 + g * 8]) : &sm.zero[0];
    const u32x4 a0 = a[0], a1 = a[(c & 11) == 0 ? 4 : 0];
    const f32x4 z = {0.f, 0.f, 0.f, 0.f};
    f32x4 d[4];
#pragma unroll
    for (int t = 0; t < 4; ++t) d[t] = mfma16(a0, bf[t], z);
#pragma unroll
    for (int t = 0; t < 4; ++t) d[t] = mfma16(a1, bf[4 + t], d[t]);
    if (lane < 32) {
#pragma unroll
      for (int t = 0; t < 4; ++t) sm.r2[(t * 16 + c) * 20 + g * 8 + wave] = d[t][0];
    }
  };
  auto mod_stage = [&]() {
    wo_prep();
    gemv_go(pack_bf(silu_f(lo_f(xs0)), silu_f(lo_f(xs1))));
    __builtin_amdgcn_s_waitcnt(0xc07f);
    if (lane == 0) *LDSP(volatile u32, (u32)(uintptr_t)&sm.mflag[wave]) = 1u;
    if (wave == 7) {
#pragma nounroll
      for (int spin = 0; spin < SPIN_MAX; ++spin) {
        const u32 f = *LDSP(volatile u32, (u32)(uintptr_t)&sm.mflag[lane & 7]);
        if (all_ok(f != 0u)) break;
        __builtin_amdgcn_s_sleep(1);
      }
      CFENCE();
      st8_sc1(wsr, G_MOD + (blk * 64 + lane) * 8, pack_bf(sum8(&sm.r2[lane * 20]), sum8(&sm.r2[lane * 20 + 8])), tag);
    }
    STAMP(9);
  };
  if (lane < 4) sm.zero[lane] = u32x4{0u, 0u, 0u, 0u};
  if (part == 2 && has_w) {
    if (tid < 8) sm.mflag[tid] = 0u;
    LDS_BARRIER();
    if (__builtin_expect(modc, 0)) {
      small_ops();
      asm volatile("s_waitcnt vmcnt(0)" ::: "memory");
      tag = next_tag(__builtin_amdgcn_readfirstlane(ep[0]));
      mod_stage();
      __builtin_amdgcn_s_barrier();
    }
  }
  SB();
  const int fd = wi * 8 + (lane & 7);
  const int ub = wave >> 2, ud = (wave & 3) * 128 + c * 8 + g * 2;
  u32x4 sg[3]; u32 sx2;
  auto side_loads = [&]() {
    const bool fin = part == 1 && wave == 0;
    sg[0] = ld16<AUX_SC1>(wsr, is_up ? G_MOD + (1024 + ud) * 8 : (fin ? G_MOD + (1024 + fd) * 8 : OOB), 0);
    sg[1] = ld16<AUX_SC1>(wsr, is_up ? G_MOD + (1536 + ud) * 8 : (fin ? G_MOD + (2560 + fd) * 8 : OOB), 0);
    sg[2] = ld16<AUX_SC1>(wsr, is_up ? G_MOD + (2048 + ud) * 8 : OOB, 0);
    sx2 = __builtin_amdgcn_raw_buffer_load_b32(mk_rsrc(x, (is_up || fin) ? 2048u : 0u), is_up ? ub * 1024 + ud * 2 : (g & 1) * 1024 + (fd >> 1) * 4, 0, 0);
  };
  auto side_pin = [&]() { PIN(sg[0]); PIN(sg[1]); PIN(sg[2]); PIN(sx2); };
  const u32 mlo = G_ML + (head * 2 + b) * 64 + (lane & 3) * 16;
  float fmrg = 0.f;
  bool fok = wave != 0;
  auto merge_factor = [&](const u32x4& ml) {
    const float mi = u2f(ml[0]);
    float M = fmaxf(mi, dpp<XOR1>(mi)); M = fmaxf(M, dpp<XOR2>(M));
    const float e = __expf(mi - M);
    float L = e * u2f(ml[2]);
    L += dpp<XOR1>(L); L += dpp<XOR2>(L);
    fmrg = rdl(e * __builtin_amdgcn_rcpf(L), part);
  };
  f32x2 ga = {0.f, 0.f}, xin = {0.f, 0.f}, cc1 = {0.f, 0.f};
  float Af = 0.f, Bf = 0.f;
  const int hpos = ub * 512 + ud;
  auto up_prep = [&]() {
    if (!all_ok((sg[0][1] == tag) & (sg[0][3] == tag) & (sg[1][1] == tag) & (sg[1][3] == tag) & (sg[2][1] == tag) & (sg[2][3] == tag))) gather16<3>(wsr, G_MOD + (1024 + ud) * 8, 512 * 8, tag, sg);
    ga = f32x2{ub ? hi_f(sg[0][0]) : lo_f(sg[0][0]), ub ? hi_f(sg[0][2]) : lo_f(sg[0][2])};
    cc1 = f32x2{rbf(1.f + (ub ? hi_f(sg[2][0]) : lo_f(sg[2][0]))), rbf(1.f + (ub ? hi_f(sg[2][2]) : lo_f(sg[2][2])))};
    xin = f32x2{lo_f(sx2), hi_f(sx2)};
    u32* hp = reinterpret_cast<u32*>(&sm.hb[hpos]);
    hp[512] = pack_bf(cc1[0], cc1[1]);
    hp[1024] = ub ? perm_hi(sg[1][0], sg[1][2]) : perm_lo(sg[1][0], sg[1][2]);
  };
  auto up_frags = [&]() {
    const int pp = lane & 3;
    const u32 a = wimg + (pp & 1) * 32768 + ((pp >> 1) * 32 + g * 8 + ((lane >> 2) & 3)) * 64 + (((wave >> 1) ^ ((g & 1) | (pp & 2))) * 16) + (wave & 1) * 8;
#pragma unroll
    for (int rb = 0; rb < 8; ++rb) {
      const u32x2 lo = tr_read(a + rb * 4096), hi = tr_read(a + rb * 4096 + 256);
      bf[rb] = u32x4{lo[0], lo[1], hi[0], hi[1]};
    }
  };
  const u32x4* ap = reinterpret_cast<const u32x4*>(&sm.hb[((lane >> 3) & 1) * 512 + (lane & 1) * 32 + g * 8]);
  const bool par = lane & 8;
  float l0 = 0.f, l1 = 0.f;
  u32x2 gmd[2], tbg;
  int t1 = 0;
#pragma unroll
  for (int i = NCMIN / 8; i < NL; ++i) kr[i] = u32x4{0u, 0u, 0u, 0u};
  sfor<0, NL>([&](auto ic) {
    constexpr int i = ic.v;
    if (i >= NCMIN / 8 && i >= nl) return false;
    const u32 so = __builtin_amdgcn_readlane(sot, i);
    gate();
    SB();
    kr[i] = ld16<AUX_NT>(rk, lane * 16, so);
    if constexpr (i == 0) small_ops();
    if constexpr (i == TBL) tbg = ld8<AUX_SC1>(wsr, wave == 7 ? G_TB + xcd * 128 + (lane & 7) * 8 : OOB, 0);
    if constexpr (i == QP) { gmd[0] = ld8<AUX_SC1>(wsr, part == 1 ? G_MOD + tid * 8 : OOB, 0); gmd[1] = ld8<AUX_SC1>(wsr, part == 1 ? G_MOD + tid * 8 : OOB, 4096); }
    SB();
    SB();
    if (i >= WK) { WAIT_VM(WK); SB(); }
    if (i == 0) STAMP(25);
    if (i == 8) STAMP(12);
    if (i == 20) STAMP(21);
    if (i == 25) STAMP(22);
    if (i == TBP) {
      if (part == 3 && i8 == 0 && wave == 7) { tag = next_tag(__builtin_amdgcn_readfirstlane(ep[0])); if (lane < 8) st8_sc1(wsr, G_TB + lane * 128 + xcd * 8, t0 + (((tka - t0) * P.GA) >> 3) - P.GS, tag); }
      SB();
    }
    if (i == TBU) {
      if (wave == 7) {
        const bool ok = tbg[1] == tag;
        float lf = ok ? (float)min(max((int)(t0 - tbg[0]), -300), 300) : 0.f, nf = ok ? 1.f : 0.f;
        lf += dpp<XOR1>(lf); nf += dpp<XOR1>(nf); lf += dpp<XOR2>(lf); nf += dpp<XOR2>(nf); lf += dpp<XOR7>(lf); nf += dpp<XOR7>(nf);
        if (lane == 0) sm.bc = u32x2{tag, t0 - (u32)(int)(lf * __builtin_amdgcn_rcpf(fmaxf(nf, 1.f)))};
      }
      LDS_BARRIER();
      const u32x2 bc = sm.bc;
      tag = __builtin_amdgcn_readfirstlane(bc[0]);
      t1 = (__builtin_amdgcn_readfirstlane((int)(bc[1] - t0)) + TEND + P.X1) * 256;
      tau -= __builtin_amdgcn_readfirstlane((int)((float)(int)(t0 - bc[1]) * 256.f * __builtin_amdgcn_rcpf((float)(2 * nl - TBU - 2))));
      SB();
    }
    if (i == LNS) {
      tag = next_tag(__builtin_amdgcn_readfirstlane(ep[0]));
      if (part == 1) {
        float x0[8], x1[8], mb0, ri0, mb1, ri1;
#pragma unroll
        for (int k = 0; k < 4; ++k) { x0[2 * k] = lo_f(xr0[k]); x0[2 * k + 1] = hi_f(xr0[k]); x1[2 * k] = lo_f(xr1[k]); x1[2 * k + 1] = hi_f(xr1[k]); }
        ln_stats2(x0, x1, mb0, ri0, mb1, ri1);
        l0 = rbf(rbf(lo_f(xs0) - mb0) * ri0); l1 = rbf(rbf(lo_f(xs1) - mb1) * ri1);
      }
      PIN(l0); PIN(l1);
      SB();
    }
    if (i == WUSE) {
      PIN(tch1); PIN(tch2);
      if ((part == 1 || (part == 2 && has_w && !modc))) {
        WAIT_VM(WK); CFENCE();
        if (part == 2) mod_stage(); else wo_prep();
      }
#pragma unroll
      for (int k = 0; k < 8; ++k) PIN(bf[k]);
      SB();
    }
    if (i == QP + QD) {
      if (part == 1) {
        u32 gm[2] = {gmd[0][0], gmd[1][0]};
        if (!all_ok((gmd[0][1] == tag) & (gmd[1][1] == tag))) { STAMP(11); gather8<2>(wsr, G_MOD + tid * 8, 512 * 8, tag, gm); }
        STAMP(5);
        gemv_go(pack_bf(rbf(l0 * rbf(1.f + lo_f(gm[1]))) + lo_f(gm[0]), rbf(l1 * rbf(1.f + hi_f(gm[1]))) + hi_f(gm[0])));
      }
      SB();
    }
    if (i == QP + QD + 1) {
      if (part == 1) {
        LDS_BARRIER();
        if (wave == 0) {
          const int bb = lane >> 5, jq = lane & 31;
          st8_sc1(wsr, G_Q + (bb * 2048 + blk * 32 + jq) * 8, pack_bf(sum8(&sm.r2[2 * jq * 20 + bb * 8]), sum8(&sm.r2[(2 * jq + 1) * 20 + bb * 8])), tag);
        }
        STAMP(6);
      }
      SB();
    }
    return true;
  });
  PIN(sot);
  const u32 qoff = G_Q + ((b * 32 + head) * 64 + c * 4) * 8;
  u32x4 qg[2], vr[NL], fl2[3];
  u32 qq[4];
  constexpr int NB = (NL + 15) / 16;
  float xlo, sc[NB], pr[NB], m = 0.f;
  f32x4 acc = {0.f, 0.f, 0.f, 0.f};
  u32 pb[NB], pm[4], cpf;
  const u32 adst0 = lds_addr(lane < 32 ? &sm.red[wave][c * 8 + g * 4] : &sm.r2[(lane - 32) * 4]);
  u32 po, rdst, rsrc, xo;
  auto wo_addr = [&]() {
    po = G_P + (((c >> 2 & 1) * 32 + xcd * 4 + (wave >> 1)) * 4 + (c & 3)) * 256 + (wave & 1) * 128 + (c >> 3) * 64 + g * 16;
    rdst = lane < 32 ? (u32)(uintptr_t)&sm.red2[wave][0] + lane * 16 : (u32)(uintptr_t)&sm.r2[0] + (lane - 32) * 16;
    rsrc = (u32)(uintptr_t)&sm.red2[0][((wave & 1) * 16 + (lane & 15)) * 4 + (lane >> 4)];
    xo = (lane & 1) ? OOB : G_XP + xcd * 2048 + (wave & 1) * 1024 + (blk * 64 + lane) * 2;
    PIN(po); PIN(rdst); PIN(rsrc); PIN(xo);
  };
  u32 pso = wave == 0 ? pslot + lane * 4 : OOB;
  PIN(pso);
  const u32 nadd = (te0 >> 20) ? P.LW : (is_up ? P.LU : 0);
  u32 nto = tid == 0 ? G_NT + bid * 8 : OOB;
  PIN(nto);
  auto notices = [&](auto nc_, u32 off, u32x4 (&gn)[nc_.v]) -> int {
    constexpr int N = nc_.v;
#pragma nounroll
    for (int spin = 0; spin < SPIN_MAX; ++spin) {
      bool ok = true;
#pragma unroll
      for (int i = 0; i < N; ++i) ok &= (gn[i][1] == tag) & (gn[i][3] == tag);
      if (all_ok(ok)) break;
      __builtin_amdgcn_s_sleep(10);
      CFENCE();
#pragma unroll
      for (int i = 0; i < N; ++i) gn[i] = ld16<AUX_SC1>(wsr, off, i * 1024);
    }
    int mx = 0;
#pragma unroll
    for (int i = 0; i < N; ++i) mx = max(mx, max((int)(gn[i][0] - t0), (int)(gn[i][2] - t0)));
    return __builtin_amdgcn_readfirstlane((int)wave_max((float)min(mx, 1 << 20)));
  };
  const int side = nl >= SIDE + 13 ? SIDE + 8 : (nl >= SIDE + 9 ? SIDE + 4 : SIDE);
  sfor<0, NL + WV>([&](auto ic) {
    constexpr int v = ic.v;
    if (cons_end(v - 1) >= NCMIN / 8 && cons_end(v - 1) >= nl) return false;
    if constexpr (v < NL) {
      const u32 so = __builtin_amdgcn_readlane(sot, v);
      if constexpr (v >= 12) { if (v == nl - P.NE) tau = (tau * P.FE) >> 4; }
      if constexpr (v >= NCMIN / 8) { if (v < nl) gate(); } else gate();
      SB();
      vr[v] = ld16<AUX_NT>(rv, lane * 16, so);
      SB();
      SB();
    }
    if constexpr (v == NCMIN / 8 - 1) {
#pragma unroll
      for (int i = NCMIN / 8; i < NL; ++i) asm volatile("" : "=v"(vr[i]));
      asm volatile("" : "=v"(fl2[1]), "=v"(fl2[2]));
    }
    if constexpr (v == VQ - 4) { qg[0] = ld16<AUX_SC1>(wsr, qoff, 0); qg[1] = ld16<AUX_SC1>(wsr, qoff + 16, 0); }
    if constexpr (v == VSIDE) side_loads();
    if constexpr (v == CPF) {
      u64 pc; u32 dlt, len;
      asm volatile("s_getpc_b64 %0\n.Lcpf_here:\n\ts_mov_b32 %1, .Ltail_start-.Lcpf_here\n\ts_mov_b32 %2, .Lend_a-.Ltail_start\n\ts_max_i32 %2, %2, .Lend_b-.Ltail_start\n\ts_max_i32 %2, %2, .Lend_c-.Ltail_start\n\ts_max_i32 %2, %2, .Lend_d-.Ltail_start\n\ts_max_i32 %2, %2, .Lend_e-.Ltail_start" : "=s"(pc), "=s"(dlt), "=s"(len) :: "scc");
      pc += dlt;
      const rsrc_t rc = mk_rsrc((const void*)pc, (part == 3 && i8 == 1) ? len : 0u);
      cpf = __builtin_amdgcn_raw_buffer_load_b32(rc, tid * 64, 0, 0);
    }
    if constexpr (v == CPF + WV + 1) PIN(cpf);
    if constexpr (v == SIDE || v == SIDE + 4 || v == SIDE + 8) fl2[(v - SIDE) / 4] = ld16<AUX_SC1>(wsr, (wave == 0 && v == side) ? mlo : OOB, 0);
    SB();
    if constexpr (v < WV) { WAIT_VM(WK); SB(); } else if constexpr (v < NL && cons_end(v) < v - WV + 1) { WAIT_VM(WV); SB(); }
    if constexpr (v == VQ) {
      if (!all_ok((qg[0][1] == tag) & (qg[0][3] == tag) & (qg[1][1] == tag) & (qg[1][3] == tag))) { STAMP(20); gather16<2>(wsr, qoff, 16, tag, qg); }
      STAMP(1); WSTAMP(1);
      qq[0] = qg[0][0]; qq[1] = qg[0][2]; qq[2] = qg[1][0]; qq[3] = qg[1][2];
    }
    if constexpr (v >= VQ && v < VS) {
      constexpr int sl = v - VQ, bt = sl >> 1;
      float xs[8];
#pragma unroll
      for (int i = 0; i < 8; ++i) {
        float d = 0.f;
        if (sl * 8 + i < NL) {
#pragma unroll
          for (int k = 0; k < 4; ++k) d = dot2(kr[sl * 8 + i][k], qq[k], d);
        }
        xs[i] = d;
      }
#pragma unroll
      for (int h = 0; h < 4; ++h) xs[h] = sel_add<XOR7>(xs[h], xs[h + 4], c & 4);
#pragma unroll
      for (int h = 0; h < 2; ++h) xs[h] = sel_add<XOR2>(xs[h], xs[h + 2], c & 2);
      xs[0] = sel_add<XOR1>(xs[0], xs[1], c & 1);
      if constexpr ((sl & 1) || sl == NSL - 1) {
        const float t = (sl & 1) ? sel_add<XOR8>(xlo, xs[0], c & 8) : xs[0] + dpp<XOR8>(xs[0]);
        const float s = rbf(rbf(t) * 0.08838834764831845f);
        sc[bt] = (bt * 16 + c < nl) ? s : -__builtin_inff();
        PIN(sc[bt]);
      } else {
        xlo = xs[0];
        PIN(xlo);
      }
    }
    if constexpr (v == VS) {
      float mloc = sc[0];
#pragma unroll
      for (int bt = 1; bt < NB; ++bt) mloc = fmaxf(mloc, sc[bt]);
      m = fmaxf(wave_max(mloc), -3.0e38f);
      float ls = 0.f;
#pragma unroll
      for (int bt = 0; bt < NB; ++bt) { pr[bt] = __expf(sc[bt] - m); ls += pr[bt]; }
      const float l = wave_sum(ls);
      if (lane == 0) { sm.ml[wave][0] = m; sm.ml[wave][1] = l; }
      STAMP(2); WSTAMP(2);
    }
    sfor<cons_end(v - 1), cons_end(v)>([&](auto jc) {
      constexpr int i = jc.v;
      if (i < NCMIN / 8 || i < nl) {
        const u32 pv = __float_as_uint(bcast16(u2f(pb[i >> 4]), i & 15));
        acc = mfma16(u32x4{pv & pm[0], pv & pm[1], pv & pm[2], pv & pm[3]}, vr[i], acc);
      }
      PIN(acc);
      return true;
    });
    if constexpr (v == MLS - 1) { side_pin(); if (is_up) up_prep(); PIN(ga); PIN(xin); PIN(cc1); }
    if constexpr (v == UPREP - 1) wo_addr();
    if constexpr (v == UPREP) {
#pragma unroll
      for (int k = 0; k < 8; ++k) asm volatile("" : "=v"(bf[k]));
      if (part == 0) wo_prep(0, 1); else if (is_up) up_frags();
#pragma unroll
      for (int k = 0; k < 8; ++k) PIN(bf[k]);
    }
    if constexpr (v == UPREP + 1) {
      if (is_up) {
        const u32x4* a1 = ap + 128 + ((lane >> 1) & 1) * 128;
        f32x4 d0 = {0.f, 0.f, 0.f, 0.f}, d1 = {0.f, 0.f, 0.f, 0.f};
#pragma unroll
        for (int rb = 0; rb < 4; ++rb) { d0 = mfma16(a1[rb * 8], bf[rb], d0); d1 = mfma16(a1[(rb + 4) * 8], bf[rb + 4], d1); }
        Af = par ? d0[1] + d1[1] : d0[0] + d1[0]; Bf = par ? d0[3] + d1[3] : d0[2] + d1[2];
        Af += dpp<XOR8>(Af); Bf += dpp<XOR8>(Bf);
      }
      PIN(Af); PIN(Bf);
    }
    if constexpr (v == SIDE + WV + 1 || v == SIDE + WV + 5 || v == SIDE + WV + 9) {
      if (wave == 0 && v == side + WV + 1) {
        const u32x4 ml = fl2[(v - SIDE - WV - 1) / 4];
        fok = all_ok((ml[1] == tag) & (ml[3] == tag));
        merge_factor(ml);
      }
    }
    if (v % 6 == 4 && v >= 10 && v < 30) STAMP(15 + v / 6);
    if constexpr (v == MLS) {
      LDS_BARRIER();
      float mm[8], ll[8];
#pragma unroll
      for (int w = 0; w < 8; ++w) { const f32x2 t = *reinterpret_cast<const f32x2*>(&sm.ml[w][0]); mm[w] = t[0]; ll[w] = t[1]; }
      float Mwg = mm[0], Lwg = 0.f;
#pragma unroll
      for (int w = 1; w < 8; ++w) Mwg = fmaxf(Mwg, mm[w]);
      if (wave == 0) {
#pragma unroll
        for (int w = 0; w < 8; ++w) Lwg = fmaf(__expf(mm[w] - Mwg), ll[w], Lwg);
        if (lane == 0) st16_sc1(wsr, G_ML + ((head * 2 + b) * 4 + part) * 16, __float_as_uint(Mwg), __float_as_uint(Lwg), tag);
      }
      const float fw = __expf(m - Mwg);
#pragma unroll
      for (int bt = 0; bt < NB; ++bt) { pb[bt] = pack_bf(pr[bt] * fw, pr[bt] * fw); PIN(pb[bt]); }
#pragma unroll
      for (int k = 0; k < 4; ++k) { pm[k] = (c >> 1) == k ? ((c & 1) ? 0xffff0000u : 0x0000ffffu) : 0u; PIN(pm[k]); }
    }
    SB();
    return true;
  });
  asm volatile(".Ltail_start:");
  *LDSP(f32x4, adst0) = acc;
  STAMP(14); WSTAMP(14);
  if (!fok) {
    u32x4 ml;
#pragma nounroll
    for (int spin = 0; spin < SPIN_MAX; ++spin) {
      CFENCE();
      ml = ld16<AUX_SC1>(wsr, mlo, 0);
      if (all_ok((ml[1] == tag) & (ml[3] == tag))) break;
    }
    merge_factor(ml);
  }
  STAMP(15);
  LDS_BARRIER();
  STAMP(3);
  {
    u32 pw = 0;
    if (wave == 0) {
      f32x2 t[8];
#pragma unroll
      for (int w = 0; w < 8; ++w) t[w] = *reinterpret_cast<const f32x2*>(&sm.red[w][2 * lane]);
      const f32x2 o = ((t[0] + t[1]) + (t[2] + t[3])) + ((t[4] + t[5]) + (t[6] + t[7]));
      pw = min(pack_bf(o[0] * fmrg, o[1] * fmrg), SENT - 1u);
    }
    __builtin_amdgcn_raw_buffer_store_b32(pw, wsr, pso, 0, AUX_SC1);
    st8_sc1(wsr, nto, (u32)wall_clock64() + nadd, tag);
    SB();
#pragma unroll
    for (int i = NCMIN / 8; i < NL; ++i) PIN(vr[i]);
    PIN(fl2[0]); PIN(fl2[1]); PIN(fl2[2]);
  }
  STAMP(4);

  if (part == 2) FINISH();
  if (part == 0) {
    __builtin_amdgcn_s_barrier();
#pragma nounroll
    while (wleft > 0) piece();
    until(t1);
    STAMP(11);
    CFENCE();
    u32x4 og[2];
    og[0] = ld16<AUX_SC1>(wsr, po, 0);
    SB();
    WAIT_VM(1); CFENCE();
    wo_prep(1, 2);
#pragma unroll
    for (int k = 4; k < 8; ++k) PIN(bf[k]);
    SB();
    f32x4 d[4];
    auto wo_mm = [&]() {
      const f32x4 z = {0.f, 0.f, 0.f, 0.f};
#pragma unroll
      for (int t = 0; t < 4; ++t) { d[t] = mfma16(og[0], bf[t], z); og[1][t] = __float_as_uint(dpp<XOR8>(u2f(og[0][t]))); }
      SB();
#pragma unroll
      for (int t = 0; t < 4; ++t) d[t] = mfma16(og[1], bf[4 + t], d[t]);
    };
    auto p_bad = [&]() { return !all_ok(max(max(og[0][0], og[0][1]), max(og[0][2], og[0][3])) != SENT); };
    wo_mm();
    if (__builtin_expect(p_bad(), 0)) {
#pragma nounroll
      for (int spin = 0; spin < SPIN_MAX; ++spin) {
        __builtin_amdgcn_s_sleep(1);
        CFENCE();
        og[0] = ld16<AUX_SC1>(wsr, po, 0);
        if (!p_bad()) break;
      }
      wo_mm();
      SB();
      asm volatile(".Lend_d:");
    }
    STAMP(5); WSTAMP(5);
    {
      SB();
      f32x4 y; f32x2 h[4];
#pragma unroll
      for (int t = 0; t < 4; ++t) { h[t] = d[t].xy + d[t].zw; PIN(h[t]); }
#pragma unroll
      for (int t = 0; t < 4; ++t) { y[t] = h[t][0] + h[t][1]; PIN(y[t]); }
      *LDSP(f32x4, rdst) = y;
    }
    STAMP(16);
    LDS_BARRIER();
    STAMP(17);
    if (wave < 2) {
      f32x2 v[4];
#pragma unroll
      for (int w = 0; w < 4; ++w) v[w] = f32x2{LDSP(float, rsrc)[w * 256], LDSP(float, rsrc)[w * 256 + 128]};
      const f32x2 h = (v[0] + v[1]) + (v[2] + v[3]);
      const float y = h[0] + h[1];
      __builtin_amdgcn_raw_buffer_store_b32(min(pack_bf(y, dpp<XOR1>(y)), SENT - 1u), wsr, xo, 0, AUX_SC1);
      asm volatile(".Lend_a:");
    }
    STAMP(6);
    FINISH();
  }
  if (part == 3) {
    if (!has_w) FINISH();
    u32x4 b2[4];
    {
      __builtin_amdgcn_s_barrier();
      const rsrc_t r2 = mk_rsrc(w2 + (size_t)(blk * 32 + wave * 4) * 512, 4096u);
      const u32 l2 = (u32)(uintptr_t)(&sm.w2b[wave * 4][0]);
#pragma unroll
      for (int n = 0; n < 4; ++n) dma16(r2, (lane ^ (n * 2) ^ ((wave & 2) << 2)) * 16, n * 1024, l2 + n * 1024);
    }
    const u32 tdst = lds_addr(&sm.hb[hpos]);
    const u32 sdst = lds_addr(&sm.stat[wave][lane]);
    const u32 ssrc = lds_addr(&sm.stat[(g >> 1) * 4 + ((lane >> 1) & 3)][(lane & 1) * 16]);
    const u32 adst = lds_addr((c < 4 && (g & 1) == 0) ? &sm.actl[g >> 1][wave * 4 + c] : reinterpret_cast<u16*>(&sm.dummy[lane]));
    const u32 asrc = lds_addr(&sm.actl[lane & 1][g * 8]);
    const u32 apa = lds_addr(&sm.hb[(c >> 3) * 512 + (c & 3) * 64 + ((c >> 2) & 1) * 32 + g * 8]);
    u32 xoff = G_XP + g * 2048 + ub * 1024 + (wave & 3) * 256 + c * 16;
    u32x4 one;
#pragma unroll
    for (int k = 0; k < 4; ++k) one[k] = ((c & 2) == 0 && (c >> 2) == k) ? ((c & 1) ? 0x3f800000u : 0x00003f80u) : 0u;
    PIN(one);
    u32 ooff = G_OP + (blk * 512 + tid) * 8;
    float Afp = Af, Bfp = Bf;
    PIN(xoff); PIN(ooff); PIN(Afp); PIN(Bfp);
    PIN(ga); PIN(xin); PIN(cc1);
    u32 w2a = (u32)(uintptr_t)(&sm.w2b[0][0]) + (g * 8 + ((lane >> 2) & 3)) * 1024 + (wave ^ (g & 1)) * 128 + (lane & 15) * 8;
    PIN(w2a);
    if (wave == 1) {
      due = t1 + P.N1 * 256; gate();
      u32x4 gn[2] = {ld16<AUX_SC1>(wsr, G_NT + lane * 16, 0), ld16<AUX_SC1>(wsr, G_NT + lane * 16, 1024)};
      const int tn = notices(IC<2>{}, G_NT + lane * 16, gn) * 256;
      due = (tn > t1 + 2 * 256 ? max(tn, t1 + P.L0 * 256) + P.L1 * 256 : t1) + P.X2 * 256;
      until(due);
    }
    asm volatile("s_waitcnt vmcnt(0)" ::: "memory");
    STAMP(9);
    __builtin_amdgcn_s_barrier();
    STAMP(11);
    CFENCE();
    u32x4 xg[2] = {ld16<AUX_SC1>(wsr, xoff, 0), ld16<AUX_SC1>(wsr, xoff, 8192)};
    SB();
#pragma unroll
    for (int t = 0; t < 4; ++t) {
      const u32x2 lo = tr_read(w2a ^ (t * 32)), hi = tr_read((w2a ^ (t * 32)) + 4096);
      b2[t] = u32x4{lo[0], lo[1], hi[0], hi[1]};
    }
#pragma unroll
    for (int t = 0; t < 4; ++t) PIN(b2[t]);
    SB();
    const f32x4 z4 = {0.f, 0.f, 0.f, 0.f};
    auto xp_bad = [&]() { return !all_ok(max(max(max(xg[0][0], xg[0][1]), max(xg[0][2], xg[0][3])), max(max(xg[1][0], xg[1][1]), max(xg[1][2], xg[1][3]))) != SENT); };
    f32x4 v = mfma16(one, xg[1], mfma16(one, xg[0], z4));
    if (__builtin_expect(xp_bad(), 0)) {
#pragma nounroll
      for (int spin = 0; spin < SPIN_MAX; ++spin) {
        __builtin_amdgcn_s_sleep(1);
        CFENCE();
        xg[0] = ld16<AUX_SC1>(wsr, xoff, 0); xg[1] = ld16<AUX_SC1>(wsr, xoff, 8192);
        if (!xp_bad()) break;
      }
      v = mfma16(one, xg[1], mfma16(one, xg[0], z4));
      SB();
      asm volatile(".Lend_e:");
    }
    STAMP(5); WSTAMP(5);
    {
      const float a0 = v[0], a1 = v[1];
      const float x10 = fmaf(ga[0], a0, xin[0]), x11 = fmaf(ga[1], a1, xin[1]);
      *LDSP(u32, tdst) = pack_bf(x10 * cc1[0], x11 * cc1[1]);
      float u = swap32_add(x10 + x11, fmaf(x10, x10, x11 * x11));
      u += dpp<XOR1>(u); u += dpp<XOR2>(u); u += dpp<XOR7>(u); u += dpp<XOR8>(u);
      *LDSP(float, sdst) = u;
    }
    STAMP(16);
    LDS_BARRIER();
    STAMP(17);
    {
      const f32x4 z = {0.f, 0.f, 0.f, 0.f};
      const u32x4 af0 = LDSP(u32x4, apa)[0], af1 = LDSP(u32x4, apa)[32];
      float s = LDSP(float, ssrc)[0], q = LDSP(float, ssrc)[32];
      SB();
      f32x4 d[4];
#pragma unroll
      for (int j = 0; j < 4; ++j) d[j] = mfma16(af0, bf[j], z);
#pragma unroll
      for (int j = 0; j < 4; ++j) d[j] = mfma16(af1, bf[4 + j], d[j]);
      s += dpp<XOR1>(s); q += dpp<XOR1>(q); s += dpp<XOR2>(s); q += dpp<XOR2>(q); s += dpp<XOR7>(s); q += dpp<XOR7>(q);
      const float mean = s * (1.f / 512.f);
      const float var = fmaf(q, 1.f / 512.f, -mean * mean);
      const float ri = __builtin_amdgcn_rsqf(fmaxf(var, 0.f) + 1e-5f);
      const float cf = fmaf(-ri * mean, Afp, Bfp);
      float y = (d[0][0] + d[1][1]) + (d[2][2] + d[3][3]);
      y = __builtin_bit_cast(float, __builtin_amdgcn_update_dpp(__builtin_bit_cast(int, y), __builtin_bit_cast(int, y), XOR8, 0xa, 0xf, false));
      y = swap16_add(y, y);
      y = fmaf(y, ri, cf);
      const float up = dpp<0x104>(y);
      *LDSP(u16, adst) = (u16)pack_bf(silu_f(y) * up, 0.f);
    }
    STAMP(18);
    LDS_BARRIER();
    STAMP(10);
    {
      const u32x4 af = *LDSP(u32x4, asrc);
      const f32x4 z = {0.f, 0.f, 0.f, 0.f};
      const f32x4 d0 = mfma16(af, b2[0], z), d1 = mfma16(af, b2[1], z), d2 = mfma16(af, b2[2], z), d3 = mfma16(af, b2[3], z);
      const bool g1 = g & 1, g2 = g & 2;
      const float e0 = g1 ? d1[0] : d0[0], e1 = g1 ? d1[1] : d0[1], f0 = g1 ? d3[0] : d2[0], f1 = g1 ? d3[1] : d2[1];
      st8_sc1(wsr, ooff, pack_bf(g2 ? f0 : e0, g2 ? f1 : e1), tag);
    }
    asm volatile(".Lend_b:");
    STAMP(6);
    FINISH();
  }
  if (wave == 0) {
    const int d = fd, ul = lane >> 3;
    u32 gm[2] = {sg[0][0], sg[1][0]};
    if (!all_ok((sg[0][1] == tag) & (sg[1][1] == tag))) gather8<2>(wsr, G_MOD + (1024 + d) * 8, 1536 * 8, tag, gm);
    u32 xa0, xa1;
    {
      due = t1 + P.N2 * 256; gate();
      u32x4 gn[2] = {ld16<AUX_SC1>(wsr, G_NT + lane * 16, 0), ld16<AUX_SC1>(wsr, G_NT + lane * 16, 1024)};
      const int tn = notices(IC<2>{}, G_NT + lane * 16, gn) * 256;
      due = (tn > t1 + 2 * 256 ? max(tn, t1 + P.L0 * 256) + P.L1 * 256 : t1) + P.X3 * 256;
      until(due);
    }
#pragma nounroll
    for (int spin = 0; spin < SPIN_MAX; ++spin) {
      CFENCE();
      xa0 = __builtin_amdgcn_raw_buffer_load_b32(wsr, G_XP + ul * 2048 + (d >> 1) * 4, 0, AUX_SC1);
      xa1 = __builtin_amdgcn_raw_buffer_load_b32(wsr, G_XP + ul * 2048 + (d >> 1) * 4, 1024, AUX_SC1);
      if (all_ok(max(xa0, xa1) != SENT)) break;
      __builtin_amdgcn_s_sleep(1);
    }
    float a0 = (d & 1) ? hi_f(xa0) : lo_f(xa0), a1 = (d & 1) ? hi_f(xa1) : lo_f(xa1);
    a0 += dpp<XOR8>(a0); a1 += dpp<XOR8>(a1);
    a0 = swap16_add(a0, a0); a1 = swap16_add(a1, a1);
    a0 = swap32_add(a0, a0); a1 = swap32_add(a1, a1);
    const bool hb = g & 1;
    float xv = rbf(((d & 1) ? hi_f(sx2) : lo_f(sx2)) + rbf((hb ? hi_f(gm[0]) : lo_f(gm[0])) * rbf(hb ? a1 : a0)));
    float gv = hb ? hi_f(gm[1]) : lo_f(gm[1]);
    PIN(xv); PIN(gv);
    if (wi == 0 && lane == 0) __builtin_amdgcn_raw_buffer_store_b32(tag, wsr, WS_EPOCH, 0, AUX_SC1);
    STAMP(10);
    due = max(due + P.X4 * 256, (int)(((u32)wall_clock64() - t0) << 8) + P.GX * 256);
    const bool last = ul + 40 < NUP;
    u32 o0 = G_OP + (ul * 512 + d) * 8, o5 = last ? o0 : OOB, tg5 = last ? tag : 0u;
    const rsrc_t rout = mk_rsrc(out, 2048u);
    u32 oo = (lane & 40) == 0 ? (g * 512 + d) * 2 : OOB;
    const u32 pk = c == 0 ? 0x00003f80u : (c == 4 ? 0x3f800000u : 0u);
    u32x4 one = {pk, 0u, pk, 0u};
    PIN(o0); PIN(o5); PIN(tg5); PIN(oo); PIN(one);
    __builtin_amdgcn_raw_buffer_store_b16((u16)pack_bf(xv, 0.f), rout, oo, 0, AUX_SC1);
    until(due);
    f32x4 sv;
#pragma nounroll
    for (int spin = 0; spin < SPIN_MAX; ++spin) {
      CFENCE();
      u32x2 og[6];
#pragma unroll
      for (int k = 0; k < 5; ++k) og[k] = ld8<AUX_SC1>(wsr, o0, k * 32768);
      og[5] = ld8<AUX_SC1>(wsr, o5, 5 * 32768);
      sv = f32x4{0.f, 0.f, 0.f, 0.f};
#pragma unroll
      for (int k = 0; k < 3; ++k) sv = mfma16(one, u32x4{og[2 * k][0], og[2 * k][1], og[2 * k + 1][0], og[2 * k + 1][1]}, sv);
      const u32 bad = ((og[0][1] ^ tag) | (og[1][1] ^ tag) | (og[2][1] ^ tag)) | ((og[3][1] ^ tag) | (og[4][1] ^ tag) | (og[5][1] ^ tg5));
      PIN(sv);
      if (all_ok(bad == 0u)) break;
      __builtin_amdgcn_s_sleep(1);
    }
    STAMP(7);
    const float s = sv[0] + dpp<XOR8>(sv[0]);
    __builtin_amdgcn_raw_buffer_store_b16((u16)pack_bf(fmaf(gv, s, xv), 0.f), rout, oo, 0, AUX_SC1);
    asm volatile(".Lend_c:");
  }
  FINISH();
}

extern "C" void kernel_launch(void* const* d_in, const int* in_sizes, int n_in,
                              void* d_out, int out_size, void* d_ws, size_t ws_size,
                              hipStream_t stream) {
  fused_kernel<<<256, 512, 0, stream>>>((const u16*)d_in[0], (const u16*)d_in[1], (const u16*)d_in[2], (const u16*)d_in[3], (const u16*)d_in[4],
                                        (const u16*)d_in[6], (const u16*)d_in[7], (const u16*)d_in[8], (const u16*)d_in[9], (const u16*)d_in[10],
                                        (unsigned char*)d_ws, (u16*)d_out
    );
}
```

```cpp
#include <hip/hip_runtime.h>
#include <stdint.h>

typedef unsigned int u32;
typedef unsigned short u16;
typedef unsigned long long u64;
typedef __attribute__((ext_vector_type(4))) unsigned u32x4;
typedef __attribute__((ext_vector_type(2))) unsigned u32x2;
typedef __attribute__((ext_vector_type(2))) __bf16 bf16x2;
typedef __attribute__((ext_vector_type(4))) float f32x4;
typedef __attribute__((ext_vector_type(2))) float f32x2;
typedef __amdgpu_buffer_rsrc_t rsrc_t;

constexpr int NH = 32, SK = 16384, DH = 128, WIN = 4096, FF = 1376;
struct Prm { int E0, ES, E1, E2, E2C, EU, TEND, DT_UP, L0, L1, X1, X2, X3, X4, NE, FE, M0, M1, M2, M3, M4, GS, GX, N1, N2, GA, WD, DT_WO, LW, LU, EW, PM; };
constexpr Prm PRM0 = {32, 24, 32, 32, 56, 64, 2030, 60, 30, 25, 92, 86, 131, 88, 4, 12, 5, 8, 21, 4, 32, 30, 56, 28, 70, 12, 4, 100, 45, 15, 8, 0x24310};
#define PRM_ARG
#define TABP (&TAB.e[0])
constexpr int NL = 37;
constexpr int NCMIN = 192;
constexpr int WK = 8, WV = 5;
constexpr int WUSE = 12, LNS = 9;
constexpr int QP = 14, QD = 4;
constexpr int VQ = 6;
constexpr int NSL = (NL + 7) / 8;
constexpr int VS = VQ + NSL;
constexpr int MLS = VS + 2;
constexpr int VSIDE = MLS - 5, UPREP = MLS + 8;
constexpr int SIDE = NCMIN / 8 - 4;
constexpr int CPF = 18;
constexpr int TBP = 6, TBL = 10, TBU = 15;
constexpr int cons_end(int v) {
  const int a = v - WV + 1, c = (v - MLS) * 4;
  const int m = a < c ? a : c;
  return m < 0 ? 0 : (m > NL ? NL : m);
}
constexpr int KF = 3;
struct Tab { u32 e[2048]; };
constexpr int popc(u32 v) { int n = 0; for (; v; v >>= 1) n += v & 1; return n; }
constexpr u32 step_off(const Prm& P, u32 s0, u32 bh) {
  u32 sb = s0 & 96u;
  for (int k = 0; k < 5; ++k) sb |= (s0 >> k & 1u) << (P.PM >> (4 * k) & 7);
  const u32 src = (sb >> 5 & 3u) | bh << 2;
  sb ^= (popc(src & P.M0) & 1) | (popc(src & P.M1) & 1) << 1 | (popc(src & P.M2) & 1) << 2 | (popc(src & P.M3) & 1) << 3 | (popc(src & P.M4) & 1) << 4;
  return (sb & 7u) << 10 | (sb >> 3) << 16;
}
constexpr Tab make_tab(const Prm& P) {
  Tab T{};
  for (int bid = 0; bid < 256; ++bid) {
    const int xcd = bid & 7, idx = bid >> 3, part = idx & 3, i8 = idx >> 2, wi = i8 * 8 + xcd;
    const bool has_mod = wi < 48, has_up = wi >= 21;
    const int cb0 = (i8 + 5) & 7, cb1 = (i8 + 4) & 7, ms = wi - 20;
    const int mi = wi < 8 ? wi : (wi < 16 ? 8 + ((xcd + 4) & 7) : (wi < 20 ? (wi - 14) * 8 + 3 : 16 + ms / 7 * 8 + ms % 7 + (ms % 7 >= 3 ? 1 : 0)));
    const int ui = xcd * 5 + (xcd > 5 ? xcd - 5 : 0) + (i8 >= 3 ? (xcd >= 5 ? 1 : 0) + i8 - 3 : 0);
    const int wd = cb0 == 3 ? 0 : P.WD;
    const int e[4] = {P.E0 - 4 * wd + (cb0 == 3 ? P.ES : 0), P.E1 + (cb1 == 3 ? P.ES : 0), has_mod ? (wi < 16 ? P.E2C : P.E2) + ((mi & 7) == 3 ? P.ES : 0) : 0, has_up ? P.EU : 0};
    const int ew = wd ? P.EW : 0, pa = (1024 + e[0] + ew + e[1] + e[2] + e[3]) / 4;
    int c[5] = {0, 0, 0, 0, 128};
    for (int k = 0; k < 3; ++k) c[k + 1] = c[k] + (pa - e[k] - (k == 0 ? ew : 0) + 4) / 8;
    const int nl = c[part + 1] - c[part], wst = e[part] / 4;
    const bool has_w = part < 2 || (part == 2 ? has_mod : has_up);
    const int blk = !has_w ? 0 : (part == 0 ? cb0 : (part == 1 ? xcd * 8 + cb1 : (part == 2 ? mi : ui)));
    const int tau = (part == 3 && has_up ? P.TEND - P.DT_UP : (part == 0 && wd ? P.TEND - P.DT_WO : P.TEND)) * 256 / (2 * nl + wst);
    T.e[8 * bid] = (u32)(c[part] | nl << 7 | blk << 13 | (has_w ? 1 : 0) << 19 | (part == 0 ? wd : 0) << 20);
    T.e[8 * bid + 1] = (u32)(tau | (tau * wst / (part == 0 ? 8 - wd : 8)) << 16);
    const u32 bh = (idx >> 2 & 1) * 32 + xcd * 4 + (((idx >> 3) & 1) << 1 | idx >> 4);
    for (int k = 0; k < KF; ++k) T.e[8 * bid + 2 + k] = (bh * SK + (SK - WIN)) * DH * 2 + step_off(P, c[part] + k, bh);
    const u32 pitch = part == 0 ? 1024u : (part == 1 ? 8192u : (part == 2 ? 6144u : 2752u));
    T.e[8 * bid + 5] = (part == 0 ? xcd * 512u * pitch : 0u) + blk * (part == 3 ? 64u : 128u);
    T.e[8 * bid + 6] = pitch;
    T.e[8 * bid + 7] = (part == 0 ? 4096u : 512u) * pitch;
  }
  return T;
}
constexpr int step_range(const Prm& P, bool mx_) {
  int mx = 0, mn = 1 << 20;
  const Tab T = make_tab(P);
  for (int bid = 0; bid < 256; ++bid) { const int n = T.e[8 * bid] >> 7 & 63; mx = n > mx ? n : mx; mn = n < mn ? n : mn; }
  return mx_ ? mx : mn;
}
__constant__ __attribute__((aligned(64))) const Tab TAB = make_tab(PRM0);
static_assert(step_range(PRM0, true) <= NL && step_range(PRM0, false) >= NCMIN / 8, "a wave's key range does not fit its load steps");
static_assert(cons_end(SIDE + WV + 1) <= NCMIN / 8 && cons_end(UPREP) <= NCMIN / 8 && SIDE > MLS + 3, "steps every wave runs");
constexpr int NUP = FF / 32;
constexpr int SPIN_MAX = 1 << 17;

constexpr u32 WS_EPOCH = 0;
constexpr u32 G_TB = 1024;
constexpr u32 G_NT = 2048;
constexpr u32 G_MOD = 4096;
constexpr u32 G_Q = G_MOD + 3072 * 8;
constexpr u32 G_ML = G_Q + 4096 * 8;
constexpr u32 G_P = G_ML + 256 * 16;
constexpr u32 G_XP = G_P + 256 * 256;
constexpr u32 G_OP = G_XP + 8 * 512 * 4;
constexpr u32 WS_END = G_OP + 43 * 512 * 8;
constexpr u32 SENT = 0xffffffffu;
constexpr u32 OOB = 0x40000000u;
#define WST 0

constexpr int AUX_NT = 2, AUX_SC1 = 16;

__device__ __forceinline__ float lo_f(u32 v) { return __uint_as_float(v << 16); }
__device__ __forceinline__ float hi_f(u32 v) { return __uint_as_float(v & 0xffff0000u); }
__device__ __forceinline__ float rbf(float x) { return (float)(__bf16)x; }
__device__ __forceinline__ u32 pack_bf(float lo, float hi) {
  bf16x2 r; r[0] = (__bf16)lo; r[1] = (__bf16)hi; return __builtin_bit_cast(u32, r);
}
__device__ __forceinline__ float dot2(u32 k, u32 q, float acc) {
  return __builtin_amdgcn_fdot2_f32_bf16(__builtin_bit_cast(bf16x2, k), __builtin_bit_cast(bf16x2, q), acc, false);
}
template <int CTRL> __device__ __forceinline__ float dpp(float x) {
  return __builtin_bit_cast(float, __builtin_amdgcn_mov_dpp(__builtin_bit_cast(int, x), CTRL, 0xf, 0xf, true));
}
constexpr int XOR1 = 0xB1, XOR2 = 0x4E, XOR7 = 0x141, XOR8 = 0x128;
__device__ __forceinline__ float swap32_add(float a, float b) {
  auto t = __builtin_amdgcn_permlane32_swap(__float_as_uint(a), __float_as_uint(b), false, false);
  return __uint_as_float(t[0]) + __uint_as_float(t[1]);
}
__device__ __forceinline__ float swap16_add(float a, float b) {
  auto t = __builtin_amdgcn_permlane16_swap(__float_as_uint(a), __float_as_uint(b), false, false);
  return __uint_as_float(t[0]) + __uint_as_float(t[1]);
}
__device__ __forceinline__ float wave_sum(float x) {
  x += dpp<XOR1>(x); x += dpp<XOR2>(x); x += dpp<XOR7>(x); x += dpp<XOR8>(x);
  x = swap16_add(x, x);
  return swap32_add(x, x);
}
__device__ __forceinline__ float wave_max(float x) {
  x = fmaxf(x, dpp<XOR1>(x)); x = fmaxf(x, dpp<XOR2>(x)); x = fmaxf(x, dpp<XOR7>(x)); x = fmaxf(x, dpp<XOR8>(x));
  auto s = __builtin_amdgcn_permlane16_swap(__float_as_uint(x), __float_as_uint(x), false, false);
  x = fmaxf(__uint_as_float(s[0]), __uint_as_float(s[1]));
  auto t = __builtin_amdgcn_permlane32_swap(__float_as_uint(x), __float_as_uint(x), false, false);
  return fmaxf(__uint_as_float(t[0]), __uint_as_float(t[1]));
}
template <int CTRL> __device__ __forceinline__ float sel_add(float lo, float hi, bool up) {
  const float t1 = lo + dpp<CTRL>(lo), t2 = hi + dpp<CTRL>(hi);
  return up ? t2 : t1;
}
template <int I> __device__ __forceinline__ float bc16(float x) {
  return __builtin_bit_cast(float, __builtin_amdgcn_update_dpp(0, __builtin_bit_cast(int, x), 0x150 + I, 0xf, 0xf, false));
}
__device__ __forceinline__ float bcast16(float x, int i) {
  switch (i) {
    case 0: return bc16<0>(x); case 1: return bc16<1>(x); case 2: return bc16<2>(x); case 3: return bc16<3>(x);
    case 4: return bc16<4>(x); case 5: return bc16<5>(x); case 6: return bc16<6>(x); case 7: return bc16<7>(x);
    case 8: return bc16<8>(x); case 9: return bc16<9>(x); case 10: return bc16<10>(x); case 11: return bc16<11>(x);
    case 12: return bc16<12>(x); case 13: return bc16<13>(x); case 14: return bc16<14>(x); default: return bc16<15>(x);
  }
}
__device__ __forceinline__ float silu_f(float x) { return x * __builtin_amdgcn_rcpf(1.f + __expf(-x)); }

template <int AUX> __device__ __forceinline__ u32x4 ld16(rsrc_t r, u32 voff, u32 soff) {
  return __builtin_amdgcn_raw_buffer_load_b128(r, voff, soff, AUX);
}
template <int AUX> __device__ __forceinline__ u32x2 ld8(rsrc_t r, u32 voff, u32 soff) {
  return __builtin_amdgcn_raw_buffer_load_b64(r, voff, soff, AUX);
}
__device__ __forceinline__ void st8_sc1(rsrc_t r, u32 voff, u32 v, u32 tag) {
  __builtin_amdgcn_raw_buffer_store_b64(u32x2{v, tag}, r, voff, 0, AUX_SC1);
}
__device__ __forceinline__ void st16_sc1(rsrc_t r, u32 voff, u32 v0, u32 v1, u32 tag) {
  __builtin_amdgcn_raw_buffer_store_b128(u32x4{v0, tag, v1, tag}, r, voff, 0, AUX_SC1);
}
__device__ __forceinline__ rsrc_t mk_rsrc(const void* p, u32 bytes) {
  return __builtin_amdgcn_make_buffer_rsrc(const_cast<void*>(p), 0, bytes, 0x00020000);
}
#define WAIT_VM(n) __builtin_amdgcn_s_waitcnt(((n) & 15) | (((n) >> 4) << 14) | (7 << 4) | (15 << 8))
#define SB() __builtin_amdgcn_sched_barrier(0)
#define CFENCE() asm volatile("" ::: "memory")
__device__ __forceinline__ bool all_ok(bool ok) { return __builtin_amdgcn_ballot_w64(!ok) == 0ull; }

template <int N> __device__ __forceinline__ void gather16(rsrc_t ws, u32 voff, u32 stride, u32 tag, u32x4 (&g)[N]) {
#pragma nounroll
  for (int spin = 0; spin < SPIN_MAX; ++spin) {
    CFENCE();
    bool ok = true;
#pragma unroll
    for (int i = 0; i < N; ++i) g[i] = ld16<AUX_SC1>(ws, voff + i * stride, 0);
#pragma unroll
    for (int i = 0; i < N; ++i) ok &= (g[i][1] == tag) & (g[i][3] == tag);
    if (all_ok(ok)) break;
    __builtin_amdgcn_s_sleep(8);
  }
}
template <int N> __device__ __forceinline__ void gather8(rsrc_t ws, u32 voff, u32 stride, u32 tag, u32 (&g)[N], bool act = true) {
#pragma nounroll
  for (int spin = 0; spin < SPIN_MAX; ++spin) {
    CFENCE();
    bool ok = true;
    u32x2 t[N];
#pragma unroll
    for (int i = 0; i < N; ++i) t[i] = ld8<AUX_SC1>(ws, voff + i * stride, 0);
#pragma unroll
    for (int i = 0; i < N; ++i) { ok &= (t[i][1] == tag); g[i] = t[i][0]; }
    if (all_ok(ok | !act)) break;
    __builtin_amdgcn_s_sleep(8);
  }
}

struct Smem {
  float red[8][128];
  float ml[8][2];
  float r2[64 * 20];
  u16 hb[3 * 1024];
  float stat[8][64];
  float red2[8][128];
  u32 dummy[64];
  u32x2 bc;
  u32x4 zero[4];
  u32 mflag[8];
  u32x4 wbuf[8][8][64];
  __attribute__((aligned(256))) u16 w2b[32][512];
  u16 actl[2][32];
};

#define STAMP(k) do {} while (0)
#define WSTAMP(k) do {} while (0)
#define FINISH() return
#define PIN(x) asm volatile("" : "+v"(x))
#define LDS_BARRIER() do { __builtin_amdgcn_s_waitcnt(0xc07f); __builtin_amdgcn_s_barrier(); CFENCE(); } while (0)
#define WAVE_SYNC() do { __builtin_amdgcn_fence(__ATOMIC_RELEASE, "wavefront"); __builtin_amdgcn_wave_barrier(); __builtin_amdgcn_fence(__ATOMIC_ACQUIRE, "wavefront"); } while (0)

__device__ __forceinline__ void dma16(rsrc_t r, u32 voff, u32 soff, u32 lds_dst) {
  u32 keep;
  asm volatile("s_mov_b32 %0, m0\n\ts_mov_b32 m0, %4\n\ts_nop 4\n\tbuffer_load_dwordx4 %1, %2, %3 offen nt lds\n\ts_mov_b32 m0, %0"
               : "=&s"(keep) : "v"(voff), "s"(r), "s"(soff), "s"(lds_dst) : "memory");
}
__device__ __forceinline__ u32 perm_lo(u32 a, u32 b) { return __builtin_amdgcn_perm(b, a, 0x05040100u); }
__device__ __forceinline__ u32 perm_hi(u32 a, u32 b) { return __builtin_amdgcn_perm(b, a, 0x07060302u); }
__device__ __forceinline__ float rdl(float x, int l) { return __int_as_float(__builtin_amdgcn_readlane(__float_as_int(x), l)); }

typedef __attribute__((ext_vector_type(8))) __bf16 bf16x8;
__device__ __forceinline__ f32x4 mfma16(u32x4 a, u32x4 b, f32x4 c) {
  return __builtin_amdgcn_mfma_f32_16x16x32_bf16(__builtin_bit_cast(bf16x8, a), __builtin_bit_cast(bf16x8, b), c, 0, 0, 0);
}
__device__ __forceinline__ float sum8(const float* p) {
  const f32x4 a = *reinterpret_cast<const f32x4*>(p), b = *reinterpret_cast<const f32x4*>(p + 4);
  return ((a[0] + b[0]) + (a[1] + b[1])) + ((a[2] + b[2]) + (a[3] + b[3]));
}
__device__ __forceinline__ float ln_ri(float var) {
  return __builtin_amdgcn_rcpf(rbf(__builtin_amdgcn_sqrtf(rbf(rbf(fmaxf(var, 0.f)) + 1e-5f))));
}
__device__ __forceinline__ void ln_stats2(const float (&x0)[8], const float (&x1)[8], float& mb0, float& ri0, float& mb1, float& ri1) {
  const float k0 = rdl(x0[0], 0), k1 = rdl(x1[0], 0);
  float s0 = 0.f, s1 = 0.f, q0 = 0.f, q1 = 0.f;
#pragma unroll
  for (int k = 0; k < 8; ++k) {
    const float d0 = x0[k] - k0, d1 = x1[k] - k1;
    s0 += d0; s1 += d1; q0 = fmaf(d0, d0, q0); q1 = fmaf(d1, d1, q1);
  }
  float t = swap16_add(swap32_add(s0, s1), swap32_add(q0, q1));
  t += dpp<XOR1>(t); t += dpp<XOR2>(t); t += dpp<XOR7>(t); t += dpp<XOR8>(t);
  auto u = __builtin_amdgcn_permlane16_swap(__float_as_uint(t), __float_as_uint(t), false, false);
  const float ms = __uint_as_float(u[0]) * (1.f / 512.f), mq = __uint_as_float(u[1]) * (1.f / 512.f);
  const float ri = ln_ri(fmaf(-ms, ms, mq));
  mb0 = rbf(k0 + rdl(ms, 0)); mb1 = rbf(k1 + rdl(ms, 32));
  ri0 = rdl(ri, 0); ri1 = rdl(ri, 32);
}
template <int I> struct IC { static constexpr int v = I; };
template <int I, int N, class F> __device__ __forceinline__ void sfor(F&& f) {
  if constexpr (I < N) { if (f(IC<I>{})) sfor<I + 1, N>(f); }
}
typedef __attribute__((ext_vector_type(4))) short s16x4;
typedef __attribute__((address_space(3))) s16x4* lds_s16x4p;
__device__ __forceinline__ u32x2 tr_read(u32 lds_addr) {
  return __builtin_bit_cast(u32x2, __builtin_amdgcn_ds_read_tr16_b64_v4i16((lds_s16x4p)lds_addr));
}
__device__ __forceinline__ u32 next_tag(u32 e) { return ((e | 0x40004000u) + 1u) & ~0x40004000u; }
__device__ __forceinline__ float u2f(u32 v) { return __uint_as_float(v); }
#define LDSP(T, a) ((__attribute__((address_space(3))) T*)(a))
template <class T> __device__ __forceinline__ u32 lds_addr(T* p) { u32 a = (u32)(uintptr_t)p; PIN(a); return a; }

typedef __attribute__((ext_vector_type(16))) unsigned u32x16;
typedef __attribute__((ext_vector_type(8))) unsigned u32x8;
__global__ void __launch_bounds__(512, 2) fused_kernel(const u16* x_, const u16* kc_, const u16* vc_, const u16* wq_, const u16* wo_, const u16* w1_,
                                                       const u16* w3_, const u16* w2_, const u16* cnd_, const u16* wmod_, unsigned char* ws_, u16* out_ PRM_ARG) {
  __shared__ __attribute__((aligned(256))) Smem sm;
  constexpr Prm P = PRM0;
  const int TEND = P.TEND;
  const int bid = blockIdx.x, tid = threadIdx.x;
  u32x4 kr[NL];
  u32x16 ka; u32x8 kb, te; u64 pt0; u32 tch2, va, vb;
  asm volatile("s_load_dwordx16 %0, %4, 0x0\n\ts_load_dwordx8 %2, %5, %6\n\ts_load_dwordx8 %1, %4, 0x40\n\ts_memrealtime %3"
               : "=&s"(ka), "=&s"(kb), "=&s"(te), "=&s"(pt0) : "s"(__builtin_amdgcn_kernarg_segment_ptr()), "s"(TABP), "s"(bid << 5));
  SB();
  {
    u64 pc;
    asm volatile("s_getpc_b64 %3\n.Lcpf_first:\n\tv_lshrrev_b32 %1, 6, %4\n\tv_add_u32 %2, %5, %1\n\tv_mul_u32_u24 %2, ((.Lend_c-.Lcpf_first)>>10)<<2, %2\n\tglobal_load_dword %0, %2, %3\n\t"
                 "v_lshlrev_b32 %1, 13, %1\n\tv_and_b32 %2, 63, %4\n\tv_lshl_or_b32 %1, %2, 4, %1"
                 : "=&v"(tch2), "=&v"(va), "=&v"(vb), "=&s"(pc) : "v"(tid), "s"(bid & ~7) : "memory");
  }
  SB();
  asm volatile("s_waitcnt lgkmcnt(0)" : "+s"(ka), "+s"(kb), "+s"(te), "+s"(pt0));
  SB();
#pragma unroll
  for (int k = 0; k < KF; ++k)
    asm volatile("v_add_u32 %1, %2, %3\n\tglobal_load_dwordx4 %0, %1, %4 nt" : "=&v"(kr[k]), "=&v"(vb) : "s"(te[2 + k]), "v"(va), "s"((u64)ka[2] | (u64)ka[3] << 32) : "memory");
  SB();
  const u32 t0 = (u32)pt0, tka = (u32)wall_clock64();
  auto kap = [&](int k) { return k < 8 ? (u64)ka[2 * k] | (u64)ka[2 * k + 1] << 32 : (u64)kb[2 * k - 16] | (u64)kb[2 * k - 15] << 32; };
  const u16 *x = (const u16*)kap(0), *kc = (const u16*)kap(1), *vc = (const u16*)kap(2), *w2 = (const u16*)kap(7), *cnd = (const u16*)kap(8);
  unsigned char* ws = (unsigned char*)kap(10); u16* out = (u16*)kap(11);
  const int xcd = bid & 7, idx = bid >> 3, part = idx & 3;
  const int lane = tid & 63, wave = __builtin_amdgcn_readfirstlane(tid >> 6);
  const bool p3 = part == 3;
  const u64 wa = part == 0 ? kap(4) : (part == 1 ? kap(3) : (part == 2 ? kap(9) : ((wave & 1) ? kap(6) : kap(5))));
  const u32 te0 = te[0], te1 = te[1];
  const u32 pitch = te[6], wsz = te[7];
  const int sh = p3 ? 2 : 3;
  u32 wv0 = (u32)((p3 ? (wave >> 1) * 128 : wave * 64) + (lane >> sh)) * pitch + ((lane & (p3 ? 3 : 7)) ^ (lane >> (p3 ? 5 : 3))) * 16u;
  const u32 wimg = (u32)(uintptr_t)(&sm.wbuf[0][0][0]);
  u32 wld = wimg + (p3 ? (wave & 1) * 32768 + (wave >> 1) * 8192 : wave * 8192);
  const int bl0 = te0 & 127, nl = te0 >> 7 & 63, blk = te0 >> 13 & 63;
  const bool has_w = te0 >> 19 & 1;
  const rsrc_t rw = mk_rsrc((const void*)wa, wsz);
  const u32 wstep = pitch << (6 - sh), wflip = p3 ? 32u : 0u;
  u32 wso = te[5];
  int wleft = 8;
  auto piece = [&]() {
    dma16(rw, wv0, wso, wld);
    wso += wstep; wld += 1024;
    wv0 ^= (wleft & 1) ? wflip : 0u;
    --wleft;
  };
  int tau = te1 & 0xffff;
  const int tauw = te1 >> 16;
  int due = KF * tau, now = 0;
  auto gate = [&]() {
    while (now < due) { now = (int)(((u32)wall_clock64() - t0) << 8); if (now < due) __builtin_amdgcn_s_sleep(1); }
    due += tau;
  };
  const bool modc = part == 2 && bid < 64;
  if (has_w) {
    if (modc) now = 1 << 30;
#pragma nounroll
    while (wleft > (int)(te0 >> 20)) { gate(); due += tauw - tau; piece(); }
    now = 0;
  }
  SB();
  STAMP(23);
  const int b = idx >> 2 & 1, head = xcd * 4 + (((idx >> 3) & 1) << 1 | idx >> 4);
  const rsrc_t wsr = mk_rsrc(ws, WS_END);
  const int g = lane >> 4, c = lane & 15;
  const int i8 = idx >> 2, wi = i8 * 8 + xcd;
  const bool is_up = p3 && has_w;
  const size_t base = ((size_t)(b * NH + head) * SK + (SK - WIN)) * DH;
  const rsrc_t rk = mk_rsrc(kc + base, WIN * DH * 2), rv = mk_rsrc(vc + base, WIN * DH * 2);
  u32 sot;
  {
    const u32 s0 = bl0 + lane;
    u32 sb = s0 & 96u;
#pragma unroll
    for (int k = 0; k < 5; ++k) sb |= (s0 >> k & 1u) << (P.PM >> (4 * k) & 7);
    const u32 src = (sb >> 5 & 3u) | (u32)(b * 32 + head) << 2;
    sb ^= (__builtin_popcount(src & P.M0) & 1) | (__builtin_popcount(src & P.M1) & 1) << 1 | (__builtin_popcount(src & P.M2) & 1) << 2 | (__builtin_popcount(src & P.M3) & 1) << 3 | (__builtin_popcount(src & P.M4) & 1) << 4;
    sot = (sb & 7u) << 10 | (u32)wave << 13 | (sb >> 3) << 16;
    sot = lane < nl ? sot : OOB;
    PIN(sot);
  }
  auto until = [&](int t) { while ((int)(((u32)wall_clock64() - t0) << 8) < t); };
  const u32 pslot = G_P + ((b * 32 + head) * 4 + part) * 256;
  const rsrc_t rx = mk_rsrc(part == 2 ? cnd : x, (part == 1 || (part == 2 && has_w)) ? 2048u : 0u);
  u32x2 ep; u32 xs0, xs1, tch1; u32x4 xr0, xr1;
  auto small_ops = [&]() {
    ep = ld8<AUX_SC1>(wsr, wave == 7 ? WS_EPOCH : OOB, 0);
    xs0 = __builtin_amdgcn_raw_buffer_load_b16(rx, tid * 2, 0, 0); xs1 = __builtin_amdgcn_raw_buffer_load_b16(rx, tid * 2, 1024, 0);
    xr0 = ld16<0>(rx, part == 1 ? lane * 16 : OOB, 0); xr1 = ld16<0>(rx, part == 1 ? lane * 16 : OOB, 1024);
    tch1 = __builtin_amdgcn_raw_buffer_load_b32(mk_rsrc(w2 + (size_t)blk * 32 * 512, is_up ? 64u : 0u), tid == 384 ? 0 : OOB, 0, AUX_NT);
  };
  if (modc) __builtin_amdgcn_raw_buffer_store_b128(u32x4{SENT, SENT, SENT, SENT}, wsr, tid < 320 ? G_P + wi * 5120 + tid * 16 : OOB, 0, AUX_SC1);
  SB();
  u32 tag = 0;
  u32x4 bf[8];
  auto wo_prep = [&](int s0 = 0, int s1 = 2) {
    const u32 wi_ = wimg + wave * 8192;
    const int q = (lane >> 2) & 3, pp = lane & 3;
#pragma unroll
    for (int s = s0; s < s1; ++s) {
#pragma unroll
      for (int t = 0; t < 4; ++t) {
        const int r0 = s * 32 + g * 8 + q, r1 = r0 + 4, ch = t * 2 + (pp >> 1);
        const u32x2 lo = tr_read(wi_ + r0 * 128 + ((ch ^ (r0 & 7)) * 16) + (pp & 1) * 8), hi = tr_read(wi_ + r1 * 128 + ((ch ^ (r1 & 7)) * 16) + (pp & 1) * 8);
        bf[s * 4 + t] = u32x4{lo[0], lo[1], hi[0], hi[1]};
      }
    }
  };
  auto gemv_go = [&](u32 hh) {
    sm.hb[tid] = (u16)hh; sm.hb[512 + tid] = (u16)(hh >> 16);
    WAVE_SYNC();
    const u32x4* a = (c & 11) == 0 ? reinterpret_cast<const u32x4*>(&sm.hb[(c >> 2) * 512 + wave * 64 + g * 8]) : &sm.zero[0];
    const u32x4 a0 = a[0], a1 = a[(c & 11) == 0 ? 4 : 0];
    const f32x4 z = {0.f, 0.f, 0.f, 0.f};
    f32x4 d[4];
#pragma unroll
    for (int t = 0; t < 4; ++t) d[t] = mfma16(a0, bf[t], z);
#pragma unroll
    for (int t = 0; t < 4; ++t) d[t] = mfma16(a1, bf[4 + t], d[t]);
    if (lane < 32) {
#pragma unroll
      for (int t = 0; t < 4; ++t) sm.r2[(t * 16 + c) * 20 + g * 8 + wave] = d[t][0];
    }
  };
  auto mod_stage = [&]() {
    wo_prep();
    gemv_go(pack_bf(silu_f(lo_f(xs0)), silu_f(lo_f(xs1))));
    __builtin_amdgcn_s_waitcnt(0xc07f);
    if (lane == 0) *LDSP(volatile u32, (u32)(uintptr_t)&sm.mflag[wave]) = 1u;
    if (wave == 7) {
#pragma nounroll
      for (int spin = 0; spin < SPIN_MAX; ++spin) {
        const u32 f = *LDSP(volatile u32, (u32)(uintptr_t)&sm.mflag[lane & 7]);
        if (all_ok(f != 0u)) break;
        __builtin_amdgcn_s_sleep(1);
      }
      CFENCE();
      st8_sc1(wsr, G_MOD + (blk * 64 + lane) * 8, pack_bf(sum8(&sm.r2[lane * 20]), sum8(&sm.r2[lane * 20 + 8])), tag);
    }
    STAMP(9);
  };
  if (lane < 4) sm.zero[lane] = u32x4{0u, 0u, 0u, 0u};
  if (part == 2 && has_w) {
    if (tid < 8) sm.mflag[tid] = 0u;
    LDS_BARRIER();
    if (__builtin_expect(modc, 0)) {
      small_ops();
      asm volatile("s_waitcnt vmcnt(0)" ::: "memory");
      tag = next_tag(__builtin_amdgcn_readfirstlane(ep[0]));
      mod_stage();
      __builtin_amdgcn_s_barrier();
    }
  }
  SB();
  const int fd = wi * 8 + (lane & 7);
  const int ub = wave >> 2, ud = (wave & 3) * 128 + c * 8 + g * 2;
  u32x4 sg[3]; u32 sx2;
  auto side_loads = [&]() {
    const bool fin = part == 1 && wave == 0;
    sg[0] = ld16<AUX_SC1>(wsr, is_up ? G_MOD + (1024 + ud) * 8 : (fin ? G_MOD + (1024 + fd) * 8 : OOB), 0);
    sg[1] = ld16<AUX_SC1>(wsr, is_up ? G_MOD + (1536 + ud) * 8 : (fin ? G_MOD + (2560 + fd) * 8 : OOB), 0);
    sg[2] = ld16<AUX_SC1>(wsr, is_up ? G_MOD + (2048 + ud) * 8 : OOB, 0);
    sx2 = __builtin_amdgcn_raw_buffer_load_b32(mk_rsrc(x, (is_up || fin) ? 2048u : 0u), is_up ? ub * 1024 + ud * 2 : (g & 1) * 1024 + (fd >> 1) * 4, 0, 0);
  };
  auto side_pin = [&]() { PIN(sg[0]); PIN(sg[1]); PIN(sg[2]); PIN(sx2); };
  const u32 mlo = G_ML + (head * 2 + b) * 64 + (lane & 3) * 16;
  float fmrg = 0.f;
  bool fok = wave != 0;
  auto merge_factor = [&](const u32x4& ml) {
    const float mi = u2f(ml[0]);
    float M = fmaxf(mi, dpp<XOR1>(mi)); M = fmaxf(M, dpp<XOR2>(M));
    const float e = __expf(mi - M);
    float L = e * u2f(ml[2]);
    L += dpp<XOR1>(L); L += dpp<XOR2>(L);
    fmrg = rdl(e * __builtin_amdgcn_rcpf(L), part);
  };
  f32x2 ga = {0.f, 0.f}, xin = {0.f, 0.f}, cc1 = {0.f, 0.f};
  float Af = 0.f, Bf = 0.f;
  const int hpos = ub * 512 + ud;
  auto up_prep = [&]() {
    if (!all_ok((sg[0][1] == tag) & (sg[0][3] == tag) & (sg[1][1] == tag) & (sg[1][3] == tag) & (sg[2][1] == tag) & (sg[2][3] == tag))) gather16<3>(wsr, G_MOD + (1024 + ud) * 8, 512 * 8, tag, sg);
    ga = f32x2{ub ? hi_f(sg[0][0]) : lo_f(sg[0][0]), ub ? hi_f(sg[0][2]) : lo_f(sg[0][2])};
    cc1 = f32x2{rbf(1.f + (ub ? hi_f(sg[2][0]) : lo_f(sg[2][0]))), rbf(1.f + (ub ? hi_f(sg[2][2]) : lo_f(sg[2][2])))};
    xin = f32x2{lo_f(sx2), hi_f(sx2)};
    u32* hp = reinterpret_cast<u32*>(&sm.hb[hpos]);
    hp[512] = pack_bf(cc1[0], cc1[1]);
    hp[1024] = ub ? perm_hi(sg[1][0], sg[1][2]) : perm_lo(sg[1][0], sg[1][2]);
  };
  auto up_frags = [&]() {
    const int pp = lane & 3;
    const u32 a = wimg + (pp & 1) * 32768 + ((pp >> 1) * 32 + g * 8 + ((lane >> 2) & 3)) * 64 + (((wave >> 1) ^ ((g & 1) | (pp & 2))) * 16) + (wave & 1) * 8;
#pragma unroll
    for (int rb = 0; rb < 8; ++rb) {
      const u32x2 lo = tr_read(a + rb * 4096), hi = tr_read(a + rb * 4096 + 256);
      bf[rb] = u32x4{lo[0], lo[1], hi[0], hi[1]};
    }
  };
  const u32x4* ap = reinterpret_cast<const u32x4*>(&sm.hb[((lane >> 3) & 1) * 512 + (lane & 1) * 32 + g * 8]);
  const bool par = lane & 8;
  float l0 = 0.f, l1 = 0.f;
  u32x2 gmd[2], tbg;
  int t1 = 0;
#pragma unroll
  for (int i = NCMIN / 8; i < NL; ++i) kr[i] = u32x4{0u, 0u, 0u, 0u};
  sfor<KF, NL>([&](auto ic) {
    constexpr int i = ic.v;
    if (i >= NCMIN / 8 && i >= nl) return false;
    const u32 so = __builtin_amdgcn_readlane(sot, i);
    gate();
    SB();
    kr[i] = ld16<AUX_NT>(rk, lane * 16, so);
    if constexpr (i == KF) small_ops();
    if constexpr (i == TBL) tbg = ld8<AUX_SC1>(wsr, wave == 7 ? G_TB + xcd * 128 + (lane & 7) * 8 : OOB, 0);
    if constexpr (i == QP) { gmd[0] = ld8<AUX_SC1>(wsr, part == 1 ? G_MOD + tid * 8 : OOB, 0); gmd[1] = ld8<AUX_SC1>(wsr, part == 1 ? G_MOD + tid * 8 : OOB, 4096); }
    SB();
    SB();
    if (i >= WK) { WAIT_VM(WK); SB(); }
    if (i == KF) STAMP(25);
    if (i == 8) STAMP(12);
    if (i == 20) STAMP(21);
    if (i == 25) STAMP(22);
    if (i == TBP) {
      if (part == 3 && i8 == 0 && wave == 7) { tag = next_tag(__builtin_amdgcn_readfirstlane(ep[0])); if (lane < 8) st8_sc1(wsr, G_TB + lane * 128 + xcd * 8, t0 + (((tka - t0) * P.GA) >> 3) - P.GS, tag); }
      SB();
    }
    if (i == TBU) {
      if (wave == 7) {
        const bool ok = tbg[1] == tag;
        float lf = ok ? (float)min(max((int)(t0 - tbg[0]), -300), 300) : 0.f, nf = ok ? 1.f : 0.f;
        lf += dpp<XOR1>(lf); nf += dpp<XOR1>(nf); lf += dpp<XOR2>(lf); nf += dpp<XOR2>(nf); lf += dpp<XOR7>(lf); nf += dpp<XOR7>(nf);
        if (lane == 0) sm.bc = u32x2{tag, t0 - (u32)(int)(lf * __builtin_amdgcn_rcpf(fmaxf(nf, 1.f)))};
      }
      LDS_BARRIER();
      const u32x2 bc = sm.bc;
      tag = __builtin_amdgcn_readfirstlane(bc[0]);
      t1 = (__builtin_amdgcn_readfirstlane((int)(bc[1] - t0)) + TEND + P.X1) * 256;
      tau -= __builtin_amdgcn_readfirstlane((int)((float)(int)(t0 - bc[1]) * 256.f * __builtin_amdgcn_rcpf((float)(2 * nl - TBU - 2))));
      SB();
    }
    if (i == LNS) {
      tag = next_tag(__builtin_amdgcn_readfirstlane(ep[0]));
      if (part == 1) {
        float x0[8], x1[8], mb0, ri0, mb1, ri1;
#pragma unroll
        for (int k = 0; k < 4; ++k) { x0[2 * k] = lo_f(xr0[k]); x0[2 * k + 1] = hi_f(xr0[k]); x1[2 * k] = lo_f(xr1[k]); x1[2 * k + 1] = hi_f(xr1[k]); }
        ln_stats2(x0, x1, mb0, ri0, mb1, ri1);
        l0 = rbf(rbf(lo_f(xs0) - mb0) * ri0); l1 = rbf(rbf(lo_f(xs1) - mb1) * ri1);
      }
      PIN(l0); PIN(l1);
      SB();
    }
    if (i == WUSE) {
      PIN(tch1); PIN(tch2);
      if ((part == 1 || (part == 2 && has_w && !modc))) {
        WAIT_VM(WK); CFENCE();
        if (part == 2) mod_stage(); else wo_prep();
      }
#pragma unroll
      for (int k = 0; k < 8; ++k) PIN(bf[k]);
      SB();
    }
    if (i == QP + QD) {
      if (part == 1) {
        u32 gm[2] = {gmd[0][0], gmd[1][0]};
        if (!all_ok((gmd[0][1] == tag) & (gmd[1][1] == tag))) { STAMP(11); gather8<2>(wsr, G_MOD + tid * 8, 512 * 8, tag, gm); }
        STAMP(5);
        gemv_go(pack_bf(rbf(l0 * rbf(1.f + lo_f(gm[1]))) + lo_f(gm[0]), rbf(l1 * rbf(1.f + hi_f(gm[1]))) + hi_f(gm[0])));
      }
      SB();
    }
    if (i == QP + QD + 1) {
      if (part == 1) {
        LDS_BARRIER();
        if (wave == 0) {
          const int bb = lane >> 5, jq = lane & 31;
          st8_sc1(wsr, G_Q + (bb * 2048 + blk * 32 + jq) * 8, pack_bf(sum8(&sm.r2[2 * jq * 20 + bb * 8]), sum8(&sm.r2[(2 * jq + 1) * 20 + bb * 8])), tag);
        }
        STAMP(6);
      }
      SB();
    }
    return true;
  });
  PIN(sot);
  const u32 qoff = G_Q + ((b * 32 + head) * 64 + c * 4) * 8;
  u32x4 qg[2], vr[NL], fl2[3];
  u32 qq[4];
  constexpr int NB = (NL + 15) / 16;
  float xlo, sc[NB], pr[NB], m = 0.f;
  f32x4 acc = {0.f, 0.f, 0.f, 0.f};
  u32 pb[NB], pm[4], cpf;
  const u32 adst0 = lds_addr(lane < 32 ? &sm.red[wave][c * 8 + g * 4] : &sm.r2[(lane - 32) * 4]);
  u32 po, rdst, rsrc, xo;
  auto wo_addr = [&]() {
    po = G_P + (((c >> 2 & 1) * 32 + xcd * 4 + (wave >> 1)) * 4 + (c & 3)) * 256 + (wave & 1) * 128 + (c >> 3) * 64 + g * 16;
    rdst = lane < 32 ? (u32)(uintptr_t)&sm.red2[wave][0] + lane * 16 : (u32)(uintptr_t)&sm.r2[0] + (lane - 32) * 16;
    rsrc = (u32)(uintptr_t)&sm.red2[0][((wave & 1) * 16 + (lane & 15)) * 4 + (lane >> 4)];
    xo = (lane & 1) ? OOB : G_XP + xcd * 2048 + (wave & 1) * 1024 + (blk * 64 + lane) * 2;
    PIN(po); PIN(rdst); PIN(rsrc); PIN(xo);
  };
  u32 pso = wave == 0 ? pslot + lane * 4 : OOB;
  PIN(pso);
  const u32 nadd = (te0 >> 20) ? P.LW : (is_up ? P.LU : 0);
  u32 nto = tid == 0 ? G_NT + bid * 8 : OOB;
  PIN(nto);
  auto notices = [&](auto nc_, u32 off, u32x4 (&gn)[nc_.v]) -> int {
    constexpr int N = nc_.v;
#pragma nounroll
    for (int spin = 0; spin < SPIN_MAX; ++spin) {
      bool ok = true;
#pragma unroll
      for (int i = 0; i < N; ++i) ok &= (gn[i][1] == tag) & (gn[i][3] == tag);
      if (all_ok(ok)) break;
      __builtin_amdgcn_s_sleep(10);
      CFENCE();
#pragma unroll
      for (int i = 0; i < N; ++i) gn[i] = ld16<AUX_SC1>(wsr, off, i * 1024);
    }
    int mx = 0;
#pragma unroll
    for (int i = 0; i < N; ++i) mx = max(mx, max((int)(gn[i][0] - t0), (int)(gn[i][2] - t0)));
    return __builtin_amdgcn_readfirstlane((int)wave_max((float)min(mx, 1 << 20)));
  };
  const int side = nl >= SIDE + 13 ? SIDE + 8 : (nl >= SIDE + 9 ? SIDE + 4 : SIDE);
  sfor<0, NL + WV>([&](auto ic) {
    constexpr int v = ic.v;
    if (cons_end(v - 1) >= NCMIN / 8 && cons_end(v - 1) >= nl) return false;
    if constexpr (v < NL) {
      const u32 so = __builtin_amdgcn_readlane(sot, v);
      if constexpr (v >= 12) { if (v == nl - P.NE) tau = (tau * P.FE) >> 4; }
      if constexpr (v >= NCMIN / 8) { if (v < nl) gate(); } else gate();
      SB();
      vr[v] = ld16<AUX_NT>(rv, lane * 16, so);
      SB();
      SB();
    }
    if constexpr (v == NCMIN / 8 - 1) {
#pragma unroll
      for (int i = NCMIN / 8; i < NL; ++i) asm volatile("" : "=v"(vr[i]));
      asm volatile("" : "=v"(fl2[1]), "=v"(fl2[2]));
    }
    if constexpr (v == VQ - 4) { qg[0] = ld16<AUX_SC1>(wsr, qoff, 0); qg[1] = ld16<AUX_SC1>(wsr, qoff + 16, 0); }
    if constexpr (v == VSIDE) side_loads();
    if constexpr (v == CPF) {
      u64 pc; u32 dlt, len;
      asm volatile("s_getpc_b64 %0\n.Lcpf_here:\n\ts_mov_b32 %1, .Ltail_start-.Lcpf_here\n\ts_mov_b32 %2, .Lend_a-.Ltail_start\n\ts_max_i32 %2, %2, .Lend_b-.Ltail_start\n\ts_max_i32 %2, %2, .Lend_c-.Ltail_start\n\ts_max_i32 %2, %2, .Lend_d-.Ltail_start\n\ts_max_i32 %2, %2, .Lend_e-.Ltail_start" : "=s"(pc), "=s"(dlt), "=s"(len) :: "scc");
      pc += dlt;
      const rsrc_t rc = mk_rsrc((const void*)pc, (part == 3 && i8 == 1) ? len : 0u);
      cpf = __builtin_amdgcn_raw_buffer_load_b32(rc, tid * 64, 0, 0);
    }
    if constexpr (v == CPF + WV + 1) PIN(cpf);
    if constexpr (v == SIDE || v == SIDE + 4 || v == SIDE + 8) fl2[(v - SIDE) / 4] = ld16<AUX_SC1>(wsr, (wave == 0 && v == side) ? mlo : OOB, 0);
    SB();
    if constexpr (v < WV) { WAIT_VM(WK); SB(); } else if constexpr (v < NL && cons_end(v) < v - WV + 1) { WAIT_VM(WV); SB(); }
    if constexpr (v == VQ) {
      if (!all_ok((qg[0][1] == tag) & (qg[0][3] == tag) & (qg[1][1] == tag) & (qg[1][3] == tag))) { STAMP(20); gather16<2>(wsr, qoff, 16, tag, qg); }
      STAMP(1); WSTAMP(1);
      qq[0] = qg[0][0]; qq[1] = qg[0][2]; qq[2] = qg[1][0]; qq[3] = qg[1][2];
    }
    if constexpr (v >= VQ && v < VS) {
      constexpr int sl = v - VQ, bt = sl >> 1;
      float xs[8];
#pragma unroll
      for (int i = 0; i < 8; ++i) {
        float d = 0.f;
        if (sl * 8 + i < NL) {
#pragma unroll
          for (int k = 0; k < 4; ++k) d = dot2(kr[sl * 8 + i][k], qq[k], d);
        }
        xs[i] = d;
      }
#pragma unroll
      for (int h = 0; h < 4; ++h) xs[h] = sel_add<XOR7>(xs[h], xs[h + 4], c & 4);
#pragma unroll
      for (int h = 0; h < 2; ++h) xs[h] = sel_add<XOR2>(xs[h], xs[h + 2], c & 2);
      xs[0] = sel_add<XOR1>(xs[0], xs[1], c & 1);
      if constexpr ((sl & 1) || sl == NSL - 1) {
        const float t = (sl & 1) ? sel_add<XOR8>(xlo, xs[0], c & 8) : xs[0] + dpp<XOR8>(xs[0]);
        const float s = rbf(rbf(t) * 0.08838834764831845f);
        sc[bt] = (bt * 16 + c < nl) ? s : -__builtin_inff();
        PIN(sc[bt]);
      } else {
        xlo = xs[0];
        PIN(xlo);
      }
    }
    if constexpr (v == VS) {
      float mloc = sc[0];
#pragma unroll
      for (int bt = 1; bt < NB; ++bt) mloc = fmaxf(mloc, sc[bt]);
      m = fmaxf(wave_max(mloc), -3.0e38f);
      float ls = 0.f;
#pragma unroll
      for (int bt = 0; bt < NB; ++bt) { pr[bt] = __expf(sc[bt] - m); ls += pr[bt]; }
      const float l = wave_sum(ls);
      if (lane == 0) { sm.ml[wave][0] = m; sm.ml[wave][1] = l; }
      STAMP(2); WSTAMP(2);
    }
    sfor<cons_end(v - 1), cons_end(v)>([&](auto jc) {
      constexpr int i = jc.v;
      if (i < NCMIN / 8 || i < nl) {
        const u32 pv = __float_as_uint(bcast16(u2f(pb[i >> 4]), i & 15));
        acc = mfma16(u32x4{pv & pm[0], pv & pm[1], pv & pm[2], pv & pm[3]}, vr[i], acc);
      }
      PIN(acc);
      return true;
    });
    if constexpr (v == MLS - 1) { side_pin(); if (is_up) up_prep(); PIN(ga); PIN(xin); PIN(cc1); }
    if constexpr (v == UPREP - 1) wo_addr();
    if constexpr (v == UPREP) {
#pragma unroll
      for (int k = 0; k < 8; ++k) asm volatile("" : "=v"(bf[k]));
      if (part == 0) wo_prep(0, 1); else if (is_up) up_frags();
#pragma unroll
      for (int k = 0; k < 8; ++k) PIN(bf[k]);
    }
    if constexpr (v == UPREP + 1) {
      if (is_up) {
        const u32x4* a1 = ap + 128 + ((lane >> 1) & 1) * 128;
        f32x4 d0 = {0.f, 0.f, 0.f, 0.f}, d1 = {0.f, 0.f, 0.f, 0.f};
#pragma unroll
        for (int rb = 0; rb < 4; ++rb) { d0 = mfma16(a1[rb * 8], bf[rb], d0); d1 = mfma16(a1[(rb + 4) * 8], bf[rb + 4], d1); }
        Af = par ? d0[1] + d1[1] : d0[0] + d1[0]; Bf = par ? d0[3] + d1[3] : d0[2] + d1[2];
        Af += dpp<XOR8>(Af); Bf += dpp<XOR8>(Bf);
      }
      PIN(Af); PIN(Bf);
    }
    if constexpr (v == SIDE + WV + 1 || v == SIDE + WV + 5 || v == SIDE + WV + 9) {
      if (wave == 0 && v == side + WV + 1) {
        const u32x4 ml = fl2[(v - SIDE - WV - 1) / 4];
        fok = all_ok((ml[1] == tag) & (ml[3] == tag));
        merge_factor(ml);
      }
    }
    if (v % 6 == 4 && v >= 10 && v < 30) STAMP(15 + v / 6);
    if constexpr (v == MLS) {
      LDS_BARRIER();
      float mm[8], ll[8];
#pragma unroll
      for (int w = 0; w < 8; ++w) { const f32x2 t = *reinterpret_cast<const f32x2*>(&sm.ml[w][0]); mm[w] = t[0]; ll[w] = t[1]; }
      float Mwg = mm[0], Lwg = 0.f;
#pragma unroll
      for (int w = 1; w < 8; ++w) Mwg = fmaxf(Mwg, mm[w]);
      if (wave == 0) {
#pragma unroll
        for (int w = 0; w < 8; ++w) Lwg = fmaf(__expf(mm[w] - Mwg), ll[w], Lwg);
        if (lane == 0) st16_sc1(wsr, G_ML + ((head * 2 + b) * 4 + part) * 16, __float_as_uint(Mwg), __float_as_uint(Lwg), tag);
      }
      const float fw = __expf(m - Mwg);
#pragma unroll
      for (int bt = 0; bt < NB; ++bt) { pb[bt] = pack_bf(pr[bt] * fw, pr[bt] * fw); PIN(pb[bt]); }
#pragma unroll
      for (int k = 0; k < 4; ++k) { pm[k] = (c >> 1) == k ? ((c & 1) ? 0xffff0000u : 0x0000ffffu) : 0u; PIN(pm[k]); }
    }
    SB();
    return true;
  });
  asm volatile(".Ltail_start:");
  *LDSP(f32x4, adst0) = acc;
  STAMP(14); WSTAMP(14);
  if (!fok) {
    u32x4 ml;
#pragma nounroll
    for (int spin = 0; spin < SPIN_MAX; ++spin) {
      CFENCE();
      ml = ld16<AUX_SC1>(wsr, mlo, 0);
      if (all_ok((ml[1] == tag) & (ml[3] == tag))) break;
    }
    merge_factor(ml);
  }
  STAMP(15);
  LDS_BARRIER();
  STAMP(3);
  {
    u32 pw = 0;
    if (wave == 0) {
      f32x2 t[8];
#pragma unroll
      for (int w = 0; w < 8; ++w) t[w] = *reinterpret_cast<const f32x2*>(&sm.red[w][2 * lane]);
      const f32x2 o = ((t[0] + t[1]) + (t[2] + t[3])) + ((t[4] + t[5]) + (t[6] + t[7]));
      pw = min(pack_bf(o[0] * fmrg, o[1] * fmrg), SENT - 1u);
    }
    __builtin_amdgcn_raw_buffer_store_b32(pw, wsr, pso, 0, AUX_SC1);
    st8_sc1(wsr, nto, (u32)wall_clock64() + nadd, tag);
    SB();
#pragma unroll
    for (int i = NCMIN / 8; i < NL; ++i) PIN(vr[i]);
    PIN(fl2[0]); PIN(fl2[1]); PIN(fl2[2]);
  }
  STAMP(4);

  if (part == 2) FINISH();
  if (part == 0) {
    __builtin_amdgcn_s_barrier();
#pragma nounroll
    while (wleft > 0) piece();
    until(t1);
    STAMP(11);
    CFENCE();
    u32x4 og[2];
    og[0] = ld16<AUX_SC1>(wsr, po, 0);
    SB();
    WAIT_VM(1); CFENCE();
    wo_prep(1, 2);
#pragma unroll
    for (int k = 4; k < 8; ++k) PIN(bf[k]);
    SB();
    f32x4 d[4];
    auto wo_mm = [&]() {
      const f32x4 z = {0.f, 0.f, 0.f, 0.f};
#pragma unroll
      for (int t = 0; t < 4; ++t) { d[t] = mfma16(og[0], bf[t], z); og[1][t] = __float_as_uint(dpp<XOR8>(u2f(og[0][t]))); }
      SB();
#pragma unroll
      for (int t = 0; t < 4; ++t) d[t] = mfma16(og[1], bf[4 + t], d[t]);
    };
    auto p_bad = [&]() { return !all_ok(max(max(og[0][0], og[0][1]), max(og[0][2], og[0][3])) != SENT); };
    wo_mm();
    if (__builtin_expect(p_bad(), 0)) {
#pragma nounroll
      for (int spin = 0; spin < SPIN_MAX; ++spin) {
        __builtin_amdgcn_s_sleep(1);
        CFENCE();
        og[0] = ld16<AUX_SC1>(wsr, po, 0);
        if (!p_bad()) break;
      }
      wo_mm();
      SB();
      asm volatile(".Lend_d:");
    }
    STAMP(5); WSTAMP(5);
    {
      SB();
      f32x4 y; f32x2 h[4];
#pragma unroll
      for (int t = 0; t < 4; ++t) { h[t] = d[t].xy + d[t].zw; PIN(h[t]); }
#pragma unroll
      for (int t = 0; t < 4; ++t) { y[t] = h[t][0] + h[t][1]; PIN(y[t]); }
      *LDSP(f32x4, rdst) = y;
    }
    STAMP(16);
    LDS_BARRIER();
    STAMP(17);
    if (wave < 2) {
      f32x2 v[4];
#pragma unroll
      for (int w = 0; w < 4; ++w) v[w] = f32x2{LDSP(float, rsrc)[w * 256], LDSP(float, rsrc)[w * 256 + 128]};
      const f32x2 h = (v[0] + v[1]) + (v[2] + v[3]);
      const float y = h[0] + h[1];
      __builtin_amdgcn_raw_buffer_store_b32(min(pack_bf(y, dpp<XOR1>(y)), SENT - 1u), wsr, xo, 0, AUX_SC1);
      asm volatile(".Lend_a:");
    }
    STAMP(6);
    FINISH();
  }
  if (part == 3) {
    if (!has_w) FINISH();
    u32x4 b2[4];
    {
      __builtin_amdgcn_s_barrier();
      const rsrc_t r2 = mk_rsrc(w2 + (size_t)(blk * 32 + wave * 4) * 512, 4096u);
      const u32 l2 = (u32)(uintptr_t)(&sm.w2b[wave * 4][0]);
#pragma unroll
      for (int n = 0; n < 4; ++n) dma16(r2, (lane ^ (n * 2) ^ ((wave & 2) << 2)) * 16, n * 1024, l2 + n * 1024);
    }
    const u32 tdst = lds_addr(&sm.hb[hpos]);
    const u32 sdst = lds_addr(&sm.stat[wave][lane]);
    const u32 ssrc = lds_addr(&sm.stat[(g >> 1) * 4 + ((lane >> 1) & 3)][(lane & 1) * 16]);
    const u32 adst = lds_addr((c < 4 && (g & 1) == 0) ? &sm.actl[g >> 1][wave * 4 + c] : reinterpret_cast<u16*>(&sm.dummy[lane]));
    const u32 asrc = lds_addr(&sm.actl[lane & 1][g * 8]);
    const u32 apa = lds_addr(&sm.hb[(c >> 3) * 512 + (c & 3) * 64 + ((c >> 2) & 1) * 32 + g * 8]);
    u32 xoff = G_XP + g * 2048 + ub * 1024 + (wave & 3) * 256 + c * 16;
    u32x4 one;
#pragma unroll
    for (int k = 0; k < 4; ++k) one[k] = ((c & 2) == 0 && (c >> 2) == k) ? ((c & 1) ? 0x3f800000u : 0x00003f80u) : 0u;
    PIN(one);
    u32 ooff = G_OP + (blk * 512 + tid) * 8;
    float Afp = Af, Bfp = Bf;
    PIN(xoff); PIN(ooff); PIN(Afp); PIN(Bfp);
    PIN(ga); PIN(xin); PIN(cc1);
    u32 w2a = (u32)(uintptr_t)(&sm.w2b[0][0]) + (g * 8 + ((lane >> 2) & 3)) * 1024 + (wave ^ (g & 1)) * 128 + (lane & 15) * 8;
    PIN(w2a);
    if (wave == 1) {
      due = t1 + P.N1 * 256; gate();
      u32x4 gn[2] = {ld16<AUX_SC1>(wsr, G_NT + lane * 16, 0), ld16<AUX_SC1>(wsr, G_NT + lane * 16, 1024)};
      const int tn = notices(IC<2>{}, G_NT + lane * 16, gn) * 256;
      due = (tn > t1 + 2 * 256 ? max(tn, t1 + P.L0 * 256) + P.L1 * 256 : t1) + P.X2 * 256;
      until(due);
    }
    asm volatile("s_waitcnt vmcnt(0)" ::: "memory");
    STAMP(9);
    __builtin_amdgcn_s_barrier();
    STAMP(11);
    CFENCE();
    u32x4 xg[2] = {ld16<AUX_SC1>(wsr, xoff, 0), ld16<AUX_SC1>(wsr, xoff, 8192)};
    SB();
#pragma unroll
    for (int t = 0; t < 4; ++t) {
      const u32x2 lo = tr_read(w2a ^ (t * 32)), hi = tr_read((w2a ^ (t * 32)) + 4096);
      b2[t] = u32x4{lo[0], lo[1], hi[0], hi[1]};
    }
#pragma unroll
    for (int t = 0; t < 4; ++t) PIN(b2[t]);
    SB();
    const f32x4 z4 = {0.f, 0.f, 0.f, 0.f};
    auto xp_bad = [&]() { return !all_ok(max(max(max(xg[0][0], xg[0][1]), max(xg[0][2], xg[0][3])), max(max(xg[1][0], xg[1][1]), max(xg[1][2], xg[1][3]))) != SENT); };
    f32x4 v = mfma16(one, xg[1], mfma16(one, xg[0], z4));
    if (__builtin_expect(xp_bad(), 0)) {
#pragma nounroll
      for (int spin = 0; spin < SPIN_MAX; ++spin) {
        __builtin_amdgcn_s_sleep(1);
        CFENCE();
        xg[0] = ld16<AUX_SC1>(wsr, xoff, 0); xg[1] = ld16<AUX_SC1>(wsr, xoff, 8192);
        if (!xp_bad()) break;
      }
      v = mfma16(one, xg[1], mfma16(one, xg[0], z4));
      SB();
      asm volatile(".Lend_e:");
    }
    STAMP(5); WSTAMP(5);
    {
      const float a0 = v[0], a1 = v[1];
      const float x10 = fmaf(ga[0], a0, xin[0]), x11 = fmaf(ga[1], a1, xin[1]);
      *LDSP(u32, tdst) = pack_bf(x10 * cc1[0], x11 * cc1[1]);
      float u = swap32_add(x10 + x11, fmaf(x10, x10, x11 * x11));
      u += dpp<XOR1>(u); u += dpp<XOR2>(u); u += dpp<XOR7>(u); u += dpp<XOR8>(u);
      *LDSP(float, sdst) = u;
    }
    STAMP(16);
    LDS_BARRIER();
    STAMP(17);
    {
      const f32x4 z = {0.f, 0.f, 0.f, 0.f};
      const u32x4 af0 = LDSP(u32x4, apa)[0], af1 = LDSP(u32x4, apa)[32];
      float s = LDSP(float, ssrc)[0], q = LDSP(float, ssrc)[32];
      SB();
      f32x4 d[4];
#pragma unroll
      for (int j = 0; j < 4; ++j) d[j] = mfma16(af0, bf[j], z);
#pragma unroll
      for (int j = 0; j < 4; ++j) d[j] = mfma16(af1, bf[4 + j], d[j]);
      s += dpp<XOR1>(s); q += dpp<XOR1>(q); s += dpp<XOR2>(s); q += dpp<XOR2>(q); s += dpp<XOR7>(s); q += dpp<XOR7>(q);
      const float mean = s * (1.f / 512.f);
      const float var = fmaf(q, 1.f / 512.f, -mean * mean);
      const float ri = __builtin_amdgcn_rsqf(fmaxf(var, 0.f) + 1e-5f);
      const float cf = fmaf(-ri * mean, Afp, Bfp);
      float y = (d[0][0] + d[1][1]) + (d[2][2] + d[3][3]);
      y = __builtin_bit_cast(float, __builtin_amdgcn_update_dpp(__builtin_bit_cast(int, y), __builtin_bit_cast(int, y), XOR8, 0xa, 0xf, false));
      y = swap16_add(y, y);
      y = fmaf(y, ri, cf);
      const float up = dpp<0x104>(y);
      *LDSP(u16, adst) = (u16)pack_bf(silu_f(y) * up, 0.f);
    }
    STAMP(18);
    LDS_BARRIER();
    STAMP(10);
    {
      const u32x4 af = *LDSP(u32x4, asrc);
      const f32x4 z = {0.f, 0.f, 0.f, 0.f};
      const f32x4 d0 = mfma16(af, b2[0], z), d1 = mfma16(af, b2[1], z), d2 = mfma16(af, b2[2], z), d3 = mfma16(af, b2[3], z);
      const bool g1 = g & 1, g2 = g & 2;
      const float e0 = g1 ? d1[0] : d0[0], e1 = g1 ? d1[1] : d0[1], f0 = g1 ? d3[0] : d2[0], f1 = g1 ? d3[1] : d2[1];
      st8_sc1(wsr, ooff, pack_bf(g2 ? f0 : e0, g2 ? f1 : e1), tag);
    }
    asm volatile(".Lend_b:");
    STAMP(6);
    FINISH();
  }
  if (wave == 0) {
    const int d = fd, ul = lane >> 3;
    u32 gm[2] = {sg[0][0], sg[1][0]};
    if (!all_ok((sg[0][1] == tag) & (sg[1][1] == tag))) gather8<2>(wsr, G_MOD + (1024 + d) * 8, 1536 * 8, tag, gm);
    u32 xa0, xa1;
    {
      due = t1 + P.N2 * 256; gate();
      u32x4 gn[2] = {ld16<AUX_SC1>(wsr, G_NT + lane * 16, 0), ld16<AUX_SC1>(wsr, G_NT + lane * 16, 1024)};
      const int tn = notices(IC<2>{}, G_NT + lane * 16, gn) * 256;
      due = (tn > t1 + 2 * 256 ? max(tn, t1 + P.L0 * 256) + P.L1 * 256 : t1) + P.X3 * 256;
      until(due);
    }
#pragma nounroll
    for (int spin = 0; spin < SPIN_MAX; ++spin) {
      CFENCE();
      xa0 = __builtin_amdgcn_raw_buffer_load_b32(wsr, G_XP + ul * 2048 + (d >> 1) * 4, 0, AUX_SC1);
      xa1 = __builtin_amdgcn_raw_buffer_load_b32(wsr, G_XP + ul * 2048 + (d >> 1) * 4, 1024, AUX_SC1);
      if (all_ok(max(xa0, xa1) != SENT)) break;
      __builtin_amdgcn_s_sleep(1);
    }
    float a0 = (d & 1) ? hi_f(xa0) : lo_f(xa0), a1 = (d & 1) ? hi_f(xa1) : lo_f(xa1);
    a0 += dpp<XOR8>(a0); a1 += dpp<XOR8>(a1);
    a0 = swap16_add(a0, a0); a1 = swap16_add(a1, a1);
    a0 = swap32_add(a0, a0); a1 = swap32_add(a1, a1);
    const bool hb = g & 1;
    float xv = rbf(((d & 1) ? hi_f(sx2) : lo_f(sx2)) + rbf((hb ? hi_f(gm[0]) : lo_f(gm[0])) * rbf(hb ? a1 : a0)));
    float gv = hb ? hi_f(gm[1]) : lo_f(gm[1]);
    PIN(xv); PIN(gv);
    if (wi == 0 && lane == 0) __builtin_amdgcn_raw_buffer_store_b32(tag, wsr, WS_EPOCH, 0, AUX_SC1);
    STAMP(10);
    due = max(due + P.X4 * 256, (int)(((u32)wall_clock64() - t0) << 8) + P.GX * 256);
    const bool last = ul + 40 < NUP;
    u32 o0 = G_OP + (ul * 512 + d) * 8, o5 = last ? o0 : OOB, tg5 = last ? tag : 0u;
    const rsrc_t rout = mk_rsrc(out, 2048u);
    u32 oo = (lane & 40) == 0 ? (g * 512 + d) * 2 : OOB;
    const u32 pk = c == 0 ? 0x00003f80u : (c == 4 ? 0x3f800000u : 0u);
    u32x4 one = {pk, 0u, pk, 0u};
    PIN(o0); PIN(o5); PIN(tg5); PIN(oo); PIN(one);
    __builtin_amdgcn_raw_buffer_store_b16((u16)pack_bf(xv, 0.f), rout, oo, 0, AUX_SC1);
    until(due);
    f32x4 sv;
#pragma nounroll
    for (int spin = 0; spin < SPIN_MAX; ++spin) {
      CFENCE();
      u32x2 og[6];
#pragma unroll
      for (int k = 0; k < 5; ++k) og[k] = ld8<AUX_SC1>(wsr, o0, k * 32768);
      og[5] = ld8<AUX_SC1>(wsr, o5, 5 * 32768);
      sv = f32x4{0.f, 0.f, 0.f, 0.f};
#pragma unroll
      for (int k = 0; k < 3; ++k) sv = mfma16(one, u32x4{og[2 * k][0], og[2 * k][1], og[2 * k + 1][0], og[2 * k + 1][1]}, sv);
      const u32 bad = ((og[0][1] ^ tag) | (og[1][1] ^ tag) | (og[2][1] ^ tag)) | ((og[3][1] ^ tag) | (og[4][1] ^ tag) | (og[5][1] ^ tg5));
      PIN(sv);
      if (all_ok(bad == 0u)) break;
      __builtin_amdgcn_s_sleep(1);
    }
    STAMP(7);
    const float s = sv[0] + dpp<XOR8>(sv[0]);
    __builtin_amdgcn_raw_buffer_store_b16((u16)pack_bf(fmaf(gv, s, xv), 0.f), rout, oo, 0, AUX_SC1);
    asm volatile(".Lend_c:");
  }
  FINISH();
}

extern "C" void kernel_launch(void* const* d_in, const int* in_sizes, int n_in,
                              void* d_out, int out_size, void* d_ws, size_t ws_size,
                              hipStream_t stream) {
  fused_kernel<<<256, 512, 0, stream>>>((const u16*)d_in[0], (const u16*)d_in[1], (const u16*)d_in[2], (const u16*)d_in[3], (const u16*)d_in[4],
                                        (const u16*)d_in[6], (const u16*)d_in[7], (const u16*)d_in[8], (const u16*)d_in[9], (const u16*)d_in[10],
                                        (unsigned char*)d_ws, (u16*)d_out
    );
}
```

```cpp
#include <hip/hip_runtime.h>
#include <stdint.h>

typedef unsigned int u32;
typedef unsigned short u16;
typedef unsigned long long u64;
typedef __attribute__((ext_vector_type(4))) unsigned u32x4;
typedef __attribute__((ext_vector_type(2))) unsigned u32x2;
typedef __attribute__((ext_vector_type(2))) __bf16 bf16x2;
typedef __attribute__((ext_vector_type(4))) float f32x4;
typedef __attribute__((ext_vector_type(2))) float f32x2;
typedef __amdgpu_buffer_rsrc_t rsrc_t;

constexpr int NH = 32, SK = 16384, DH = 128, WIN = 4096, FF = 1376;
struct Prm { int E0, ES, E1, E2, E2C, EU, TEND, DT_UP, L0, L1, X1, X2, X3, X4, NE, FE, M0, M1, M2, M3, M4, GS, GX, N1, N2, GA, WD, DT_WO, LW, LU, EW, PM; };
constexpr Prm PRM0 = {32, 24, 32, 32, 56, 64, 2030, 60, 30, 25, 92, 86, 131, 88, 4, 12, 5, 8, 21, 4, 32, 25, 56, 28, 70, 8, 4, 100, 45, 15, 8, 0x24310};
#define PRM_ARG
#define TABP (&TAB.e[0])
constexpr int NL = 37;
constexpr int NCMIN = 192;
constexpr int WK = 8, WV = 5;
constexpr int WUSE = 13, LNS = 10;
constexpr int QP = 14, QD = 4;
constexpr int VQ = 6;
constexpr int NSL = (NL + 7) / 8;
constexpr int VS = VQ + NSL;
constexpr int MLS = VS + 2;
constexpr int VSIDE = MLS - 5, UPREP = MLS + 8;
constexpr int SIDE = NCMIN / 8 - 4;
constexpr int CPF = 18;
constexpr int TBP = 7, TBL = 11, TBU = 15;
constexpr int cons_end(int v) {
  const int a = v - WV + 1, c = (v - MLS) * 4;
  const int m = a < c ? a : c;
  return m < 0 ? 0 : (m > NL ? NL : m);
}
constexpr int KF = 4, KF0 = 8;
struct Tab { u32 e[1024]; };
#define popc __builtin_popcount
__host__ __device__ constexpr u32 step_off(const Prm& P, u32 s0, u32 bh) {
  u32 sb = s0 & 96u;
  for (int k = 0; k < 5; ++k) sb |= (s0 >> k & 1u) << (P.PM >> (4 * k) & 7);
  const u32 src = (sb >> 5 & 3u) | bh << 2;
  sb ^= (popc(src & P.M0) & 1) | (popc(src & P.M1) & 1) << 1 | (popc(src & P.M2) & 1) << 2 | (popc(src & P.M3) & 1) << 3 | (popc(src & P.M4) & 1) << 4;
  return (sb & 7u) << 10 | (sb >> 3) << 16;
}
constexpr Tab make_tab(const Prm& P) {
  Tab T{};
  for (int bid = 0; bid < 256; ++bid) {
    const int xcd = bid & 7, idx = bid >> 3, part = idx & 3, i8 = idx >> 2, wi = i8 * 8 + xcd;
    const bool has_mod = wi < 48, has_up = wi >= 21;
    const int cb0 = (i8 + 5) & 7, cb1 = (i8 + 4) & 7, ms = wi - 20;
    const int mi = wi < 8 ? wi : (wi < 16 ? 8 + ((xcd + 4) & 7) : (wi < 20 ? (wi - 14) * 8 + 3 : 16 + ms / 7 * 8 + ms % 7 + (ms % 7 >= 3 ? 1 : 0)));
    const int ui = xcd * 5 + (xcd > 5 ? xcd - 5 : 0) + (i8 >= 3 ? (xcd >= 5 ? 1 : 0) + i8 - 3 : 0);
    const int wd = cb0 == 3 ? 0 : P.WD;
    const int e[4] = {P.E0 - 4 * wd + (cb0 == 3 ? P.ES : 0), P.E1 + (cb1 == 3 ? P.ES : 0), has_mod ? (wi < 16 ? P.E2C : P.E2) + ((mi & 7) == 3 ? P.ES : 0) : 0, has_up ? P.EU : 0};
    const int ew = wd ? P.EW : 0, pa = (1024 + e[0] + ew + e[1] + e[2] + e[3]) / 4;
    int c[5] = {0, 0, 0, 0, 128};
    for (int k = 0; k < 3; ++k) c[k + 1] = c[k] + (pa - e[k] - (k == 0 ? ew : 0) + 4) / 8;
    const int nl = c[part + 1] - c[part], wst = e[part] / 4;
    const bool has_w = part < 2 || (part == 2 ? has_mod : has_up);
    const int blk = !has_w ? 0 : (part == 0 ? cb0 : (part == 1 ? xcd * 8 + cb1 : (part == 2 ? mi : ui)));
    const int tau = (part == 3 && has_up ? P.TEND - P.DT_UP : (part == 0 && wd ? P.TEND - P.DT_WO : P.TEND)) * 256 / (2 * nl + wst);
    T.e[4 * bid] = (u32)(c[part] | nl << 7 | blk << 13 | (has_w ? 1 : 0) << 19 | (part == 0 ? wd : 0) << 20);
    T.e[4 * bid + 1] = (u32)(tau | (tau * wst / (part == 0 ? 8 - wd : 8)) << 16);
    if (c[part] > 32 * part + KF0 || c[part + 1] < 32 * part + KF0 + 4) T.e[4 * bid] = 0;
    const u32 pitch = part == 0 ? 1024u : (part == 1 ? 8192u : (part == 2 ? 6144u : 2752u));
    T.e[4 * bid + 2] = (part == 0 ? xcd * 512u * pitch : 0u) + blk * (part == 3 ? 64u : 128u);
    T.e[4 * bid + 3] = pitch;
  }
  return T;
}
constexpr int step_range(const Prm& P, bool mx_) {
  int mx = 0, mn = 1 << 20;
  const Tab T = make_tab(P);
  for (int bid = 0; bid < 256; ++bid) { const int n = T.e[4 * bid] >> 7 & 63; mx = n > mx ? n : mx; mn = n < mn ? n : mn; }
  return mx_ ? mx : mn;
}
__constant__ __attribute__((aligned(64))) const Tab TAB = make_tab(PRM0);
static_assert(step_range(PRM0, true) <= NL && step_range(PRM0, false) >= NCMIN / 8, "a wave's key range does not fit its load steps");
static_assert(cons_end(SIDE + WV + 1) <= NCMIN / 8 && cons_end(UPREP) <= NCMIN / 8 && SIDE > MLS + 3, "steps every wave runs");
constexpr int NUP = FF / 32;
constexpr int SPIN_MAX = 1 << 17;

constexpr u32 WS_EPOCH = 0;
constexpr u32 G_TB = 1024;
constexpr u32 G_NT = 2048;
constexpr u32 G_MOD = 4096;
constexpr u32 G_Q = G_MOD + 3072 * 8;
constexpr u32 G_ML = G_Q + 4096 * 8;
constexpr u32 G_P = G_ML + 256 * 16;
constexpr u32 G_XP = G_P + 256 * 256;
constexpr u32 G_OP = G_XP + 8 * 512 * 4;
constexpr u32 WS_END = G_OP + 43 * 512 * 8;
constexpr u32 SENT = 0xffffffffu;
constexpr u32 OOB = 0x40000000u;
#define WST 0

constexpr int AUX_NT = 2, AUX_SC1 = 16;

__device__ __forceinline__ float lo_f(u32 v) { return __uint_as_float(v << 16); }
__device__ __forceinline__ float hi_f(u32 v) { return __uint_as_float(v & 0xffff0000u); }
__device__ __forceinline__ float rbf(float x) { return (float)(__bf16)x; }
__device__ __forceinline__ u32 pack_bf(float lo, float hi) {
  bf16x2 r; r[0] = (__bf16)lo; r[1] = (__bf16)hi; return __builtin_bit_cast(u32, r);
}
__device__ __forceinline__ float dot2(u32 k, u32 q, float acc) {
  return __builtin_amdgcn_fdot2_f32_bf16(__builtin_bit_cast(bf16x2, k), __builtin_bit_cast(bf16x2, q), acc, false);
}
template <int CTRL> __device__ __forceinline__ float dpp(float x) {
  return __builtin_bit_cast(float, __builtin_amdgcn_mov_dpp(__builtin_bit_cast(int, x), CTRL, 0xf, 0xf, true));
}
constexpr int XOR1 = 0xB1, XOR2 = 0x4E, XOR7 = 0x141, XOR8 = 0x128;
__device__ __forceinline__ float swap32_add(float a, float b) {
  auto t = __builtin_amdgcn_permlane32_swap(__float_as_uint(a), __float_as_uint(b), false, false);
  return __uint_as_float(t[0]) + __uint_as_float(t[1]);
}
__device__ __forceinline__ float swap16_add(float a, float b) {
  auto t = __builtin_amdgcn_permlane16_swap(__float_as_uint(a), __float_as_uint(b), false, false);
  return __uint_as_float(t[0]) + __uint_as_float(t[1]);
}
__device__ __forceinline__ float wave_sum(float x) {
  x += dpp<XOR1>(x); x += dpp<XOR2>(x); x += dpp<XOR7>(x); x += dpp<XOR8>(x);
  x = swap16_add(x, x);
  return swap32_add(x, x);
}
__device__ __forceinline__ float wave_max(float x) {
  x = fmaxf(x, dpp<XOR1>(x)); x = fmaxf(x, dpp<XOR2>(x)); x = fmaxf(x, dpp<XOR7>(x)); x = fmaxf(x, dpp<XOR8>(x));
  auto s = __builtin_amdgcn_permlane16_swap(__float_as_uint(x), __float_as_uint(x), false, false);
  x = fmaxf(__uint_as_float(s[0]), __uint_as_float(s[1]));
  auto t = __builtin_amdgcn_permlane32_swap(__float_as_uint(x), __float_as_uint(x), false, false);
  return fmaxf(__uint_as_float(t[0]), __uint_as_float(t[1]));
}
template <int CTRL> __device__ __forceinline__ float sel_add(float lo, float hi, bool up) {
  const float t1 = lo + dpp<CTRL>(lo), t2 = hi + dpp<CTRL>(hi);
  return up ? t2 : t1;
}
template <int I> __device__ __forceinline__ float bc16(float x) {
  return __builtin_bit_cast(float, __builtin_amdgcn_update_dpp(0, __builtin_bit_cast(int, x), 0x150 + I, 0xf, 0xf, false));
}
__device__ __forceinline__ float bcast16(float x, int i) {
  switch (i) {
    case 0: return bc16<0>(x); case 1: return bc16<1>(x); case 2: return bc16<2>(x); case 3: return bc16<3>(x);
    case 4: return bc16<4>(x); case 5: return bc16<5>(x); case 6: return bc16<6>(x); case 7: return bc16<7>(x);
    case 8: return bc16<8>(x); case 9: return bc16<9>(x); case 10: return bc16<10>(x); case 11: return bc16<11>(x);
    case 12: return bc16<12>(x); case 13: return bc16<13>(x); case 14: return bc16<14>(x); default: return bc16<15>(x);
  }
}
__device__ __forceinline__ float silu_f(float x) { return x * __builtin_amdgcn_rcpf(1.f + __expf(-x)); }

template <int AUX> __device__ __forceinline__ u32x4 ld16(rsrc_t r, u32 voff, u32 soff) {
  return __builtin_amdgcn_raw_buffer_load_b128(r, voff, soff, AUX);
}
template <int AUX> __device__ __forceinline__ u32x2 ld8(rsrc_t r, u32 voff, u32 soff) {
  return __builtin_amdgcn_raw_buffer_load_b64(r, voff, soff, AUX);
}
__device__ __forceinline__ void st8_sc1(rsrc_t r, u32 voff, u32 v, u32 tag) {
  __builtin_amdgcn_raw_buffer_store_b64(u32x2{v, tag}, r, voff, 0, AUX_SC1);
}
__device__ __forceinline__ void st16_sc1(rsrc_t r, u32 voff, u32 v0, u32 v1, u32 tag) {
  __builtin_amdgcn_raw_buffer_store_b128(u32x4{v0, tag, v1, tag}, r, voff, 0, AUX_SC1);
}
__device__ __forceinline__ rsrc_t mk_rsrc(const void* p, u32 bytes) {
  return __builtin_amdgcn_make_buffer_rsrc(const_cast<void*>(p), 0, bytes, 0x00020000);
}
#define WAIT_VM(n) __builtin_amdgcn_s_waitcnt(((n) & 15) | (((n) >> 4) << 14) | (7 << 4) | (15 << 8))
#define SB() __builtin_amdgcn_sched_barrier(0)
#define CFENCE() asm volatile("" ::: "memory")
__device__ __forceinline__ bool all_ok(bool ok) { return __builtin_amdgcn_ballot_w64(!ok) == 0ull; }

template <int N> __device__ __forceinline__ void gather16(rsrc_t ws, u32 voff, u32 stride, u32 tag, u32x4 (&g)[N]) {
#pragma nounroll
  for (int spin = 0; spin < SPIN_MAX; ++spin) {
    CFENCE();
    bool ok = true;
#pragma unroll
    for (int i = 0; i < N; ++i) g[i] = ld16<AUX_SC1>(ws, voff + i * stride, 0);
#pragma unroll
    for (int i = 0; i < N; ++i) ok &= (g[i][1] == tag) & (g[i][3] == tag);
    if (all_ok(ok)) break;
    __builtin_amdgcn_s_sleep(8);
  }
}
template <int N> __device__ __forceinline__ void gather8(rsrc_t ws, u32 voff, u32 stride, u32 tag, u32 (&g)[N], bool act = true) {
#pragma nounroll
  for (int spin = 0; spin < SPIN_MAX; ++spin) {
    CFENCE();
    bool ok = true;
    u32x2 t[N];
#pragma unroll
    for (int i = 0; i < N; ++i) t[i] = ld8<AUX_SC1>(ws, voff + i * stride, 0);
#pragma unroll
    for (int i = 0; i < N; ++i) { ok &= (t[i][1] == tag); g[i] = t[i][0]; }
    if (all_ok(ok | !act)) break;
    __builtin_amdgcn_s_sleep(8);
  }
}

struct Smem {
  float red[8][128];
  float ml[8][2];
  float r2[64 * 20];
  u16 hb[3 * 1024];
  float stat[8][64];
  float red2[8][128];
  u32 dummy[64];
  u32x2 bc;
  u32x4 zero[4];
  u32 mflag[8];
  u32x4 wbuf[8][8][64];
  __attribute__((aligned(256))) u16 w2b[32][512];
  u16 actl[2][32];
};

#define STAMP(k) do {} while (0)
#define WSTAMP(k) do {} while (0)
#define FINISH() return
#define PIN(x) asm volatile("" : "+v"(x))
#define LDS_BARRIER() do { __builtin_amdgcn_s_waitcnt(0xc07f); __builtin_amdgcn_s_barrier(); CFENCE(); } while (0)
#define WAVE_SYNC() do { __builtin_amdgcn_fence(__ATOMIC_RELEASE, "wavefront"); __builtin_amdgcn_wave_barrier(); __builtin_amdgcn_fence(__ATOMIC_ACQUIRE, "wavefront"); } while (0)

__device__ __forceinline__ void dma16(rsrc_t r, u32 voff, u32 soff, u32 lds_dst) {
  u32 keep;
  asm volatile("s_mov_b32 %0, m0\n\ts_mov_b32 m0, %4\n\ts_nop 4\n\tbuffer_load_dwordx4 %1, %2, %3 offen nt lds\n\ts_mov_b32 m0, %0"
               : "=&s"(keep) : "v"(voff), "s"(r), "s"(soff), "s"(lds_dst) : "memory");
}
__device__ __forceinline__ u32 perm_lo(u32 a, u32 b) { return __builtin_amdgcn_perm(b, a, 0x05040100u); }
__device__ __forceinline__ u32 perm_hi(u32 a, u32 b) { return __builtin_amdgcn_perm(b, a, 0x07060302u); }
__device__ __forceinline__ float rdl(float x, int l) { return __int_as_float(__builtin_amdgcn_readlane(__float_as_int(x), l)); }

typedef __attribute__((ext_vector_type(8))) __bf16 bf16x8;
__device__ __forceinline__ f32x4 mfma16(u32x4 a, u32x4 b, f32x4 c) {
  return __builtin_amdgcn_mfma_f32_16x16x32_bf16(__builtin_bit_cast(bf16x8, a), __builtin_bit_cast(bf16x8, b), c, 0, 0, 0);
}
__device__ __forceinline__ float sum8(const float* p) {
  const f32x4 a = *reinterpret_cast<const f32x4*>(p), b = *reinterpret_cast<const f32x4*>(p + 4);
  return ((a[0] + b[0]) + (a[1] + b[1])) + ((a[2] + b[2]) + (a[3] + b[3]));
}
__device__ __forceinline__ float ln_ri(float var) {
  return __builtin_amdgcn_rcpf(rbf(__builtin_amdgcn_sqrtf(rbf(rbf(fmaxf(var, 0.f)) + 1e-5f))));
}
__device__ __forceinline__ void ln_stats2(const float (&x0)[8], const float (&x1)[8], float& mb0, float& ri0, float& mb1, float& ri1) {
  const float k0 = rdl(x0[0], 0), k1 = rdl(x1[0], 0);
  float s0 = 0.f, s1 = 0.f, q0 = 0.f, q1 = 0.f;
#pragma unroll
  for (int k = 0; k < 8; ++k) {
    const float d0 = x0[k] - k0, d1 = x1[k] - k1;
    s0 += d0; s1 += d1; q0 = fmaf(d0, d0, q0); q1 = fmaf(d1, d1, q1);
  }
  float t = swap16_add(swap32_add(s0, s1), swap32_add(q0, q1));
  t += dpp<XOR1>(t); t += dpp<XOR2>(t); t += dpp<XOR7>(t); t += dpp<XOR8>(t);
  auto u = __builtin_amdgcn_permlane16_swap(__float_as_uint(t), __float_as_uint(t), false, false);
  const float ms = __uint_as_float(u[0]) * (1.f / 512.f), mq = __uint_as_float(u[1]) * (1.f / 512.f);
  const float ri = ln_ri(fmaf(-ms, ms, mq));
  mb0 = rbf(k0 + rdl(ms, 0)); mb1 = rbf(k1 + rdl(ms, 32));
  ri0 = rdl(ri, 0); ri1 = rdl(ri, 32);
}
template <int I> struct IC { static constexpr int v = I; };
template <int I, int N, class F> __device__ __forceinline__ void sfor(F&& f) {
  if constexpr (I < N) { if (f(IC<I>{})) sfor<I + 1, N>(f); }
}
typedef __attribute__((ext_vector_type(4))) short s16x4;
typedef __attribute__((address_space(3))) s16x4* lds_s16x4p;
__device__ __forceinline__ u32x2 tr_read(u32 lds_addr) {
  return __builtin_bit_cast(u32x2, __builtin_amdgcn_ds_read_tr16_b64_v4i16((lds_s16x4p)lds_addr));
}
__device__ __forceinline__ u32 next_tag(u32 e) { return ((e | 0x40004000u) + 1u) & ~0x40004000u; }
__device__ __forceinline__ float u2f(u32 v) { return __uint_as_float(v); }
#define LDSP(T, a) ((__attribute__((address_space(3))) T*)(a))
template <class T> __device__ __forceinline__ u32 lds_addr(T* p) { u32 a = (u32)(uintptr_t)p; PIN(a); return a; }

typedef __attribute__((ext_vector_type(16))) unsigned u32x16;
typedef __attribute__((ext_vector_type(8))) unsigned u32x8;
__global__ void __launch_bounds__(512, 2) fused_kernel(const u16* x_, const u16* kc_, const u16* vc_, const u16* wq_, const u16* wo_, const u16* w1_,
                                                       const u16* w3_, const u16* w2_, const u16* cnd_, const u16* wmod_, unsigned char* ws_, u16* out_ PRM_ARG) {
  __shared__ __attribute__((aligned(256))) Smem sm;
  constexpr Prm P = PRM0;
  const int TEND = P.TEND;
  const int bid = blockIdx.x, tid = threadIdx.x;
  u32x4 kr[NL];
  u32x16 ka; u32x8 kb; u32x4 tev; u64 pt0; u32 tch2;
  asm volatile("s_load_dwordx16 %0, %3, 0x0\n\ts_load_dwordx8 %1, %3, 0x40\n\ts_memrealtime %2"
               : "=&s"(ka), "=&s"(kb), "=&s"(pt0) : "s"(__builtin_amdgcn_kernarg_segment_ptr()));
  SB();
  {
    u64 pc; u32 vb;
    asm volatile("s_getpc_b64 %2\n.Lcpf_first:\n\tv_mul_u32_u24 %1, ((.Lend_c-.Lcpf_first)>>10)<<2, %3\n\tglobal_load_dword %0, %1, %2"
                 : "=&v"(tch2), "=&v"(vb), "=&s"(pc) : "v"((bid & ~7) + (tid >> 6)) : "memory");
    asm volatile("global_load_dwordx4 %0, %1, %2" : "=&v"(tev) : "v"(bid << 4), "s"(TABP) : "memory");
  }
  SB();
  const int xcd = bid & 7, idx = bid >> 3, part = idx & 3;
  const int b = idx >> 2 & 1, head = xcd * 4 + (((idx >> 3) & 1) << 1 | idx >> 4);
  const u32 so0 = step_off(P, 32 * part + KF0, b * 32 + head) & ~0xc00u;
  u32 va = (((b * 32 + head) * SK + (SK - WIN)) * DH * 2 + so0) | (tid >> 6) << 13 | (tid & 63) << 4;
  PIN(va);
  SB();
  asm volatile("s_waitcnt lgkmcnt(0)" : "+s"(ka), "+s"(kb), "+s"(pt0));
  SB();
  asm volatile("global_load_dwordx4 %0, %4, %5 nt\n\tglobal_load_dwordx4 %1, %4, %5 offset:1024 nt\n\tglobal_load_dwordx4 %2, %4, %5 offset:2048 nt\n\tglobal_load_dwordx4 %3, %4, %5 offset:3072 nt"
               : "=&v"(kr[0]), "=&v"(kr[1]), "=&v"(kr[2]), "=&v"(kr[3]) : "v"(va), "s"((u64)ka[2] | (u64)ka[3] << 32) : "memory");
  SB();
  const u32 t0 = (u32)pt0, tka = (u32)wall_clock64();
  auto kap = [&](int k) { return k < 8 ? (u64)ka[2 * k] | (u64)ka[2 * k + 1] << 32 : (u64)kb[2 * k - 16] | (u64)kb[2 * k - 15] << 32; };
  const u16 *x = (const u16*)kap(0), *kc = (const u16*)kap(1), *vc = (const u16*)kap(2), *w2 = (const u16*)kap(7), *cnd = (const u16*)kap(8);
  unsigned char* ws = (unsigned char*)kap(10); u16* out = (u16*)kap(11);
  const int lane = tid & 63, wave = __builtin_amdgcn_readfirstlane(tid >> 6);
  const bool p3 = part == 3;
  const u64 wa = part == 0 ? kap(4) : (part == 1 ? kap(3) : (part == 2 ? kap(9) : ((wave & 1) ? kap(6) : kap(5))));
  asm volatile("s_waitcnt vmcnt(4)" : "+v"(tev));
  const u32 te0 = __builtin_amdgcn_readfirstlane(tev[0]), te1 = __builtin_amdgcn_readfirstlane(tev[1]);
  const u32 pitch = __builtin_amdgcn_readfirstlane(tev[3]), wsz = pitch << (part == 0 ? 12 : 9);
  const int sh = p3 ? 2 : 3;
  u32 wv0 = (u32)((p3 ? (wave >> 1) * 128 : wave * 64) + (lane >> sh)) * pitch + ((lane & (p3 ? 3 : 7)) ^ (lane >> (p3 ? 5 : 3))) * 16u;
  const u32 wimg = (u32)(uintptr_t)(&sm.wbuf[0][0][0]);
  u32 wld = wimg + (p3 ? (wave & 1) * 32768 + (wave >> 1) * 8192 : wave * 8192);
  const int bl0 = te0 & 127, nl = te0 >> 7 & 63, blk = te0 >> 13 & 63;
  const bool has_w = te0 >> 19 & 1;
  const rsrc_t rw = mk_rsrc((const void*)wa, wsz);
  const u32 wstep = pitch << (6 - sh), wflip = p3 ? 32u : 0u;
  u32 wso = __builtin_amdgcn_readfirstlane(tev[2]);
  int wleft = 8;
  auto piece = [&]() {
    dma16(rw, wv0, wso, wld);
    wso += wstep; wld += 1024;
    wv0 ^= (wleft & 1) ? wflip : 0u;
    --wleft;
  };
  int tau = te1 & 0xffff;
  const int tauw = te1 >> 16;
  int due = KF * tau, now = 0;
  auto gate = [&]() {
    while (now < due) { now = (int)(((u32)wall_clock64() - t0) << 8); if (now < due) __builtin_amdgcn_s_sleep(1); }
    due += tau;
  };
  const bool modc = part == 2 && bid < 64;
  if (has_w) {
    if (modc) now = 1 << 30;
#pragma nounroll
    while (wleft > (int)(te0 >> 20)) { gate(); due += tauw - tau; piece(); }
    now = 0;
  }
  SB();
  STAMP(23);
  const rsrc_t wsr = mk_rsrc(ws, WS_END);
  const int g = lane >> 4, c = lane & 15;
  const int i8 = idx >> 2, wi = i8 * 8 + xcd;
  const bool is_up = p3 && has_w;
  const size_t base = ((size_t)(b * NH + head) * SK + (SK - WIN)) * DH;
  const rsrc_t rk = mk_rsrc(kc + base, WIN * DH * 2), rv = mk_rsrc(vc + base, WIN * DH * 2);
  u32 sot;
  {
    u32 s0 = bl0 + lane - KF;
    s0 += s0 >= (u32)(32 * part + KF0) ? KF : 0;
    sot = (lane < KF ? so0 | (u32)lane << 10 : step_off(P, s0, b * 32 + head)) | (u32)wave << 13;
    sot = lane < nl ? sot : OOB;
    PIN(sot);
  }
  auto until = [&](int t) { while ((int)(((u32)wall_clock64() - t0) << 8) < t); };
  const u32 pslot = G_P + ((b * 32 + head) * 4 + part) * 256;
  const rsrc_t rx = mk_rsrc(part == 2 ? cnd : x, (part == 1 || (part == 2 && has_w)) ? 2048u : 0u);
  u32x2 ep; u32 xs0, xs1, tch1; u32x4 xr0, xr1;
  auto small_ops = [&]() {
    ep = ld8<AUX_SC1>(wsr, wave == 7 ? WS_EPOCH : OOB, 0);
    xs0 = __builtin_amdgcn_raw_buffer_load_b16(rx, tid * 2, 0, 0); xs1 = __builtin_amdgcn_raw_buffer_load_b16(rx, tid * 2, 1024, 0);
    xr0 = ld16<0>(rx, part == 1 ? lane * 16 : OOB, 0); xr1 = ld16<0>(rx, part == 1 ? lane * 16 : OOB, 1024);
    tch1 = __builtin_amdgcn_raw_buffer_load_b32(mk_rsrc(w2 + (size_t)blk * 32 * 512, is_up ? 64u : 0u), tid == 384 ? 0 : OOB, 0, AUX_NT);
  };
  if (modc) __builtin_amdgcn_raw_buffer_store_b128(u32x4{SENT, SENT, SENT, SENT}, wsr, tid < 320 ? G_P + wi * 5120 + tid * 16 : OOB, 0, AUX_SC1);
  SB();
  u32 tag = 0;
  u32x4 bf[8];
  auto wo_prep = [&](int s0 = 0, int s1 = 2) {
    const u32 wi_ = wimg + wave * 8192;
    const int q = (lane >> 2) & 3, pp = lane & 3;
#pragma unroll
    for (int s = s0; s < s1; ++s) {
#pragma unroll
      for (int t = 0; t < 4; ++t) {
        const int r0 = s * 32 + g * 8 + q, r1 = r0 + 4, ch = t * 2 + (pp >> 1);
        const u32x2 lo = tr_read(wi_ + r0 * 128 + ((ch ^ (r0 & 7)) * 16) + (pp & 1) * 8), hi = tr_read(wi_ + r1 * 128 + ((ch ^ (r1 & 7)) * 16) + (pp & 1) * 8);
        bf[s * 4 + t] = u32x4{lo[0], lo[1], hi[0], hi[1]};
      }
    }
  };
  auto gemv_go = [&](u32 hh) {
    sm.hb[tid] = (u16)hh; sm.hb[512 + tid] = (u16)(hh >> 16);
    WAVE_SYNC();
    const u32x4* a = (c & 11) == 0 ? reinterpret_cast<const u32x4*>(&sm.hb[(c >> 2) * 512 + wave * 64 + g * 8]) : &sm.zero[0];
    const u32x4 a0 = a[0], a1 = a[(c & 11) == 0 ? 4 : 0];
    const f32x4 z = {0.f, 0.f, 0.f, 0.f};
    f32x4 d[4];
#pragma unroll
    for (int t = 0; t < 4; ++t) d[t] = mfma16(a0, bf[t], z);
#pragma unroll
    for (int t = 0; t < 4; ++t) d[t] = mfma16(a1, bf[4 + t], d[t]);
    if (lane < 32) {
#pragma unroll
      for (int t = 0; t < 4; ++t) sm.r2[(t * 16 + c) * 20 + g * 8 + wave] = d[t][0];
    }
  };
  auto mod_stage = [&]() {
    wo_prep();
    gemv_go(pack_bf(silu_f(lo_f(xs0)), silu_f(lo_f(xs1))));
    __builtin_amdgcn_s_waitcnt(0xc07f);
    if (lane == 0) *LDSP(volatile u32, (u32)(uintptr_t)&sm.mflag[wave]) = 1u;
    if (wave == 7) {
#pragma nounroll
      for (int spin = 0; spin < SPIN_MAX; ++spin) {
        const u32 f = *LDSP(volatile u32, (u32)(uintptr_t)&sm.mflag[lane & 7]);
        if (all_ok(f != 0u)) break;
        __builtin_amdgcn_s_sleep(1);
      }
      CFENCE();
      st8_sc1(wsr, G_MOD + (blk * 64 + lane) * 8, pack_bf(sum8(&sm.r2[lane * 20]), sum8(&sm.r2[lane * 20 + 8])), tag);
    }
    STAMP(9);
  };
  if (lane < 4) sm.zero[lane] = u32x4{0u, 0u, 0u, 0u};
  if (part == 2 && has_w) {
    if (tid < 8) sm.mflag[tid] = 0u;
    LDS_BARRIER();
    if (__builtin_expect(modc, 0)) {
      small_ops();
      asm volatile("s_waitcnt vmcnt(0)" ::: "memory");
      tag = next_tag(__builtin_amdgcn_readfirstlane(ep[0]));
      mod_stage();
      __builtin_amdgcn_s_barrier();
    }
  }
  SB();
  const int fd = wi * 8 + (lane & 7);
  const int ub = wave >> 2, ud = (wave & 3) * 128 + c * 8 + g * 2;
  u32x4 sg[3]; u32 sx2;
  auto side_loads = [&]() {
    const bool fin = part == 1 && wave == 0;
    sg[0] = ld16<AUX_SC1>(wsr, is_up ? G_MOD + (1024 + ud) * 8 : (fin ? G_MOD + (1024 + fd) * 8 : OOB), 0);
    sg[1] = ld16<AUX_SC1>(wsr, is_up ? G_MOD + (1536 + ud) * 8 : (fin ? G_MOD + (2560 + fd) * 8 : OOB), 0);
    sg[2] = ld16<AUX_SC1>(wsr, is_up ? G_MOD + (2048 + ud) * 8 : OOB, 0);
    sx2 = __builtin_amdgcn_raw_buffer_load_b32(mk_rsrc(x, (is_up || fin) ? 2048u : 0u), is_up ? ub * 1024 + ud * 2 : (g & 1) * 1024 + (fd >> 1) * 4, 0, 0);
  };
  auto side_pin = [&]() { PIN(sg[0]); PIN(sg[1]); PIN(sg[2]); PIN(sx2); };
  const u32 mlo = G_ML + (head * 2 + b) * 64 + (lane & 3) * 16;
  float fmrg = 0.f;
  bool fok = wave != 0;
  auto merge_factor = [&](const u32x4& ml) {
    const float mi = u2f(ml[0]);
    float M = fmaxf(mi, dpp<XOR1>(mi)); M = fmaxf(M, dpp<XOR2>(M));
    const float e = __expf(mi - M);
    float L = e * u2f(ml[2]);
    L += dpp<XOR1>(L); L += dpp<XOR2>(L);
    fmrg = rdl(e * __builtin_amdgcn_rcpf(L), part);
  };
  f32x2 ga = {0.f, 0.f}, xin = {0.f, 0.f}, cc1 = {0.f, 0.f};
  float Af = 0.f, Bf = 0.f;
  const int hpos = ub * 512 + ud;
  auto up_prep = [&]() {
    if (!all_ok((sg[0][1] == tag) & (sg[0][3] == tag) & (sg[1][1] == tag) & (sg[1][3] == tag) & (sg[2][1] == tag) & (sg[2][3] == tag))) gather16<3>(wsr, G_MOD + (1024 + ud) * 8, 512 * 8, tag, sg);
    ga = f32x2{ub ? hi_f(sg[0][0]) : lo_f(sg[0][0]), ub ? hi_f(sg[0][2]) : lo_f(sg[0][2])};
    cc1 = f32x2{rbf(1.f + (ub ? hi_f(sg[2][0]) : lo_f(sg[2][0]))), rbf(1.f + (ub ? hi_f(sg[2][2]) : lo_f(sg[2][2])))};
    xin = f32x2{lo_f(sx2), hi_f(sx2)};
    u32* hp = reinterpret_cast<u32*>(&sm.hb[hpos]);
    hp[512] = pack_bf(cc1[0], cc1[1]);
    hp[1024] = ub ? perm_hi(sg[1][0], sg[1][2]) : perm_lo(sg[1][0], sg[1][2]);
  };
  auto up_frags = [&]() {
    const int pp = lane & 3;
    const u32 a = wimg + (pp & 1) * 32768 + ((pp >> 1) * 32 + g * 8 + ((lane >> 2) & 3)) * 64 + (((wave >> 1) ^ ((g & 1) | (pp & 2))) * 16) + (wave & 1) * 8;
#pragma unroll
    for (int rb = 0; rb < 8; ++rb) {
      const u32x2 lo = tr_read(a + rb * 4096), hi = tr_read(a + rb * 4096 + 256);
      bf[rb] = u32x4{lo[0], lo[1], hi[0], hi[1]};
    }
  };
  const u32x4* ap = reinterpret_cast<const u32x4*>(&sm.hb[((lane >> 3) & 1) * 512 + (lane & 1) * 32 + g * 8]);
  const bool par = lane & 8;
  float l0 = 0.f, l1 = 0.f;
  u32x2 gmd[2], tbg;
  int t1 = 0;
#pragma unroll
  for (int i = NCMIN / 8; i < NL; ++i) kr[i] = u32x4{0u, 0u, 0u, 0u};
  sfor<KF, NL>([&](auto ic) {
    constexpr int i = ic.v;
    if (i >= NCMIN / 8 && i >= nl) return false;
    const u32 so = __builtin_amdgcn_readlane(sot, i);
    gate();
    SB();
    kr[i] = ld16<AUX_NT>(rk, lane * 16, so);
    if constexpr (i == KF) small_ops();
    if constexpr (i == TBL) tbg = ld8<AUX_SC1>(wsr, wave == 7 ? G_TB + xcd * 128 + (lane & 7) * 8 : OOB, 0);
    if constexpr (i == QP) { gmd[0] = ld8<AUX_SC1>(wsr, part == 1 ? G_MOD + tid * 8 : OOB, 0); gmd[1] = ld8<AUX_SC1>(wsr, part == 1 ? G_MOD + tid * 8 : OOB, 4096); }
    SB();
    SB();
    if (i >= WK) { WAIT_VM(WK); SB(); }
    if (i == KF) STAMP(25);
    if (i == 8) STAMP(12);
    if (i == 20) STAMP(21);
    if (i == 25) STAMP(22);
    if (i == TBP) {
      if (part == 3 && i8 == 0 && wave == 7) { tag = next_tag(__builtin_amdgcn_readfirstlane(ep[0])); if (lane < 8) st8_sc1(wsr, G_TB + lane * 128 + xcd * 8, t0 + (((tka - t0) * P.GA) >> 3) - P.GS, tag); }
      SB();
    }
    if (i == TBU) {
      if (wave == 7) {
        const bool ok = tbg[1] == tag;
        float lf = ok ? (float)min(max((int)(t0 - tbg[0]), -300), 300) : 0.f, nf = ok ? 1.f : 0.f;
        lf += dpp<XOR1>(lf); nf += dpp<XOR1>(nf); lf += dpp<XOR2>(lf); nf += dpp<XOR2>(nf); lf += dpp<XOR7>(lf); nf += dpp<XOR7>(nf);
        if (lane == 0) sm.bc = u32x2{tag, t0 - (u32)(int)(lf * __builtin_amdgcn_rcpf(fmaxf(nf, 1.f)))};
      }
      LDS_BARRIER();
      const u32x2 bc = sm.bc;
      tag = __builtin_amdgcn_readfirstlane(bc[0]);
      t1 = (__builtin_amdgcn_readfirstlane((int)(bc[1] - t0)) + TEND + P.X1) * 256;
      tau -= __builtin_amdgcn_readfirstlane((int)((float)(int)(t0 - bc[1]) * 256.f * __builtin_amdgcn_rcpf((float)(2 * nl - TBU - 2))));
      SB();
    }
    if (i == LNS) {
      tag = next_tag(__builtin_amdgcn_readfirstlane(ep[0]));
      if (part == 1) {
        float x0[8], x1[8], mb0, ri0, mb1, ri1;
#pragma unroll
        for (int k = 0; k < 4; ++k) { x0[2 * k] = lo_f(xr0[k]); x0[2 * k + 1] = hi_f(xr0[k]); x1[2 * k] = lo_f(xr1[k]); x1[2 * k + 1] = hi_f(xr1[k]); }
        ln_stats2(x0, x1, mb0, ri0, mb1, ri1);
        l0 = rbf(rbf(lo_f(xs0) - mb0) * ri0); l1 = rbf(rbf(lo_f(xs1) - mb1) * ri1);
      }
      PIN(l0); PIN(l1);
      SB();
    }
    if (i == WUSE) {
      PIN(tch1); PIN(tch2);
      if ((part == 1 || (part == 2 && has_w && !modc))) {
        WAIT_VM(WK); CFENCE();
        if (part == 2) mod_stage(); else wo_prep();
      }
#pragma unroll
      for (int k = 0; k < 8; ++k) PIN(bf[k]);
      SB();
    }
    if (i == QP + QD) {
      if (part == 1) {
        u32 gm[2] = {gmd[0][0], gmd[1][0]};
        if (!all_ok((gmd[0][1] == tag) & (gmd[1][1] == tag))) { STAMP(11); gather8<2>(wsr, G_MOD + tid * 8, 512 * 8, tag, gm); }
        STAMP(5);
        gemv_go(pack_bf(rbf(l0 * rbf(1.f + lo_f(gm[1]))) + lo_f(gm[0]), rbf(l1 * rbf(1.f + hi_f(gm[1]))) + hi_f(gm[0])));
      }
      SB();
    }
    if (i == QP + QD + 1) {
      if (part == 1) {
        LDS_BARRIER();
        if (wave == 0) {
          const int bb = lane >> 5, jq = lane & 31;
          st8_sc1(wsr, G_Q + (bb * 2048 + blk * 32 + jq) * 8, pack_bf(sum8(&sm.r2[2 * jq * 20 + bb * 8]), sum8(&sm.r2[(2 * jq + 1) * 20 + bb * 8])), tag);
        }
        STAMP(6);
      }
      SB();
    }
    return true;
  });
  PIN(sot);
  const u32 qoff = G_Q + ((b * 32 + head) * 64 + c * 4) * 8;
  u32x4 qg[2], vr[NL], fl2[3];
  u32 qq[4];
  constexpr int NB = (NL + 15) / 16;
  float xlo, sc[NB], pr[NB], m = 0.f;
  f32x4 acc = {0.f, 0.f, 0.f, 0.f};
  u32 pb[NB], pm[4], cpf;
  const u32 adst0 = lds_addr(lane < 32 ? &sm.red[wave][c * 8 + g * 4] : &sm.r2[(lane - 32) * 4]);
  u32 po, rdst, rsrc, xo;
  auto wo_addr = [&]() {
    po = G_P + (((c >> 2 & 1) * 32 + xcd * 4 + (wave >> 1)) * 4 + (c & 3)) * 256 + (wave & 1) * 128 + (c >> 3) * 64 + g * 16;
    rdst = lane < 32 ? (u32)(uintptr_t)&sm.red2[wave][0] + lane * 16 : (u32)(uintptr_t)&sm.r2[0] + (lane - 32) * 16;
    rsrc = (u32)(uintptr_t)&sm.red2[0][((wave & 1) * 16 + (lane & 15)) * 4 + (lane >> 4)];
    xo = (lane & 1) ? OOB : G_XP + xcd * 2048 + (wave & 1) * 1024 + (blk * 64 + lane) * 2;
    PIN(po); PIN(rdst); PIN(rsrc); PIN(xo);
  };
  u32 pso = wave == 0 ? pslot + lane * 4 : OOB;
  PIN(pso);
  const u32 nadd = (te0 >> 20) ? P.LW : (is_up ? P.LU : 0);
  u32 nto = tid == 0 ? G_NT + bid * 8 : OOB;
  PIN(nto);
  auto notices = [&](auto nc_, u32 off, u32x4 (&gn)[nc_.v]) -> int {
    constexpr int N = nc_.v;
#pragma nounroll
    for (int spin = 0; spin < SPIN_MAX; ++spin) {
      bool ok = true;
#pragma unroll
      for (int i = 0; i < N; ++i) ok &= (gn[i][1] == tag) & (gn[i][3] == tag);
      if (all_ok(ok)) break;
      __builtin_amdgcn_s_sleep(10);
      CFENCE();
#pragma unroll
      for (int i = 0; i < N; ++i) gn[i] = ld16<AUX_SC1>(wsr, off, i * 1024);
    }
    int mx = 0;
#pragma unroll
    for (int i = 0; i < N; ++i) mx = max(mx, max((int)(gn[i][0] - t0), (int)(gn[i][2] - t0)));
    return __builtin_amdgcn_readfirstlane((int)wave_max((float)min(mx, 1 << 20)));
  };
  const int side = nl >= SIDE + 13 ? SIDE + 8 : (nl >= SIDE + 9 ? SIDE + 4 : SIDE);
  sfor<0, NL + WV>([&](auto ic) {
    constexpr int v = ic.v;
    if (cons_end(v - 1) >= NCMIN / 8 && cons_end(v - 1) >= nl) return false;
    if constexpr (v < NL) {
      const u32 so = __builtin_amdgcn_readlane(sot, v);
      if constexpr (v >= 12) { if (v == nl - P.NE) tau = (tau * P.FE) >> 4; }
      if constexpr (v >= NCMIN / 8) { if (v < nl) gate(); } else gate();
      SB();
      vr[v] = ld16<AUX_NT>(rv, lane * 16, so);
      SB();
      SB();
    }
    if constexpr (v == NCMIN / 8 - 1) {
#pragma unroll
      for (int i = NCMIN / 8; i < NL; ++i) asm volatile("" : "=v"(vr[i]));
      asm volatile("" : "=v"(fl2[1]), "=v"(fl2[2]));
    }
    if constexpr (v == VQ - 4) { qg[0] = ld16<AUX_SC1>(wsr, qoff, 0); qg[1] = ld16<AUX_SC1>(wsr, qoff + 16, 0); }
    if constexpr (v == VSIDE) side_loads();
    if constexpr (v == CPF) {
      u64 pc; u32 dlt, len;
      asm volatile("s_getpc_b64 %0\n.Lcpf_here:\n\ts_mov_b32 %1, .Ltail_start-.Lcpf_here\n\ts_mov_b32 %2, .Lend_a-.Ltail_start\n\ts_max_i32 %2, %2, .Lend_b-.Ltail_start\n\ts_max_i32 %2, %2, .Lend_c-.Ltail_start\n\ts_max_i32 %2, %2, .Lend_d-.Ltail_start\n\ts_max_i32 %2, %2, .Lend_e-.Ltail_start" : "=s"(pc), "=s"(dlt), "=s"(len) :: "scc");
      pc += dlt;
      const rsrc_t rc = mk_rsrc((const void*)pc, (part == 3 && i8 == 1) ? len : 0u);
      cpf = __builtin_amdgcn_raw_buffer_load_b32(rc, tid * 64, 0, 0);
    }
    if constexpr (v == CPF + WV + 1) PIN(cpf);
    if constexpr (v == SIDE || v == SIDE + 4 || v == SIDE + 8) fl2[(v - SIDE) / 4] = ld16<AUX_SC1>(wsr, (wave == 0 && v == side) ? mlo : OOB, 0);
    SB();
    if constexpr (v < WV) { WAIT_VM(WK); SB(); } else if constexpr (v < NL && cons_end(v) < v - WV + 1) { WAIT_VM(WV); SB(); }
    if constexpr (v == VQ) {
      if (!all_ok((qg[0][1] == tag) & (qg[0][3] == tag) & (qg[1][1] == tag) & (qg[1][3] == tag))) { STAMP(20); gather16<2>(wsr, qoff, 16, tag, qg); }
      STAMP(1); WSTAMP(1);
      qq[0] = qg[0][0]; qq[1] = qg[0][2]; qq[2] = qg[1][0]; qq[3] = qg[1][2];
    }
    if constexpr (v >= VQ && v < VS) {
      constexpr int sl = v - VQ, bt = sl >> 1;
      float xs[8];
#pragma unroll
      for (int i = 0; i < 8; ++i) {
        float d = 0.f;
        if (sl * 8 + i < NL) {
#pragma unroll
          for (int k = 0; k < 4; ++k) d = dot2(kr[sl * 8 + i][k], qq[k], d);
        }
        xs[i] = d;
      }
#pragma unroll
      for (int h = 0; h < 4; ++h) xs[h] = sel_add<XOR7>(xs[h], xs[h + 4], c & 4);
#pragma unroll
      for (int h = 0; h < 2; ++h) xs[h] = sel_add<XOR2>(xs[h], xs[h + 2], c & 2);
      xs[0] = sel_add<XOR1>(xs[0], xs[1], c & 1);
      if constexpr ((sl & 1) || sl == NSL - 1) {
        const float t = (sl & 1) ? sel_add<XOR8>(xlo, xs[0], c & 8) : xs[0] + dpp<XOR8>(xs[0]);
        const float s = rbf(rbf(t) * 0.08838834764831845f);
        sc[bt] = (bt * 16 + c < nl) ? s : -__builtin_inff();
        PIN(sc[bt]);
      } else {
        xlo = xs[0];
        PIN(xlo);
      }
    }
    if constexpr (v == VS) {
      float mloc = sc[0];
#pragma unroll
      for (int bt = 1; bt < NB; ++bt) mloc = fmaxf(mloc, sc[bt]);
      m = fmaxf(wave_max(mloc), -3.0e38f);
      float ls = 0.f;
#pragma unroll
      for (int bt = 0; bt < NB; ++bt) { pr[bt] = __expf(sc[bt] - m); ls += pr[bt]; }
      const float l = wave_sum(ls);
      if (lane == 0) { sm.ml[wave][0] = m; sm.ml[wave][1] = l; }
      STAMP(2); WSTAMP(2);
    }
    sfor<cons_end(v - 1), cons_end(v)>([&](auto jc) {
      constexpr int i = jc.v;
      if (i < NCMIN / 8 || i < nl) {
        const u32 pv = __float_as_uint(bcast16(u2f(pb[i >> 4]), i & 15));
        acc = mfma16(u32x4{pv & pm[0], pv & pm[1], pv & pm[2], pv & pm[3]}, vr[i], acc);
      }
      PIN(acc);
      return true;
    });
    if constexpr (v == MLS - 1) { side_pin(); if (is_up) up_prep(); PIN(ga); PIN(xin); PIN(cc1); }
    if constexpr (v == UPREP - 1) wo_addr();
    if constexpr (v == UPREP) {
#pragma unroll
      for (int k = 0; k < 8; ++k) asm volatile("" : "=v"(bf[k]));
      if (part == 0) wo_prep(0, 1); else if (is_up) up_frags();
#pragma unroll
      for (int k = 0; k < 8; ++k) PIN(bf[k]);
    }
    if constexpr (v == UPREP + 1) {
      if (is_up) {
        const u32x4* a1 = ap + 128 + ((lane >> 1) & 1) * 128;
        f32x4 d0 = {0.f, 0.f, 0.f, 0.f}, d1 = {0.f, 0.f, 0.f, 0.f};
#pragma unroll
        for (int rb = 0; rb < 4; ++rb) { d0 = mfma16(a1[rb * 8], bf[rb], d0); d1 = mfma16(a1[(rb + 4) * 8], bf[rb + 4], d1); }
        Af = par ? d0[1] + d1[1] : d0[0] + d1[0]; Bf = par ? d0[3] + d1[3] : d0[2] + d1[2];
        Af += dpp<XOR8>(Af); Bf += dpp<XOR8>(Bf);
      }
      PIN(Af); PIN(Bf);
    }
    if constexpr (v == SIDE + WV + 1 || v == SIDE + WV + 5 || v == SIDE + WV + 9) {
      if (wave == 0 && v == side + WV + 1) {
        const u32x4 ml = fl2[(v - SIDE - WV - 1) / 4];
        fok = all_ok((ml[1] == tag) & (ml[3] == tag));
        merge_factor(ml);
      }
    }
    if (v % 6 == 4 && v >= 10 && v < 30) STAMP(15 + v / 6);
    if constexpr (v == MLS) {
      LDS_BARRIER();
      float mm[8], ll[8];
#pragma unroll
      for (int w = 0; w < 8; ++w) { const f32x2 t = *reinterpret_cast<const f32x2*>(&sm.ml[w][0]); mm[w] = t[0]; ll[w] = t[1]; }
      float Mwg = mm[0], Lwg = 0.f;
#pragma unroll
      for (int w = 1; w < 8; ++w) Mwg = fmaxf(Mwg, mm[w]);
      if (wave == 0) {
#pragma unroll
        for (int w = 0; w < 8; ++w) Lwg = fmaf(__expf(mm[w] - Mwg), ll[w], Lwg);
        if (lane == 0) st16_sc1(wsr, G_ML + ((head * 2 + b) * 4 + part) * 16, __float_as_uint(Mwg), __float_as_uint(Lwg), tag);
      }
      const float fw = __expf(m - Mwg);
#pragma unroll
      for (int bt = 0; bt < NB; ++bt) { pb[bt] = pack_bf(pr[bt] * fw, pr[bt] * fw); PIN(pb[bt]); }
#pragma unroll
      for (int k = 0; k < 4; ++k) { pm[k] = (c >> 1) == k ? ((c & 1) ? 0xffff0000u : 0x0000ffffu) : 0u; PIN(pm[k]); }
    }
    SB();
    return true;
  });
  asm volatile(".Ltail_start:");
  *LDSP(f32x4, adst0) = acc;
  STAMP(14); WSTAMP(14);
  if (!fok) {
    u32x4 ml;
#pragma nounroll
    for (int spin = 0; spin < SPIN_MAX; ++spin) {
      CFENCE();
      ml = ld16<AUX_SC1>(wsr, mlo, 0);
      if (all_ok((ml[1] == tag) & (ml[3] == tag))) break;
    }
    merge_factor(ml);
  }
  STAMP(15);
  LDS_BARRIER();
  STAMP(3);
  {
    u32 pw = 0;
    if (wave == 0) {
      f32x2 t[8];
#pragma unroll
      for (int w = 0; w < 8; ++w) t[w] = *reinterpret_cast<const f32x2*>(&sm.red[w][2 * lane]);
      const f32x2 o = ((t[0] + t[1]) + (t[2] + t[3])) + ((t[4] + t[5]) + (t[6] + t[7]));
      pw = min(pack_bf(o[0] * fmrg, o[1] * fmrg), SENT - 1u);
    }
    __builtin_amdgcn_raw_buffer_store_b32(pw, wsr, pso, 0, AUX_SC1);
    st8_sc1(wsr, nto, (u32)wall_clock64() + nadd, tag);
    SB();
#pragma unroll
    for (int i = NCMIN / 8; i < NL; ++i) PIN(vr[i]);
    PIN(fl2[0]); PIN(fl2[1]); PIN(fl2[2]);
  }
  STAMP(4);

  if (part == 2) FINISH();
  if (part == 0) {
    __builtin_amdgcn_s_barrier();
#pragma nounroll
    while (wleft > 0) piece();
    until(t1);
    STAMP(11);
    CFENCE();
    u32x4 og[2];
    og[0] = ld16<AUX_SC1>(wsr, po, 0);
    SB();
    WAIT_VM(1); CFENCE();
    wo_prep(1, 2);
#pragma unroll
    for (int k = 4; k < 8; ++k) PIN(bf[k]);
    SB();
    f32x4 d[4];
    auto wo_mm = [&]() {
      const f32x4 z = {0.f, 0.f, 0.f, 0.f};
#pragma unroll
      for (int t = 0; t < 4; ++t) { d[t] = mfma16(og[0], bf[t], z); og[1][t] = __float_as_uint(dpp<XOR8>(u2f(og[0][t]))); }
      SB();
#pragma unroll
      for (int t = 0; t < 4; ++t) d[t] = mfma16(og[1], bf[4 + t], d[t]);
    };
    auto p_bad = [&]() { return !all_ok(max(max(og[0][0], og[0][1]), max(og[0][2], og[0][3])) != SENT); };
    wo_mm();
    if (__builtin_expect(p_bad(), 0)) {
#pragma nounroll
      for (int spin = 0; spin < SPIN_MAX; ++spin) {
        __builtin_amdgcn_s_sleep(1);
        CFENCE();
        og[0] = ld16<AUX_SC1>(wsr, po, 0);
        if (!p_bad()) break;
      }
      wo_mm();
      SB();
      asm volatile(".Lend_d:");
    }
    STAMP(5); WSTAMP(5);
    {
      SB();
      f32x4 y; f32x2 h[4];
#pragma unroll
      for (int t = 0; t < 4; ++t) { h[t] = d[t].xy + d[t].zw; PIN(h[t]); }
#pragma unroll
      for (int t = 0; t < 4; ++t) { y[t] = h[t][0] + h[t][1]; PIN(y[t]); }
      *LDSP(f32x4, rdst) = y;
    }
    STAMP(16);
    LDS_BARRIER();
    STAMP(17);
    if (wave < 2) {
      f32x2 v[4];
#pragma unroll
      for (int w = 0; w < 4; ++w) v[w] = f32x2{LDSP(float, rsrc)[w * 256], LDSP(float, rsrc)[w * 256 + 128]};
      const f32x2 h = (v[0] + v[1]) + (v[2] + v[3]);
      const float y = h[0] + h[1];
      __builtin_amdgcn_raw_buffer_store_b32(min(pack_bf(y, dpp<XOR1>(y)), SENT - 1u), wsr, xo, 0, AUX_SC1);
      asm volatile(".Lend_a:");
    }
    STAMP(6);
    FINISH();
  }
  if (part == 3) {
    if (!has_w) FINISH();
    u32x4 b2[4];
    {
      __builtin_amdgcn_s_barrier();
      const rsrc_t r2 = mk_rsrc(w2 + (size_t)(blk * 32 + wave * 4) * 512, 4096u);
      const u32 l2 = (u32)(uintptr_t)(&sm.w2b[wave * 4][0]);
#pragma unroll
      for (int n = 0; n < 4; ++n) dma16(r2, (lane ^ (n * 2) ^ ((wave & 2) << 2)) * 16, n * 1024, l2 + n * 1024);
    }
    const u32 tdst = lds_addr(&sm.hb[hpos]);
    const u32 sdst = lds_addr(&sm.stat[wave][lane]);
    const u32 ssrc = lds_addr(&sm.stat[(g >> 1) * 4 + ((lane >> 1) & 3)][(lane & 1) * 16]);
    const u32 adst = lds_addr((c < 4 && (g & 1) == 0) ? &sm.actl[g >> 1][wave * 4 + c] : reinterpret_cast<u16*>(&sm.dummy[lane]));
    const u32 asrc = lds_addr(&sm.actl[lane & 1][g * 8]);
    const u32 apa = lds_addr(&sm.hb[(c >> 3) * 512 + (c & 3) * 64 + ((c >> 2) & 1) * 32 + g * 8]);
    u32 xoff = G_XP + g * 2048 + ub * 1024 + (wave & 3) * 256 + c * 16;
    u32x4 one;
#pragma unroll
    for (int k = 0; k < 4; ++k) one[k] = ((c & 2) == 0 && (c >> 2) == k) ? ((c & 1) ? 0x3f800000u : 0x00003f80u) : 0u;
    PIN(one);
    u32 ooff = G_OP + (blk * 512 + tid) * 8;
    float Afp = Af, Bfp = Bf;
    PIN(xoff); PIN(ooff); PIN(Afp); PIN(Bfp);
    PIN(ga); PIN(xin); PIN(cc1);
    u32 w2a = (u32)(uintptr_t)(&sm.w2b[0][0]) + (g * 8 + ((lane >> 2) & 3)) * 1024 + (wave ^ (g & 1)) * 128 + (lane & 15) * 8;
    PIN(w2a);
    if (wave == 1) {
      due = t1 + P.N1 * 256; gate();
      u32x4 gn[2] = {ld16<AUX_SC1>(wsr, G_NT + lane * 16, 0), ld16<AUX_SC1>(wsr, G_NT + lane * 16, 1024)};
      const int tn = notices(IC<2>{}, G_NT + lane * 16, gn) * 256;
      due = (tn > t1 + 2 * 256 ? max(tn, t1 + P.L0 * 256) + P.L1 * 256 : t1) + P.X2 * 256;
      until(due);
    }
    asm volatile("s_waitcnt vmcnt(0)" ::: "memory");
    STAMP(9);
    __builtin_amdgcn_s_barrier();
    STAMP(11);
    CFENCE();
    u32x4 xg[2] = {ld16<AUX_SC1>(wsr, xoff, 0), ld16<AUX_SC1>(wsr, xoff, 8192)};
    SB();
#pragma unroll
    for (int t = 0; t < 4; ++t) {
      const u32x2 lo = tr_read(w2a ^ (t * 32)), hi = tr_read((w2a ^ (t * 32)) + 4096);
      b2[t] = u32x4{lo[0], lo[1], hi[0], hi[1]};
    }
#pragma unroll
    for (int t = 0; t < 4; ++t) PIN(b2[t]);
    SB();
    const f32x4 z4 = {0.f, 0.f, 0.f, 0.f};
    auto xp_bad = [&]() { return !all_ok(max(max(max(xg[0][0], xg[0][1]), max(xg[0][2], xg[0][3])), max(max(xg[1][0], xg[1][1]), max(xg[1][2], xg[1][3]))) != SENT); };
    f32x4 v = mfma16(one, xg[1], mfma16(one, xg[0], z4));
    if (__builtin_expect(xp_bad(), 0)) {
#pragma nounroll
      for (int spin = 0; spin < SPIN_MAX; ++spin) {
        __builtin_amdgcn_s_sleep(1);
        CFENCE();
        xg[0] = ld16<AUX_SC1>(wsr, xoff, 0); xg[1] = ld16<AUX_SC1>(wsr, xoff, 8192);
        if (!xp_bad()) break;
      }
      v = mfma16(one, xg[1], mfma16(one, xg[0], z4));
      SB();
      asm volatile(".Lend_e:");
    }
    STAMP(5); WSTAMP(5);
    {
      const float a0 = v[0], a1 = v[1];
      const float x10 = fmaf(ga[0], a0, xin[0]), x11 = fmaf(ga[1], a1, xin[1]);
      *LDSP(u32, tdst) = pack_bf(x10 * cc1[0], x11 * cc1[1]);
      float u = swap32_add(x10 + x11, fmaf(x10, x10, x11 * x11));
      u += dpp<XOR1>(u); u += dpp<XOR2>(u); u += dpp<XOR7>(u); u += dpp<XOR8>(u);
      *LDSP(float, sdst) = u;
    }
    STAMP(16);
    LDS_BARRIER();
    STAMP(17);
    {
      const f32x4 z = {0.f, 0.f, 0.f, 0.f};
      const u32x4 af0 = LDSP(u32x4, apa)[0], af1 = LDSP(u32x4, apa)[32];
      float s = LDSP(float, ssrc)[0], q = LDSP(float, ssrc)[32];
      SB();
      f32x4 d[4];
#pragma unroll
      for (int j = 0; j < 4; ++j) d[j] = mfma16(af0, bf[j], z);
#pragma unroll
      for (int j = 0; j < 4; ++j) d[j] = mfma16(af1, bf[4 + j], d[j]);
      s += dpp<XOR1>(s); q += dpp<XOR1>(q); s += dpp<XOR2>(s); q += dpp<XOR2>(q); s += dpp<XOR7>(s); q += dpp<XOR7>(q);
      const float mean = s * (1.f / 512.f);
      const float var = fmaf(q, 1.f / 512.f, -mean * mean);
      const float ri = __builtin_amdgcn_rsqf(fmaxf(var, 0.f) + 1e-5f);
      const float cf = fmaf(-ri * mean, Afp, Bfp);
      float y = (d[0][0] + d[1][1]) + (d[2][2] + d[3][3]);
      y = __builtin_bit_cast(float, __builtin_amdgcn_update_dpp(__builtin_bit_cast(int, y), __builtin_bit_cast(int, y), XOR8, 0xa, 0xf, false));
      y = swap16_add(y, y);
      y = fmaf(y, ri, cf);
      const float up = dpp<0x104>(y);
      *LDSP(u16, adst) = (u16)pack_bf(silu_f(y) * up, 0.f);
    }
    STAMP(18);
    LDS_BARRIER();
    STAMP(10);
    {
      const u32x4 af = *LDSP(u32x4, asrc);
      const f32x4 z = {0.f, 0.f, 0.f, 0.f};
      const f32x4 d0 = mfma16(af, b2[0], z), d1 = mfma16(af, b2[1], z), d2 = mfma16(af, b2[2], z), d3 = mfma16(af, b2[3], z);
      const bool g1 = g & 1, g2 = g & 2;
      const float e0 = g1 ? d1[0] : d0[0], e1 = g1 ? d1[1] : d0[1], f0 = g1 ? d3[0] : d2[0], f1 = g1 ? d3[1] : d2[1];
      st8_sc1(wsr, ooff, pack_bf(g2 ? f0 : e0, g2 ? f1 : e1), tag);
    }
    asm volatile(".Lend_b:");
    STAMP(6);
    FINISH();
  }
  if (wave == 0) {
    const int d = fd, ul = lane >> 3;
    u32 gm[2] = {sg[0][0], sg[1][0]};
    if (!all_ok((sg[0][1] == tag) & (sg[1][1] == tag))) gather8<2>(wsr, G_MOD + (1024 + d) * 8, 1536 * 8, tag, gm);
    u32 xa0, xa1;
    {
      due = t1 + P.N2 * 256; gate();
      u32x4 gn[2] = {ld16<AUX_SC1>(wsr, G_NT + lane * 16, 0), ld16<AUX_SC1>(wsr, G_NT + lane * 16, 1024)};
      const int tn = notices(IC<2>{}, G_NT + lane * 16, gn) * 256;
      due = (tn > t1 + 2 * 256 ? max(tn, t1 + P.L0 * 256) + P.L1 * 256 : t1) + P.X3 * 256;
      until(due);
    }
#pragma nounroll
    for (int spin = 0; spin < SPIN_MAX; ++spin) {
      CFENCE();
      xa0 = __builtin_amdgcn_raw_buffer_load_b32(wsr, G_XP + ul * 2048 + (d >> 1) * 4, 0, AUX_SC1);
      xa1 = __builtin_amdgcn_raw_buffer_load_b32(wsr, G_XP + ul * 2048 + (d >> 1) * 4, 1024, AUX_SC1);
      if (all_ok(max(xa0, xa1) != SENT)) break;
      __builtin_amdgcn_s_sleep(1);
    }
    float a0 = (d & 1) ? hi_f(xa0) : lo_f(xa0), a1 = (d & 1) ? hi_f(xa1) : lo_f(xa1);
    a0 += dpp<XOR8>(a0); a1 += dpp<XOR8>(a1);
    a0 = swap16_add(a0, a0); a1 = swap16_add(a1, a1);
    a0 = swap32_add(a0, a0); a1 = swap32_add(a1, a1);
    const bool hb = g & 1;
    float xv = rbf(((d & 1) ? hi_f(sx2) : lo_f(sx2)) + rbf((hb ? hi_f(gm[0]) : lo_f(gm[0])) * rbf(hb ? a1 : a0)));
    float gv = hb ? hi_f(gm[1]) : lo_f(gm[1]);
    PIN(xv); PIN(gv);
    if (wi == 0 && lane == 0) __builtin_amdgcn_raw_buffer_store_b32(tag, wsr, WS_EPOCH, 0, AUX_SC1);
    STAMP(10);
    due = max(due + P.X4 * 256, (int)(((u32)wall_clock64() - t0) << 8) + P.GX * 256);
    const bool last = ul + 40 < NUP;
    u32 o0 = G_OP + (ul * 512 + d) * 8, o5 = last ? o0 : OOB, tg5 = last ? tag : 0u;
    const rsrc_t rout = mk_rsrc(out, 2048u);
    u32 oo = (lane & 40) == 0 ? (g * 512 + d) * 2 : OOB;
    const u32 pk = c == 0 ? 0x00003f80u : (c == 4 ? 0x3f800000u : 0u);
    u32x4 one = {pk, 0u, pk, 0u};
    PIN(o0); PIN(o5); PIN(tg5); PIN(oo); PIN(one);
    __builtin_amdgcn_raw_buffer_store_b16((u16)pack_bf(xv, 0.f), rout, oo, 0, AUX_SC1);
    until(due);
    f32x4 sv;
#pragma nounroll
    for (int spin = 0; spin < SPIN_MAX; ++spin) {
      CFENCE();
      u32x2 og[6];
#pragma unroll
      for (int k = 0; k < 5; ++k) og[k] = ld8<AUX_SC1>(wsr, o0, k * 32768);
      og[5] = ld8<AUX_SC1>(wsr, o5, 5 * 32768);
      sv = f32x4{0.f, 0.f, 0.f, 0.f};
#pragma unroll
      for (int k = 0; k < 3; ++k) sv = mfma16(one, u32x4{og[2 * k][0], og[2 * k][1], og[2 * k + 1][0], og[2 * k + 1][1]}, sv);
      const u32 bad = ((og[0][1] ^ tag) | (og[1][1] ^ tag) | (og[2][1] ^ tag)) | ((og[3][1] ^ tag) | (og[4][1] ^ tag) | (og[5][1] ^ tg5));
      PIN(sv);
      if (all_ok(bad == 0u)) break;
      __builtin_amdgcn_s_sleep(1);
    }
    STAMP(7);
    const float s = sv[0] + dpp<XOR8>(sv[0]);
    __builtin_amdgcn_raw_buffer_store_b16((u16)pack_bf(fmaf(gv, s, xv), 0.f), rout, oo, 0, AUX_SC1);
    asm volatile(".Lend_c:");
  }
  FINISH();
}

extern "C" void kernel_launch(void* const* d_in, const int* in_sizes, int n_in,
                              void* d_out, int out_size, void* d_ws, size_t ws_size,
                              hipStream_t stream) {
  fused_kernel<<<256, 512, 0, stream>>>((const u16*)d_in[0], (const u16*)d_in[1], (const u16*)d_in[2], (const u16*)d_in[3], (const u16*)d_in[4],
                                        (const u16*)d_in[6], (const u16*)d_in[7], (const u16*)d_in[8], (const u16*)d_in[9], (const u16*)d_in[10],
                                        (unsigned char*)d_ws, (u16*)d_out
    );
}
```

```cpp
#define PROBE_MUL 10
#include <hip/hip_runtime.h>
#include <stdint.h>

typedef unsigned int u32;
typedef unsigned short u16;
typedef unsigned long long u64;
typedef __attribute__((ext_vector_type(4))) unsigned u32x4;
typedef __attribute__((ext_vector_type(2))) unsigned u32x2;
typedef __attribute__((ext_vector_type(2))) __bf16 bf16x2;
typedef __attribute__((ext_vector_type(4))) float f32x4;
typedef __attribute__((ext_vector_type(2))) float f32x2;
typedef __amdgpu_buffer_rsrc_t rsrc_t;

constexpr int NH = 32, SK = 16384, DH = 128, WIN = 4096, FF = 1376;
struct Prm { int E0, ES, E1, E2, E2C, EU, TEND, DT_UP, L0, L1, X1, X2, X3, X4, NE, FE, M0, M1, M2, M3, M4, GS, GX, N1, N2, GA, WD, DT_WO, LW, LU, EW, PM; };
constexpr Prm PRM0 = {32, 24, 32, 32, 56, 64, 2030, 60, 30, 25, 92, 86, 131, 88, 4, 12, 5, 8, 21, 4, 32, 10, 56, 28, 70, 8, 4, 100, 45, 15, 8, 0x24310};
#define PRM_ARG
#define TABP (&TAB.e[0])
constexpr int NL = 37;
constexpr int NCMIN = 192;
constexpr int WK = 8, WV = 5;
constexpr int WUSE = 13, LNS = 10;
constexpr int QP = 14, QD = 4;
constexpr int VQ = 6;
constexpr int NSL = (NL + 7) / 8;
constexpr int VS = VQ + NSL;
constexpr int MLS = VS + 2;
constexpr int VSIDE = MLS - 5, UPREP = MLS + 8;
constexpr int SIDE = NCMIN / 8 - 4;
constexpr int CPF = 18;
constexpr int TBP = 7, TBL = 11, TBU = 15;
constexpr int cons_end(int v) {
  const int a = v - WV + 1, c = (v - MLS) * 4;
  const int m = a < c ? a : c;
  return m < 0 ? 0 : (m > NL ? NL : m);
}
constexpr int KF = 4, KF0 = 8;
struct Tab { u32 e[1024]; };
#define popc __builtin_popcount
__host__ __device__ constexpr u32 step_off(const Prm& P, u32 s0, u32 bh) {
  u32 sb = s0 & 96u;
  for (int k = 0; k < 5; ++k) sb |= (s0 >> k & 1u) << (P.PM >> (4 * k) & 7);
  const u32 src = (sb >> 5 & 3u) | bh << 2;
  sb ^= (popc(src & P.M0) & 1) | (popc(src & P.M1) & 1) << 1 | (popc(src & P.M2) & 1) << 2 | (popc(src & P.M3) & 1) << 3 | (popc(src & P.M4) & 1) << 4;
  return (sb & 7u) << 10 | (sb >> 3) << 16;
}
constexpr Tab make_tab(const Prm& P) {
  Tab T{};
  for (int bid = 0; bid < 256; ++bid) {
    const int xcd = bid & 7, idx = bid >> 3, part = idx & 3, i8 = idx >> 2, wi = i8 * 8 + xcd;
    const bool has_mod = wi < 48, has_up = wi >= 21;
    const int cb0 = (i8 + 5) & 7, cb1 = (i8 + 4) & 7, ms = wi - 20;
    const int mi = wi < 8 ? wi : (wi < 16 ? 8 + ((xcd + 4) & 7) : (wi < 20 ? (wi - 14) * 8 + 3 : 16 + ms / 7 * 8 + ms % 7 + (ms % 7 >= 3 ? 1 : 0)));
    const int ui = xcd * 5 + (xcd > 5 ? xcd - 5 : 0) + (i8 >= 3 ? (xcd >= 5 ? 1 : 0) + i8 - 3 : 0);
    const int wd = cb0 == 3 ? 0 : P.WD;
    const int e[4] = {P.E0 - 4 * wd + (cb0 == 3 ? P.ES : 0), P.E1 + (cb1 == 3 ? P.ES : 0), has_mod ? (wi < 16 ? P.E2C : P.E2) + ((mi & 7) == 3 ? P.ES : 0) : 0, has_up ? P.EU : 0};
    const int ew = wd ? P.EW : 0, pa = (1024 + e[0] + ew + e[1] + e[2] + e[3]) / 4;
    int c[5] = {0, 0, 0, 0, 128};
    for (int k = 0; k < 3; ++k) c[k + 1] = c[k] + (pa - e[k] - (k == 0 ? ew : 0) + 4) / 8;
    const int nl = c[part + 1] - c[part], wst = e[part] / 4;
    const bool has_w = part < 2 || (part == 2 ? has_mod : has_up);
    const int blk = !has_w ? 0 : (part == 0 ? cb0 : (part == 1 ? xcd * 8 + cb1 : (part == 2 ? mi : ui)));
    const int tau = (part == 3 && has_up ? P.TEND - P.DT_UP : (part == 0 && wd ? P.TEND - P.DT_WO : P.TEND)) * 256 / (2 * nl + wst);
    T.e[4 * bid] = (u32)(c[part] | nl << 7 | blk << 13 | (has_w ? 1 : 0) << 19 | (part == 0 ? wd : 0) << 20);
    T.e[4 * bid + 1] = (u32)(tau | (tau * wst / (part == 0 ? 8 - wd : 8)) << 16);
    if (c[part] > 32 * part + KF0 || c[part + 1] < 32 * part + KF0 + 4) T.e[4 * bid] = 0;
    const u32 pitch = part == 0 ? 1024u : (part == 1 ? 8192u : (part == 2 ? 6144u : 2752u));
    T.e[4 * bid + 2] = (part == 0 ? xcd * 512u * pitch : 0u) + blk * (part == 3 ? 64u : 128u);
    T.e[4 * bid + 3] = pitch;
  }
  return T;
}
constexpr int step_range(const Prm& P, bool mx_) {
  int mx = 0, mn = 1 << 20;
  const Tab T = make_tab(P);
  for (int bid = 0; bid < 256; ++bid) { const int n = T.e[4 * bid] >> 7 & 63; mx = n > mx ? n : mx; mn = n < mn ? n : mn; }
  return mx_ ? mx : mn;
}
__constant__ __attribute__((aligned(64))) const Tab TAB = make_tab(PRM0);
static_assert(step_range(PRM0, true) <= NL && step_range(PRM0, false) >= NCMIN / 8, "a wave's key range does not fit its load steps");
static_assert(cons_end(SIDE + WV + 1) <= NCMIN / 8 && cons_end(UPREP) <= NCMIN / 8 && SIDE > MLS + 3, "steps every wave runs");
constexpr int NUP = FF / 32;
constexpr int SPIN_MAX = 1 << 17;

constexpr u32 WS_EPOCH = 0;
constexpr u32 G_TB = 1024;
constexpr u32 G_NT = 2048;
constexpr u32 G_MOD = 4096;
constexpr u32 G_Q = G_MOD + 3072 * 8;
constexpr u32 G_ML = G_Q + 4096 * 8;
constexpr u32 G_P = G_ML + 256 * 16;
constexpr u32 G_XP = G_P + 256 * 256;
constexpr u32 G_OP = G_XP + 8 * 512 * 4;
constexpr u32 WS_END = G_OP + 43 * 512 * 8;
constexpr u32 SENT = 0xffffffffu;
constexpr u32 OOB = 0x40000000u;
#define WST 0

constexpr int AUX_NT = 2, AUX_SC1 = 16;

__device__ __forceinline__ float lo_f(u32 v) { return __uint_as_float(v << 16); }
__device__ __forceinline__ float hi_f(u32 v) { return __uint_as_float(v & 0xffff0000u); }
__device__ __forceinline__ float rbf(float x) { return (float)(__bf16)x; }
__device__ __forceinline__ u32 pack_bf(float lo, float hi) {
  bf16x2 r; r[0] = (__bf16)lo; r[1] = (__bf16)hi; return __builtin_bit_cast(u32, r);
}
__device__ __forceinline__ float dot2(u32 k, u32 q, float acc) {
  return __builtin_amdgcn_fdot2_f32_bf16(__builtin_bit_cast(bf16x2, k), __builtin_bit_cast(bf16x2, q), acc, false);
}
template <int CTRL> __device__ __forceinline__ float dpp(float x) {
  return __builtin_bit_cast(float, __builtin_amdgcn_mov_dpp(__builtin_bit_cast(int, x), CTRL, 0xf, 0xf, true));
}
constexpr int XOR1 = 0xB1, XOR2 = 0x4E, XOR7 = 0x141, XOR8 = 0x128;
__device__ __forceinline__ float swap32_add(float a, float b) {
  auto t = __builtin_amdgcn_permlane32_swap(__float_as_uint(a), __float_as_uint(b), false, false);
  return __uint_as_float(t[0]) + __uint_as_float(t[1]);
}
__device__ __forceinline__ float swap16_add(float a, float b) {
  auto t = __builtin_amdgcn_permlane16_swap(__float_as_uint(a), __float_as_uint(b), false, false);
  return __uint_as_float(t[0]) + __uint_as_float(t[1]);
}
__device__ __forceinline__ float wave_sum(float x) {
  x += dpp<XOR1>(x); x += dpp<XOR2>(x); x += dpp<XOR7>(x); x += dpp<XOR8>(x);
  x = swap16_add(x, x);
  return swap32_add(x, x);
}
__device__ __forceinline__ float wave_max(float x) {
  x = fmaxf(x, dpp<XOR1>(x)); x = fmaxf(x, dpp<XOR2>(x)); x = fmaxf(x, dpp<XOR7>(x)); x = fmaxf(x, dpp<XOR8>(x));
  auto s = __builtin_amdgcn_permlane16_swap(__float_as_uint(x), __float_as_uint(x), false, false);
  x = fmaxf(__uint_as_float(s[0]), __uint_as_float(s[1]));
  auto t = __builtin_amdgcn_permlane32_swap(__float_as_uint(x), __float_as_uint(x), false, false);
  return fmaxf(__uint_as_float(t[0]), __uint_as_float(t[1]));
}
template <int CTRL> __device__ __forceinline__ float sel_add(float lo, float hi, bool up) {
  const float t1 = lo + dpp<CTRL>(lo), t2 = hi + dpp<CTRL>(hi);
  return up ? t2 : t1;
}
template <int I> __device__ __forceinline__ float bc16(float x) {
  return __builtin_bit_cast(float, __builtin_amdgcn_update_dpp(0, __builtin_bit_cast(int, x), 0x150 + I, 0xf, 0xf, false));
}
__device__ __forceinline__ float bcast16(float x, int i) {
  switch (i) {
    case 0: return bc16<0>(x); case 1: return bc16<1>(x); case 2: return bc16<2>(x); case 3: return bc16<3>(x);
    case 4: return bc16<4>(x); case 5: return bc16<5>(x); case 6: return bc16<6>(x); case 7: return bc16<7>(x);
    case 8: return bc16<8>(x); case 9: return bc16<9>(x); case 10: return bc16<10>(x); case 11: return bc16<11>(x);
    case 12: return bc16<12>(x); case 13: return bc16<13>(x); case 14: return bc16<14>(x); default: return bc16<15>(x);
  }
}
__device__ __forceinline__ float silu_f(float x) { return x * __builtin_amdgcn_rcpf(1.f + __expf(-x)); }

template <int AUX> __device__ __forceinline__ u32x4 ld16(rsrc_t r, u32 voff, u32 soff) {
  return __builtin_amdgcn_raw_buffer_load_b128(r, voff, soff, AUX);
}
template <int AUX> __device__ __forceinline__ u32x2 ld8(rsrc_t r, u32 voff, u32 soff) {
  return __builtin_amdgcn_raw_buffer_load_b64(r, voff, soff, AUX);
}
__device__ __forceinline__ void st8_sc1(rsrc_t r, u32 voff, u32 v, u32 tag) {
  __builtin_amdgcn_raw_buffer_store_b64(u32x2{v, tag}, r, voff, 0, AUX_SC1);
}
__device__ __forceinline__ void st16_sc1(rsrc_t r, u32 voff, u32 v0, u32 v1, u32 tag) {
  __builtin_amdgcn_raw_buffer_store_b128(u32x4{v0, tag, v1, tag}, r, voff, 0, AUX_SC1);
}
__device__ __forceinline__ rsrc_t mk_rsrc(const void* p, u32 bytes) {
  return __builtin_amdgcn_make_buffer_rsrc(const_cast<void*>(p), 0, bytes, 0x00020000);
}
#define WAIT_VM(n) __builtin_amdgcn_s_waitcnt(((n) & 15) | (((n) >> 4) << 14) | (7 << 4) | (15 << 8))
#define SB() __builtin_amdgcn_sched_barrier(0)
#define CFENCE() asm volatile("" ::: "memory")
__device__ __forceinline__ bool all_ok(bool ok) { return __builtin_amdgcn_ballot_w64(!ok) == 0ull; }

template <int N> __device__ __forceinline__ void gather16(rsrc_t ws, u32 voff, u32 stride, u32 tag, u32x4 (&g)[N]) {
#pragma nounroll
  for (int spin = 0; spin < SPIN_MAX; ++spin) {
    CFENCE();
    bool ok = true;
#pragma unroll
    for (int i = 0; i < N; ++i) g[i] = ld16<AUX_SC1>(ws, voff + i * stride, 0);
#pragma unroll
    for (int i = 0; i < N; ++i) ok &= (g[i][1] == tag) & (g[i][3] == tag);
    if (all_ok(ok)) break;
    __builtin_amdgcn_s_sleep(8);
  }
}
template <int N> __device__ __forceinline__ void gather8(rsrc_t ws, u32 voff, u32 stride, u32 tag, u32 (&g)[N], bool act = true) {
#pragma nounroll
  for (int spin = 0; spin < SPIN_MAX; ++spin) {
    CFENCE();
    bool ok = true;
    u32x2 t[N];
#pragma unroll
    for (int i = 0; i < N; ++i) t[i] = ld8<AUX_SC1>(ws, voff + i * stride, 0);
#pragma unroll
    for (int i = 0; i < N; ++i) { ok &= (t[i][1] == tag); g[i] = t[i][0]; }
    if (all_ok(ok | !act)) break;
    __builtin_amdgcn_s_sleep(8);
  }
}

struct Smem {
  float red[8][128];
  float ml[8][2];
  float r2[64 * 20];
  u16 hb[3 * 1024];
  float stat[8][64];
  float red2[8][128];
  u32 dummy[64];
  u32x2 bc;
  u32x4 zero[4];
  u32 mflag[8];
  u32x4 wbuf[8][8][64];
  __attribute__((aligned(256))) u16 w2b[32][512];
  u16 actl[2][32];
};

#define PROBE_K 100
#define PROBE_OP 1
#define PROBE_PART -1
#define STAMP(k) do { if ((k) == PROBE_K && tid == 0) pst = wall_clock64(); } while (0)
#define WSTAMP(k) do {} while (0)
#define FINISH() do { STAMP(8); if (tid == 0) { __hip_atomic_store(reinterpret_cast<u64*>(ws + (1u << 20)) + bid * 2, pt0, __ATOMIC_RELAXED, __HIP_MEMORY_SCOPE_AGENT); \
   __hip_atomic_store(reinterpret_cast<u64*>(ws + (1u << 20)) + bid * 2 + 1, (PROBE_PART < 0 || part == PROBE_PART) ? pst : 0ull, __ATOMIC_RELAXED, __HIP_MEMORY_SCOPE_AGENT); } \
   if (bid == 8 && wave == 0) probe_end(ws, pt0, lane); return; } while (0)
#define PIN(x) asm volatile("" : "+v"(x))
#define LDS_BARRIER() do { __builtin_amdgcn_s_waitcnt(0xc07f); __builtin_amdgcn_s_barrier(); CFENCE(); } while (0)
#define WAVE_SYNC() do { __builtin_amdgcn_fence(__ATOMIC_RELEASE, "wavefront"); __builtin_amdgcn_wave_barrier(); __builtin_amdgcn_fence(__ATOMIC_ACQUIRE, "wavefront"); } while (0)

__device__ __forceinline__ void dma16(rsrc_t r, u32 voff, u32 soff, u32 lds_dst) {
  u32 keep;
  asm volatile("s_mov_b32 %0, m0\n\ts_mov_b32 m0, %4\n\ts_nop 4\n\tbuffer_load_dwordx4 %1, %2, %3 offen nt lds\n\ts_mov_b32 m0, %0"
               : "=&s"(keep) : "v"(voff), "s"(r), "s"(soff), "s"(lds_dst) : "memory");
}
__device__ __forceinline__ u32 perm_lo(u32 a, u32 b) { return __builtin_amdgcn_perm(b, a, 0x05040100u); }
__device__ __forceinline__ u32 perm_hi(u32 a, u32 b) { return __builtin_amdgcn_perm(b, a, 0x07060302u); }
__device__ __forceinline__ float rdl(float x, int l) { return __int_as_float(__builtin_amdgcn_readlane(__float_as_int(x), l)); }

typedef __attribute__((ext_vector_type(8))) __bf16 bf16x8;
__device__ __forceinline__ f32x4 mfma16(u32x4 a, u32x4 b, f32x4 c) {
  return __builtin_amdgcn_mfma_f32_16x16x32_bf16(__builtin_bit_cast(bf16x8, a), __builtin_bit_cast(bf16x8, b), c, 0, 0, 0);
}
__device__ __forceinline__ float sum8(const float* p) {
  const f32x4 a = *reinterpret_cast<const f32x4*>(p), b = *reinterpret_cast<const f32x4*>(p + 4);
  return ((a[0] + b[0]) + (a[1] + b[1])) + ((a[2] + b[2]) + (a[3] + b[3]));
}
__device__ __forceinline__ float ln_ri(float var) {
  return __builtin_amdgcn_rcpf(rbf(__builtin_amdgcn_sqrtf(rbf(rbf(fmaxf(var, 0.f)) + 1e-5f))));
}
__device__ __forceinline__ void ln_stats2(const float (&x0)[8], const float (&x1)[8], float& mb0, float& ri0, float& mb1, float& ri1) {
  const float k0 = rdl(x0[0], 0), k1 = rdl(x1[0], 0);
  float s0 = 0.f, s1 = 0.f, q0 = 0.f, q1 = 0.f;
#pragma unroll
  for (int k = 0; k < 8; ++k) {
    const float d0 = x0[k] - k0, d1 = x1[k] - k1;
    s0 += d0; s1 += d1; q0 = fmaf(d0, d0, q0); q1 = fmaf(d1, d1, q1);
  }
  float t = swap16_add(swap32_add(s0, s1), swap32_add(q0, q1));
  t += dpp<XOR1>(t); t += dpp<XOR2>(t); t += dpp<XOR7>(t); t += dpp<XOR8>(t);
  auto u = __builtin_amdgcn_permlane16_swap(__float_as_uint(t), __float_as_uint(t), false, false);
  const float ms = __uint_as_float(u[0]) * (1.f / 512.f), mq = __uint_as_float(u[1]) * (1.f / 512.f);
  const float ri = ln_ri(fmaf(-ms, ms, mq));
  mb0 = rbf(k0 + rdl(ms, 0)); mb1 = rbf(k1 + rdl(ms, 32));
  ri0 = rdl(ri, 0); ri1 = rdl(ri, 32);
}
template <int I> struct IC { static constexpr int v = I; };
template <int I, int N, class F> __device__ __forceinline__ void sfor(F&& f) {
  if constexpr (I < N) { if (f(IC<I>{})) sfor<I + 1, N>(f); }
}
typedef __attribute__((ext_vector_type(4))) short s16x4;
typedef __attribute__((address_space(3))) s16x4* lds_s16x4p;
__device__ __forceinline__ u32x2 tr_read(u32 lds_addr) {
  return __builtin_bit_cast(u32x2, __builtin_amdgcn_ds_read_tr16_b64_v4i16((lds_s16x4p)lds_addr));
}
__device__ __forceinline__ u32 next_tag(u32 e) { return ((e | 0x40004000u) + 1u) & ~0x40004000u; }
__device__ __forceinline__ float u2f(u32 v) { return __uint_as_float(v); }
#define LDSP(T, a) ((__attribute__((address_space(3))) T*)(a))
template <class T> __device__ __forceinline__ u32 lds_addr(T* p) { u32 a = (u32)(uintptr_t)p; PIN(a); return a; }

typedef __attribute__((ext_vector_type(16))) unsigned u32x16;
typedef __attribute__((ext_vector_type(8))) unsigned u32x8;
__device__ __forceinline__ void probe_end(unsigned char* ws, u64 pt0, int lane) {
  while (wall_clock64() < pt0 + 5500ull) __builtin_amdgcn_s_sleep(8);
  u64 a[4], c2[4];
  for (int i = 0; i < 4; ++i) {
    a[i] = __hip_atomic_load(reinterpret_cast<u64*>(ws + (1u << 20)) + (i * 64 + lane) * 2, __ATOMIC_RELAXED, __HIP_MEMORY_SCOPE_AGENT);
    c2[i] = __hip_atomic_load(reinterpret_cast<u64*>(ws + (1u << 20)) + (i * 64 + lane) * 2 + 1, __ATOMIC_RELAXED, __HIP_MEMORY_SCOPE_AGENT);
  }
  int mn0 = 1 << 30, mx1 = -(1 << 30), mn1 = 1 << 30, sum = 0, cnt = 0;
  for (int i = 0; i < 4; ++i) {
    const int d0 = (int)(a[i] - pt0);
    mn0 = min(mn0, d0);
    if (c2[i] != 0ull) { const int d1 = (PROBE_K >= 29 && PROBE_K <= 31) ? (int)(c2[i] - a[i]) : (int)(c2[i] - pt0); mx1 = max(mx1, d1); mn1 = min(mn1, d1); sum += d1; cnt += 1; }
  }
  for (int o = 1; o < 64; o <<= 1) { mn0 = min(mn0, __shfl_xor(mn0, o)); mx1 = max(mx1, __shfl_xor(mx1, o)); mn1 = min(mn1, __shfl_xor(mn1, o)); sum += __shfl_xor(sum, o); cnt += __shfl_xor(cnt, o); }
  int X = PROBE_OP == 0 ? mx1 : (PROBE_OP == 1 ? sum / max(cnt, 1) : mn1);
  if (PROBE_K == 102) X = mn0;
  X = (PROBE_K >= 29 && PROBE_K <= 31) ? mn0 + X * PROBE_MUL : mn0 + (X - mn0) * PROBE_MUL;
  const u64 tg = pt0 + (u64)(long long)(X + 8000);
  for (int i = 0; i < 400000 && wall_clock64() < tg; ++i) __builtin_amdgcn_s_sleep(1);
}
__global__ void __launch_bounds__(512, 2) fused_kernel(const u16* x_, const u16* kc_, const u16* vc_, const u16* wq_, const u16* wo_, const u16* w1_,
                                                       const u16* w3_, const u16* w2_, const u16* cnd_, const u16* wmod_, unsigned char* ws_, u16* out_ PRM_ARG) {
  __shared__ __attribute__((aligned(256))) Smem sm;
  constexpr Prm P = PRM0;
  const int TEND = P.TEND;
  const int bid = blockIdx.x, tid = threadIdx.x;
  u32x4 kr[NL];
  u32x16 ka; u32x8 kb; u32x4 tev; u64 pt0; u32 tch2;
  asm volatile("s_load_dwordx16 %0, %3, 0x0\n\ts_load_dwordx8 %1, %3, 0x40\n\ts_memrealtime %2"
               : "=&s"(ka), "=&s"(kb), "=&s"(pt0) : "s"(__builtin_amdgcn_kernarg_segment_ptr()));
  SB();
  {
    u64 pc; u32 vb;
    asm volatile("s_getpc_b64 %2\n.Lcpf_first:\n\tv_mul_u32_u24 %1, ((.Lend_c-.Lcpf_first)>>10)<<2, %3\n\tglobal_load_dword %0, %1, %2"
                 : "=&v"(tch2), "=&v"(vb), "=&s"(pc) : "v"((bid & ~7) + (tid >> 6)) : "memory");
    asm volatile("global_load_dwordx4 %0, %1, %2" : "=&v"(tev) : "v"(bid << 4), "s"(TABP) : "memory");
  }
  SB();
  const int xcd = bid & 7, idx = bid >> 3, part = idx & 3;
  const int b = idx >> 2 & 1, head = xcd * 4 + (((idx >> 3) & 1) << 1 | idx >> 4);
  const u32 so0 = step_off(P, 32 * part + KF0, b * 32 + head) & ~0xc00u;
  u32 va = (((b * 32 + head) * SK + (SK - WIN)) * DH * 2 + so0) | (tid >> 6) << 13 | (tid & 63) << 4;
  PIN(va);
  SB();
  asm volatile("s_waitcnt lgkmcnt(0)" : "+s"(ka), "+s"(kb), "+s"(pt0));
  SB();
  asm volatile("global_load_dwordx4 %0, %4, %5 nt\n\tglobal_load_dwordx4 %1, %4, %5 offset:1024 nt\n\tglobal_load_dwordx4 %2, %4, %5 offset:2048 nt\n\tglobal_load_dwordx4 %3, %4, %5 offset:3072 nt"
               : "=&v"(kr[0]), "=&v"(kr[1]), "=&v"(kr[2]), "=&v"(kr[3]) : "v"(va), "s"((u64)ka[2] | (u64)ka[3] << 32) : "memory");
  SB();
  const u32 t0 = (u32)pt0, tka = (u32)wall_clock64();
  u64 pst = (PROBE_K == 100 || PROBE_K == 102) ? pt0 : (PROBE_K == 101 ? pt0 + (tka - t0) : 0); const u64 pmin = pt0;
  auto kap = [&](int k) { return k < 8 ? (u64)ka[2 * k] | (u64)ka[2 * k + 1] << 32 : (u64)kb[2 * k - 16] | (u64)kb[2 * k - 15] << 32; };
  const u16 *x = (const u16*)kap(0), *kc = (const u16*)kap(1), *vc = (const u16*)kap(2), *w2 = (const u16*)kap(7), *cnd = (const u16*)kap(8);
  unsigned char* ws = (unsigned char*)kap(10); u16* out = (u16*)kap(11);
  const int lane = tid & 63, wave = __builtin_amdgcn_readfirstlane(tid >> 6);
  const bool p3 = part == 3;
  const u64 wa = part == 0 ? kap(4) : (part == 1 ? kap(3) : (part == 2 ? kap(9) : ((wave & 1) ? kap(6) : kap(5))));
  asm volatile("s_waitcnt vmcnt(4)" : "+v"(tev));
  const u32 te0 = __builtin_amdgcn_readfirstlane(tev[0]), te1 = __builtin_amdgcn_readfirstlane(tev[1]);
  const u32 pitch = __builtin_amdgcn_readfirstlane(tev[3]), wsz = pitch << (part == 0 ? 12 : 9);
  const int sh = p3 ? 2 : 3;
  u32 wv0 = (u32)((p3 ? (wave >> 1) * 128 : wave * 64) + (lane >> sh)) * pitch + ((lane & (p3 ? 3 : 7)) ^ (lane >> (p3 ? 5 : 3))) * 16u;
  const u32 wimg = (u32)(uintptr_t)(&sm.wbuf[0][0][0]);
  u32 wld = wimg + (p3 ? (wave & 1) * 32768 + (wave >> 1) * 8192 : wave * 8192);
  const int bl0 = te0 & 127, nl = te0 >> 7 & 63, blk = te0 >> 13 & 63;
  const bool has_w = te0 >> 19 & 1;
  const rsrc_t rw = mk_rsrc((const void*)wa, wsz);
  const u32 wstep = pitch << (6 - sh), wflip = p3 ? 32u : 0u;
  u32 wso = __builtin_amdgcn_readfirstlane(tev[2]);
  int wleft = 8;
  auto piece = [&]() {
    dma16(rw, wv0, wso, wld);
    wso += wstep; wld += 1024;
    wv0 ^= (wleft & 1) ? wflip : 0u;
    --wleft;
  };
  int tau = te1 & 0xffff;
  const int tauw = te1 >> 16;
  int due = KF * tau, now = 0;
  auto gate = [&]() {
    while (now < due) { now = (int)(((u32)wall_clock64() - t0) << 8); if (now < due) __builtin_amdgcn_s_sleep(1); }
    due += tau;
  };
  const bool modc = part == 2 && bid < 64;
  if (has_w) {
    if (modc) now = 1 << 30;
#pragma nounroll
    while (wleft > (int)(te0 >> 20)) { gate(); due += tauw - tau; piece(); }
    now = 0;
  }
  SB();
  STAMP(23);
  const rsrc_t wsr = mk_rsrc(ws, WS_END);
  const int g = lane >> 4, c = lane & 15;
  const int i8 = idx >> 2, wi = i8 * 8 + xcd;
  const bool is_up = p3 && has_w;
  const size_t base = ((size_t)(b * NH + head) * SK + (SK - WIN)) * DH;
  const rsrc_t rk = mk_rsrc(kc + base, WIN * DH * 2), rv = mk_rsrc(vc + base, WIN * DH * 2);
  u32 sot;
  {
    u32 s0 = bl0 + lane - KF;
    s0 += s0 >= (u32)(32 * part + KF0) ? KF : 0;
    sot = (lane < KF ? so0 | (u32)lane << 10 : step_off(P, s0, b * 32 + head)) | (u32)wave << 13;
    sot = lane < nl ? sot : OOB;
    PIN(sot);
  }
  auto until = [&](int t) { while ((int)(((u32)wall_clock64() - t0) << 8) < t); };
  const u32 pslot = G_P + ((b * 32 + head) * 4 + part) * 256;
  const rsrc_t rx = mk_rsrc(part == 2 ? cnd : x, (part == 1 || (part == 2 && has_w)) ? 2048u : 0u);
  u32x2 ep; u32 xs0, xs1, tch1; u32x4 xr0, xr1;
  auto small_ops = [&]() {
    ep = ld8<AUX_SC1>(wsr, wave == 7 ? WS_EPOCH : OOB, 0);
    xs0 = __builtin_amdgcn_raw_buffer_load_b16(rx, tid * 2, 0, 0); xs1 = __builtin_amdgcn_raw_buffer_load_b16(rx, tid * 2, 1024, 0);
    xr0 = ld16<0>(rx, part == 1 ? lane * 16 : OOB, 0); xr1 = ld16<0>(rx, part == 1 ? lane * 16 : OOB, 1024);
    tch1 = __builtin_amdgcn_raw_buffer_load_b32(mk_rsrc(w2 + (size_t)blk * 32 * 512, is_up ? 64u : 0u), tid == 384 ? 0 : OOB, 0, AUX_NT);
  };
  if (modc) __builtin_amdgcn_raw_buffer_store_b128(u32x4{SENT, SENT, SENT, SENT}, wsr, tid < 320 ? G_P + wi * 5120 + tid * 16 : OOB, 0, AUX_SC1);
  SB();
  u32 tag = 0;
  u32x4 bf[8];
  auto wo_prep = [&](int s0 = 0, int s1 = 2) {
    const u32 wi_ = wimg + wave * 8192;
    const int q = (lane >> 2) & 3, pp = lane & 3;
#pragma unroll
    for (int s = s0; s < s1; ++s) {
#pragma unroll
      for (int t = 0; t < 4; ++t) {
        const int r0 = s * 32 + g * 8 + q, r1 = r0 + 4, ch = t * 2 + (pp >> 1);
        const u32x2 lo = tr_read(wi_ + r0 * 128 + ((ch ^ (r0 & 7)) * 16) + (pp & 1) * 8), hi = tr_read(wi_ + r1 * 128 + ((ch ^ (r1 & 7)) * 16) + (pp & 1) * 8);
        bf[s * 4 + t] = u32x4{lo[0], lo[1], hi[0], hi[1]};
      }
    }
  };
  auto gemv_go = [&](u32 hh) {
    sm.hb[tid] = (u16)hh; sm.hb[512 + tid] = (u16)(hh >> 16);
    WAVE_SYNC();
    const u32x4* a = (c & 11) == 0 ? reinterpret_cast<const u32x4*>(&sm.hb[(c >> 2) * 512 + wave * 64 + g * 8]) : &sm.zero[0];
    const u32x4 a0 = a[0], a1 = a[(c & 11) == 0 ? 4 : 0];
    const f32x4 z = {0.f, 0.f, 0.f, 0.f};
    f32x4 d[4];
#pragma unroll
    for (int t = 0; t < 4; ++t) d[t] = mfma16(a0, bf[t], z);
#pragma unroll
    for (int t = 0; t < 4; ++t) d[t] = mfma16(a1, bf[4 + t], d[t]);
    if (lane < 32) {
#pragma unroll
      for (int t = 0; t < 4; ++t) sm.r2[(t * 16 + c) * 20 + g * 8 + wave] = d[t][0];
    }
  };
  auto mod_stage = [&]() {
    wo_prep();
    gemv_go(pack_bf(silu_f(lo_f(xs0)), silu_f(lo_f(xs1))));
    __builtin_amdgcn_s_waitcnt(0xc07f);
    if (lane == 0) *LDSP(volatile u32, (u32)(uintptr_t)&sm.mflag[wave]) = 1u;
    if (wave == 7) {
#pragma nounroll
      for (int spin = 0; spin < SPIN_MAX; ++spin) {
        const u32 f = *LDSP(volatile u32, (u32)(uintptr_t)&sm.mflag[lane & 7]);
        if (all_ok(f != 0u)) break;
        __builtin_amdgcn_s_sleep(1);
      }
      CFENCE();
      st8_sc1(wsr, G_MOD + (blk * 64 + lane) * 8, pack_bf(sum8(&sm.r2[lane * 20]), sum8(&sm.r2[lane * 20 + 8])), tag);
    }
    STAMP(9);
  };
  if (lane < 4) sm.zero[lane] = u32x4{0u, 0u, 0u, 0u};
  if (part == 2 && has_w) {
    if (tid < 8) sm.mflag[tid] = 0u;
    LDS_BARRIER();
    if (__builtin_expect(modc, 0)) {
      small_ops();
      asm volatile("s_waitcnt vmcnt(0)" ::: "memory");
      tag = next_tag(__builtin_amdgcn_readfirstlane(ep[0]));
      mod_stage();
      __builtin_amdgcn_s_barrier();
    }
  }
  SB();
  const int fd = wi * 8 + (lane & 7);
  const int ub = wave >> 2, ud = (wave & 3) * 128 + c * 8 + g * 2;
  u32x4 sg[3]; u32 sx2;
  auto side_loads = [&]() {
    const bool fin = part == 1 && wave == 0;
    sg[0] = ld16<AUX_SC1>(wsr, is_up ? G_MOD + (1024 + ud) * 8 : (fin ? G_MOD + (1024 + fd) * 8 : OOB), 0);
    sg[1] = ld16<AUX_SC1>(wsr, is_up ? G_MOD + (1536 + ud) * 8 : (fin ? G_MOD + (2560 + fd) * 8 : OOB), 0);
    sg[2] = ld16<AUX_SC1>(wsr, is_up ? G_MOD + (2048 + ud) * 8 : OOB, 0);
    sx2 = __builtin_amdgcn_raw_buffer_load_b32(mk_rsrc(x, (is_up || fin) ? 2048u : 0u), is_up ? ub * 1024 + ud * 2 : (g & 1) * 1024 + (fd >> 1) * 4, 0, 0);
  };
  auto side_pin = [&]() { PIN(sg[0]); PIN(sg[1]); PIN(sg[2]); PIN(sx2); };
  const u32 mlo = G_ML + (head * 2 + b) * 64 + (lane & 3) * 16;
  float fmrg = 0.f;
  bool fok = wave != 0;
  auto merge_factor = [&](const u32x4& ml) {
    const float mi = u2f(ml[0]);
    float M = fmaxf(mi, dpp<XOR1>(mi)); M = fmaxf(M, dpp<XOR2>(M));
    const float e = __expf(mi - M);
    float L = e * u2f(ml[2]);
    L += dpp<XOR1>(L); L += dpp<XOR2>(L);
    fmrg = rdl(e * __builtin_amdgcn_rcpf(L), part);
  };
  f32x2 ga = {0.f, 0.f}, xin = {0.f, 0.f}, cc1 = {0.f, 0.f};
  float Af = 0.f, Bf = 0.f;
  const int hpos = ub * 512 + ud;
  auto up_prep = [&]() {
    if (!all_ok((sg[0][1] == tag) & (sg[0][3] == tag) & (sg[1][1] == tag) & (sg[1][3] == tag) & (sg[2][1] == tag) & (sg[2][3] == tag))) gather16<3>(wsr, G_MOD + (1024 + ud) * 8, 512 * 8, tag, sg);
    ga = f32x2{ub ? hi_f(sg[0][0]) : lo_f(sg[0][0]), ub ? hi_f(sg[0][2]) : lo_f(sg[0][2])};
    cc1 = f32x2{rbf(1.f + (ub ? hi_f(sg[2][0]) : lo_f(sg[2][0]))), rbf(1.f + (ub ? hi_f(sg[2][2]) : lo_f(sg[2][2])))};
    xin = f32x2{lo_f(sx2), hi_f(sx2)};
    u32* hp = reinterpret_cast<u32*>(&sm.hb[hpos]);
    hp[512] = pack_bf(cc1[0], cc1[1]);
    hp[1024] = ub ? perm_hi(sg[1][0], sg[1][2]) : perm_lo(sg[1][0], sg[1][2]);
  };
  auto up_frags = [&]() {
    const int pp = lane & 3;
    const u32 a = wimg + (pp & 1) * 32768 + ((pp >> 1) * 32 + g * 8 + ((lane >> 2) & 3)) * 64 + (((wave >> 1) ^ ((g & 1) | (pp & 2))) * 16) + (wave & 1) * 8;
#pragma unroll
    for (int rb = 0; rb < 8; ++rb) {
      const u32x2 lo = tr_read(a + rb * 4096), hi = tr_read(a + rb * 4096 + 256);
      bf[rb] = u32x4{lo[0], lo[1], hi[0], hi[1]};
    }
  };
  const u32x4* ap = reinterpret_cast<const u32x4*>(&sm.hb[((lane >> 3) & 1) * 512 + (lane & 1) * 32 + g * 8]);
  const bool par = lane & 8;
  float l0 = 0.f, l1 = 0.f;
  u32x2 gmd[2], tbg;
  int t1 = 0;
#pragma unroll
  for (int i = NCMIN / 8; i < NL; ++i) kr[i] = u32x4{0u, 0u, 0u, 0u};
  sfor<KF, NL>([&](auto ic) {
    constexpr int i = ic.v;
    if (i >= NCMIN / 8 && i >= nl) return false;
    const u32 so = __builtin_amdgcn_readlane(sot, i);
    gate();
    SB();
    kr[i] = ld16<AUX_NT>(rk, lane * 16, so);
    if constexpr (i == KF) small_ops();
    if constexpr (i == TBL) tbg = ld8<AUX_SC1>(wsr, wave == 7 ? G_TB + xcd * 128 + (lane & 7) * 8 : OOB, 0);
    if constexpr (i == QP) { gmd[0] = ld8<AUX_SC1>(wsr, part == 1 ? G_MOD + tid * 8 : OOB, 0); gmd[1] = ld8<AUX_SC1>(wsr, part == 1 ? G_MOD + tid * 8 : OOB, 4096); }
    SB();
    SB();
    if (i >= WK) { WAIT_VM(WK); SB(); }
    if (i == KF) STAMP(25);
    if (i == 8) STAMP(12);
    if (i == 20) STAMP(21);
    if (i == 25) STAMP(22);
    if (i == TBP) {
      if (part == 3 && i8 == 0 && wave == 7) { tag = next_tag(__builtin_amdgcn_readfirstlane(ep[0])); if (lane < 8) st8_sc1(wsr, G_TB + lane * 128 + xcd * 8, t0 + (((tka - t0) * P.GA) >> 3) - P.GS, tag); }
      SB();
    }
    if (i == TBU) {
      if (wave == 7) {
        const bool ok = tbg[1] == tag;
        float lf = ok ? (float)min(max((int)(t0 - tbg[0]), -300), 300) : 0.f, nf = ok ? 1.f : 0.f;
        lf += dpp<XOR1>(lf); nf += dpp<XOR1>(nf); lf += dpp<XOR2>(lf); nf += dpp<XOR2>(nf); lf += dpp<XOR7>(lf); nf += dpp<XOR7>(nf);
        if (lane == 0) sm.bc = u32x2{tag, t0 - (u32)(int)(lf * __builtin_amdgcn_rcpf(fmaxf(nf, 1.f)))};
      }
      LDS_BARRIER();
      const u32x2 bc = sm.bc;
      tag = __builtin_amdgcn_readfirstlane(bc[0]);
      t1 = (__builtin_amdgcn_readfirstlane((int)(bc[1] - t0)) + TEND + P.X1) * 256;
      tau -= __builtin_amdgcn_readfirstlane((int)((float)(int)(t0 - bc[1]) * 256.f * __builtin_amdgcn_rcpf((float)(2 * nl - TBU - 2))));
      SB();
    }
    if (i == LNS) {
      tag = next_tag(__builtin_amdgcn_readfirstlane(ep[0]));
      if (part == 1) {
        float x0[8], x1[8], mb0, ri0, mb1, ri1;
#pragma unroll
        for (int k = 0; k < 4; ++k) { x0[2 * k] = lo_f(xr0[k]); x0[2 * k + 1] = hi_f(xr0[k]); x1[2 * k] = lo_f(xr1[k]); x1[2 * k + 1] = hi_f(xr1[k]); }
        ln_stats2(x0, x1, mb0, ri0, mb1, ri1);
        l0 = rbf(rbf(lo_f(xs0) - mb0) * ri0); l1 = rbf(rbf(lo_f(xs1) - mb1) * ri1);
      }
      PIN(l0); PIN(l1);
      SB();
    }
    if (i == WUSE) {
      PIN(tch1); PIN(tch2);
      if ((part == 1 || (part == 2 && has_w && !modc))) {
        WAIT_VM(WK); CFENCE();
        if (part == 2) mod_stage(); else wo_prep();
      }
#pragma unroll
      for (int k = 0; k < 8; ++k) PIN(bf[k]);
      SB();
    }
    if (i == QP + QD) {
      if (part == 1) {
        u32 gm[2] = {gmd[0][0], gmd[1][0]};
        if (!all_ok((gmd[0][1] == tag) & (gmd[1][1] == tag))) { STAMP(11); gather8<2>(wsr, G_MOD + tid * 8, 512 * 8, tag, gm); }
        STAMP(5);
        gemv_go(pack_bf(rbf(l0 * rbf(1.f + lo_f(gm[1]))) + lo_f(gm[0]), rbf(l1 * rbf(1.f + hi_f(gm[1]))) + hi_f(gm[0])));
      }
      SB();
    }
    if (i == QP + QD + 1) {
      if (part == 1) {
        LDS_BARRIER();
        if (wave == 0) {
          const int bb = lane >> 5, jq = lane & 31;
          st8_sc1(wsr, G_Q + (bb * 2048 + blk * 32 + jq) * 8, pack_bf(sum8(&sm.r2[2 * jq * 20 + bb * 8]), sum8(&sm.r2[(2 * jq + 1) * 20 + bb * 8])), tag);
        }
        STAMP(6);
      }
      SB();
    }
    return true;
  });
  PIN(sot);
  const u32 qoff = G_Q + ((b * 32 + head) * 64 + c * 4) * 8;
  u32x4 qg[2], vr[NL], fl2[3];
  u32 qq[4];
  constexpr int NB = (NL + 15) / 16;
  float xlo, sc[NB], pr[NB], m = 0.f;
  f32x4 acc = {0.f, 0.f, 0.f, 0.f};
  u32 pb[NB], pm[4], cpf;
  const u32 adst0 = lds_addr(lane < 32 ? &sm.red[wave][c * 8 + g * 4] : &sm.r2[(lane - 32) * 4]);
  u32 po, rdst, rsrc, xo;
  auto wo_addr = [&]() {
    po = G_P + (((c >> 2 & 1) * 32 + xcd * 4 + (wave >> 1)) * 4 + (c & 3)) * 256 + (wave & 1) * 128 + (c >> 3) * 64 + g * 16;
    rdst = lane < 32 ? (u32)(uintptr_t)&sm.red2[wave][0] + lane * 16 : (u32)(uintptr_t)&sm.r2[0] + (lane - 32) * 16;
    rsrc = (u32)(uintptr_t)&sm.red2[0][((wave & 1) * 16 + (lane & 15)) * 4 + (lane >> 4)];
    xo = (lane & 1) ? OOB : G_XP + xcd * 2048 + (wave & 1) * 1024 + (blk * 64 + lane) * 2;
    PIN(po); PIN(rdst); PIN(rsrc); PIN(xo);
  };
  u32 pso = wave == 0 ? pslot + lane * 4 : OOB;
  PIN(pso);
  const u32 nadd = (te0 >> 20) ? P.LW : (is_up ? P.LU : 0);
  u32 nto = tid == 0 ? G_NT + bid * 8 : OOB;
  PIN(nto);
  auto notices = [&](auto nc_, u32 off, u32x4 (&gn)[nc_.v]) -> int {
    constexpr int N = nc_.v;
#pragma nounroll
    for (int spin = 0; spin < SPIN_MAX; ++spin) {
      bool ok = true;
#pragma unroll
      for (int i = 0; i < N; ++i) ok &= (gn[i][1] == tag) & (gn[i][3] == tag);
      if (all_ok(ok)) break;
      __builtin_amdgcn_s_sleep(10);
      CFENCE();
#pragma unroll
      for (int i = 0; i < N; ++i) gn[i] = ld16<AUX_SC1>(wsr, off, i * 1024);
    }
    int mx = 0;
#pragma unroll
    for (int i = 0; i < N; ++i) mx = max(mx, max((int)(gn[i][0] - t0), (int)(gn[i][2] - t0)));
    return __builtin_amdgcn_readfirstlane((int)wave_max((float)min(mx, 1 << 20)));
  };
  const int side = nl >= SIDE + 13 ? SIDE + 8 : (nl >= SIDE + 9 ? SIDE + 4 : SIDE);
  sfor<0, NL + WV>([&](auto ic) {
    constexpr int v = ic.v;
    if (cons_end(v - 1) >= NCMIN / 8 && cons_end(v - 1) >= nl) return false;
    if constexpr (v < NL) {
      const u32 so = __builtin_amdgcn_readlane(sot, v);
      if constexpr (v >= 12) { if (v == nl - P.NE) tau = (tau * P.FE) >> 4; }
      if constexpr (v >= NCMIN / 8) { if (v < nl) gate(); } else gate();
      SB();
      vr[v] = ld16<AUX_NT>(rv, lane * 16, so);
      SB();
      SB();
    }
    if constexpr (v == NCMIN / 8 - 1) {
#pragma unroll
      for (int i = NCMIN / 8; i < NL; ++i) asm volatile("" : "=v"(vr[i]));
      asm volatile("" : "=v"(fl2[1]), "=v"(fl2[2]));
    }
    if constexpr (v == VQ - 4) { qg[0] = ld16<AUX_SC1>(wsr, qoff, 0); qg[1] = ld16<AUX_SC1>(wsr, qoff + 16, 0); }
    if constexpr (v == VSIDE) side_loads();
    if constexpr (v == CPF) {
      u64 pc; u32 dlt, len;
      asm volatile("s_getpc_b64 %0\n.Lcpf_here:\n\ts_mov_b32 %1, .Ltail_start-.Lcpf_here\n\ts_mov_b32 %2, .Lend_a-.Ltail_start\n\ts_max_i32 %2, %2, .Lend_b-.Ltail_start\n\ts_max_i32 %2, %2, .Lend_c-.Ltail_start\n\ts_max_i32 %2, %2, .Lend_d-.Ltail_start\n\ts_max_i32 %2, %2, .Lend_e-.Ltail_start" : "=s"(pc), "=s"(dlt), "=s"(len) :: "scc");
      pc += dlt;
      const rsrc_t rc = mk_rsrc((const void*)pc, (part == 3 && i8 == 1) ? len : 0u);
      cpf = __builtin_amdgcn_raw_buffer_load_b32(rc, tid * 64, 0, 0);
    }
    if constexpr (v == CPF + WV + 1) PIN(cpf);
    if constexpr (v == SIDE || v == SIDE + 4 || v == SIDE + 8) fl2[(v - SIDE) / 4] = ld16<AUX_SC1>(wsr, (wave == 0 && v == side) ? mlo : OOB, 0);
    SB();
    if constexpr (v < WV) { WAIT_VM(WK); SB(); } else if constexpr (v < NL && cons_end(v) < v - WV + 1) { WAIT_VM(WV); SB(); }
    if constexpr (v == VQ) {
      if (!all_ok((qg[0][1] == tag) & (qg[0][3] == tag) & (qg[1][1] == tag) & (qg[1][3] == tag))) { STAMP(20); gather16<2>(wsr, qoff, 16, tag, qg); }
      STAMP(1); WSTAMP(1);
      qq[0] = qg[0][0]; qq[1] = qg[0][2]; qq[2] = qg[1][0]; qq[3] = qg[1][2];
    }
    if constexpr (v >= VQ && v < VS) {
      constexpr int sl = v - VQ, bt = sl >> 1;
      float xs[8];
#pragma unroll
      for (int i = 0; i < 8; ++i) {
        float d = 0.f;
        if (sl * 8 + i < NL) {
#pragma unroll
          for (int k = 0; k < 4; ++k) d = dot2(kr[sl * 8 + i][k], qq[k], d);
        }
        xs[i] = d;
      }
#pragma unroll
      for (int h = 0; h < 4; ++h) xs[h] = sel_add<XOR7>(xs[h], xs[h + 4], c & 4);
#pragma unroll
      for (int h = 0; h < 2; ++h) xs[h] = sel_add<XOR2>(xs[h], xs[h + 2], c & 2);
      xs[0] = sel_add<XOR1>(xs[0], xs[1], c & 1);
      if constexpr ((sl & 1) || sl == NSL - 1) {
        const float t = (sl & 1) ? sel_add<XOR8>(xlo, xs[0], c & 8) : xs[0] + dpp<XOR8>(xs[0]);
        const float s = rbf(rbf(t) * 0.08838834764831845f);
        sc[bt] = (bt * 16 + c < nl) ? s : -__builtin_inff();
        PIN(sc[bt]);
      } else {
        xlo = xs[0];
        PIN(xlo);
      }
    }
    if constexpr (v == VS) {
      float mloc = sc[0];
#pragma unroll
      for (int bt = 1; bt < NB; ++bt) mloc = fmaxf(mloc, sc[bt]);
      m = fmaxf(wave_max(mloc), -3.0e38f);
      float ls = 0.f;
#pragma unroll
      for (int bt = 0; bt < NB; ++bt) { pr[bt] = __expf(sc[bt] - m); ls += pr[bt]; }
      const float l = wave_sum(ls);
      if (lane == 0) { sm.ml[wave][0] = m; sm.ml[wave][1] = l; }
      STAMP(2); WSTAMP(2);
    }
    sfor<cons_end(v - 1), cons_end(v)>([&](auto jc) {
      constexpr int i = jc.v;
      if (i < NCMIN / 8 || i < nl) {
        const u32 pv = __float_as_uint(bcast16(u2f(pb[i >> 4]), i & 15));
        acc = mfma16(u32x4{pv & pm[0], pv & pm[1], pv & pm[2], pv & pm[3]}, vr[i], acc);
      }
      PIN(acc);
      return true;
    });
    if constexpr (v == MLS - 1) { side_pin(); if (is_up) up_prep(); PIN(ga); PIN(xin); PIN(cc1); }
    if constexpr (v == UPREP - 1) wo_addr();
    if constexpr (v == UPREP) {
#pragma unroll
      for (int k = 0; k < 8; ++k) asm volatile("" : "=v"(bf[k]));
      if (part == 0) wo_prep(0, 1); else if (is_up) up_frags();
#pragma unroll
      for (int k = 0; k < 8; ++k) PIN(bf[k]);
    }
    if constexpr (v == UPREP + 1) {
      if (is_up) {
        const u32x4* a1 = ap + 128 + ((lane >> 1) & 1) * 128;
        f32x4 d0 = {0.f, 0.f, 0.f, 0.f}, d1 = {0.f, 0.f, 0.f, 0.f};
#pragma unroll
        for (int rb = 0; rb < 4; ++rb) { d0 = mfma16(a1[rb * 8], bf[rb], d0); d1 = mfma16(a1[(rb + 4) * 8], bf[rb + 4], d1); }
        Af = par ? d0[1] + d1[1] : d0[0] + d1[0]; Bf = par ? d0[3] + d1[3] : d0[2] + d1[2];
        Af += dpp<XOR8>(Af); Bf += dpp<XOR8>(Bf);
      }
      PIN(Af); PIN(Bf);
    }
    if constexpr (v == SIDE + WV + 1 || v == SIDE + WV + 5 || v == SIDE + WV + 9) {
      if (wave == 0 && v == side + WV + 1) {
        const u32x4 ml = fl2[(v - SIDE - WV - 1) / 4];
        fok = all_ok((ml[1] == tag) & (ml[3] == tag));
        merge_factor(ml);
      }
    }
    if (v % 6 == 4 && v >= 10 && v < 30) STAMP(15 + v / 6);
    if constexpr (v == MLS) {
      LDS_BARRIER();
      float mm[8], ll[8];
#pragma unroll
      for (int w = 0; w < 8; ++w) { const f32x2 t = *reinterpret_cast<const f32x2*>(&sm.ml[w][0]); mm[w] = t[0]; ll[w] = t[1]; }
      float Mwg = mm[0], Lwg = 0.f;
#pragma unroll
      for (int w = 1; w < 8; ++w) Mwg = fmaxf(Mwg, mm[w]);
      if (wave == 0) {
#pragma unroll
        for (int w = 0; w < 8; ++w) Lwg = fmaf(__expf(mm[w] - Mwg), ll[w], Lwg);
        if (lane == 0) st16_sc1(wsr, G_ML + ((head * 2 + b) * 4 + part) * 16, __float_as_uint(Mwg), __float_as_uint(Lwg), tag);
      }
      const float fw = __expf(m - Mwg);
#pragma unroll
      for (int bt = 0; bt < NB; ++bt) { pb[bt] = pack_bf(pr[bt] * fw, pr[bt] * fw); PIN(pb[bt]); }
#pragma unroll
      for (int k = 0; k < 4; ++k) { pm[k] = (c >> 1) == k ? ((c & 1) ? 0xffff0000u : 0x0000ffffu) : 0u; PIN(pm[k]); }
    }
    SB();
    return true;
  });
  asm volatile(".Ltail_start:");
  *LDSP(f32x4, adst0) = acc;
  STAMP(14); WSTAMP(14);
  if (!fok) {
    u32x4 ml;
#pragma nounroll
    for (int spin = 0; spin < SPIN_MAX; ++spin) {
      CFENCE();
      ml = ld16<AUX_SC1>(wsr, mlo, 0);
      if (all_ok((ml[1] == tag) & (ml[3] == tag))) break;
    }
    merge_factor(ml);
  }
  STAMP(15);
  LDS_BARRIER();
  STAMP(3);
  {
    u32 pw = 0;
    if (wave == 0) {
      f32x2 t[8];
#pragma unroll
      for (int w = 0; w < 8; ++w) t[w] = *reinterpret_cast<const f32x2*>(&sm.red[w][2 * lane]);
      const f32x2 o = ((t[0] + t[1]) + (t[2] + t[3])) + ((t[4] + t[5]) + (t[6] + t[7]));
      pw = min(pack_bf(o[0] * fmrg, o[1] * fmrg), SENT - 1u);
    }
    __builtin_amdgcn_raw_buffer_store_b32(pw, wsr, pso, 0, AUX_SC1);
    st8_sc1(wsr, nto, (u32)wall_clock64() + nadd, tag);
    SB();
#pragma unroll
    for (int i = NCMIN / 8; i < NL; ++i) PIN(vr[i]);
    PIN(fl2[0]); PIN(fl2[1]); PIN(fl2[2]);
  }
  STAMP(4);

  if (part == 2) FINISH();
  if (part == 0) {
    __builtin_amdgcn_s_barrier();
#pragma nounroll
    while (wleft > 0) piece();
    until(t1);
    STAMP(11);
    CFENCE();
    u32x4 og[2];
    og[0] = ld16<AUX_SC1>(wsr, po, 0);
    SB();
    WAIT_VM(1); CFENCE();
    wo_prep(1, 2);
#pragma unroll
    for (int k = 4; k < 8; ++k) PIN(bf[k]);
    SB();
    f32x4 d[4];
    auto wo_mm = [&]() {
      const f32x4 z = {0.f, 0.f, 0.f, 0.f};
#pragma unroll
      for (int t = 0; t < 4; ++t) { d[t] = mfma16(og[0], bf[t], z); og[1][t] = __float_as_uint(dpp<XOR8>(u2f(og[0][t]))); }
      SB();
#pragma unroll
      for (int t = 0; t < 4; ++t) d[t] = mfma16(og[1], bf[4 + t], d[t]);
    };
    auto p_bad = [&]() { return !all_ok(max(max(og[0][0], og[0][1]), max(og[0][2], og[0][3])) != SENT); };
    wo_mm();
    if (__builtin_expect(p_bad(), 0)) {
#pragma nounroll
      for (int spin = 0; spin < SPIN_MAX; ++spin) {
        __builtin_amdgcn_s_sleep(1);
        CFENCE();
        og[0] = ld16<AUX_SC1>(wsr, po, 0);
        if (!p_bad()) break;
      }
      wo_mm();
      SB();
      asm volatile(".Lend_d:");
    }
    STAMP(5); WSTAMP(5);
    {
      SB();
      f32x4 y; f32x2 h[4];
#pragma unroll
      for (int t = 0; t < 4; ++t) { h[t] = d[t].xy + d[t].zw; PIN(h[t]); }
#pragma unroll
      for (int t = 0; t < 4; ++t) { y[t] = h[t][0] + h[t][1]; PIN(y[t]); }
      *LDSP(f32x4, rdst) = y;
    }
    STAMP(16);
    LDS_BARRIER();
    STAMP(17);
    if (wave < 2) {
      f32x2 v[4];
#pragma unroll
      for (int w = 0; w < 4; ++w) v[w] = f32x2{LDSP(float, rsrc)[w * 256], LDSP(float, rsrc)[w * 256 + 128]};
      const f32x2 h = (v[0] + v[1]) + (v[2] + v[3]);
      const float y = h[0] + h[1];
      __builtin_amdgcn_raw_buffer_store_b32(min(pack_bf(y, dpp<XOR1>(y)), SENT - 1u), wsr, xo, 0, AUX_SC1);
      asm volatile(".Lend_a:");
    }
    STAMP(6);
    FINISH();
  }
  if (part == 3) {
    if (!has_w) FINISH();
    u32x4 b2[4];
    {
      __builtin_amdgcn_s_barrier();
      const rsrc_t r2 = mk_rsrc(w2 + (size_t)(blk * 32 + wave * 4) * 512, 4096u);
      const u32 l2 = (u32)(uintptr_t)(&sm.w2b[wave * 4][0]);
#pragma unroll
      for (int n = 0; n < 4; ++n) dma16(r2, (lane ^ (n * 2) ^ ((wave & 2) << 2)) * 16, n * 1024, l2 + n * 1024);
    }
    const u32 tdst = lds_addr(&sm.hb[hpos]);
    const u32 sdst = lds_addr(&sm.stat[wave][lane]);
    const u32 ssrc = lds_addr(&sm.stat[(g >> 1) * 4 + ((lane >> 1) & 3)][(lane & 1) * 16]);
    const u32 adst = lds_addr((c < 4 && (g & 1) == 0) ? &sm.actl[g >> 1][wave * 4 + c] : reinterpret_cast<u16*>(&sm.dummy[lane]));
    const u32 asrc = lds_addr(&sm.actl[lane & 1][g * 8]);
    const u32 apa = lds_addr(&sm.hb[(c >> 3) * 512 + (c & 3) * 64 + ((c >> 2) & 1) * 32 + g * 8]);
    u32 xoff = G_XP + g * 2048 + ub * 1024 + (wave & 3) * 256 + c * 16;
    u32x4 one;
#pragma unroll
    for (int k = 0; k < 4; ++k) one[k] = ((c & 2) == 0 && (c >> 2) == k) ? ((c & 1) ? 0x3f800000u : 0x00003f80u) : 0u;
    PIN(one);
    u32 ooff = G_OP + (blk * 512 + tid) * 8;
    float Afp = Af, Bfp = Bf;
    PIN(xoff); PIN(ooff); PIN(Afp); PIN(Bfp);
    PIN(ga); PIN(xin); PIN(cc1);
    u32 w2a = (u32)(uintptr_t)(&sm.w2b[0][0]) + (g * 8 + ((lane >> 2) & 3)) * 1024 + (wave ^ (g & 1)) * 128 + (lane & 15) * 8;
    PIN(w2a);
    if (wave == 1) {
      due = t1 + P.N1 * 256; gate();
      u32x4 gn[2] = {ld16<AUX_SC1>(wsr, G_NT + lane * 16, 0), ld16<AUX_SC1>(wsr, G_NT + lane * 16, 1024)};
      const int tn = notices(IC<2>{}, G_NT + lane * 16, gn) * 256;
      due = (tn > t1 + 2 * 256 ? max(tn, t1 + P.L0 * 256) + P.L1 * 256 : t1) + P.X2 * 256;
      until(due);
    }
    asm volatile("s_waitcnt vmcnt(0)" ::: "memory");
    STAMP(9);
    __builtin_amdgcn_s_barrier();
    STAMP(11);
    CFENCE();
    u32x4 xg[2] = {ld16<AUX_SC1>(wsr, xoff, 0), ld16<AUX_SC1>(wsr, xoff, 8192)};
    SB();
#pragma unroll
    for (int t = 0; t < 4; ++t) {
      const u32x2 lo = tr_read(w2a ^ (t * 32)), hi = tr_read((w2a ^ (t * 32)) + 4096);
      b2[t] = u32x4{lo[0], lo[1], hi[0], hi[1]};
    }
#pragma unroll
    for (int t = 0; t < 4; ++t) PIN(b2[t]);
    SB();
    const f32x4 z4 = {0.f, 0.f, 0.f, 0.f};
    auto xp_bad = [&]() { return !all_ok(max(max(max(xg[0][0], xg[0][1]), max(xg[0][2], xg[0][3])), max(max(xg[1][0], xg[1][1]), max(xg[1][2], xg[1][3]))) != SENT); };
    f32x4 v = mfma16(one, xg[1], mfma16(one, xg[0], z4));
    if (__builtin_expect(xp_bad(), 0)) {
#pragma nounroll
      for (int spin = 0; spin < SPIN_MAX; ++spin) {
        __builtin_amdgcn_s_sleep(1);
        CFENCE();
        xg[0] = ld16<AUX_SC1>(wsr, xoff, 0); xg[1] = ld16<AUX_SC1>(wsr, xoff, 8192);
        if (!xp_bad()) break;
      }
      v = mfma16(one, xg[1], mfma16(one, xg[0], z4));
      SB();
      asm volatile(".Lend_e:");
    }
    STAMP(5); WSTAMP(5);
    {
      const float a0 = v[0], a1 = v[1];
      const float x10 = fmaf(ga[0], a0, xin[0]), x11 = fmaf(ga[1], a1, xin[1]);
      *LDSP(u32, tdst) = pack_bf(x10 * cc1[0], x11 * cc1[1]);
      float u = swap32_add(x10 + x11, fmaf(x10, x10, x11 * x11));
      u += dpp<XOR1>(u); u += dpp<XOR2>(u); u += dpp<XOR7>(u); u += dpp<XOR8>(u);
      *LDSP(float, sdst) = u;
    }
    STAMP(16);
    LDS_BARRIER();
    STAMP(17);
    {
      const f32x4 z = {0.f, 0.f, 0.f, 0.f};
      const u32x4 af0 = LDSP(u32x4, apa)[0], af1 = LDSP(u32x4, apa)[32];
      float s = LDSP(float, ssrc)[0], q = LDSP(float, ssrc)[32];
      SB();
      f32x4 d[4];
#pragma unroll
      for (int j = 0; j < 4; ++j) d[j] = mfma16(af0, bf[j], z);
#pragma unroll
      for (int j = 0; j < 4; ++j) d[j] = mfma16(af1, bf[4 + j], d[j]);
      s += dpp<XOR1>(s); q += dpp<XOR1>(q); s += dpp<XOR2>(s); q += dpp<XOR2>(q); s += dpp<XOR7>(s); q += dpp<XOR7>(q);
      const float mean = s * (1.f / 512.f);
      const float var = fmaf(q, 1.f / 512.f, -mean * mean);
      const float ri = __builtin_amdgcn_rsqf(fmaxf(var, 0.f) + 1e-5f);
      const float cf = fmaf(-ri * mean, Afp, Bfp);
      float y = (d[0][0] + d[1][1]) + (d[2][2] + d[3][3]);
      y = __builtin_bit_cast(float, __builtin_amdgcn_update_dpp(__builtin_bit_cast(int, y), __builtin_bit_cast(int, y), XOR8, 0xa, 0xf, false));
      y = swap16_add(y, y);
      y = fmaf(y, ri, cf);
      const float up = dpp<0x104>(y);
      *LDSP(u16, adst) = (u16)pack_bf(silu_f(y) * up, 0.f);
    }
    STAMP(18);
    LDS_BARRIER();
    STAMP(10);
    {
      const u32x4 af = *LDSP(u32x4, asrc);
      const f32x4 z = {0.f, 0.f, 0.f, 0.f};
      const f32x4 d0 = mfma16(af, b2[0], z), d1 = mfma16(af, b2[1], z), d2 = mfma16(af, b2[2], z), d3 = mfma16(af, b2[3], z);
      const bool g1 = g & 1, g2 = g & 2;
      const float e0 = g1 ? d1[0] : d0[0], e1 = g1 ? d1[1] : d0[1], f0 = g1 ? d3[0] : d2[0], f1 = g1 ? d3[1] : d2[1];
      st8_sc1(wsr, ooff, pack_bf(g2 ? f0 : e0, g2 ? f1 : e1), tag);
    }
    asm volatile(".Lend_b:");
    STAMP(6);
    FINISH();
  }
  if (wave == 0) {
    const int d = fd, ul = lane >> 3;
    u32 gm[2] = {sg[0][0], sg[1][0]};
    if (!all_ok((sg[0][1] == tag) & (sg[1][1] == tag))) gather8<2>(wsr, G_MOD + (1024 + d) * 8, 1536 * 8, tag, gm);
    u32 xa0, xa1;
    {
      due = t1 + P.N2 * 256; gate();
      u32x4 gn[2] = {ld16<AUX_SC1>(wsr, G_NT + lane * 16, 0), ld16<AUX_SC1>(wsr, G_NT + lane * 16, 1024)};
      const int tn = notices(IC<2>{}, G_NT + lane * 16, gn) * 256;
      due = (tn > t1 + 2 * 256 ? max(tn, t1 + P.L0 * 256) + P.L1 * 256 : t1) + P.X3 * 256;
      until(due);
    }
#pragma nounroll
    for (int spin = 0; spin < SPIN_MAX; ++spin) {
      CFENCE();
      xa0 = __builtin_amdgcn_raw_buffer_load_b32(wsr, G_XP + ul * 2048 + (d >> 1) * 4, 0, AUX_SC1);
      xa1 = __builtin_amdgcn_raw_buffer_load_b32(wsr, G_XP + ul * 2048 + (d >> 1) * 4, 1024, AUX_SC1);
      if (all_ok(max(xa0, xa1) != SENT)) break;
      __builtin_amdgcn_s_sleep(1);
    }
    float a0 = (d & 1) ? hi_f(xa0) : lo_f(xa0), a1 = (d & 1) ? hi_f(xa1) : lo_f(xa1);
    a0 += dpp<XOR8>(a0); a1 += dpp<XOR8>(a1);
    a0 = swap16_add(a0, a0); a1 = swap16_add(a1, a1);
    a0 = swap32_add(a0, a0); a1 = swap32_add(a1, a1);
    const bool hb = g & 1;
    float xv = rbf(((d & 1) ? hi_f(sx2) : lo_f(sx2)) + rbf((hb ? hi_f(gm[0]) : lo_f(gm[0])) * rbf(hb ? a1 : a0)));
    float gv = hb ? hi_f(gm[1]) : lo_f(gm[1]);
    PIN(xv); PIN(gv);
    if (wi == 0 && lane == 0) __builtin_amdgcn_raw_buffer_store_b32(tag, wsr, WS_EPOCH, 0, AUX_SC1);
    STAMP(10);
    due = max(due + P.X4 * 256, (int)(((u32)wall_clock64() - t0) << 8) + P.GX * 256);
    const bool last = ul + 40 < NUP;
    u32 o0 = G_OP + (ul * 512 + d) * 8, o5 = last ? o0 : OOB, tg5 = last ? tag : 0u;
    const rsrc_t rout = mk_rsrc(out, 2048u);
    u32 oo = (lane & 40) == 0 ? (g * 512 + d) * 2 : OOB;
    const u32 pk = c == 0 ? 0x00003f80u : (c == 4 ? 0x3f800000u : 0u);
    u32x4 one = {pk, 0u, pk, 0u};
    PIN(o0); PIN(o5); PIN(tg5); PIN(oo); PIN(one);
    __builtin_amdgcn_raw_buffer_store_b16((u16)pack_bf(xv, 0.f), rout, oo, 0, AUX_SC1);
    until(due);
    f32x4 sv;
#pragma nounroll
    for (int spin = 0; spin < SPIN_MAX; ++spin) {
      CFENCE();
      u32x2 og[6];
#pragma unroll
      for (int k = 0; k < 5; ++k) og[k] = ld8<AUX_SC1>(wsr, o0, k * 32768);
      og[5] = ld8<AUX_SC1>(wsr, o5, 5 * 32768);
      sv = f32x4{0.f, 0.f, 0.f, 0.f};
#pragma unroll
      for (int k = 0; k < 3; ++k) sv = mfma16(one, u32x4{og[2 * k][0], og[2 * k][1], og[2 * k + 1][0], og[2 * k + 1][1]}, sv);
      const u32 bad = ((og[0][1] ^ tag) | (og[1][1] ^ tag) | (og[2][1] ^ tag)) | ((og[3][1] ^ tag) | (og[4][1] ^ tag) | (og[5][1] ^ tg5));
      PIN(sv);
      if (all_ok(bad == 0u)) break;
      __builtin_amdgcn_s_sleep(1);
    }
    STAMP(7);
    const float s = sv[0] + dpp<XOR8>(sv[0]);
    __builtin_amdgcn_raw_buffer_store_b16((u16)pack_bf(fmaf(gv, s, xv), 0.f), rout, oo, 0, AUX_SC1);
    asm volatile(".Lend_c:");
  }
  FINISH();
}

extern "C" void kernel_launch(void* const* d_in, const int* in_sizes, int n_in,
                              void* d_out, int out_size, void* d_ws, size_t ws_size,
                              hipStream_t stream) {
  fused_kernel<<<256, 512, 0, stream>>>((const u16*)d_in[0], (const u16*)d_in[1], (const u16*)d_in[2], (const u16*)d_in[3], (const u16*)d_in[4],
                                        (const u16*)d_in[6], (const u16*)d_in[7], (const u16*)d_in[8], (const u16*)d_in[9], (const u16*)d_in[10],
                                        (unsigned char*)d_ws, (u16*)d_out
    );
}
```

```cpp
#include <hip/hip_runtime.h>
#include <stdint.h>

typedef unsigned int u32;
typedef unsigned short u16;
typedef unsigned long long u64;
typedef __attribute__((ext_vector_type(4))) unsigned u32x4;
typedef __attribute__((ext_vector_type(2))) unsigned u32x2;
typedef __attribute__((ext_vector_type(2))) __bf16 bf16x2;
typedef __attribute__((ext_vector_type(4))) float f32x4;
typedef __attribute__((ext_vector_type(2))) float f32x2;
typedef __amdgpu_buffer_rsrc_t rsrc_t;

constexpr int NH = 32, SK = 16384, DH = 128, WIN = 4096, FF = 1376;
struct Prm { int E0, ES, E1, E2, E2C, EU, TEND, DT_UP, L0, L1, X1, X2, X3, X4, NE, FE, M0, M1, M2, M3, M4, GS, GX, N1, N2, GA, WD, DT_WO, LW, LU, EW, PM, D0, FK, FW, FU, FN, F0, DS, FE2, TS, WM, PW; };
constexpr Prm PRM0 = {32, 24, 32, 32, 48, 64, 2028, 66, 30, 25, 68, 85, 115, 96, 5, 12, 12, 5, 12, 32, 28, 30, 56, 24, 58, 8, 4, 100, 45, 15, 8, 0x6827941, 30, 60, 61, 60, 61, 59, 116, 16, 500, 0x53, 0};
#define PRM_ARG
#define TABP (&TAB.e[0])
constexpr int NL = 37;
constexpr int NCMIN = 192;
constexpr int WK = 6, WV = 5;
constexpr int WUSE = 13, LNS = 10;
#define QPA 12
#define QPB 18
#define QDD 5
constexpr int QP1 = QPA, QP2 = QPB, QD = QDD;
constexpr int VQ = 6;
constexpr int NSL = (NL + 7) / 8;
constexpr int VS = VQ + NSL;
constexpr int MLS = VS + 2;
constexpr int VSIDE = MLS - 5, UPREP = MLS + 8;
constexpr int SIDE = NCMIN / 8 - 4;
constexpr int CPF = 18;
constexpr int TBP = 7, TBL = 11, TBU = 15;
constexpr int cons_end(int v) {
  const int a = v - WV + 1, c = (v - MLS) * 4;
  const int m = a < c ? a : c;
  return m < 0 ? 0 : (m > NL ? NL : m);
}
constexpr int KF = 4, KF0 = 8;
struct Tab { u32 e[1024]; };
#define popc __builtin_popcount
__host__ __device__ constexpr u32 place(u32 v, u32 m, int n) {
  u32 r = 0;
  for (int k = 0; k < n; ++k) r |= (v >> k & 1u) << (10 + (m >> (4 * k) & 15));
  return r;
}
__host__ __device__ constexpr u32 step_off(const Prm& P, u32 s0, u32 bh) {
  const u32 src = (s0 >> 5 & 3u) | bh << 2;
  return place(s0 ^ ((popc(src & P.M0) & 1) | (popc(src & P.M1) & 1) << 1 | (popc(src & P.M2) & 1) << 2 | (popc(src & P.M3) & 1) << 3 | (popc(src & P.M4) & 1) << 4), P.PM, 7);
}
constexpr Tab make_tab(const Prm& P) {
  Tab T{};
  for (int bid = 0; bid < 256; ++bid) {
    const int xcd = bid & 7, idx = bid >> 3, part = idx & 3, i8 = idx >> 2, wi = i8 * 8 + xcd;
    const bool has_mod = wi < 48, has_up = wi >= 21;
    const int cb0 = (i8 + 5) & 7, cb1 = (i8 + 4) & 7, ms = wi - 20;
    const int mi = wi < 8 ? wi : (wi < 16 ? 8 + ((xcd + 4) & 7) : (wi < 20 ? (wi - 14) * 8 + 3 : 16 + ms / 7 * 8 + ms % 7 + (ms % 7 >= 3 ? 1 : 0)));
    const int ui = xcd * 5 + (xcd > 5 ? xcd - 5 : 0) + (i8 >= 3 ? (xcd >= 5 ? 1 : 0) + i8 - 3 : 0);
    const int wd = cb0 == 3 ? 0 : P.WD;
    const int e[4] = {P.E0 - 4 * wd + (cb0 == 3 ? P.ES : 0), P.E1 + (cb1 == 3 ? P.ES : 0), has_mod ? (wi < 16 ? P.E2C : P.E2) + ((mi & 7) == 3 ? P.ES : 0) : 0, has_up ? P.EU : 0};
    const int ew = wd ? P.EW : 0, pa = (1024 + e[0] + ew + e[1] + e[2] + e[3]) / 4;
    int c[5] = {0, 0, 0, 0, 128};
    for (int k = 0; k < 3; ++k) c[k + 1] = c[k] + (pa - e[k] - (k == 0 ? ew : 0) + 4) / 8;
    const int nl = c[part + 1] - c[part], wst = e[part] / 4;
    const bool has_w = part < 2 || (part == 2 ? has_mod : has_up);
    const int blk = !has_w ? 0 : (part == 0 ? cb0 : (part == 1 ? xcd * 8 + cb1 : (part == 2 ? mi : ui)));
    const int span = (part == 3 && has_up ? P.TEND - P.DT_UP : (part == 0 && wd ? P.TEND - P.DT_WO : P.TEND)) * 256;
    const int np = part == 0 ? 8 - wd : 8, tau0 = span / (2 * nl + wst);
    const bool up = part == 3 && has_up;
    const int tau = tau0 * (up ? P.FU : (part == 0 && wd ? P.F0 : (has_w ? P.FK : P.FN))) / 64, tauw = tau0 * wst / np * (up ? P.FU : P.FW) / 64;
    int sw = KF, at = P.DS * 256 + (has_w ? np * tauw : 0);
    while (sw < NCMIN / 8 - 1 && at < P.TS * 256) { at += tau; ++sw; }
    const int tau2 = (span - at) / (2 * nl - sw);
    T.e[4 * bid] = (u32)(c[part] | nl << 7 | blk << 13 | (has_w ? 1 : 0) << 19 | (part == 0 ? wd : 0) << 20 | sw << 24);
    T.e[4 * bid + 1] = (u32)(tau | tauw << 16);
    if ((part == 1 && nl <= QP2 + QD + 1) || c[part] > 32 * part + KF0 || c[part + 1] < 32 * part + KF0 + 4) T.e[4 * bid] = 0;
    const u32 pitch = part == 0 ? 1024u : (part == 1 ? 8192u : (part == 2 ? 6144u : 2752u));
    T.e[4 * bid + 2] = (part == 0 ? xcd * 512u * pitch : 0u) + blk * (part == 3 ? 64u : 128u);
    T.e[4 * bid + 3] = pitch | (u32)tau2 << 16;
  }
  return T;
}
constexpr int step_range(const Prm& P, bool mx_) {
  int mx = 0, mn = 1 << 20;
  const Tab T = make_tab(P);
  for (int bid = 0; bid < 256; ++bid) { const int n = T.e[4 * bid] >> 7 & 63; mx = n > mx ? n : mx; mn = n < mn ? n : mn; }
  return mx_ ? mx : mn;
}
__constant__ __attribute__((aligned(64))) const Tab TAB = make_tab(PRM0);
static_assert(step_range(PRM0, true) <= NL && step_range(PRM0, false) >= NCMIN / 8, "a wave's key range does not fit its load steps");
static_assert(cons_end(SIDE + WV + 1) <= NCMIN / 8 && cons_end(UPREP) <= NCMIN / 8 && SIDE > MLS + 3, "steps every wave runs");
constexpr int NUP = FF / 32;
constexpr int SPIN_MAX = 1 << 17;

constexpr u32 WS_EPOCH = 0;
constexpr u32 G_TB = 1024;
constexpr u32 G_NT = 2048;
constexpr u32 G_MOD = 4096;
constexpr u32 G_Q = G_MOD + 3072 * 8;
constexpr u32 G_ML = G_Q + 4096 * 8;
constexpr u32 G_P = G_ML + 256 * 16;
constexpr u32 G_XP = G_P + 256 * 256;
constexpr u32 G_OP = G_XP + 8 * 512 * 4;
constexpr u32 WS_END = G_OP + 43 * 512 * 8;
constexpr u32 SENT = 0xffffffffu;
constexpr u32 OOB = 0x40000000u;
#define WST 0

constexpr int AUX_NT = 2, AUX_SC1 = 16;

__device__ __forceinline__ float lo_f(u32 v) { return __uint_as_float(v << 16); }
__device__ __forceinline__ float hi_f(u32 v) { return __uint_as_float(v & 0xffff0000u); }
__device__ __forceinline__ float rbf(float x) { return (float)(__bf16)x; }
__device__ __forceinline__ u32 pack_bf(float lo, float hi) {
  bf16x2 r; r[0] = (__bf16)lo; r[1] = (__bf16)hi; return __builtin_bit_cast(u32, r);
}
__device__ __forceinline__ float dot2(u32 k, u32 q, float acc) {
  return __builtin_amdgcn_fdot2_f32_bf16(__builtin_bit_cast(bf16x2, k), __builtin_bit_cast(bf16x2, q), acc, false);
}
template <int CTRL> __device__ __forceinline__ float dpp(float x) {
  return __builtin_bit_cast(float, __builtin_amdgcn_mov_dpp(__builtin_bit_cast(int, x), CTRL, 0xf, 0xf, true));
}
constexpr int XOR1 = 0xB1, XOR2 = 0x4E, XOR7 = 0x141, XOR8 = 0x128;
__device__ __forceinline__ float swap32_add(float a, float b) {
  auto t = __builtin_amdgcn_permlane32_swap(__float_as_uint(a), __float_as_uint(b), false, false);
  return __uint_as_float(t[0]) + __uint_as_float(t[1]);
}
__device__ __forceinline__ float swap16_add(float a, float b) {
  auto t = __builtin_amdgcn_permlane16_swap(__float_as_uint(a), __float_as_uint(b), false, false);
  return __uint_as_float(t[0]) + __uint_as_float(t[1]);
}
__device__ __forceinline__ float wave_sum(float x) {
  x += dpp<XOR1>(x); x += dpp<XOR2>(x); x += dpp<XOR7>(x); x += dpp<XOR8>(x);
  x = swap16_add(x, x);
  return swap32_add(x, x);
}
__device__ __forceinline__ float wave_max(float x) {
  x = fmaxf(x, dpp<XOR1>(x)); x = fmaxf(x, dpp<XOR2>(x)); x = fmaxf(x, dpp<XOR7>(x)); x = fmaxf(x, dpp<XOR8>(x));
  auto s = __builtin_amdgcn_permlane16_swap(__float_as_uint(x), __float_as_uint(x), false, false);
  x = fmaxf(__uint_as_float(s[0]), __uint_as_float(s[1]));
  auto t = __builtin_amdgcn_permlane32_swap(__float_as_uint(x), __float_as_uint(x), false, false);
  return fmaxf(__uint_as_float(t[0]), __uint_as_float(t[1]));
}
template <int CTRL> __device__ __forceinline__ float sel_add(float lo, float hi, bool up) {
  const float t1 = lo + dpp<CTRL>(lo), t2 = hi + dpp<CTRL>(hi);
  return up ? t2 : t1;
}
template <int I> __device__ __forceinline__ float bc16(float x) {
  return __builtin_bit_cast(float, __builtin_amdgcn_update_dpp(0, __builtin_bit_cast(int, x), 0x150 + I, 0xf, 0xf, false));
}
__device__ __forceinline__ float bcast16(float x, int i) {
  switch (i) {
    case 0: return bc16<0>(x); case 1: return bc16<1>(x); case 2: return bc16<2>(x); case 3: return bc16<3>(x);
    case 4: return bc16<4>(x); case 5: return bc16<5>(x); case 6: return bc16<6>(x); case 7: return bc16<7>(x);
    case 8: return bc16<8>(x); case 9: return bc16<9>(x); case 10: return bc16<10>(x); case 11: return bc16<11>(x);
    case 12: return bc16<12>(x); case 13: return bc16<13>(x); case 14: return bc16<14>(x); default: return bc16<15>(x);
  }
}
__device__ __forceinline__ float silu_f(float x) { return x * __builtin_amdgcn_rcpf(1.f + __expf(-x)); }

template <int AUX> __device__ __forceinline__ u32x4 ld16(rsrc_t r, u32 voff, u32 soff) {
  return __builtin_amdgcn_raw_buffer_load_b128(r, voff, soff, AUX);
}
template <int AUX> __device__ __forceinline__ u32x2 ld8(rsrc_t r, u32 voff, u32 soff) {
  return __builtin_amdgcn_raw_buffer_load_b64(r, voff, soff, AUX);
}
__device__ __forceinline__ void st8_sc1(rsrc_t r, u32 voff, u32 v, u32 tag) {
  __builtin_amdgcn_raw_buffer_store_b64(u32x2{v, tag}, r, voff, 0, AUX_SC1);
}
__device__ __forceinline__ void st16_sc1(rsrc_t r, u32 voff, u32 v0, u32 v1, u32 tag) {
  __builtin_amdgcn_raw_buffer_store_b128(u32x4{v0, tag, v1, tag}, r, voff, 0, AUX_SC1);
}
__device__ __forceinline__ rsrc_t mk_rsrc(const void* p, u32 bytes) {
  return __builtin_amdgcn_make_buffer_rsrc(const_cast<void*>(p), 0, bytes, 0x00020000);
}
#define WAIT_VM(n) __builtin_amdgcn_s_waitcnt(((n) & 15) | (((n) >> 4) << 14) | (7 << 4) | (15 << 8))
#define SB() __builtin_amdgcn_sched_barrier(0)
#define CFENCE() asm volatile("" ::: "memory")
__device__ __forceinline__ bool all_ok(bool ok) { return __builtin_amdgcn_ballot_w64(!ok) == 0ull; }

template <int N> __device__ __forceinline__ void gather16(rsrc_t ws, u32 voff, u32 stride, u32 tag, u32x4 (&g)[N]) {
#pragma nounroll
  for (int spin = 0; spin < SPIN_MAX; ++spin) {
    CFENCE();
    bool ok = true;
#pragma unroll
    for (int i = 0; i < N; ++i) g[i] = ld16<AUX_SC1>(ws, voff + i * stride, 0);
#pragma unroll
    for (int i = 0; i < N; ++i) ok &= (g[i][1] == tag) & (g[i][3] == tag);
    if (all_ok(ok)) break;
    __builtin_amdgcn_s_sleep(8);
  }
}
template <int N> __device__ __forceinline__ void gather8(rsrc_t ws, u32 voff, u32 stride, u32 tag, u32 (&g)[N], bool act = true) {
#pragma nounroll
  for (int spin = 0; spin < SPIN_MAX; ++spin) {
    CFENCE();
    bool ok = true;
    u32x2 t[N];
#pragma unroll
    for (int i = 0; i < N; ++i) t[i] = ld8<AUX_SC1>(ws, voff + i * stride, 0);
#pragma unroll
    for (int i = 0; i < N; ++i) { ok &= (t[i][1] == tag); g[i] = t[i][0]; }
    if (all_ok(ok | !act)) break;
    __builtin_amdgcn_s_sleep(8);
  }
}

struct Smem {
  float red[8][128];
  float ml[8][2];
  float r2[64 * 20];
  u16 hb[3 * 1024];
  float stat[8][64];
  float red2[8][128];
  u32 dummy[64];
  u32x2 bc;
  u32x4 zero[4];
  u32 mflag[8];
  u32x4 wbuf[8][8][64];
  __attribute__((aligned(256))) u16 w2b[32][512];
  u16 actl[2][32];
};

#define STAMP(k) do {} while (0)
#define WSTAMP(k) do {} while (0)
#define FINISH() return
#define PIN(x) asm volatile("" : "+v"(x))
#define LDS_BARRIER() do { __builtin_amdgcn_s_waitcnt(0xc07f); __builtin_amdgcn_s_barrier(); CFENCE(); } while (0)
#define WAVE_SYNC() do { __builtin_amdgcn_fence(__ATOMIC_RELEASE, "wavefront"); __builtin_amdgcn_wave_barrier(); __builtin_amdgcn_fence(__ATOMIC_ACQUIRE, "wavefront"); } while (0)

__device__ __forceinline__ void dma16(rsrc_t r, u32 voff, u32 soff, u32 lds_dst) {
  u32 keep;
  asm volatile("s_mov_b32 %0, m0\n\ts_mov_b32 m0, %4\n\ts_nop 4\n\tbuffer_load_dwordx4 %1, %2, %3 offen nt lds\n\ts_mov_b32 m0, %0"
               : "=&s"(keep) : "v"(voff), "s"(r), "s"(soff), "s"(lds_dst) : "memory");
}
__device__ __forceinline__ u32 perm_lo(u32 a, u32 b) { return __builtin_amdgcn_perm(b, a, 0x05040100u); }
__device__ __forceinline__ u32 perm_hi(u32 a, u32 b) { return __builtin_amdgcn_perm(b, a, 0x07060302u); }
__device__ __forceinline__ float rdl(float x, int l) { return __int_as_float(__builtin_amdgcn_readlane(__float_as_int(x), l)); }

typedef __attribute__((ext_vector_type(8))) __bf16 bf16x8;
__device__ __forceinline__ f32x4 mfma16(u32x4 a, u32x4 b, f32x4 c) {
  return __builtin_amdgcn_mfma_f32_16x16x32_bf16(__builtin_bit_cast(bf16x8, a), __builtin_bit_cast(bf16x8, b), c, 0, 0, 0);
}
__device__ __forceinline__ float sum8(const float* p) {
  const f32x4 a = *reinterpret_cast<const f32x4*>(p), b = *reinterpret_cast<const f32x4*>(p + 4);
  return ((a[0] + b[0]) + (a[1] + b[1])) + ((a[2] + b[2]) + (a[3] + b[3]));
}
__device__ __forceinline__ float ln_ri(float var) {
  return __builtin_amdgcn_rcpf(rbf(__builtin_amdgcn_sqrtf(rbf(rbf(fmaxf(var, 0.f)) + 1e-5f))));
}
__device__ __forceinline__ void ln_stats2(const float (&x0)[8], const float (&x1)[8], float& mb0, float& ri0, float& mb1, float& ri1) {
  const float k0 = rdl(x0[0], 0), k1 = rdl(x1[0], 0);
  float s0 = 0.f, s1 = 0.f, q0 = 0.f, q1 = 0.f;
#pragma unroll
  for (int k = 0; k < 8; ++k) {
    const float d0 = x0[k] - k0, d1 = x1[k] - k1;
    s0 += d0; s1 += d1; q0 = fmaf(d0, d0, q0); q1 = fmaf(d1, d1, q1);
  }
  float t = swap16_add(swap32_add(s0, s1), swap32_add(q0, q1));
  t += dpp<XOR1>(t); t += dpp<XOR2>(t); t += dpp<XOR7>(t); t += dpp<XOR8>(t);
  auto u = __builtin_amdgcn_permlane16_swap(__float_as_uint(t), __float_as_uint(t), false, false);
  const float ms = __uint_as_float(u[0]) * (1.f / 512.f), mq = __uint_as_float(u[1]) * (1.f / 512.f);
  const float ri = ln_ri(fmaf(-ms, ms, mq));
  mb0 = rbf(k0 + rdl(ms, 0)); mb1 = rbf(k1 + rdl(ms, 32));
  ri0 = rdl(ri, 0); ri1 = rdl(ri, 32);
}
template <int I> struct IC { static constexpr int v = I; };
template <int I, int N, class F> __device__ __forceinline__ void sfor(F&& f) {
  if constexpr (I < N) { if (f(IC<I>{})) sfor<I + 1, N>(f); }
}
typedef __attribute__((ext_vector_type(4))) short s16x4;
typedef __attribute__((address_space(3))) s16x4* lds_s16x4p;
__device__ __forceinline__ u32x2 tr_read(u32 lds_addr) {
  return __builtin_bit_cast(u32x2, __builtin_amdgcn_ds_read_tr16_b64_v4i16((lds_s16x4p)lds_addr));
}
__device__ __forceinline__ u32 next_tag(u32 e) { return ((e | 0x40004000u) + 1u) & ~0x40004000u; }
__device__ __forceinline__ float u2f(u32 v) { return __uint_as_float(v); }
#define LDSP(T, a) ((__attribute__((address_space(3))) T*)(a))
template <class T> __device__ __forceinline__ u32 lds_addr(T* p) { u32 a = (u32)(uintptr_t)p; PIN(a); return a; }

typedef __attribute__((ext_vector_type(16))) unsigned u32x16;
typedef __attribute__((ext_vector_type(8))) unsigned u32x8;
__global__ void __launch_bounds__(512, 2) fused_kernel(const u16* x_, const u16* kc_, const u16* vc_, const u16* wq_, const u16* wo_, const u16* w1_,
                                                       const u16* w3_, const u16* w2_, const u16* cnd_, const u16* wmod_, unsigned char* ws_, u16* out_ PRM_ARG) {
  __shared__ __attribute__((aligned(256))) Smem sm;
  constexpr Prm P = PRM0;
  const int TEND = P.TEND;
  const int bid = blockIdx.x, tid = threadIdx.x;
  u32x4 kr[NL];
  u32x16 ka; u32x8 kb; u32x4 tev; u64 pt0; u32 tch2;
  asm volatile("s_load_dwordx16 %0, %3, 0x0\n\ts_load_dwordx8 %1, %3, 0x40\n\ts_memrealtime %2"
               : "=&s"(ka), "=&s"(kb), "=&s"(pt0) : "s"(__builtin_amdgcn_kernarg_segment_ptr()));
  SB();
  {
    u64 pc; u32 vb;
    asm volatile("s_getpc_b64 %2\n.Lcpf_first:\n\tv_mul_u32_u24 %1, ((.Lend_c-.Lcpf_first)>>10)<<2, %3\n\tglobal_load_dword %0, %1, %2"
                 : "=&v"(tch2), "=&v"(vb), "=&s"(pc) : "v"((bid & ~7) + (tid >> 6)) : "memory");
    asm volatile("global_load_dwordx4 %0, %1, %2" : "=&v"(tev) : "v"(bid << 4), "s"(TABP) : "memory");
  }
  SB();
  const int xcd = bid & 7, idx = bid >> 3, part = idx & 3;
  const int b = idx >> 2 & 1, head = xcd * 4 + (((idx >> 3) & 1) << 1 | idx >> 4);
  const u32 so0 = step_off(P, 32 * part + KF0, b * 32 + head) & ~place(3, P.PM, 2);
  u32 va = (((b * 32 + head) * SK + (SK - WIN)) * DH * 2 + so0) | place(tid >> 6, P.WM, 3) | (tid & 63) << 4;
  const u32 kx1 = place(1, P.PM, 2), kx2 = place(2, P.PM, 2);
  PIN(va);
  u32 va1 = va | kx1, va2 = va | kx2, va3 = va | kx1 | kx2;
  PIN(va1); PIN(va2); PIN(va3);
  int mc = bid & 0xd8;
  asm volatile("" : "+v"(va), "+s"(mc));
  SB();
  asm volatile("s_waitcnt lgkmcnt(0)" : "+s"(ka), "+s"(kb), "+s"(pt0));
  SB();
#define K_FIRST "global_load_dwordx4 %0, %4, %5 nt\n\tglobal_load_dwordx4 %1, %6, %5 nt\n\tglobal_load_dwordx4 %2, %7, %5 nt\n\tglobal_load_dwordx4 %3, %8, %5 nt"
  asm volatile("s_cmp_eq_u32 %9, 16\n\ts_cbranch_scc1 .Lk_later\n\t" K_FIRST "\n.Lk_later:"
               : "=&v"(kr[0]), "=&v"(kr[1]), "=&v"(kr[2]), "=&v"(kr[3]) : "v"(va), "s"((u64)ka[2] | (u64)ka[3] << 32), "v"(va1), "v"(va2), "v"(va3), "s"(mc) : "memory", "scc");
  SB();
  const u32 tka = (u32)wall_clock64(), t0 = tka - P.D0 + (__builtin_amdgcn_readfirstlane(tid >> 6) * 2 - 7) * P.PW / 16;
  auto kap = [&](int k) { return k < 8 ? (u64)ka[2 * k] | (u64)ka[2 * k + 1] << 32 : (u64)kb[2 * k - 16] | (u64)kb[2 * k - 15] << 32; };
  const u16 *x = (const u16*)kap(0), *kc = (const u16*)kap(1), *vc = (const u16*)kap(2), *w2 = (const u16*)kap(7), *cnd = (const u16*)kap(8);
  unsigned char* ws = (unsigned char*)kap(10); u16* out = (u16*)kap(11);
  const int lane = tid & 63, wave = __builtin_amdgcn_readfirstlane(tid >> 6);
  const bool p3 = part == 3;
  const u64 wa = part == 0 ? kap(4) : (part == 1 ? kap(3) : (part == 2 ? kap(9) : ((wave & 1) ? kap(6) : kap(5))));
  const bool modc = mc == 16;
  if (modc) asm volatile("s_waitcnt vmcnt(0)" ::: "memory");
  asm volatile("s_waitcnt vmcnt(4)" : "+v"(tev));
  const u32 te0 = __builtin_amdgcn_readfirstlane(tev[0]), te1 = __builtin_amdgcn_readfirstlane(tev[1]);
  const u32 te3 = __builtin_amdgcn_readfirstlane(tev[3]), pitch = te3 & 0xffff, wsz = pitch << (part == 0 ? 12 : 9);
  const int sh = p3 ? 2 : 3;
  u32 wv0 = (u32)((p3 ? (wave >> 1) * 128 : wave * 64) + (lane >> sh)) * pitch + ((lane & (p3 ? 3 : 7)) ^ (lane >> (p3 ? 5 : 3))) * 16u;
  const u32 wimg = (u32)(uintptr_t)(&sm.wbuf[0][0][0]);
  u32 wld = wimg + (p3 ? (wave & 1) * 32768 + (wave >> 1) * 8192 : wave * 8192);
  const int bl0 = te0 & 127, nl = te0 >> 7 & 63, blk = te0 >> 13 & 63;
  const bool has_w = te0 >> 19 & 1;
  const rsrc_t rw = mk_rsrc((const void*)wa, wsz);
  const u32 wstep = pitch << (6 - sh), wflip = p3 ? 32u : 0u;
  u32 wso = __builtin_amdgcn_readfirstlane(tev[2]);
  int wleft = 8;
  auto piece = [&]() {
    dma16(rw, wv0, wso, wld);
    wso += wstep; wld += 1024;
    wv0 ^= (wleft & 1) ? wflip : 0u;
    --wleft;
  };
  int tau = te1 & 0xffff;
  const int tauw = te1 >> 16, sw = te0 >> 24;
  int tau2 = te3 >> 16;
  int due = P.DS * 256, now = 0;
  auto gate = [&]() {
    while (now < due) { now = (int)(((u32)wall_clock64() - t0) << 8); if (now < due) __builtin_amdgcn_s_sleep(1); }
    due += tau;
  };
  if (has_w) {
    if (modc) now = 1 << 30;
#pragma nounroll
    while (wleft > (int)(te0 >> 20 & 7)) { gate(); due += tauw - tau; piece(); }
    now = 0;
  }
  SB();
  STAMP(23);
  const rsrc_t wsr = mk_rsrc(ws, WS_END);
  const int g = lane >> 4, c = lane & 15;
  const int i8 = idx >> 2, wi = i8 * 8 + xcd;
  const bool is_up = p3 && has_w;
  const size_t base = ((size_t)(b * NH + head) * SK + (SK - WIN)) * DH;
  const rsrc_t rk = mk_rsrc(kc + base, WIN * DH * 2), rv = mk_rsrc(vc + base, WIN * DH * 2);
  u32 sot;
  {
    u32 s0 = bl0 + lane - KF;
    s0 += s0 >= (u32)(32 * part + KF0) ? KF : 0;
    sot = (lane < KF ? so0 | place(lane, P.PM, 2) : step_off(P, s0, b * 32 + head)) | place(wave, P.WM, 3);
    sot = lane < nl ? sot : OOB;
    PIN(sot);
  }
  auto until = [&](int t) { while ((int)(((u32)wall_clock64() - t0) << 8) < t); };
  const u32 pslot = G_P + ((b * 32 + head) * 4 + part) * 256;
  const rsrc_t rx = mk_rsrc(part == 2 ? cnd : x, (part == 1 || (part == 2 && has_w)) ? 2048u : 0u);
  u32x2 ep; u32 xs0, xs1, tch1; u32x4 xr0, xr1;
  auto small_ops = [&]() {
    ep = ld8<AUX_SC1>(wsr, wave == 7 ? WS_EPOCH : OOB, 0);
    xs0 = __builtin_amdgcn_raw_buffer_load_b16(rx, tid * 2, 0, 0); xs1 = __builtin_amdgcn_raw_buffer_load_b16(rx, tid * 2, 1024, 0);
    xr0 = ld16<0>(rx, part == 1 ? lane * 16 : OOB, 0); xr1 = ld16<0>(rx, part == 1 ? lane * 16 : OOB, 1024);
    tch1 = __builtin_amdgcn_raw_buffer_load_b32(mk_rsrc(w2 + (size_t)blk * 32 * 512, is_up ? 64u : 0u), tid == 384 ? 0 : OOB, 0, AUX_NT);
  };
  if (modc) __builtin_amdgcn_raw_buffer_store_b128(u32x4{SENT, SENT, SENT, SENT}, wsr, tid < 320 ? G_P + wi * 5120 + tid * 16 : OOB, 0, AUX_SC1);
  SB();
  u32 tag = 0;
  u32x4 bf[8];
  auto wo_prep = [&](int s0 = 0, int s1 = 2) {
    const u32 wi_ = wimg + wave * 8192;
    const int q = (lane >> 2) & 3, pp = lane & 3;
#pragma unroll
    for (int s = s0; s < s1; ++s) {
#pragma unroll
      for (int t = 0; t < 4; ++t) {
        const int r0 = s * 32 + g * 8 + q, r1 = r0 + 4, ch = t * 2 + (pp >> 1);
        const u32x2 lo = tr_read(wi_ + r0 * 128 + ((ch ^ (r0 & 7)) * 16) + (pp & 1) * 8), hi = tr_read(wi_ + r1 * 128 + ((ch ^ (r1 & 7)) * 16) + (pp & 1) * 8);
        bf[s * 4 + t] = u32x4{lo[0], lo[1], hi[0], hi[1]};
      }
    }
  };
  auto gemv_go = [&](u32 hh) {
    sm.hb[tid] = (u16)hh; sm.hb[512 + tid] = (u16)(hh >> 16);
    WAVE_SYNC();
    const u32x4* a = (c & 11) == 0 ? reinterpret_cast<const u32x4*>(&sm.hb[(c >> 2) * 512 + wave * 64 + g * 8]) : &sm.zero[0];
    const u32x4 a0 = a[0], a1 = a[(c & 11) == 0 ? 4 : 0];
    const f32x4 z = {0.f, 0.f, 0.f, 0.f};
    f32x4 d[4];
#pragma unroll
    for (int t = 0; t < 4; ++t) d[t] = mfma16(a0, bf[t], z);
#pragma unroll
    for (int t = 0; t < 4; ++t) d[t] = mfma16(a1, bf[4 + t], d[t]);
    if (lane < 32) {
#pragma unroll
      for (int t = 0; t < 4; ++t) sm.r2[(t * 16 + c) * 20 + g * 8 + wave] = d[t][0];
    }
  };
  auto mod_stage = [&]() {
    wo_prep();
    gemv_go(pack_bf(silu_f(lo_f(xs0)), silu_f(lo_f(xs1))));
    __builtin_amdgcn_s_waitcnt(0xc07f);
    if (lane == 0) *LDSP(volatile u32, (u32)(uintptr_t)&sm.mflag[wave]) = 1u;
    if (wave == 7) {
#pragma nounroll
      for (int spin = 0; spin < SPIN_MAX; ++spin) {
        const u32 f = *LDSP(volatile u32, (u32)(uintptr_t)&sm.mflag[lane & 7]);
        if (all_ok(f != 0u)) break;
        __builtin_amdgcn_s_sleep(1);
      }
      CFENCE();
      st8_sc1(wsr, G_MOD + (blk * 64 + lane) * 8, pack_bf(sum8(&sm.r2[lane * 20]), sum8(&sm.r2[lane * 20 + 8])), tag);
    }
    STAMP(9);
  };
  if (lane < 4) sm.zero[lane] = u32x4{0u, 0u, 0u, 0u};
  if (part == 2 && has_w) {
    if (tid < 8) sm.mflag[tid] = 0u;
    LDS_BARRIER();
    if (__builtin_expect(modc, 0)) {
      small_ops();
      asm volatile("s_waitcnt vmcnt(0)" ::: "memory");
      tag = next_tag(__builtin_amdgcn_readfirstlane(ep[0]));
      mod_stage();
      __builtin_amdgcn_s_barrier();
      asm volatile(K_FIRST : "+v"(kr[0]), "+v"(kr[1]), "+v"(kr[2]), "+v"(kr[3]) : "v"(va), "s"((u64)ka[2] | (u64)ka[3] << 32), "v"(va1), "v"(va2), "v"(va3) : "memory");
    }
  }
  SB();
  const int fd = wi * 8 + (lane & 7);
  const int ub = wave >> 2, ud = (wave & 3) * 128 + c * 8 + g * 2;
  u32x4 sg[3]; u32 sx2;
  auto side_loads = [&]() {
    const bool fin = part == 1 && wave == 0;
    sg[0] = ld16<AUX_SC1>(wsr, is_up ? G_MOD + (1024 + ud) * 8 : (fin ? G_MOD + (1024 + fd) * 8 : OOB), 0);
    sg[1] = ld16<AUX_SC1>(wsr, is_up ? G_MOD + (1536 + ud) * 8 : (fin ? G_MOD + (2560 + fd) * 8 : OOB), 0);
    sg[2] = ld16<AUX_SC1>(wsr, is_up ? G_MOD + (2048 + ud) * 8 : OOB, 0);
    sx2 = __builtin_amdgcn_raw_buffer_load_b32(mk_rsrc(x, (is_up || fin) ? 2048u : 0u), is_up ? ub * 1024 + ud * 2 : (g & 1) * 1024 + (fd >> 1) * 4, 0, 0);
  };
  auto side_pin = [&]() { PIN(sg[0]); PIN(sg[1]); PIN(sg[2]); PIN(sx2); };
  const u32 mlo = G_ML + (head * 2 + b) * 64 + (lane & 3) * 16;
  float fmrg = 0.f;
  bool fok = wave != 0;
  auto merge_factor = [&](const u32x4& ml) {
    const float mi = u2f(ml[0]);
    float M = fmaxf(mi, dpp<XOR1>(mi)); M = fmaxf(M, dpp<XOR2>(M));
    const float e = __expf(mi - M);
    float L = e * u2f(ml[2]);
    L += dpp<XOR1>(L); L += dpp<XOR2>(L);
    fmrg = rdl(e * __builtin_amdgcn_rcpf(L), part);
  };
  f32x2 ga = {0.f, 0.f}, xin = {0.f, 0.f}, cc1 = {0.f, 0.f};
  float Af = 0.f, Bf = 0.f;
  const int hpos = ub * 512 + ud;
  auto up_prep = [&]() {
    if (!all_ok((sg[0][1] == tag) & (sg[0][3] == tag) & (sg[1][1] == tag) & (sg[1][3] == tag) & (sg[2][1] == tag) & (sg[2][3] == tag))) gather16<3>(wsr, G_MOD + (1024 + ud) * 8, 512 * 8, tag, sg);
    ga = f32x2{ub ? hi_f(sg[0][0]) : lo_f(sg[0][0]), ub ? hi_f(sg[0][2]) : lo_f(sg[0][2])};
    cc1 = f32x2{rbf(1.f + (ub ? hi_f(sg[2][0]) : lo_f(sg[2][0]))), rbf(1.f + (ub ? hi_f(sg[2][2]) : lo_f(sg[2][2])))};
    xin = f32x2{lo_f(sx2), hi_f(sx2)};
    u32* hp = reinterpret_cast<u32*>(&sm.hb[hpos]);
    hp[512] = pack_bf(cc1[0], cc1[1]);
    hp[1024] = ub ? perm_hi(sg[1][0], sg[1][2]) : perm_lo(sg[1][0], sg[1][2]);
  };
  auto up_frags = [&]() {
    const int pp = lane & 3;
    const u32 a = wimg + (pp & 1) * 32768 + ((pp >> 1) * 32 + g * 8 + ((lane >> 2) & 3)) * 64 + (((wave >> 1) ^ ((g & 1) | (pp & 2))) * 16) + (wave & 1) * 8;
#pragma unroll
    for (int rb = 0; rb < 8; ++rb) {
      const u32x2 lo = tr_read(a + rb * 4096), hi = tr_read(a + rb * 4096 + 256);
      bf[rb] = u32x4{lo[0], lo[1], hi[0], hi[1]};
    }
  };
  const u32x4* ap = reinterpret_cast<const u32x4*>(&sm.hb[((lane >> 3) & 1) * 512 + (lane & 1) * 32 + g * 8]);
  const bool par = lane & 8;
  float l0 = 0.f, l1 = 0.f;
  u32x2 gmd[2], tbg;
  const int qp = part != 1 ? -99 : ((blk & 7) == 3 ? QP1 : QP2);
  static_assert(QP1 + QD < QP2 && QP1 + QD > WUSE && QP1 + QD + 1 > TBU, "order of the q steps");
  int t1 = 0;
#pragma unroll
  for (int i = NCMIN / 8; i < NL; ++i) kr[i] = u32x4{0u, 0u, 0u, 0u};
  sfor<KF, NL>([&](auto ic) {
    constexpr int i = ic.v;
    if (i >= NCMIN / 8 && i >= nl) return false;
    const u32 so = __builtin_amdgcn_readlane(sot, i);
    if constexpr (i < NCMIN / 8) tau = i == sw ? tau2 : tau;
    gate();
    SB();
    kr[i] = ld16<AUX_NT>(rk, lane * 16, so);
    if constexpr (i == KF) small_ops();
    if constexpr (i == TBL) tbg = ld8<AUX_SC1>(wsr, wave == 7 ? G_TB + xcd * 128 + (lane & 7) * 8 : OOB, 0);
    if constexpr (i == QP1 || i == QP2) { gmd[0] = ld8<AUX_SC1>(wsr, i == qp ? G_MOD + tid * 8 : OOB, 0); gmd[1] = ld8<AUX_SC1>(wsr, i == qp ? G_MOD + tid * 8 : OOB, 4096); }
    SB();
    SB();
    if (i >= WK) { WAIT_VM(WK); SB(); }
    if (i == KF) STAMP(25);
    if (i == 8) STAMP(12);
    if (i == 20) STAMP(21);
    if (i == 25) STAMP(22);
    if (i == TBP) {
      if (part == 3 && i8 == 0 && wave == 7) { tag = next_tag(__builtin_amdgcn_readfirstlane(ep[0])); if (lane < 8) st8_sc1(wsr, G_TB + lane * 128 + xcd * 8, (u32)pt0 + (((tka - (u32)pt0) * P.GA) >> 3) - P.GS, tag); }
      SB();
    }
    if (i == TBU) {
      if (wave == 7) {
        const bool ok = tbg[1] == tag;
        float lf = ok ? (float)min(max((int)(t0 - tbg[0]), -300), 300) : 0.f, nf = ok ? 1.f : 0.f;
        lf += dpp<XOR1>(lf); nf += dpp<XOR1>(nf); lf += dpp<XOR2>(lf); nf += dpp<XOR2>(nf); lf += dpp<XOR7>(lf); nf += dpp<XOR7>(nf);
        if (lane == 0) sm.bc = u32x2{tag, t0 - (u32)(int)(lf * __builtin_amdgcn_rcpf(fmaxf(nf, 1.f)))};
      }
      LDS_BARRIER();
      const u32x2 bc = sm.bc;
      tag = __builtin_amdgcn_readfirstlane(bc[0]);
      t1 = (__builtin_amdgcn_readfirstlane((int)(bc[1] - t0)) + TEND + P.X1) * 256;
      const int dt = __builtin_amdgcn_readfirstlane((int)((float)(int)(t0 - bc[1]) * 256.f * __builtin_amdgcn_rcpf((float)(2 * nl - TBU - 2))));
      tau -= dt; tau2 -= dt;
      SB();
    }
    if (i == LNS) {
      tag = next_tag(__builtin_amdgcn_readfirstlane(ep[0]));
      if (part == 1) {
        float x0[8], x1[8], mb0, ri0, mb1, ri1;
#pragma unroll
        for (int k = 0; k < 4; ++k) { x0[2 * k] = lo_f(xr0[k]); x0[2 * k + 1] = hi_f(xr0[k]); x1[2 * k] = lo_f(xr1[k]); x1[2 * k + 1] = hi_f(xr1[k]); }
        ln_stats2(x0, x1, mb0, ri0, mb1, ri1);
        l0 = rbf(rbf(lo_f(xs0) - mb0) * ri0); l1 = rbf(rbf(lo_f(xs1) - mb1) * ri1);
      }
      PIN(l0); PIN(l1);
      SB();
    }
    if (i == WUSE) {
      PIN(tch1); PIN(tch2);
      if ((part == 1 || (part == 2 && has_w && !modc))) {
        WAIT_VM(WK); CFENCE();
        if (part == 2) mod_stage(); else wo_prep();
      }
#pragma unroll
      for (int k = 0; k < 8; ++k) PIN(bf[k]);
      SB();
    }
    if (i == QP1 + QD || i == QP2 + QD) {
      if (i == qp + QD) {
        u32 gm[2] = {gmd[0][0], gmd[1][0]};
        if (!all_ok((gmd[0][1] == tag) & (gmd[1][1] == tag))) { STAMP(11); gather8<2>(wsr, G_MOD + tid * 8, 512 * 8, tag, gm); }
        STAMP(5);
        gemv_go(pack_bf(rbf(l0 * rbf(1.f + lo_f(gm[1]))) + lo_f(gm[0]), rbf(l1 * rbf(1.f + hi_f(gm[1]))) + hi_f(gm[0])));
      }
      SB();
    }
    if (i == QP1 + QD + 1 || i == QP2 + QD + 1) {
      if (i == qp + QD + 1) {
        LDS_BARRIER();
        if (wave == 0) {
          const int bb = lane >> 5, jq = lane & 31;
          st8_sc1(wsr, G_Q + (bb * 2048 + blk * 32 + jq) * 8, pack_bf(sum8(&sm.r2[2 * jq * 20 + bb * 8]), sum8(&sm.r2[(2 * jq + 1) * 20 + bb * 8])), tag);
        }
        STAMP(6);
      }
      SB();
    }
    return true;
  });
  PIN(sot);
  const u32 qoff = G_Q + ((b * 32 + head) * 64 + c * 4) * 8;
  u32x4 qg[2], vr[NL], fl2[3];
  u32 qq[4];
  constexpr int NB = (NL + 15) / 16;
  float xlo, sc[NB], pr[NB], m = 0.f;
  f32x4 acc = {0.f, 0.f, 0.f, 0.f};
  u32 pb[NB], pm[4], cpf;
  const u32 adst0 = lds_addr(lane < 32 ? &sm.red[wave][c * 8 + g * 4] : &sm.r2[(lane - 32) * 4]);
  u32 po, rdst, rsrc, xo;
  auto wo_addr = [&]() {
    po = G_P + (((c >> 2 & 1) * 32 + xcd * 4 + (wave >> 1)) * 4 + (c & 3)) * 256 + (wave & 1) * 128 + (c >> 3) * 64 + g * 16;
    rdst = lane < 32 ? (u32)(uintptr_t)&sm.red2[wave][0] + lane * 16 : (u32)(uintptr_t)&sm.r2[0] + (lane - 32) * 16;
    rsrc = (u32)(uintptr_t)&sm.red2[0][((wave & 1) * 16 + (lane & 15)) * 4 + (lane >> 4)];
    xo = (lane & 1) ? OOB : G_XP + xcd * 2048 + (wave & 1) * 1024 + (blk * 64 + lane) * 2;
    PIN(po); PIN(rdst); PIN(rsrc); PIN(xo);
  };
  u32 pso = wave == 0 ? pslot + lane * 4 : OOB;
  PIN(pso);
  const u32 nadd = (te0 >> 20 & 7) ? P.LW : (is_up ? P.LU : 0);
  u32 nto = tid == 0 ? G_NT + bid * 8 : OOB;
  PIN(nto);
  auto notices = [&](auto nc_, u32 off, u32x4 (&gn)[nc_.v]) -> int {
    constexpr int N = nc_.v;
#pragma nounroll
    for (int spin = 0; spin < SPIN_MAX; ++spin) {
      bool ok = true;
#pragma unroll
      for (int i = 0; i < N; ++i) ok &= (gn[i][1] == tag) & (gn[i][3] == tag);
      if (all_ok(ok)) break;
      __builtin_amdgcn_s_sleep(10);
      CFENCE();
#pragma unroll
      for (int i = 0; i < N; ++i) gn[i] = ld16<AUX_SC1>(wsr, off, i * 1024);
    }
    int mx = 0;
#pragma unroll
    for (int i = 0; i < N; ++i) mx = max(mx, max((int)(gn[i][0] - t0), (int)(gn[i][2] - t0)));
    return __builtin_amdgcn_readfirstlane((int)wave_max((float)min(mx, 1 << 20)));
  };
  const int side = nl >= SIDE + 13 ? SIDE + 8 : (nl >= SIDE + 9 ? SIDE + 4 : SIDE);
  sfor<0, NL + WV>([&](auto ic) {
    constexpr int v = ic.v;
    if (cons_end(v - 1) >= NCMIN / 8 && cons_end(v - 1) >= nl) return false;
    if constexpr (v < NL) {
      const u32 so = __builtin_amdgcn_readlane(sot, v);
      if constexpr (v >= 12) { if (v == nl - P.NE) tau = (tau * (nadd ? P.FE2 : P.FE)) >> 4; }
      if constexpr (v >= NCMIN / 8) { if (v < nl) gate(); } else gate();
      SB();
      vr[v] = ld16<AUX_NT>(rv, lane * 16, so);
      SB();
      SB();
    }
    if constexpr (v == NCMIN / 8 - 1) {
#pragma unroll
      for (int i = NCMIN / 8; i < NL; ++i) asm volatile("" : "=v"(vr[i]));
      asm volatile("" : "=v"(fl2[1]), "=v"(fl2[2]));
    }
    if constexpr (v == VQ - 4) { qg[0] = ld16<AUX_SC1>(wsr, qoff, 0); qg[1] = ld16<AUX_SC1>(wsr, qoff + 16, 0); }
    if constexpr (v == VSIDE) side_loads();
    if constexpr (v == CPF) {
      u64 pc; u32 dlt, len;
      asm volatile("s_getpc_b64 %0\n.Lcpf_here:\n\ts_mov_b32 %1, .Ltail_start-.Lcpf_here\n\ts_mov_b32 %2, .Lend_a-.Ltail_start\n\ts_max_i32 %2, %2, .Lend_b-.Ltail_start\n\ts_max_i32 %2, %2, .Lend_c-.Ltail_start\n\ts_max_i32 %2, %2, .Lend_d-.Ltail_start\n\ts_max_i32 %2, %2, .Lend_e-.Ltail_start" : "=s"(pc), "=s"(dlt), "=s"(len) :: "scc");
      pc += dlt;
      const rsrc_t rc = mk_rsrc((const void*)pc, (part == 3 && i8 == 1) ? len : 0u);
      cpf = __builtin_amdgcn_raw_buffer_load_b32(rc, tid * 64, 0, 0);
    }
    if constexpr (v == CPF + WV + 1) PIN(cpf);
    if constexpr (v == SIDE || v == SIDE + 4 || v == SIDE + 8) fl2[(v - SIDE) / 4] = ld16<AUX_SC1>(wsr, (wave == 0 && v == side) ? mlo : OOB, 0);
    SB();
    if constexpr (v < WV) { WAIT_VM(WK); SB(); } else if constexpr (v < NL && cons_end(v) < v - WV + 1) { WAIT_VM(WV); SB(); }
    if constexpr (v == VQ) {
      if (!all_ok((qg[0][1] == tag) & (qg[0][3] == tag) & (qg[1][1] == tag) & (qg[1][3] == tag))) { STAMP(20); gather16<2>(wsr, qoff, 16, tag, qg); }
      STAMP(1); WSTAMP(1);
      qq[0] = qg[0][0]; qq[1] = qg[0][2]; qq[2] = qg[1][0]; qq[3] = qg[1][2];
    }
    if constexpr (v >= VQ && v < VS) {
      constexpr int sl = v - VQ, bt = sl >> 1;
      float xs[8];
#pragma unroll
      for (int i = 0; i < 8; ++i) {
        float d = 0.f;
        if (sl * 8 + i < NL) {
#pragma unroll
          for (int k = 0; k < 4; ++k) d = dot2(kr[sl * 8 + i][k], qq[k], d);
        }
        xs[i] = d;
      }
#pragma unroll
      for (int h = 0; h < 4; ++h) xs[h] = sel_add<XOR7>(xs[h], xs[h + 4], c & 4);
#pragma unroll
      for (int h = 0; h < 2; ++h) xs[h] = sel_add<XOR2>(xs[h], xs[h + 2], c & 2);
      xs[0] = sel_add<XOR1>(xs[0], xs[1], c & 1);
      if constexpr ((sl & 1) || sl == NSL - 1) {
        const float t = (sl & 1) ? sel_add<XOR8>(xlo, xs[0], c & 8) : xs[0] + dpp<XOR8>(xs[0]);
        const float s = rbf(rbf(t) * 0.08838834764831845f);
        sc[bt] = (bt * 16 + c < nl) ? s : -__builtin_inff();
        PIN(sc[bt]);
      } else {
        xlo = xs[0];
        PIN(xlo);
      }
    }
    if constexpr (v == VS) {
      float mloc = sc[0];
#pragma unroll
      for (int bt = 1; bt < NB; ++bt) mloc = fmaxf(mloc, sc[bt]);
      m = fmaxf(wave_max(mloc), -3.0e38f);
      float ls = 0.f;
#pragma unroll
      for (int bt = 0; bt < NB; ++bt) { pr[bt] = __expf(sc[bt] - m); ls += pr[bt]; }
      const float l = wave_sum(ls);
      if (lane == 0) { sm.ml[wave][0] = m; sm.ml[wave][1] = l; }
      STAMP(2); WSTAMP(2);
    }
    sfor<cons_end(v - 1), cons_end(v)>([&](auto jc) {
      constexpr int i = jc.v;
      if (i < NCMIN / 8 || i < nl) {
        const u32 pv = __float_as_uint(bcast16(u2f(pb[i >> 4]), i & 15));
        acc = mfma16(u32x4{pv & pm[0], pv & pm[1], pv & pm[2], pv & pm[3]}, vr[i], acc);
      }
      PIN(acc);
      return true;
    });
    if constexpr (v == MLS - 1) { side_pin(); if (is_up) up_prep(); PIN(ga); PIN(xin); PIN(cc1); }
    if constexpr (v == UPREP - 1) wo_addr();
    if constexpr (v == UPREP) {
#pragma unroll
      for (int k = 0; k < 8; ++k) asm volatile("" : "=v"(bf[k]));
      if (part == 0) wo_prep(0, 1); else if (is_up) up_frags();
#pragma unroll
      for (int k = 0; k < 8; ++k) PIN(bf[k]);
    }
    if constexpr (v == UPREP + 1) {
      if (is_up) {
        const u32x4* a1 = ap + 128 + ((lane >> 1) & 1) * 128;
        f32x4 d0 = {0.f, 0.f, 0.f, 0.f}, d1 = {0.f, 0.f, 0.f, 0.f};
#pragma unroll
        for (int rb = 0; rb < 4; ++rb) { d0 = mfma16(a1[rb * 8], bf[rb], d0); d1 = mfma16(a1[(rb + 4) * 8], bf[rb + 4], d1); }
        Af = par ? d0[1] + d1[1] : d0[0] + d1[0]; Bf = par ? d0[3] + d1[3] : d0[2] + d1[2];
        Af += dpp<XOR8>(Af); Bf += dpp<XOR8>(Bf);
      }
      PIN(Af); PIN(Bf);
    }
    if constexpr (v == SIDE + WV + 1 || v == SIDE + WV + 5 || v == SIDE + WV + 9) {
      if (wave == 0 && v == side + WV + 1) {
        const u32x4 ml = fl2[(v - SIDE - WV - 1) / 4];
        fok = all_ok((ml[1] == tag) & (ml[3] == tag));
        merge_factor(ml);
      }
    }
    if (v % 6 == 4 && v >= 10 && v < 30) STAMP(15 + v / 6);
    if constexpr (v == MLS) {
      LDS_BARRIER();
      float mm[8], ll[8];
#pragma unroll
      for (int w = 0; w < 8; ++w) { const f32x2 t = *reinterpret_cast<const f32x2*>(&sm.ml[w][0]); mm[w] = t[0]; ll[w] = t[1]; }
      float Mwg = mm[0], Lwg = 0.f;
#pragma unroll
      for (int w = 1; w < 8; ++w) Mwg = fmaxf(Mwg, mm[w]);
      if (wave == 0) {
#pragma unroll
        for (int w = 0; w < 8; ++w) Lwg = fmaf(__expf(mm[w] - Mwg), ll[w], Lwg);
        if (lane == 0) st16_sc1(wsr, G_ML + ((head * 2 + b) * 4 + part) * 16, __float_as_uint(Mwg), __float_as_uint(Lwg), tag);
      }
      const float fw = __expf(m - Mwg);
#pragma unroll
      for (int bt = 0; bt < NB; ++bt) { pb[bt] = pack_bf(pr[bt] * fw, pr[bt] * fw); PIN(pb[bt]); }
#pragma unroll
      for (int k = 0; k < 4; ++k) { pm[k] = (c >> 1) == k ? ((c & 1) ? 0xffff0000u : 0x0000ffffu) : 0u; PIN(pm[k]); }
    }
    SB();
    return true;
  });
  asm volatile(".Ltail_start:");
  *LDSP(f32x4, adst0) = acc;
  STAMP(14); WSTAMP(14);
  if (!fok) {
    u32x4 ml;
#pragma nounroll
    for (int spin = 0; spin < SPIN_MAX; ++spin) {
      CFENCE();
      ml = ld16<AUX_SC1>(wsr, mlo, 0);
      if (all_ok((ml[1] == tag) & (ml[3] == tag))) break;
    }
    merge_factor(ml);
  }
  STAMP(15);
  LDS_BARRIER();
  STAMP(3);
  {
    u32 pw = 0;
    if (wave == 0) {
      f32x2 t[8];
#pragma unroll
      for (int w = 0; w < 8; ++w) t[w] = *reinterpret_cast<const f32x2*>(&sm.red[w][2 * lane]);
      const f32x2 o = ((t[0] + t[1]) + (t[2] + t[3])) + ((t[4] + t[5]) + (t[6] + t[7]));
      pw = min(pack_bf(o[0] * fmrg, o[1] * fmrg), SENT - 1u);
    }
    __builtin_amdgcn_raw_buffer_store_b32(pw, wsr, pso, 0, AUX_SC1);
    st8_sc1(wsr, nto, (u32)wall_clock64() + nadd, tag);
    SB();
#pragma unroll
    for (int i = NCMIN / 8; i < NL; ++i) PIN(vr[i]);
    PIN(fl2[0]); PIN(fl2[1]); PIN(fl2[2]);
  }
  STAMP(4);

  if (part == 2) FINISH();
  if (part == 0) {
    __builtin_amdgcn_s_barrier();
#pragma nounroll
    while (wleft > 0) piece();
    until(t1);
    STAMP(11);
    CFENCE();
    u32x4 og[2];
    og[0] = ld16<AUX_SC1>(wsr, po, 0);
    SB();
    WAIT_VM(1); CFENCE();
    wo_prep(1, 2);
#pragma unroll
    for (int k = 4; k < 8; ++k) PIN(bf[k]);
    SB();
    f32x4 d[4];
    auto wo_mm = [&]() {
      const f32x4 z = {0.f, 0.f, 0.f, 0.f};
#pragma unroll
      for (int t = 0; t < 4; ++t) { d[t] = mfma16(og[0], bf[t], z); og[1][t] = __float_as_uint(dpp<XOR8>(u2f(og[0][t]))); }
      SB();
#pragma unroll
      for (int t = 0; t < 4; ++t) d[t] = mfma16(og[1], bf[4 + t], d[t]);
    };
    auto p_bad = [&]() { return !all_ok(max(max(og[0][0], og[0][1]), max(og[0][2], og[0][3])) != SENT); };
    wo_mm();
    if (__builtin_expect(p_bad(), 0)) {
#pragma nounroll
      for (int spin = 0; spin < SPIN_MAX; ++spin) {
        __builtin_amdgcn_s_sleep(1);
        CFENCE();
        og[0] = ld16<AUX_SC1>(wsr, po, 0);
        if (!p_bad()) break;
      }
      wo_mm();
      SB();
      asm volatile(".Lend_d:");
    }
    STAMP(5); WSTAMP(5);
    {
      SB();
      f32x4 y; f32x2 h[4];
#pragma unroll
      for (int t = 0; t < 4; ++t) { h[t] = d[t].xy + d[t].zw; PIN(h[t]); }
#pragma unroll
      for (int t = 0; t < 4; ++t) { y[t] = h[t][0] + h[t][1]; PIN(y[t]); }
      *LDSP(f32x4, rdst) = y;
    }
    STAMP(16);
    LDS_BARRIER();
    STAMP(17);
    if (wave < 2) {
      f32x2 v[4];
#pragma unroll
      for (int w = 0; w < 4; ++w) v[w] = f32x2{LDSP(float, rsrc)[w * 256], LDSP(float, rsrc)[w * 256 + 128]};
      const f32x2 h = (v[0] + v[1]) + (v[2] + v[3]);
      const float y = h[0] + h[1];
      __builtin_amdgcn_raw_buffer_store_b32(min(pack_bf(y, dpp<XOR1>(y)), SENT - 1u), wsr, xo, 0, AUX_SC1);
      asm volatile(".Lend_a:");
    }
    STAMP(6);
    FINISH();
  }
  if (part == 3) {
    if (!has_w) FINISH();
    u32x4 b2[4];
    {
      __builtin_amdgcn_s_barrier();
      const rsrc_t r2 = mk_rsrc(w2 + (size_t)(blk * 32 + wave * 4) * 512, 4096u);
      const u32 l2 = (u32)(uintptr_t)(&sm.w2b[wave * 4][0]);
#pragma unroll
      for (int n = 0; n < 4; ++n) dma16(r2, (lane ^ (n * 2) ^ ((wave & 2) << 2)) * 16, n * 1024, l2 + n * 1024);
    }
    const u32 tdst = lds_addr(&sm.hb[hpos]);
    const u32 sdst = lds_addr(&sm.stat[wave][lane]);
    const u32 ssrc = lds_addr(&sm.stat[(g >> 1) * 4 + ((lane >> 1) & 3)][(lane & 1) * 16]);
    const u32 adst = lds_addr((c < 4 && (g & 1) == 0) ? &sm.actl[g >> 1][wave * 4 + c] : reinterpret_cast<u16*>(&sm.dummy[lane]));
    const u32 asrc = lds_addr(&sm.actl[lane & 1][g * 8]);
    const u32 apa = lds_addr(&sm.hb[(c >> 3) * 512 + (c & 3) * 64 + ((c >> 2) & 1) * 32 + g * 8]);
    u32 xoff = G_XP + g * 2048 + ub * 1024 + (wave & 3) * 256 + c * 16;
    u32x4 one;
#pragma unroll
    for (int k = 0; k < 4; ++k) one[k] = ((c & 2) == 0 && (c >> 2) == k) ? ((c & 1) ? 0x3f800000u : 0x00003f80u) : 0u;
    PIN(one);
    u32 ooff = G_OP + (blk * 512 + tid) * 8;
    float Afp = Af, Bfp = Bf;
    PIN(xoff); PIN(ooff); PIN(Afp); PIN(Bfp);
    PIN(ga); PIN(xin); PIN(cc1);
    u32 w2a = (u32)(uintptr_t)(&sm.w2b[0][0]) + (g * 8 + ((lane >> 2) & 3)) * 1024 + (wave ^ (g & 1)) * 128 + (lane & 15) * 8;
    PIN(w2a);
    if (wave == 1) {
      due = t1 + P.N1 * 256; gate();
      u32x4 gn[2] = {ld16<AUX_SC1>(wsr, G_NT + lane * 16, 0), ld16<AUX_SC1>(wsr, G_NT + lane * 16, 1024)};
      const int tn = notices(IC<2>{}, G_NT + lane * 16, gn) * 256;
      due = (tn > t1 + 2 * 256 ? max(tn, t1 + P.L0 * 256) + P.L1 * 256 : t1) + P.X2 * 256;
      until(due);
    }
    asm volatile("s_waitcnt vmcnt(0)" ::: "memory");
    STAMP(9);
    __builtin_amdgcn_s_barrier();
    STAMP(11);
    CFENCE();
    u32x4 xg[2] = {ld16<AUX_SC1>(wsr, xoff, 0), ld16<AUX_SC1>(wsr, xoff, 8192)};
    SB();
#pragma unroll
    for (int t = 0; t < 4; ++t) {
      const u32x2 lo = tr_read(w2a ^ (t * 32)), hi = tr_read((w2a ^ (t * 32)) + 4096);
      b2[t] = u32x4{lo[0], lo[1], hi[0], hi[1]};
    }
#pragma unroll
    for (int t = 0; t < 4; ++t) PIN(b2[t]);
    SB();
    const f32x4 z4 = {0.f, 0.f, 0.f, 0.f};
    auto xp_bad = [&]() { return !all_ok(max(max(max(xg[0][0], xg[0][1]), max(xg[0][2], xg[0][3])), max(max(xg[1][0], xg[1][1]), max(xg[1][2], xg[1][3]))) != SENT); };
    f32x4 v = mfma16(one, xg[1], mfma16(one, xg[0], z4));
    if (__builtin_expect(xp_bad(), 0)) {
#pragma nounroll
      for (int spin = 0; spin < SPIN_MAX; ++spin) {
        __builtin_amdgcn_s_sleep(1);
        CFENCE();
        xg[0] = ld16<AUX_SC1>(wsr, xoff, 0); xg[1] = ld16<AUX_SC1>(wsr, xoff, 8192);
        if (!xp_bad()) break;
      }
      v = mfma16(one, xg[1], mfma16(one, xg[0], z4));
      SB();
      asm volatile(".Lend_e:");
    }
    STAMP(5); WSTAMP(5);
    {
      const float a0 = v[0], a1 = v[1];
      const float x10 = fmaf(ga[0], a0, xin[0]), x11 = fmaf(ga[1], a1, xin[1]);
      *LDSP(u32, tdst) = pack_bf(x10 * cc1[0], x11 * cc1[1]);
      float u = swap32_add(x10 + x11, fmaf(x10, x10, x11 * x11));
      u += dpp<XOR1>(u); u += dpp<XOR2>(u); u += dpp<XOR7>(u); u += dpp<XOR8>(u);
      *LDSP(float, sdst) = u;
    }
    STAMP(16);
    LDS_BARRIER();
    STAMP(17);
    {
      const f32x4 z = {0.f, 0.f, 0.f, 0.f};
      const u32x4 af0 = LDSP(u32x4, apa)[0], af1 = LDSP(u32x4, apa)[32];
      float s = LDSP(float, ssrc)[0], q = LDSP(float, ssrc)[32];
      SB();
      f32x4 d[4];
#pragma unroll
      for (int j = 0; j < 4; ++j) d[j] = mfma16(af0, bf[j], z);
#pragma unroll
      for (int j = 0; j < 4; ++j) d[j] = mfma16(af1, bf[4 + j], d[j]);
      s += dpp<XOR1>(s); q += dpp<XOR1>(q); s += dpp<XOR2>(s); q += dpp<XOR2>(q); s += dpp<XOR7>(s); q += dpp<XOR7>(q);
      const float mean = s * (1.f / 512.f);
      const float var = fmaf(q, 1.f / 512.f, -mean * mean);
      const float ri = __builtin_amdgcn_rsqf(fmaxf(var, 0.f) + 1e-5f);
      const float cf = fmaf(-ri * mean, Afp, Bfp);
      float y = (d[0][0] + d[1][1]) + (d[2][2] + d[3][3]);
      y = __builtin_bit_cast(float, __builtin_amdgcn_update_dpp(__builtin_bit_cast(int, y), __builtin_bit_cast(int, y), XOR8, 0xa, 0xf, false));
      y = swap16_add(y, y);
      y = fmaf(y, ri, cf);
      const float up = dpp<0x104>(y);
      *LDSP(u16, adst) = (u16)pack_bf(silu_f(y) * up, 0.f);
    }
    STAMP(18);
    LDS_BARRIER();
    STAMP(10);
    {
      const u32x4 af = *LDSP(u32x4, asrc);
      const f32x4 z = {0.f, 0.f, 0.f, 0.f};
      const f32x4 d0 = mfma16(af, b2[0], z), d1 = mfma16(af, b2[1], z), d2 = mfma16(af, b2[2], z), d3 = mfma16(af, b2[3], z);
      const bool g1 = g & 1, g2 = g & 2;
      const float e0 = g1 ? d1[0] : d0[0], e1 = g1 ? d1[1] : d0[1], f0 = g1 ? d3[0] : d2[0], f1 = g1 ? d3[1] : d2[1];
      st8_sc1(wsr, ooff, pack_bf(g2 ? f0 : e0, g2 ? f1 : e1), tag);
    }
    asm volatile(".Lend_b:");
    STAMP(6);
    FINISH();
  }
  if (wave == 0) {
    const int d = fd, ul = lane >> 3;
    u32 gm[2] = {sg[0][0], sg[1][0]};
    if (!all_ok((sg[0][1] == tag) & (sg[1][1] == tag))) gather8<2>(wsr, G_MOD + (1024 + d) * 8, 1536 * 8, tag, gm);
    u32 xa0, xa1;
    {
      due = t1 + P.N2 * 256; gate();
      u32x4 gn[2] = {ld16<AUX_SC1>(wsr, G_NT + lane * 16, 0), ld16<AUX_SC1>(wsr, G_NT + lane * 16, 1024)};
      const int tn = notices(IC<2>{}, G_NT + lane * 16, gn) * 256;
      due = (tn > t1 + 2 * 256 ? max(tn, t1 + P.L0 * 256) + P.L1 * 256 : t1) + P.X3 * 256;
      until(due);
    }
#pragma nounroll
    for (int spin = 0; spin < SPIN_MAX; ++spin) {
      CFENCE();
      xa0 = __builtin_amdgcn_raw_buffer_load_b32(wsr, G_XP + ul * 2048 + (d >> 1) * 4, 0, AUX_SC1);
      xa1 = __builtin_amdgcn_raw_buffer_load_b32(wsr, G_XP + ul * 2048 + (d >> 1) * 4, 1024, AUX_SC1);
      if (all_ok(max(xa0, xa1) != SENT)) break;
      __builtin_amdgcn_s_sleep(1);
    }
    float a0 = (d & 1) ? hi_f(xa0) : lo_f(xa0), a1 = (d & 1) ? hi_f(xa1) : lo_f(xa1);
    a0 += dpp<XOR8>(a0); a1 += dpp<XOR8>(a1);
    a0 = swap16_add(a0, a0); a1 = swap16_add(a1, a1);
    a0 = swap32_add(a0, a0); a1 = swap32_add(a1, a1);
    const bool hb = g & 1;
    float xv = rbf(((d & 1) ? hi_f(sx2) : lo_f(sx2)) + rbf((hb ? hi_f(gm[0]) : lo_f(gm[0])) * rbf(hb ? a1 : a0)));
    float gv = hb ? hi_f(gm[1]) : lo_f(gm[1]);
    PIN(xv); PIN(gv);
    if (wi == 0 && lane == 0) __builtin_amdgcn_raw_buffer_store_b32(tag, wsr, WS_EPOCH, 0, AUX_SC1);
    STAMP(10);
    due = max(due + P.X4 * 256, (int)(((u32)wall_clock64() - t0) << 8) + P.GX * 256);
    const bool last = ul + 40 < NUP;
    u32 o0 = G_OP + (ul * 512 + d) * 8, o5 = last ? o0 : OOB, tg5 = last ? tag : 0u;
    const rsrc_t rout = mk_rsrc(out, 2048u);
    u32 oo = (lane & 40) == 0 ? (g * 512 + d) * 2 : OOB;
    const u32 pk = c == 0 ? 0x00003f80u : (c == 4 ? 0x3f800000u : 0u);
    u32x4 one = {pk, 0u, pk, 0u};
    PIN(o0); PIN(o5); PIN(tg5); PIN(oo); PIN(one);
    __builtin_amdgcn_raw_buffer_store_b16((u16)pack_bf(xv, 0.f), rout, oo, 0, AUX_SC1);
    until(due);
    f32x4 sv;
#pragma nounroll
    for (int spin = 0; spin < SPIN_MAX; ++spin) {
      CFENCE();
      u32x2 og[6];
#pragma unroll
      for (int k = 0; k < 5; ++k) og[k] = ld8<AUX_SC1>(wsr, o0, k * 32768);
      og[5] = ld8<AUX_SC1>(wsr, o5, 5 * 32768);
      sv = f32x4{0.f, 0.f, 0.f, 0.f};
#pragma unroll
      for (int k = 0; k < 3; ++k) sv = mfma16(one, u32x4{og[2 * k][0], og[2 * k][1], og[2 * k + 1][0], og[2 * k + 1][1]}, sv);
      const u32 bad = ((og[0][1] ^ tag) | (og[1][1] ^ tag) | (og[2][1] ^ tag)) | ((og[3][1] ^ tag) | (og[4][1] ^ tag) | (og[5][1] ^ tg5));
      PIN(sv);
      if (all_ok(bad == 0u)) break;
      __builtin_amdgcn_s_sleep(1);
    }
    STAMP(7);
    const float s = sv[0] + dpp<XOR8>(sv[0]);
    __builtin_amdgcn_raw_buffer_store_b16((u16)pack_bf(fmaf(gv, s, xv), 0.f), rout, oo, 0, AUX_SC1);
    asm volatile(".Lend_c:");
  }
  FINISH();
}

extern "C" void kernel_launch(void* const* d_in, const int* in_sizes, int n_in,
                              void* d_out, int out_size, void* d_ws, size_t ws_size,
                              hipStream_t stream) {
  fused_kernel<<<256, 512, 0, stream>>>((const u16*)d_in[0], (const u16*)d_in[1], (const u16*)d_in[2], (const u16*)d_in[3], (const u16*)d_in[4],
                                        (const u16*)d_in[6], (const u16*)d_in[7], (const u16*)d_in[8], (const u16*)d_in[9], (const u16*)d_in[10],
                                        (unsigned char*)d_ws, (u16*)d_out
    );
}
```

```cpp
#include <hip/hip_runtime.h>
#include <stdint.h>

typedef unsigned int u32;
typedef unsigned short u16;
typedef unsigned long long u64;
typedef __attribute__((ext_vector_type(4))) unsigned u32x4;
typedef __attribute__((ext_vector_type(2))) unsigned u32x2;
typedef __attribute__((ext_vector_type(2))) __bf16 bf16x2;
typedef __attribute__((ext_vector_type(4))) float f32x4;
typedef __attribute__((ext_vector_type(2))) float f32x2;
typedef __amdgpu_buffer_rsrc_t rsrc_t;

constexpr int NH = 32, SK = 16384, DH = 128, WIN = 4096, FF = 1376;
struct Prm { int E0, ES, E1, E2, E2C, EU, TEND, DT_UP, L0, L1, X1, X2, X3, X4, NE, FE, M0, M1, M2, M3, M4, GS, GX, N1, N2, GA, WD, DT_WO, LW, LU, EW, PM, D0, FK, FW, FU, FN, F0, DS, FE2, TS, WM, PW; };
constexpr Prm PRM0 = {32, 24, 32, 32, 48, 64, 2015, 66, 30, 25, 71, 85, 115, 96, 5, 12, 12, 5, 12, 32, 28, 30, 56, 24, 58, 8, 4, 100, 45, 15, 8, 0x6827941, 30, 62, 61, 60, 64, 64, 116, 16, 500, 0x53, 0};
#define PRM_ARG
#define TABP (&TAB.e[0])
constexpr int NL = 37;
constexpr int NCMIN = 192;
constexpr int WK = 6, WV = 5;
constexpr int WUSE = 13, LNS = 11;
#define QPA 12
#define QPB 18
#define QDD 5
constexpr int QP1 = QPA, QP2 = QPB, QD = QDD;
constexpr int VQ = 6;
#define UPS 7
#define MLD 2
constexpr int NSL = (NL + UPS - 2) / UPS;
constexpr int VS = VQ + NSL;
constexpr int MLS = VS + MLD;
constexpr int VSIDE = VQ + 1, UPREP = 21;
constexpr int SIDE = NCMIN / 8 - 4;
constexpr int CPF = 18;
constexpr int TBP = 7, TBL = 11, TBU = 16;
constexpr int KF = 4, KF0 = 8;
constexpr int side_k(int i) { return (i == KF || i == KF + 1) ? 3 : (i == TBL ? 1 : 0) + ((i == QP1 || i == QP2) ? 2 : 0); }
constexpr int side_v(int v) { return v == VQ - 4 ? 2 : (v == VSIDE ? 4 : 0); }
constexpr int win_k(int i) { int n = WK; for (int j = i - WK; j <= i; ++j) n += side_k(j); return n; }
constexpr int win_v(int v, int w) { int n = w; for (int j = v - w; j <= v; ++j) n += side_v(j); return n; }
constexpr int cons_end(int v) {
  const int a = v - WV + 1, c = (v - MLS) * 4;
  const int m = a < c ? a : c;
  return m < 0 ? 0 : (m > NL ? NL : m);
}
struct Tab { u32 e[1024]; };
#define popc __builtin_popcount
__host__ __device__ constexpr u32 place(u32 v, u32 m, int n) {
  u32 r = 0;
  for (int k = 0; k < n; ++k) r |= (v >> k & 1u) << (10 + (m >> (4 * k) & 15));
  return r;
}
__host__ __device__ constexpr u32 step_off(const Prm& P, u32 s0, u32 bh) {
  const u32 src = (s0 >> 5 & 3u) | bh << 2;
  return place(s0 ^ ((popc(src & P.M0) & 1) | (popc(src & P.M1) & 1) << 1 | (popc(src & P.M2) & 1) << 2 | (popc(src & P.M3) & 1) << 3 | (popc(src & P.M4) & 1) << 4), P.PM, 7);
}
constexpr Tab make_tab(const Prm& P) {
  Tab T{};
  for (int bid = 0; bid < 256; ++bid) {
    const int xcd = bid & 7, idx = bid >> 3, part = idx & 3, i8 = idx >> 2, wi = i8 * 8 + xcd;
    const bool has_mod = wi < 48, has_up = wi >= 21;
    const int cb0 = (i8 + 5) & 7, cb1 = (i8 + 4) & 7, ms = wi - 20;
    const int mi = wi < 8 ? wi : (wi < 16 ? 8 + ((xcd + 4) & 7) : (wi < 20 ? (wi - 14) * 8 + 3 : 16 + ms / 7 * 8 + ms % 7 + (ms % 7 >= 3 ? 1 : 0)));
    const int ui = xcd * 5 + (xcd > 5 ? xcd - 5 : 0) + (i8 >= 3 ? (xcd >= 5 ? 1 : 0) + i8 - 3 : 0);
    const int wd = cb0 == 3 ? 0 : P.WD;
    const int e[4] = {P.E0 - 4 * wd + (cb0 == 3 ? P.ES : 0), P.E1 + (cb1 == 3 ? P.ES : 0), has_mod ? (wi < 16 ? P.E2C : P.E2) + ((mi & 7) == 3 ? P.ES : 0) : 0, has_up ? P.EU : 0};
    const int ew = wd ? P.EW : 0, pa = (1024 + e[0] + ew + e[1] + e[2] + e[3]) / 4;
    int c[5] = {0, 0, 0, 0, 128};
    for (int k = 0; k < 3; ++k) c[k + 1] = c[k] + (pa - e[k] - (k == 0 ? ew : 0) + 4) / 8;
    const int nl = c[part + 1] - c[part], wst = e[part] / 4;
    const bool has_w = part < 2 || (part == 2 ? has_mod : has_up);
    const int blk = !has_w ? 0 : (part == 0 ? cb0 : (part == 1 ? xcd * 8 + cb1 : (part == 2 ? mi : ui)));
    const int span = (part == 3 && has_up ? P.TEND - P.DT_UP : (part == 0 && wd ? P.TEND - P.DT_WO : P.TEND)) * 256;
    const int np = part == 0 ? 8 - wd : 8, tau0 = span / (2 * nl + wst);
    const bool up = part == 3 && has_up;
    const int tau = tau0 * (up ? P.FU : (part == 0 && wd ? P.F0 : (has_w ? P.FK : P.FN))) / 64, tauw = tau0 * wst / np * (up ? P.FU : P.FW) / 64;
    int sw = KF, at = P.DS * 256 + (has_w ? np * tauw : 0);
    while (sw < NCMIN / 8 - 1 && at < P.TS * 256) { at += tau; ++sw; }
    const int tau2 = (span - at) / (2 * nl - sw);
    T.e[4 * bid] = (u32)(c[part] | nl << 7 | blk << 13 | (has_w ? 1 : 0) << 19 | (part == 0 ? wd : 0) << 20 | sw << 24);
    T.e[4 * bid + 1] = (u32)(tau | tauw << 16);
    if ((part == 1 && nl <= QP2 + QD + 1) || c[part] > 32 * part + KF0 || c[part + 1] < 32 * part + KF0 + 4) T.e[4 * bid] = 0;
    const u32 pitch = part == 0 ? 1024u : (part == 1 ? 8192u : (part == 2 ? 6144u : 2752u));
    T.e[4 * bid + 2] = (part == 0 ? xcd * 512u * pitch : 0u) + blk * (part == 3 ? 64u : 128u);
    T.e[4 * bid + 3] = pitch | (u32)tau2 << 16;
  }
  return T;
}
constexpr int step_range(const Prm& P, bool mx_) {
  int mx = 0, mn = 1 << 20;
  const Tab T = make_tab(P);
  for (int bid = 0; bid < 256; ++bid) { const int n = T.e[4 * bid] >> 7 & 63; mx = n > mx ? n : mx; mn = n < mn ? n : mn; }
  return mx_ ? mx : mn;
}
__constant__ __attribute__((aligned(64))) const Tab TAB = make_tab(PRM0);
static_assert(step_range(PRM0, true) <= NL && step_range(PRM0, false) >= NCMIN / 8, "a wave's key range does not fit its load steps");
static_assert(cons_end(SIDE + WV + 1) <= NCMIN / 8 && cons_end(UPREP) <= NCMIN / 8 && SIDE > MLS + 3, "steps every wave runs");
constexpr int NUP = FF / 32;
constexpr int SPIN_MAX = 1 << 17;

constexpr u32 WS_EPOCH = 0;
constexpr u32 G_TB = 1024;
constexpr u32 G_NT = 2048;
constexpr u32 G_MOD = 4096;
constexpr u32 G_Q = G_MOD + 3072 * 8;
constexpr u32 G_ML = G_Q + 4096 * 8;
constexpr u32 G_P = G_ML + 256 * 16;
constexpr u32 G_XP = G_P + 256 * 256;
constexpr u32 G_OP = G_XP + 8 * 512 * 4;
constexpr u32 WS_END = G_OP + 43 * 512 * 8;
constexpr u32 SENT = 0xffffffffu;
constexpr u32 OOB = 0x40000000u;
#define WST 0

constexpr int AUX_NT = 2, AUX_SC1 = 16;

__device__ __forceinline__ float lo_f(u32 v) { return __uint_as_float(v << 16); }
__device__ __forceinline__ float hi_f(u32 v) { return __uint_as_float(v & 0xffff0000u); }
__device__ __forceinline__ float rbf(float x) { return (float)(__bf16)x; }
__device__ __forceinline__ u32 pack_bf(float lo, float hi) {
  bf16x2 r; r[0] = (__bf16)lo; r[1] = (__bf16)hi; return __builtin_bit_cast(u32, r);
}
__device__ __forceinline__ float dot2(u32 k, u32 q, float acc) {
  return __builtin_amdgcn_fdot2_f32_bf16(__builtin_bit_cast(bf16x2, k), __builtin_bit_cast(bf16x2, q), acc, false);
}
template <int CTRL> __device__ __forceinline__ float dpp(float x) {
  return __builtin_bit_cast(float, __builtin_amdgcn_mov_dpp(__builtin_bit_cast(int, x), CTRL, 0xf, 0xf, true));
}
constexpr int XOR1 = 0xB1, XOR2 = 0x4E, XOR7 = 0x141, XOR8 = 0x128;
__device__ __forceinline__ float swap32_add(float a, float b) {
  auto t = __builtin_amdgcn_permlane32_swap(__float_as_uint(a), __float_as_uint(b), false, false);
  return __uint_as_float(t[0]) + __uint_as_float(t[1]);
}
__device__ __forceinline__ float swap16_add(float a, float b) {
  auto t = __builtin_amdgcn_permlane16_swap(__float_as_uint(a), __float_as_uint(b), false, false);
  return __uint_as_float(t[0]) + __uint_as_float(t[1]);
}
__device__ __forceinline__ float wave_sum(float x) {
  x += dpp<XOR1>(x); x += dpp<XOR2>(x); x += dpp<XOR7>(x); x += dpp<XOR8>(x);
  x = swap16_add(x, x);
  return swap32_add(x, x);
}
__device__ __forceinline__ float wave_max(float x) {
  x = fmaxf(x, dpp<XOR1>(x)); x = fmaxf(x, dpp<XOR2>(x)); x = fmaxf(x, dpp<XOR7>(x)); x = fmaxf(x, dpp<XOR8>(x));
  auto s = __builtin_amdgcn_permlane16_swap(__float_as_uint(x), __float_as_uint(x), false, false);
  x = fmaxf(__uint_as_float(s[0]), __uint_as_float(s[1]));
  auto t = __builtin_amdgcn_permlane32_swap(__float_as_uint(x), __float_as_uint(x), false, false);
  return fmaxf(__uint_as_float(t[0]), __uint_as_float(t[1]));
}
template <int CTRL> __device__ __forceinline__ float sel_add(float lo, float hi, bool up) {
  const float t1 = lo + dpp<CTRL>(lo), t2 = hi + dpp<CTRL>(hi);
  return up ? t2 : t1;
}
template <int I> __device__ __forceinline__ float bc16(float x) {
  return __builtin_bit_cast(float, __builtin_amdgcn_update_dpp(0, __builtin_bit_cast(int, x), 0x150 + I, 0xf, 0xf, false));
}
__device__ __forceinline__ float bcast16(float x, int i) {
  switch (i) {
    case 0: return bc16<0>(x); case 1: return bc16<1>(x); case 2: return bc16<2>(x); case 3: return bc16<3>(x);
    case 4: return bc16<4>(x); case 5: return bc16<5>(x); case 6: return bc16<6>(x); case 7: return bc16<7>(x);
    case 8: return bc16<8>(x); case 9: return bc16<9>(x); case 10: return bc16<10>(x); case 11: return bc16<11>(x);
    case 12: return bc16<12>(x); case 13: return bc16<13>(x); case 14: return bc16<14>(x); default: return bc16<15>(x);
  }
}
__device__ __forceinline__ float silu_f(float x) { return x * __builtin_amdgcn_rcpf(1.f + __expf(-x)); }

template <int AUX> __device__ __forceinline__ u32x4 ld16(rsrc_t r, u32 voff, u32 soff) {
  return __builtin_amdgcn_raw_buffer_load_b128(r, voff, soff, AUX);
}
template <int AUX> __device__ __forceinline__ u32x2 ld8(rsrc_t r, u32 voff, u32 soff) {
  return __builtin_amdgcn_raw_buffer_load_b64(r, voff, soff, AUX);
}
__device__ __forceinline__ void st8_sc1(rsrc_t r, u32 voff, u32 v, u32 tag) {
  __builtin_amdgcn_raw_buffer_store_b64(u32x2{v, tag}, r, voff, 0, AUX_SC1);
}
__device__ __forceinline__ void st16_sc1(rsrc_t r, u32 voff, u32 v0, u32 v1, u32 tag) {
  __builtin_amdgcn_raw_buffer_store_b128(u32x4{v0, tag, v1, tag}, r, voff, 0, AUX_SC1);
}
__device__ __forceinline__ rsrc_t mk_rsrc(const void* p, u32 bytes) {
  return __builtin_amdgcn_make_buffer_rsrc(const_cast<void*>(p), 0, bytes, 0x00020000);
}
#define WAIT_VM(n) __builtin_amdgcn_s_waitcnt(((n) & 15) | (((n) >> 4) << 14) | (7 << 4) | (15 << 8))
#define SB() __builtin_amdgcn_sched_barrier(0)
#define CFENCE() asm volatile("" ::: "memory")
__device__ __forceinline__ bool all_ok(bool ok) { return __builtin_amdgcn_ballot_w64(!ok) == 0ull; }

template <int N> __device__ __forceinline__ void gather16(rsrc_t ws, u32 voff, u32 stride, u32 tag, u32x4 (&g)[N]) {
#pragma nounroll
  for (int spin = 0; spin < SPIN_MAX; ++spin) {
    CFENCE();
    bool ok = true;
#pragma unroll
    for (int i = 0; i < N; ++i) g[i] = ld16<AUX_SC1>(ws, voff + i * stride, 0);
#pragma unroll
    for (int i = 0; i < N; ++i) ok &= (g[i][1] == tag) & (g[i][3] == tag);
    if (all_ok(ok)) break;
    __builtin_amdgcn_s_sleep(8);
  }
}
template <int N> __device__ __forceinline__ void gather8(rsrc_t ws, u32 voff, u32 stride, u32 tag, u32 (&g)[N], bool act = true) {
#pragma nounroll
  for (int spin = 0; spin < SPIN_MAX; ++spin) {
    CFENCE();
    bool ok = true;
    u32x2 t[N];
#pragma unroll
    for (int i = 0; i < N; ++i) t[i] = ld8<AUX_SC1>(ws, voff + i * stride, 0);
#pragma unroll
    for (int i = 0; i < N; ++i) { ok &= (t[i][1] == tag); g[i] = t[i][0]; }
    if (all_ok(ok | !act)) break;
    __builtin_amdgcn_s_sleep(8);
  }
}

struct Smem {
  float red[8][128];
  float ml[8][2];
  float r2[64 * 20];
  u16 hb[3 * 1024];
  float stat[8][64];
  float red2[8][128];
  u32 dummy[64];
  u32x2 bc;
  u32x4 zero[4];
  u32x4 wbuf[8][8][64];
  __attribute__((aligned(256))) u16 w2b[32][512];
  u16 actl[2][32];
};

#define STAMP(k) do {} while (0)
#define WSTAMP(k) do {} while (0)
#define FINISH() return
#define PIN(x) asm volatile("" : "+v"(x))
#define LDS_BARRIER() do { __builtin_amdgcn_s_waitcnt(0xc07f); __builtin_amdgcn_s_barrier(); CFENCE(); } while (0)
#define WAVE_SYNC() do { __builtin_amdgcn_fence(__ATOMIC_RELEASE, "wavefront"); __builtin_amdgcn_wave_barrier(); __builtin_amdgcn_fence(__ATOMIC_ACQUIRE, "wavefront"); } while (0)

__device__ __forceinline__ void dma16(rsrc_t r, u32 voff, u32 soff, u32 lds_dst) {
  u32 keep;
  asm volatile("s_mov_b32 %0, m0\n\ts_mov_b32 m0, %4\n\ts_nop 4\n\tbuffer_load_dwordx4 %1, %2, %3 offen nt lds\n\ts_mov_b32 m0, %0"
               : "=&s"(keep) : "v"(voff), "s"(r), "s"(soff), "s"(lds_dst) : "memory");
}
__device__ __forceinline__ u32 perm_lo(u32 a, u32 b) { return __builtin_amdgcn_perm(b, a, 0x05040100u); }
__device__ __forceinline__ u32 perm_hi(u32 a, u32 b) { return __builtin_amdgcn_perm(b, a, 0x07060302u); }
__device__ __forceinline__ float rdl(float x, int l) { return __int_as_float(__builtin_amdgcn_readlane(__float_as_int(x), l)); }

typedef __attribute__((ext_vector_type(8))) __bf16 bf16x8;
__device__ __forceinline__ f32x4 mfma16(u32x4 a, u32x4 b, f32x4 c) {
  return __builtin_amdgcn_mfma_f32_16x16x32_bf16(__builtin_bit_cast(bf16x8, a), __builtin_bit_cast(bf16x8, b), c, 0, 0, 0);
}
__device__ __forceinline__ float sum8(const float* p) {
  const f32x4 a = *reinterpret_cast<const f32x4*>(p), b = *reinterpret_cast<const f32x4*>(p + 4);
  return ((a[0] + b[0]) + (a[1] + b[1])) + ((a[2] + b[2]) + (a[3] + b[3]));
}
__device__ __forceinline__ float ln_ri(float var) {
  return __builtin_amdgcn_rcpf(rbf(__builtin_amdgcn_sqrtf(rbf(rbf(fmaxf(var, 0.f)) + 1e-5f))));
}
__device__ __forceinline__ void ln_stats2(const float (&x0)[8], const float (&x1)[8], float& mb0, float& ri0, float& mb1, float& ri1) {
  const float k0 = rdl(x0[0], 0), k1 = rdl(x1[0], 0);
  float s0 = 0.f, s1 = 0.f, q0 = 0.f, q1 = 0.f;
#pragma unroll
  for (int k = 0; k < 8; ++k) {
    const float d0 = x0[k] - k0, d1 = x1[k] - k1;
    s0 += d0; s1 += d1; q0 = fmaf(d0, d0, q0); q1 = fmaf(d1, d1, q1);
  }
  float t = swap16_add(swap32_add(s0, s1), swap32_add(q0, q1));
  t += dpp<XOR1>(t); t += dpp<XOR2>(t); t += dpp<XOR7>(t); t += dpp<XOR8>(t);
  auto u = __builtin_amdgcn_permlane16_swap(__float_as_uint(t), __float_as_uint(t), false, false);
  const float ms = __uint_as_float(u[0]) * (1.f / 512.f), mq = __uint_as_float(u[1]) * (1.f / 512.f);
  const float ri = ln_ri(fmaf(-ms, ms, mq));
  mb0 = rbf(k0 + rdl(ms, 0)); mb1 = rbf(k1 + rdl(ms, 32));
  ri0 = rdl(ri, 0); ri1 = rdl(ri, 32);
}
template <int I> struct IC { static constexpr int v = I; };
template <int I, int N, class F> __device__ __forceinline__ void sfor(F&& f) {
  if constexpr (I < N) { if (f(IC<I>{})) sfor<I + 1, N>(f); }
}
typedef __attribute__((ext_vector_type(4))) short s16x4;
typedef __attribute__((address_space(3))) s16x4* lds_s16x4p;
__device__ __forceinline__ u32x2 tr_read(u32 lds_addr) {
  return __builtin_bit_cast(u32x2, __builtin_amdgcn_ds_read_tr16_b64_v4i16((lds_s16x4p)lds_addr));
}
__device__ __forceinline__ u32 next_tag(u32 e) { return ((e | 0x40004000u) + 1u) & ~0x40004000u; }
__device__ __forceinline__ float u2f(u32 v) { return __uint_as_float(v); }
#define LDSP(T, a) ((__attribute__((address_space(3))) T*)(a))
template <class T> __device__ __forceinline__ u32 lds_addr(T* p) { u32 a = (u32)(uintptr_t)p; PIN(a); return a; }

typedef __attribute__((ext_vector_type(16))) unsigned u32x16;
typedef __attribute__((ext_vector_type(8))) unsigned u32x8;
__global__ void __launch_bounds__(512, 2) fused_kernel(const u16* x_, const u16* kc_, const u16* vc_, const u16* wq_, const u16* wo_, const u16* w1_,
                                                       const u16* w3_, const u16* w2_, const u16* cnd_, const u16* wmod_, unsigned char* ws_, u16* out_ PRM_ARG) {
  __shared__ __attribute__((aligned(256))) Smem sm;
  constexpr Prm P = PRM0;
  const int TEND = P.TEND;
  const int bid = blockIdx.x, tid = threadIdx.x;
  u32x4 kr[NL];
  u32x16 ka; u32x8 kb; u32x4 tev; u64 pt0; u32 tch2;
  asm volatile("s_load_dwordx16 %0, %3, 0x0\n\ts_load_dwordx8 %1, %3, 0x40\n\ts_memrealtime %2"
               : "=&s"(ka), "=&s"(kb), "=&s"(pt0) : "s"(__builtin_amdgcn_kernarg_segment_ptr()));
  SB();
  {
    u64 pc; u32 vb;
    asm volatile("s_getpc_b64 %2\n.Lcpf_first:\n\tv_mul_u32_u24 %1, ((.Lend_c-.Lcpf_first)>>10)<<2, %3\n\tglobal_load_dword %0, %1, %2"
                 : "=&v"(tch2), "=&v"(vb), "=&s"(pc) : "v"((bid & ~7) + (tid >> 6)) : "memory");
    asm volatile("global_load_dwordx4 %0, %1, %2" : "=&v"(tev) : "v"(bid << 4), "s"(TABP) : "memory");
  }
  SB();
  const int xcd = bid & 7, idx = bid >> 3, part = idx & 3;
  const int b = idx >> 2 & 1, head = xcd * 4 + (((idx >> 3) & 1) << 1 | idx >> 4);
  const u32 so0 = step_off(P, 32 * part + KF0, b * 32 + head) & ~place(3, P.PM, 2);
  u32 va = (((b * 32 + head) * SK + (SK - WIN)) * DH * 2 + so0) | place(tid >> 6, P.WM, 3) | (tid & 63) << 4;
  const u32 kx1 = place(1, P.PM, 2), kx2 = place(2, P.PM, 2);
  PIN(va);
  u32 va1 = va | kx1, va2 = va | kx2, va3 = va | kx1 | kx2;
  PIN(va1); PIN(va2); PIN(va3);
  int mc = bid & 0xd8;
  asm volatile("" : "+v"(va), "+s"(mc));
  SB();
  asm volatile("s_waitcnt lgkmcnt(0)" : "+s"(ka), "+s"(kb), "+s"(pt0));
  SB();
#define K_FIRST "global_load_dwordx4 %0, %4, %5 nt\n\tglobal_load_dwordx4 %1, %6, %5 nt\n\tglobal_load_dwordx4 %2, %7, %5 nt\n\tglobal_load_dwordx4 %3, %8, %5 nt"
  asm volatile("s_cmp_eq_u32 %9, 16\n\ts_cbranch_scc1 .Lk_later\n\t" K_FIRST "\n.Lk_later:"
               : "=&v"(kr[0]), "=&v"(kr[1]), "=&v"(kr[2]), "=&v"(kr[3]) : "v"(va), "s"((u64)ka[2] | (u64)ka[3] << 32), "v"(va1), "v"(va2), "v"(va3), "s"(mc) : "memory", "scc");
  SB();
  const u32 tka = (u32)wall_clock64(), t0 = tka - P.D0 + (__builtin_amdgcn_readfirstlane(tid >> 6) * 2 - 7) * P.PW / 16;
  auto kap = [&](int k) { return k < 8 ? (u64)ka[2 * k] | (u64)ka[2 * k + 1] << 32 : (u64)kb[2 * k - 16] | (u64)kb[2 * k - 15] << 32; };
  const u16 *x = (const u16*)kap(0), *kc = (const u16*)kap(1), *vc = (const u16*)kap(2), *w2 = (const u16*)kap(7), *cnd = (const u16*)kap(8);
  unsigned char* ws = (unsigned char*)kap(10); u16* out = (u16*)kap(11);
  const int lane = tid & 63, wave = __builtin_amdgcn_readfirstlane(tid >> 6);
  const bool p3 = part == 3;
  const u64 wa = part == 0 ? kap(4) : (part == 1 ? kap(3) : (part == 2 ? kap(9) : ((wave & 1) ? kap(6) : kap(5))));
  const bool modc = mc == 16;
  if (modc) asm volatile("s_waitcnt vmcnt(0)" ::: "memory");
  asm volatile("s_waitcnt vmcnt(4)" : "+v"(tev));
  const u32 te0 = __builtin_amdgcn_readfirstlane(tev[0]), te1 = __builtin_amdgcn_readfirstlane(tev[1]);
  const u32 te3 = __builtin_amdgcn_readfirstlane(tev[3]), pitch = te3 & 0xffff, wsz = pitch << (part == 0 ? 12 : 9);
  const int sh = p3 ? 2 : 3;
  u32 wv0 = (u32)((p3 ? (wave >> 1) * 128 : wave * 64) + (lane >> sh)) * pitch + ((lane & (p3 ? 3 : 7)) ^ (lane >> (p3 ? 5 : 3))) * 16u;
  const u32 wimg = (u32)(uintptr_t)(&sm.wbuf[0][0][0]);
  u32 wld = wimg + (p3 ? (wave & 1) * 32768 + (wave >> 1) * 8192 : wave * 8192);
  const int bl0 = te0 & 127, nl = te0 >> 7 & 63, blk = te0 >> 13 & 63;
  const bool has_w = te0 >> 19 & 1;
  const rsrc_t rw = mk_rsrc((const void*)wa, wsz);
  const u32 wstep = pitch << (6 - sh), wflip = p3 ? 32u : 0u;
  u32 wso = __builtin_amdgcn_readfirstlane(tev[2]);
  int wleft = 8;
  auto piece = [&]() {
    dma16(rw, wv0, wso, wld);
    wso += wstep; wld += 1024;
    wv0 ^= (wleft & 1) ? wflip : 0u;
    --wleft;
  };
  int tau = te1 & 0xffff;
  const int tauw = te1 >> 16, sw = te0 >> 24;
  int tau2 = te3 >> 16;
  int due = P.DS * 256, now = 0;
  auto gate = [&]() {
    while (now < due) { now = (int)(((u32)wall_clock64() - t0) << 8); if (now < due) __builtin_amdgcn_s_sleep(1); }
    due += tau;
  };
  const rsrc_t wsr = mk_rsrc(ws, WS_END);
  const int g = lane >> 4, c = lane & 15;
  const int i8 = idx >> 2, wi = i8 * 8 + xcd;
  const bool is_up = p3 && has_w;
  const size_t base = ((size_t)(b * NH + head) * SK + (SK - WIN)) * DH;
  const rsrc_t rk = mk_rsrc(kc + base, WIN * DH * 2), rv = mk_rsrc(vc + base, WIN * DH * 2);
  u32 sot;
  {
    u32 s0 = bl0 + lane - KF;
    s0 += s0 >= (u32)(32 * part + KF0) ? KF : 0;
    sot = (lane < KF ? so0 | place(lane, P.PM, 2) : step_off(P, s0, b * 32 + head)) | place(wave, P.WM, 3);
    sot = lane < nl ? sot : OOB;
    PIN(sot);
  }
  auto until = [&](int t) { while ((int)(((u32)wall_clock64() - t0) << 8) < t); };
  const u32 pslot = G_P + ((b * 32 + head) * 4 + part) * 256;
  const rsrc_t rx = mk_rsrc(part == 2 ? cnd : x, (part == 1 || (part == 2 && has_w)) ? 2048u : 0u);
  u32x2 ep; u32 xs0, xs1, tch1; u32x4 xr0, xr1;
  auto small_ops = [&]() {
    ep = ld8<AUX_SC1>(wsr, wave == 7 ? WS_EPOCH : OOB, 0);
    xs0 = __builtin_amdgcn_raw_buffer_load_b16(rx, tid * 2, 0, 0); xs1 = __builtin_amdgcn_raw_buffer_load_b16(rx, tid * 2, 1024, 0);
  };
  auto small_ops2 = [&]() {
    xr0 = ld16<0>(rx, part == 1 ? lane * 16 : OOB, 0); xr1 = ld16<0>(rx, part == 1 ? lane * 16 : OOB, 1024);
    tch1 = __builtin_amdgcn_raw_buffer_load_b32(mk_rsrc(w2 + (size_t)blk * 32 * 512, is_up ? 64u : 0u), tid == 384 ? 0 : OOB, 0, AUX_NT);
  };
  u32 tag = 0;
  u32x4 bf[8];
  auto wo_prep = [&](int s0 = 0, int s1 = 2) {
    const u32 wi_ = wimg + wave * 8192;
    const int q = (lane >> 2) & 3, pp = lane & 3;
#pragma unroll
    for (int s = s0; s < s1; ++s) {
#pragma unroll
      for (int t = 0; t < 4; ++t) {
        const int r0 = s * 32 + g * 8 + q, r1 = r0 + 4, ch = t * 2 + (pp >> 1);
        const u32x2 lo = tr_read(wi_ + r0 * 128 + ((ch ^ (r0 & 7)) * 16) + (pp & 1) * 8), hi = tr_read(wi_ + r1 * 128 + ((ch ^ (r1 & 7)) * 16) + (pp & 1) * 8);
        bf[s * 4 + t] = u32x4{lo[0], lo[1], hi[0], hi[1]};
      }
    }
  };
  auto gemv_go = [&](u32 hh) {
    sm.hb[tid] = (u16)hh; sm.hb[512 + tid] = (u16)(hh >> 16);
    WAVE_SYNC();
    const u32x4* a = (c & 11) == 0 ? reinterpret_cast<const u32x4*>(&sm.hb[(c >> 2) * 512 + wave * 64 + g * 8]) : &sm.zero[0];
    const u32x4 a0 = a[0], a1 = a[(c & 11) == 0 ? 4 : 0];
    const f32x4 z = {0.f, 0.f, 0.f, 0.f};
    f32x4 d[4];
#pragma unroll
    for (int t = 0; t < 4; ++t) d[t] = mfma16(a0, bf[t], z);
#pragma unroll
    for (int t = 0; t < 4; ++t) d[t] = mfma16(a1, bf[4 + t], d[t]);
    if (lane < 32) {
#pragma unroll
      for (int t = 0; t < 4; ++t) sm.r2[(t * 16 + c) * 20 + g * 8 + wave] = d[t][0];
    }
  };
  auto mod_gemv = [&]() { gemv_go(pack_bf(silu_f(lo_f(xs0)), silu_f(lo_f(xs1)))); };
  auto mod_put = [&]() { st8_sc1(wsr, G_MOD + (blk * 64 + lane) * 8, pack_bf(sum8(&sm.r2[lane * 20]), sum8(&sm.r2[lane * 20 + 8])), tag); };
  auto zero_rows = [&]() { if (lane < 4) sm.zero[lane] = u32x4{0u, 0u, 0u, 0u}; };
  u32 l16 = lane * 16;
  asm volatile("" : "+v"(l16), "+v"(wv0) : "s"(rk), "s"(rx), "s"(wsr));
  SB();
  if (has_w) {
    if (modc) now = 1 << 30;
#pragma nounroll
    while (wleft > (int)(te0 >> 20 & 7)) { gate(); due += tauw - tau; piece(); }
    now = 0;
  }
  SB();
  STAMP(23);
  if (__builtin_expect(modc, 0)) {
    __builtin_amdgcn_raw_buffer_store_b128(u32x4{SENT, SENT, SENT, SENT}, wsr, tid < 320 ? G_P + wi * 5120 + tid * 16 : OOB, 0, AUX_SC1);
    small_ops();
    zero_rows();
    asm volatile("s_waitcnt vmcnt(0)" ::: "memory");
    tag = next_tag(__builtin_amdgcn_readfirstlane(ep[0]));
    wo_prep();
    mod_gemv();
    LDS_BARRIER();
    if (wave == 7) mod_put();
    STAMP(9);
    __builtin_amdgcn_s_barrier();
    asm volatile(K_FIRST :: "v"(kr[0]), "v"(kr[1]), "v"(kr[2]), "v"(kr[3]), "v"(va), "s"((u64)ka[2] | (u64)ka[3] << 32), "v"(va1), "v"(va2), "v"(va3) : "memory");
  }
  SB();
  const int fd = wi * 8 + (lane & 7);
  const int ub = wave >> 2, ud = (wave & 3) * 128 + c * 8 + g * 2;
  u32x4 sg[3]; u32 sx2;
  auto side_loads = [&]() {
    const bool fin = part == 1 && wave == 0;
    sg[0] = ld16<AUX_SC1>(wsr, is_up ? G_MOD + (1024 + ud) * 8 : (fin ? G_MOD + (1024 + fd) * 8 : OOB), 0);
    sg[1] = ld16<AUX_SC1>(wsr, is_up ? G_MOD + (1536 + ud) * 8 : (fin ? G_MOD + (2560 + fd) * 8 : OOB), 0);
    sg[2] = ld16<AUX_SC1>(wsr, is_up ? G_MOD + (2048 + ud) * 8 : OOB, 0);
    sx2 = __builtin_amdgcn_raw_buffer_load_b32(mk_rsrc(x, (is_up || fin) ? 2048u : 0u), is_up ? ub * 1024 + ud * 2 : (g & 1) * 1024 + (fd >> 1) * 4, 0, 0);
  };
  auto side_pin = [&]() { PIN(sg[0]); PIN(sg[1]); PIN(sg[2]); PIN(sx2); };
  const u32 mlo = G_ML + (head * 2 + b) * 64 + (lane & 3) * 16;
  float fmrg = 0.f;
  bool fok = wave != 0;
  auto merge_factor = [&](const u32x4& ml) {
    const float mi = u2f(ml[0]);
    float M = fmaxf(mi, dpp<XOR1>(mi)); M = fmaxf(M, dpp<XOR2>(M));
    const float e = __expf(mi - M);
    float L = e * u2f(ml[2]);
    L += dpp<XOR1>(L); L += dpp<XOR2>(L);
    fmrg = rdl(e * __builtin_amdgcn_rcpf(L), part);
  };
  f32x2 ga = {0.f, 0.f}, xin = {0.f, 0.f}, cc1 = {0.f, 0.f};
  float Af = 0.f, Bf = 0.f;
  const int hpos = ub * 512 + ud;
  auto up_prep = [&]() {
    if (!all_ok((sg[0][1] == tag) & (sg[0][3] == tag) & (sg[1][1] == tag) & (sg[1][3] == tag) & (sg[2][1] == tag) & (sg[2][3] == tag))) gather16<3>(wsr, G_MOD + (1024 + ud) * 8, 512 * 8, tag, sg);
    ga = f32x2{ub ? hi_f(sg[0][0]) : lo_f(sg[0][0]), ub ? hi_f(sg[0][2]) : lo_f(sg[0][2])};
    cc1 = f32x2{rbf(1.f + (ub ? hi_f(sg[2][0]) : lo_f(sg[2][0]))), rbf(1.f + (ub ? hi_f(sg[2][2]) : lo_f(sg[2][2])))};
    xin = f32x2{lo_f(sx2), hi_f(sx2)};
    u32* hp = reinterpret_cast<u32*>(&sm.hb[hpos]);
    hp[512] = pack_bf(cc1[0], cc1[1]);
    hp[1024] = ub ? perm_hi(sg[1][0], sg[1][2]) : perm_lo(sg[1][0], sg[1][2]);
  };
  auto up_frags = [&]() {
    const int pp = lane & 3;
    const u32 a = wimg + (pp & 1) * 32768 + ((pp >> 1) * 32 + g * 8 + ((lane >> 2) & 3)) * 64 + (((wave >> 1) ^ ((g & 1) | (pp & 2))) * 16) + (wave & 1) * 8;
#pragma unroll
    for (int rb = 0; rb < 8; ++rb) {
      const u32x2 lo = tr_read(a + rb * 4096), hi = tr_read(a + rb * 4096 + 256);
      bf[rb] = u32x4{lo[0], lo[1], hi[0], hi[1]};
    }
  };
  const u32x4* ap = reinterpret_cast<const u32x4*>(&sm.hb[((lane >> 3) & 1) * 512 + (lane & 1) * 32 + g * 8]);
  const bool par = lane & 8;
  float l0 = 0.f, l1 = 0.f;
  u32x2 gmd[2], tbg;
  const bool modw = part == 2 && has_w && !modc;
  const int qp = part != 1 ? -99 : ((blk & 7) == 3 ? QP1 : QP2);
  static_assert(QP1 + QD < QP2 && QP1 + QD > WUSE && QP1 + QD > TBU && WUSE + 1 < TBU, "order of the q steps");
  int t1 = 0;
#pragma unroll
  for (int i = NCMIN / 8; i < NL; ++i) kr[i] = u32x4{0u, 0u, 0u, 0u};
  sfor<KF, NL>([&](auto ic) {
    constexpr int i = ic.v;
    if (i >= NCMIN / 8 && i >= nl) return false;
    const u32 so = __builtin_amdgcn_readlane(sot, i);
    if constexpr (i < NCMIN / 8) tau = i == sw ? tau2 : tau;
    gate();
    SB();
    kr[i] = ld16<AUX_NT>(rk, l16, so);
    if constexpr (i == KF) small_ops();
    if constexpr (i == KF + 1) small_ops2();
    if constexpr (i == KF + 2) zero_rows();
    if constexpr (i == TBL) tbg = ld8<AUX_SC1>(wsr, wave == 7 ? G_TB + xcd * 128 + (lane & 7) * 8 : OOB, 0);
    if constexpr (i == QP1 || i == QP2) { gmd[0] = ld8<AUX_SC1>(wsr, i == qp ? G_MOD + tid * 8 : OOB, 0); gmd[1] = ld8<AUX_SC1>(wsr, i == qp ? G_MOD + tid * 8 : OOB, 4096); }
    SB();
    SB();
    if (i >= WK) { WAIT_VM(win_k(i)); SB(); }
    if (i == KF) STAMP(25);
    if (i == 8) STAMP(12);
    if (i == 20) STAMP(21);
    if (i == 25) STAMP(22);
    if (i == TBP) {
      if (part == 3 && i8 == 0 && wave == 7) { tag = next_tag(__builtin_amdgcn_readfirstlane(ep[0])); if (lane < 8) st8_sc1(wsr, G_TB + lane * 128 + xcd * 8, (u32)pt0 + (((tka - (u32)pt0) * P.GA) >> 3) - P.GS, tag); }
      SB();
    }
    if (i == TBU - 1) {
      if (wave == 7) {
        const bool ok = tbg[1] == tag;
        float lf = ok ? (float)min(max((int)(t0 - tbg[0]), -300), 300) : 0.f, nf = ok ? 1.f : 0.f;
        lf += dpp<XOR1>(lf); nf += dpp<XOR1>(nf); lf += dpp<XOR2>(lf); nf += dpp<XOR2>(nf); lf += dpp<XOR7>(lf); nf += dpp<XOR7>(nf);
        if (lane == 0) sm.bc = u32x2{tag, t0 - (u32)(int)(lf * __builtin_amdgcn_rcpf(fmaxf(nf, 1.f)))};
      }
      SB();
    }
    if (i == TBU) {
      LDS_BARRIER();
      const u32x2 bc = sm.bc;
      tag = __builtin_amdgcn_readfirstlane(bc[0]);
      t1 = (__builtin_amdgcn_readfirstlane((int)(bc[1] - t0)) + TEND + P.X1) * 256;
      const int dt = __builtin_amdgcn_readfirstlane((int)((float)(int)(t0 - bc[1]) * 256.f * __builtin_amdgcn_rcpf((float)(2 * nl - TBU - 2))));
      tau -= dt; tau2 -= dt;
      SB();
    }
    if (i == TBU + 1) {
      if (modw) { if (wave == 6) mod_put(); STAMP(9); }
      SB();
    }
    if (i == LNS) {
      tag = next_tag(__builtin_amdgcn_readfirstlane(ep[0]));
      if (part == 1) {
        float x0[8], x1[8], mb0, ri0, mb1, ri1;
#pragma unroll
        for (int k = 0; k < 4; ++k) { x0[2 * k] = lo_f(xr0[k]); x0[2 * k + 1] = hi_f(xr0[k]); x1[2 * k] = lo_f(xr1[k]); x1[2 * k + 1] = hi_f(xr1[k]); }
        ln_stats2(x0, x1, mb0, ri0, mb1, ri1);
        l0 = rbf(rbf(lo_f(xs0) - mb0) * ri0); l1 = rbf(rbf(lo_f(xs1) - mb1) * ri1);
      }
      PIN(l0); PIN(l1);
      SB();
    }
    if (i == WUSE) {
      PIN(tch1); PIN(tch2);
      static_assert(WUSE - WK >= KF, "the weight pieces are older than the steps outside the window");
      CFENCE();
      if (part == 1 || modw) wo_prep();
#pragma unroll
      for (int k = 0; k < 8; ++k) PIN(bf[k]);
      SB();
    }
    if (i == WUSE + 1) {
      if (modw) mod_gemv();
      SB();
    }
    if (i == QP1 + QD || i == QP2 + QD) {
      if (i == qp + QD) {
        u32 gm[2] = {gmd[0][0], gmd[1][0]};
        if (!all_ok((gmd[0][1] == tag) & (gmd[1][1] == tag))) { STAMP(11); gather8<2>(wsr, G_MOD + tid * 8, 512 * 8, tag, gm); }
        STAMP(5);
        gemv_go(pack_bf(rbf(l0 * rbf(1.f + lo_f(gm[1]))) + lo_f(gm[0]), rbf(l1 * rbf(1.f + hi_f(gm[1]))) + hi_f(gm[0])));
      }
      SB();
    }
    if (i == QP1 + QD + 1 || i == QP2 + QD + 1) {
      if (i == qp + QD + 1) {
        LDS_BARRIER();
        if (wave == 0) {
          const int bb = lane >> 5, jq = lane & 31;
          st8_sc1(wsr, G_Q + (bb * 2048 + blk * 32 + jq) * 8, pack_bf(sum8(&sm.r2[2 * jq * 20 + bb * 8]), sum8(&sm.r2[(2 * jq + 1) * 20 + bb * 8])), tag);
        }
        STAMP(6);
      }
      SB();
    }
    return true;
  });
  PIN(sot);
  const u32 qoff = G_Q + ((b * 32 + head) * 64 + c * 4) * 8;
  u32x4 qg[2], vr[NL], fl2[3];
  u32 qq[4];
  constexpr int NB = (NL + 15) / 16;
  float xlo, xs[4], sc[NB], pr[NB], m = 0.f;
  f32x4 acc = {0.f, 0.f, 0.f, 0.f};
  u32 pb[NB], pm[4], cpf;
  const u32 adst0 = lds_addr(lane < 32 ? &sm.red[wave][c * 8 + g * 4] : &sm.r2[(lane - 32) * 4]);
  u32 po, rdst, rsrc, xo;
  auto wo_addr = [&]() {
    po = G_P + (((c >> 2 & 1) * 32 + xcd * 4 + (wave >> 1)) * 4 + (c & 3)) * 256 + (wave & 1) * 128 + (c >> 3) * 64 + g * 16;
    rdst = lane < 32 ? (u32)(uintptr_t)&sm.red2[wave][0] + lane * 16 : (u32)(uintptr_t)&sm.r2[0] + (lane - 32) * 16;
    rsrc = (u32)(uintptr_t)&sm.red2[0][((wave & 1) * 16 + (lane & 15)) * 4 + (lane >> 4)];
    xo = (lane & 1) ? OOB : G_XP + xcd * 2048 + (wave & 1) * 1024 + (blk * 64 + lane) * 2;
    PIN(po); PIN(rdst); PIN(rsrc); PIN(xo);
  };
  u32 pso = wave == 0 ? pslot + lane * 4 : OOB;
  PIN(pso);
  const u32 nadd = (te0 >> 20 & 7) ? P.LW : (is_up ? P.LU : 0);
  u32 nto = tid == 0 ? G_NT + bid * 8 : OOB;
  PIN(nto);
  auto notices = [&](auto nc_, u32 off, u32x4 (&gn)[nc_.v]) -> int {
    constexpr int N = nc_.v;
#pragma nounroll
    for (int spin = 0; spin < SPIN_MAX; ++spin) {
      bool ok = true;
#pragma unroll
      for (int i = 0; i < N; ++i) ok &= (gn[i][1] == tag) & (gn[i][3] == tag);
      if (all_ok(ok)) break;
      __builtin_amdgcn_s_sleep(10);
      CFENCE();
#pragma unroll
      for (int i = 0; i < N; ++i) gn[i] = ld16<AUX_SC1>(wsr, off, i * 1024);
    }
    int mx = 0;
#pragma unroll
    for (int i = 0; i < N; ++i) mx = max(mx, max((int)(gn[i][0] - t0), (int)(gn[i][2] - t0)));
    return __builtin_amdgcn_readfirstlane((int)wave_max((float)min(mx, 1 << 20)));
  };
  const int side = nl >= SIDE + 13 ? SIDE + 8 : (nl >= SIDE + 9 ? SIDE + 4 : SIDE);
  sfor<0, NL + WV>([&](auto ic) {
    constexpr int v = ic.v;
    if (cons_end(v - 1) >= NCMIN / 8 && cons_end(v - 1) >= nl) return false;
    if constexpr (v < NL) {
      const u32 so = __builtin_amdgcn_readlane(sot, v);
      if constexpr (v >= 12) { if (v == nl - P.NE) tau = (tau * (nadd ? P.FE2 : P.FE)) >> 4; }
      if constexpr (v >= NCMIN / 8) { if (v < nl) gate(); } else gate();
      SB();
      vr[v] = ld16<AUX_NT>(rv, l16, so);
      SB();
      SB();
    }
    if constexpr (v == NCMIN / 8 - 1) {
#pragma unroll
      for (int i = NCMIN / 8; i < NL; ++i) asm volatile("" : "=v"(vr[i]));
      asm volatile("" : "=v"(fl2[1]), "=v"(fl2[2]));
    }
    if constexpr (v == VQ - 4) { qg[0] = ld16<AUX_SC1>(wsr, qoff, 0); qg[1] = ld16<AUX_SC1>(wsr, qoff + 16, 0); }
    if constexpr (v == VSIDE) side_loads();
    if constexpr (v == CPF) {
      u64 pc; u32 dlt, len;
      asm volatile("s_getpc_b64 %0\n.Lcpf_here:\n\ts_mov_b32 %1, .Ltail_start-.Lcpf_here\n\ts_mov_b32 %2, .Lend_a-.Ltail_start\n\ts_max_i32 %2, %2, .Lend_b-.Ltail_start\n\ts_max_i32 %2, %2, .Lend_c-.Ltail_start\n\ts_max_i32 %2, %2, .Lend_d-.Ltail_start\n\ts_max_i32 %2, %2, .Lend_e-.Ltail_start" : "=s"(pc), "=s"(dlt), "=s"(len) :: "scc");
      pc += dlt;
      const rsrc_t rc = mk_rsrc((const void*)pc, (part == 3 && i8 == 1) ? len : 0u);
      cpf = __builtin_amdgcn_raw_buffer_load_b32(rc, tid * 64, 0, 0);
    }
    if constexpr (v == CPF + WV + 1) PIN(cpf);
    if constexpr (v == SIDE || v == SIDE + 4 || v == SIDE + 8) fl2[(v - SIDE) / 4] = ld16<AUX_SC1>(wsr, (wave == 0 && v == side) ? mlo : OOB, 0);
    SB();
    if constexpr (v < WV) { WAIT_VM(win_v(v, WK)); SB(); } else if constexpr (v < NL && cons_end(v) < v - WV + 1) { WAIT_VM(win_v(v, WV)); SB(); }
    if constexpr (v == VQ) {
      if (!all_ok((qg[0][1] == tag) & (qg[0][3] == tag) & (qg[1][1] == tag) & (qg[1][3] == tag))) { STAMP(20); gather16<2>(wsr, qoff, 16, tag, qg); }
      STAMP(1); WSTAMP(1);
      qq[0] = qg[0][0]; qq[1] = qg[0][2]; qq[2] = qg[1][0]; qq[3] = qg[1][2];
    }
    if constexpr (v >= VQ && v < VS) {
      constexpr int u0 = (v - VQ) * UPS, u1 = v == VS - 1 ? (NL + 7) / 8 * 8 : u0 + UPS;
      sfor<u0, u1>([&](auto uc) {
        constexpr int u = uc.v, p = u & 7, i = (p & 1) * 4 + (p & 2) + (p >> 2), sl = u >> 3, bt = sl >> 1;
        float d = 0.f;
        if constexpr (sl * 8 + i < NL) {
#pragma unroll
          for (int k = 0; k < 4; ++k) d = dot2(kr[sl * 8 + i][k], qq[k], d);
        }
        if constexpr (p & 1) xs[i - 4] = sel_add<XOR7>(xs[i - 4], d, c & 4); else xs[i] = d;
        if constexpr ((p & 3) == 3) xs[p >> 2] = sel_add<XOR2>(xs[p >> 2], xs[(p >> 2) + 2], c & 2);
        if constexpr (p == 7) {
          xs[0] = sel_add<XOR1>(xs[0], xs[1], c & 1);
          if constexpr ((sl & 1) || u == (NL + 7) / 8 * 8 - 1) {
            const float t = (sl & 1) ? sel_add<XOR8>(xlo, xs[0], c & 8) : xs[0] + dpp<XOR8>(xs[0]);
            const float s = rbf(rbf(t) * 0.08838834764831845f);
            sc[bt] = (bt * 16 + c < nl) ? s : -__builtin_inff();
            PIN(sc[bt]);
          } else {
            xlo = xs[0];
            PIN(xlo);
          }
        }
        return true;
      });
      constexpr int pe = u1 & 7;
      if constexpr (pe >= 1) PIN(xs[0]);
      if constexpr (pe == 3) PIN(xs[2]);
      if constexpr (pe >= 5) PIN(xs[1]);
      if constexpr (pe == 7) PIN(xs[3]);
    }
    if constexpr (v == VS) {
      float mloc = sc[0];
#pragma unroll
      for (int bt = 1; bt < NB; ++bt) mloc = fmaxf(mloc, sc[bt]);
      m = fmaxf(wave_max(mloc), -3.0e38f);
      float ls = 0.f;
#pragma unroll
      for (int bt = 0; bt < NB; ++bt) { pr[bt] = __expf(sc[bt] - m); ls += pr[bt]; }
      const float l = wave_sum(ls);
      if (lane == 0) { sm.ml[wave][0] = m; sm.ml[wave][1] = l; }
      STAMP(2); WSTAMP(2);
    }
    sfor<cons_end(v - 1), cons_end(v)>([&](auto jc) {
      constexpr int i = jc.v;
      if (i < NCMIN / 8 || i < nl) {
        const u32 pv = __float_as_uint(bcast16(u2f(pb[i >> 4]), i & 15));
        acc = mfma16(u32x4{pv & pm[0], pv & pm[1], pv & pm[2], pv & pm[3]}, vr[i], acc);
      }
      PIN(acc);
      return true;
    });
    if constexpr (v == VS - 1) { side_pin(); if (is_up) up_prep(); PIN(ga); PIN(xin); PIN(cc1); }
    if constexpr (v == MLS - 1) {
#pragma unroll
      for (int k = 0; k < 4; ++k) { pm[k] = (c >> 1) == k ? ((c & 1) ? 0xffff0000u : 0x0000ffffu) : 0u; PIN(pm[k]); }
    }
    if constexpr (v == UPREP - 1) wo_addr();
    if constexpr (v == UPREP) {
#pragma unroll
      for (int k = 0; k < 8; ++k) asm volatile("" : "=v"(bf[k]));
      if (part == 0) wo_prep(0, 1); else if (is_up) up_frags();
#pragma unroll
      for (int k = 0; k < 8; ++k) PIN(bf[k]);
    }
    if constexpr (v == UPREP + 1 || v == UPREP + 2) {
      if (is_up) {
        constexpr int r0 = (v - UPREP - 1) * 4;
        const u32x4* a1 = ap + 128 + ((lane >> 1) & 1) * 128;
        f32x4 d0 = {0.f, 0.f, 0.f, 0.f}, d1 = {0.f, 0.f, 0.f, 0.f};
#pragma unroll
        for (int rb = r0; rb < r0 + 2; ++rb) { d0 = mfma16(a1[rb * 8], bf[rb], d0); d1 = mfma16(a1[(rb + 2) * 8], bf[rb + 2], d1); }
        Af += par ? d0[1] + d1[1] : d0[0] + d1[0]; Bf += par ? d0[3] + d1[3] : d0[2] + d1[2];
        if constexpr (r0 == 4) { Af += dpp<XOR8>(Af); Bf += dpp<XOR8>(Bf); }
      }
      PIN(Af); PIN(Bf);
    }
    if constexpr (v == SIDE + WV + 1 || v == SIDE + WV + 5 || v == SIDE + WV + 9) {
      if (wave == 0 && v == side + WV + 1) {
        const u32x4 ml = fl2[(v - SIDE - WV - 1) / 4];
        fok = all_ok((ml[1] == tag) & (ml[3] == tag));
        merge_factor(ml);
      }
    }
    if (v % 6 == 4 && v >= 10 && v < 30) STAMP(15 + v / 6);
    if constexpr (v == MLS) {
      LDS_BARRIER();
      float mm[8], ll[8];
#pragma unroll
      for (int w = 0; w < 8; ++w) { const f32x2 t = *reinterpret_cast<const f32x2*>(&sm.ml[w][0]); mm[w] = t[0]; ll[w] = t[1]; }
      float Mwg = mm[0], Lwg = 0.f;
#pragma unroll
      for (int w = 1; w < 8; ++w) Mwg = fmaxf(Mwg, mm[w]);
      if (wave == 0) {
#pragma unroll
        for (int w = 0; w < 8; ++w) Lwg = fmaf(__expf(mm[w] - Mwg), ll[w], Lwg);
        if (lane == 0) st16_sc1(wsr, G_ML + ((head * 2 + b) * 4 + part) * 16, __float_as_uint(Mwg), __float_as_uint(Lwg), tag);
      }
      const float fw = __expf(m - Mwg);
#pragma unroll
      for (int bt = 0; bt < NB; ++bt) { pb[bt] = pack_bf(pr[bt] * fw, pr[bt] * fw); PIN(pb[bt]); }
    }
    SB();
    return true;
  });
  asm volatile(".Ltail_start:");
  *LDSP(f32x4, adst0) = acc;
  STAMP(14); WSTAMP(14);
  if (!fok) {
    u32x4 ml;
#pragma nounroll
    for (int spin = 0; spin < SPIN_MAX; ++spin) {
      CFENCE();
      ml = ld16<AUX_SC1>(wsr, mlo, 0);
      if (all_ok((ml[1] == tag) & (ml[3] == tag))) break;
    }
    merge_factor(ml);
  }
  STAMP(15);
  LDS_BARRIER();
  STAMP(3);
  {
    u32 pw = 0;
    if (wave == 0) {
      f32x2 t[8];
#pragma unroll
      for (int w = 0; w < 8; ++w) t[w] = *reinterpret_cast<const f32x2*>(&sm.red[w][2 * lane]);
      const f32x2 o = ((t[0] + t[1]) + (t[2] + t[3])) + ((t[4] + t[5]) + (t[6] + t[7]));
      pw = min(pack_bf(o[0] * fmrg, o[1] * fmrg), SENT - 1u);
    }
    __builtin_amdgcn_raw_buffer_store_b32(pw, wsr, pso, 0, AUX_SC1);
    st8_sc1(wsr, nto, (u32)wall_clock64() + nadd, tag);
    SB();
#pragma unroll
    for (int i = NCMIN / 8; i < NL; ++i) PIN(vr[i]);
    PIN(fl2[0]); PIN(fl2[1]); PIN(fl2[2]);
  }
  STAMP(4);

  if (part == 2) FINISH();
  if (part == 0) {
    __builtin_amdgcn_s_barrier();
#pragma nounroll
    while (wleft > 0) piece();
    until(t1);
    STAMP(11);
    CFENCE();
    u32x4 og[2];
    og[0] = ld16<AUX_SC1>(wsr, po, 0);
    SB();
    WAIT_VM(1); CFENCE();
    wo_prep(1, 2);
#pragma unroll
    for (int k = 4; k < 8; ++k) PIN(bf[k]);
    SB();
    f32x4 d[4];
    auto wo_mm = [&]() {
      const f32x4 z = {0.f, 0.f, 0.f, 0.f};
#pragma unroll
      for (int t = 0; t < 4; ++t) { d[t] = mfma16(og[0], bf[t], z); og[1][t] = __float_as_uint(dpp<XOR8>(u2f(og[0][t]))); }
      SB();
#pragma unroll
      for (int t = 0; t < 4; ++t) d[t] = mfma16(og[1], bf[4 + t], d[t]);
    };
    auto p_bad = [&]() { return !all_ok(max(max(og[0][0], og[0][1]), max(og[0][2], og[0][3])) != SENT); };
    wo_mm();
    if (__builtin_expect(p_bad(), 0)) {
#pragma nounroll
      for (int spin = 0; spin < SPIN_MAX; ++spin) {
        __builtin_amdgcn_s_sleep(1);
        CFENCE();
        og[0] = ld16<AUX_SC1>(wsr, po, 0);
        if (!p_bad()) break;
      }
      wo_mm();
      SB();
      asm volatile(".Lend_d:");
    }
    STAMP(5); WSTAMP(5);
    {
      SB();
      f32x4 y; f32x2 h[4];
#pragma unroll
      for (int t = 0; t < 4; ++t) { h[t] = d[t].xy + d[t].zw; PIN(h[t]); }
#pragma unroll
      for (int t = 0; t < 4; ++t) { y[t] = h[t][0] + h[t][1]; PIN(y[t]); }
      *LDSP(f32x4, rdst) = y;
    }
    STAMP(16);
    LDS_BARRIER();
    STAMP(17);
    if (wave < 2) {
      f32x2 v[4];
#pragma unroll
      for (int w = 0; w < 4; ++w) v[w] = f32x2{LDSP(float, rsrc)[w * 256], LDSP(float, rsrc)[w * 256 + 128]};
      const f32x2 h = (v[0] + v[1]) + (v[2] + v[3]);
      const float y = h[0] + h[1];
      __builtin_amdgcn_raw_buffer_store_b32(min(pack_bf(y, dpp<XOR1>(y)), SENT - 1u), wsr, xo, 0, AUX_SC1);
      asm volatile(".Lend_a:");
    }
    STAMP(6);
    FINISH();
  }
  if (part == 3) {
    if (!has_w) FINISH();
    u32x4 b2[4];
    {
      __builtin_amdgcn_s_barrier();
      const rsrc_t r2 = mk_rsrc(w2 + (size_t)(blk * 32 + wave * 4) * 512, 4096u);
      const u32 l2 = (u32)(uintptr_t)(&sm.w2b[wave * 4][0]);
#pragma unroll
      for (int n = 0; n < 4; ++n) dma16(r2, (lane ^ (n * 2) ^ ((wave & 2) << 2)) * 16, n * 1024, l2 + n * 1024);
    }
    const u32 tdst = lds_addr(&sm.hb[hpos]);
    const u32 sdst = lds_addr(&sm.stat[wave][lane]);
    const u32 ssrc = lds_addr(&sm.stat[(g >> 1) * 4 + ((lane >> 1) & 3)][(lane & 1) * 16]);
    const u32 adst = lds_addr((c < 4 && (g & 1) == 0) ? &sm.actl[g >> 1][wave * 4 + c] : reinterpret_cast<u16*>(&sm.dummy[lane]));
    const u32 asrc = lds_addr(&sm.actl[lane & 1][g * 8]);
    const u32 apa = lds_addr(&sm.hb[(c >> 3) * 512 + (c & 3) * 64 + ((c >> 2) & 1) * 32 + g * 8]);
    u32 xoff = G_XP + g * 2048 + ub * 1024 + (wave & 3) * 256 + c * 16;
    u32x4 one;
#pragma unroll
    for (int k = 0; k < 4; ++k) one[k] = ((c & 2) == 0 && (c >> 2) == k) ? ((c & 1) ? 0x3f800000u : 0x00003f80u) : 0u;
    PIN(one);
    u32 ooff = G_OP + (blk * 512 + tid) * 8;
    float Afp = Af, Bfp = Bf;
    PIN(xoff); PIN(ooff); PIN(Afp); PIN(Bfp);
    PIN(ga); PIN(xin); PIN(cc1);
    u32 w2a = (u32)(uintptr_t)(&sm.w2b[0][0]) + (g * 8 + ((lane >> 2) & 3)) * 1024 + (wave ^ (g & 1)) * 128 + (lane & 15) * 8;
    PIN(w2a);
    if (wave == 1) {
      due = t1 + P.N1 * 256; gate();
      u32x4 gn[2] = {ld16<AUX_SC1>(wsr, G_NT + lane * 16, 0), ld16<AUX_SC1>(wsr, G_NT + lane * 16, 1024)};
      const int tn = notices(IC<2>{}, G_NT + lane * 16, gn) * 256;
      due = (tn > t1 + 2 * 256 ? max(tn, t1 + P.L0 * 256) + P.L1 * 256 : t1) + P.X2 * 256;
      until(due);
    }
    asm volatile("s_waitcnt vmcnt(0)" ::: "memory");
    STAMP(9);
    __builtin_amdgcn_s_barrier();
    STAMP(11);
    CFENCE();
    u32x4 xg[2] = {ld16<AUX_SC1>(wsr, xoff, 0), ld16<AUX_SC1>(wsr, xoff, 8192)};
    SB();
#pragma unroll
    for (int t = 0; t < 4; ++t) {
      const u32x2 lo = tr_read(w2a ^ (t * 32)), hi = tr_read((w2a ^ (t * 32)) + 4096);
      b2[t] = u32x4{lo[0], lo[1], hi[0], hi[1]};
    }
#pragma unroll
    for (int t = 0; t < 4; ++t) PIN(b2[t]);
    SB();
    const f32x4 z4 = {0.f, 0.f, 0.f, 0.f};
    auto xp_bad = [&]() { return !all_ok(max(max(max(xg[0][0], xg[0][1]), max(xg[0][2], xg[0][3])), max(max(xg[1][0], xg[1][1]), max(xg[1][2], xg[1][3]))) != SENT); };
    f32x4 v = mfma16(one, xg[1], mfma16(one, xg[0], z4));
    if (__builtin_expect(xp_bad(), 0)) {
#pragma nounroll
      for (int spin = 0; spin < SPIN_MAX; ++spin) {
        __builtin_amdgcn_s_sleep(1);
        CFENCE();
        xg[0] = ld16<AUX_SC1>(wsr, xoff, 0); xg[1] = ld16<AUX_SC1>(wsr, xoff, 8192);
        if (!xp_bad()) break;
      }
      v = mfma16(one, xg[1], mfma16(one, xg[0], z4));
      SB();
      asm volatile(".Lend_e:");
    }
    STAMP(5); WSTAMP(5);
    {
      const float a0 = v[0], a1 = v[1];
      const float x10 = fmaf(ga[0], a0, xin[0]), x11 = fmaf(ga[1], a1, xin[1]);
      *LDSP(u32, tdst) = pack_bf(x10 * cc1[0], x11 * cc1[1]);
      float u = swap32_add(x10 + x11, fmaf(x10, x10, x11 * x11));
      u += dpp<XOR1>(u); u += dpp<XOR2>(u); u += dpp<XOR7>(u); u += dpp<XOR8>(u);
      *LDSP(float, sdst) = u;
    }
    STAMP(16);
    LDS_BARRIER();
    STAMP(17);
    {
      const f32x4 z = {0.f, 0.f, 0.f, 0.f};
      const u32x4 af0 = LDSP(u32x4, apa)[0], af1 = LDSP(u32x4, apa)[32];
      float s = LDSP(float, ssrc)[0], q = LDSP(float, ssrc)[32];
      SB();
      f32x4 d[4];
#pragma unroll
      for (int j = 0; j < 4; ++j) d[j] = mfma16(af0, bf[j], z);
#pragma unroll
      for (int j = 0; j < 4; ++j) d[j] = mfma16(af1, bf[4 + j], d[j]);
      s += dpp<XOR1>(s); q += dpp<XOR1>(q); s += dpp<XOR2>(s); q += dpp<XOR2>(q); s += dpp<XOR7>(s); q += dpp<XOR7>(q);
      const float mean = s * (1.f / 512.f);
      const float var = fmaf(q, 1.f / 512.f, -mean * mean);
      const float ri = __builtin_amdgcn_rsqf(fmaxf(var, 0.f) + 1e-5f);
      const float cf = fmaf(-ri * mean, Afp, Bfp);
      float y = (d[0][0] + d[1][1]) + (d[2][2] + d[3][3]);
      y = __builtin_bit_cast(float, __builtin_amdgcn_update_dpp(__builtin_bit_cast(int, y), __builtin_bit_cast(int, y), XOR8, 0xa, 0xf, false));
      y = swap16_add(y, y);
      y = fmaf(y, ri, cf);
      const float up = dpp<0x104>(y);
      *LDSP(u16, adst) = (u16)pack_bf(silu_f(y) * up, 0.f);
    }
    STAMP(18);
    LDS_BARRIER();
    STAMP(10);
    {
      const u32x4 af = *LDSP(u32x4, asrc);
      const f32x4 z = {0.f, 0.f, 0.f, 0.f};
      const f32x4 d0 = mfma16(af, b2[0], z), d1 = mfma16(af, b2[1], z), d2 = mfma16(af, b2[2], z), d3 = mfma16(af, b2[3], z);
      const bool g1 = g & 1, g2 = g & 2;
      const float e0 = g1 ? d1[0] : d0[0], e1 = g1 ? d1[1] : d0[1], f0 = g1 ? d3[0] : d2[0], f1 = g1 ? d3[1] : d2[1];
      st8_sc1(wsr, ooff, pack_bf(g2 ? f0 : e0, g2 ? f1 : e1), tag);
    }
    asm volatile(".Lend_b:");
    STAMP(6);
    FINISH();
  }
  if (wave == 0) {
    const int d = fd, ul = lane >> 3;
    u32 gm[2] = {sg[0][0], sg[1][0]};
    if (!all_ok((sg[0][1] == tag) & (sg[1][1] == tag))) gather8<2>(wsr, G_MOD + (1024 + d) * 8, 1536 * 8, tag, gm);
    u32 xa0, xa1;
    {
      due = t1 + P.N2 * 256; gate();
      u32x4 gn[2] = {ld16<AUX_SC1>(wsr, G_NT + lane * 16, 0), ld16<AUX_SC1>(wsr, G_NT + lane * 16, 1024)};
      const int tn = notices(IC<2>{}, G_NT + lane * 16, gn) * 256;
      due = (tn > t1 + 2 * 256 ? max(tn, t1 + P.L0 * 256) + P.L1 * 256 : t1) + P.X3 * 256;
      until(due);
    }
#pragma nounroll
    for (int spin = 0; spin < SPIN_MAX; ++spin) {
      CFENCE();
      xa0 = __builtin_amdgcn_raw_buffer_load_b32(wsr, G_XP + ul * 2048 + (d >> 1) * 4, 0, AUX_SC1);
      xa1 = __builtin_amdgcn_raw_buffer_load_b32(wsr, G_XP + ul * 2048 + (d >> 1) * 4, 1024, AUX_SC1);
      if (all_ok(max(xa0, xa1) != SENT)) break;
      __builtin_amdgcn_s_sleep(1);
    }
    float a0 = (d & 1) ? hi_f(xa0) : lo_f(xa0), a1 = (d & 1) ? hi_f(xa1) : lo_f(xa1);
    a0 += dpp<XOR8>(a0); a1 += dpp<XOR8>(a1);
    a0 = swap16_add(a0, a0); a1 = swap16_add(a1, a1);
    a0 = swap32_add(a0, a0); a1 = swap32_add(a1, a1);
    const bool hb = g & 1;
    float xv = rbf(((d & 1) ? hi_f(sx2) : lo_f(sx2)) + rbf((hb ? hi_f(gm[0]) : lo_f(gm[0])) * rbf(hb ? a1 : a0)));
    float gv = hb ? hi_f(gm[1]) : lo_f(gm[1]);
    PIN(xv); PIN(gv);
    if (wi == 0 && lane == 0) __builtin_amdgcn_raw_buffer_store_b32(tag, wsr, WS_EPOCH, 0, AUX_SC1);
    STAMP(10);
    due = max(due + P.X4 * 256, (int)(((u32)wall_clock64() - t0) << 8) + P.GX * 256);
    const bool last = ul + 40 < NUP;
    u32 o0 = G_OP + (ul * 512 + d) * 8, o5 = last ? o0 : OOB, tg5 = last ? tag : 0u;
    const rsrc_t rout = mk_rsrc(out, 2048u);
    u32 oo = (lane & 40) == 0 ? (g * 512 + d) * 2 : OOB;
    const u32 pk = c == 0 ? 0x00003f80u : (c == 4 ? 0x3f800000u : 0u);
    u32x4 one = {pk, 0u, pk, 0u};
    PIN(o0); PIN(o5); PIN(tg5); PIN(oo); PIN(one);
    __builtin_amdgcn_raw_buffer_store_b16((u16)pack_bf(xv, 0.f), rout, oo, 0, AUX_SC1);
    until(due);
    f32x4 sv;
#pragma nounroll
    for (int spin = 0; spin < SPIN_MAX; ++spin) {
      CFENCE();
      u32x2 og[6];
#pragma unroll
      for (int k = 0; k < 5; ++k) og[k] = ld8<AUX_SC1>(wsr, o0, k * 32768);
      og[5] = ld8<AUX_SC1>(wsr, o5, 5 * 32768);
      sv = f32x4{0.f, 0.f, 0.f, 0.f};
#pragma unroll
      for (int k = 0; k < 3; ++k) sv = mfma16(one, u32x4{og[2 * k][0], og[2 * k][1], og[2 * k + 1][0], og[2 * k + 1][1]}, sv);
      const u32 bad = ((og[0][1] ^ tag) | (og[1][1] ^ tag) | (og[2][1] ^ tag)) | ((og[3][1] ^ tag) | (og[4][1] ^ tag) | (og[5][1] ^ tg5));
      PIN(sv);
      if (all_ok(bad == 0u)) break;
      __builtin_amdgcn_s_sleep(1);
    }
    STAMP(7);
    const float s = sv[0] + dpp<XOR8>(sv[0]);
    __builtin_amdgcn_raw_buffer_store_b16((u16)pack_bf(fmaf(gv, s, xv), 0.f), rout, oo, 0, AUX_SC1);
    asm volatile(".Lend_c:");
  }
  FINISH();
}

extern "C" void kernel_launch(void* const* d_in, const int* in_sizes, int n_in,
                              void* d_out, int out_size, void* d_ws, size_t ws_size,
                              hipStream_t stream) {
  fused_kernel<<<256, 512, 0, stream>>>((const u16*)d_in[0], (const u16*)d_in[1], (const u16*)d_in[2], (const u16*)d_in[3], (const u16*)d_in[4],
                                        (const u16*)d_in[6], (const u16*)d_in[7], (const u16*)d_in[8], (const u16*)d_in[9], (const u16*)d_in[10],
                                        (unsigned char*)d_ws, (u16*)d_out
    );
}
```

```cpp
#include <hip/hip_runtime.h>
#include <stdint.h>

typedef unsigned int u32;
typedef unsigned short u16;
typedef unsigned long long u64;
typedef __attribute__((ext_vector_type(4))) unsigned u32x4;
typedef __attribute__((ext_vector_type(2))) unsigned u32x2;
typedef __attribute__((ext_vector_type(2))) __bf16 bf16x2;
typedef __attribute__((ext_vector_type(4))) float f32x4;
typedef __attribute__((ext_vector_type(2))) float f32x2;
typedef __amdgpu_buffer_rsrc_t rsrc_t;

constexpr int NH = 32, SK = 16384, DH = 128, WIN = 4096, FF = 1376;
struct Prm { int E0, ES, E1, E2, E2C, EU, TEND, DT_UP, L0, L1, X1, X2, X3, X4, NE, FE, M0, M1, M2, M3, M4, GS, GX, N1, N2, GA, WD, DT_WO, LW, LU, EW, PM, D0, FK, FW, FU, FN, F0, DS, FE2, TS, WM, PW, BT, GM, EX, A0, AG, AC, NC, PT, SL, NR; };
constexpr Prm PRM0 = {32, 24, 32, 32, 48, 64, 2006, 94, 30, 25, 74, 76, 105, 85, 5, 12, 12, 5, 12, 32, 28, 34, 40, 18, 50, 8, 4, 100, 40, 40, 8, 0x6827941, 30, 62, 61, 60, 64, 64, 116, 16, 500, 0x53, 0, 10, 0, 8, 61, 44, 70, 1, 160, 10, 0};
#define PRM_ARG
#define TABP (&TAB.e[0])
constexpr int NL = 37;
constexpr int NCMIN = 192;
constexpr int WK = 6, WV = 5;
constexpr int WUSE = 13, LNS = 11;
#define QPA 12
#define QPB 18
#define QDD 5
constexpr int QP1 = QPA, QP2 = QPB, QD = QDD;
constexpr int VQ = 6;
#define UPS 7
#define MLD 2
constexpr int NSL = (NL + UPS - 2) / UPS;
constexpr int VS = VQ + NSL;
constexpr int MLS = VS + MLD;
constexpr int VSIDE = VQ + 1, UPREP = 21;
constexpr int SIDE = NCMIN / 8 - 4;
#define UD 1
constexpr int CPF = 18;
constexpr int TBP = 7, TBL = 11, TBU = 16;
#define ARS 0
#define ALS 9
constexpr int AR = ARS, AL = ALS;
constexpr int KF = 4, KF0 = 8;
constexpr int side_k(int i) { return (i == KF || i == KF + 1) ? 3 : (i == TBL ? 1 : 0) + ((i == QP1 || i == QP2) ? 2 : 0); }
constexpr int side_v(int v) { return v == VQ - 4 ? 2 : (v == VSIDE ? 4 : (v == AL ? 1 : 0)); }
constexpr int win_k(int i) { int n = WK; for (int j = i - WK; j <= i; ++j) n += side_k(j); return n; }
constexpr int win_v(int v, int w) { int n = w; for (int j = v - w; j <= v; ++j) n += side_v(j); return n; }
constexpr int cons_end(int v) {
  const int a = v - WV + 1, c = (v - MLS) * 4;
  const int m = a < c ? a : c;
  return m < 0 ? 0 : (m > NL ? NL : m);
}
struct Tab { u32 e[1024]; };
#define popc __builtin_popcount
__host__ __device__ constexpr u32 place(u32 v, u32 m, int n) {
  u32 r = 0;
  for (int k = 0; k < n; ++k) r |= (v >> k & 1u) << (10 + (m >> (4 * k) & 15));
  return r;
}
__host__ __device__ constexpr u32 step_off(const Prm& P, u32 s0, u32 bh) {
  const u32 src = (s0 >> 5 & 3u) | bh << 2;
  return place(s0 ^ ((popc(src & P.M0) & 1) | (popc(src & P.M1) & 1) << 1 | (popc(src & P.M2) & 1) << 2 | (popc(src & P.M3) & 1) << 3 | (popc(src & P.M4) & 1) << 4), P.PM, 7);
}
constexpr Tab make_tab(const Prm& P) {
  Tab T{};
  for (int bid = 0; bid < 256; ++bid) {
    const int xcd = bid & 7, idx = bid >> 3, part = idx & 3, i8 = idx >> 2, wi = i8 * 8 + xcd;
    const bool has_mod = wi < 48, has_up = wi >= 21;
    const int cb0 = (i8 + 5) & 7, cb1 = (i8 + 4) & 7, ms = wi - 20;
    const int mi = wi < 8 ? wi : (wi < 16 ? 8 + ((xcd + 4) & 7) : (wi < 20 ? (wi - 14) * 8 + 3 : 16 + ms / 7 * 8 + ms % 7 + (ms % 7 >= 3 ? 1 : 0)));
    const int ui = xcd * 5 + (xcd > 5 ? xcd - 5 : 0) + (i8 >= 3 ? (xcd >= 5 ? 1 : 0) + i8 - 3 : 0);
    const int wd = cb0 == 3 ? 0 : P.WD;
    const int e[4] = {P.E0 - 4 * wd + (cb0 == 3 ? P.ES : 0), P.E1 + (cb1 == 3 ? P.ES : 0), has_mod ? (wi < 16 ? P.E2C : P.E2) + ((mi & 7) == 3 ? P.ES : 0) : 0, has_up ? P.EU * (8 - UD) / 8 : 0};
    const int ew = wd ? P.EW : 0, pa = (1024 + e[0] + ew + e[1] + e[2] + e[3] + (has_up ? P.EX : 0)) / 4;
    int c[5] = {0, 0, 0, 0, 128};
    for (int k = 0; k < 3; ++k) c[k + 1] = c[k] + (pa - e[k] - (k == 0 ? ew : 0) + 4) / 8;
    const int nl = c[part + 1] - c[part], wst = e[part] / 4;
    const bool has_w = part < 2 || (part == 2 ? has_mod : has_up);
    const int blk = !has_w ? 0 : (part == 0 ? cb0 : (part == 1 ? xcd * 8 + cb1 : (part == 2 ? mi : ui)));
    const int span = (part == 3 && has_up ? P.TEND - P.DT_UP : (part == 0 && wd ? P.TEND - P.DT_WO : P.TEND)) * 256;
    const int np = part == 0 ? 8 - wd : (part == 3 ? 8 - UD : 8), tau0 = span / (2 * nl + wst);
    const bool up = part == 3 && has_up;
    const int tau = tau0 * (up ? P.FU : (part == 0 && wd ? P.F0 : (has_w ? P.FK : P.FN))) / 64, tauw = tau0 * wst / np * (up ? P.FU : P.FW) / 64;
    int sw = KF, at = P.DS * 256 + (has_w ? np * tauw : 0);
    while (sw < NCMIN / 8 - 1 && at < P.TS * 256) { at += tau; ++sw; }
    const int tau2 = (span - at) / (2 * nl - sw);
    T.e[4 * bid] = (u32)(c[part] | nl << 7 | blk << 13 | (has_w ? 1 : 0) << 19 | (8 - np) << 20 | sw << 24);
    T.e[4 * bid + 1] = (u32)(tau | tauw << 16);
    if ((part == 1 && nl <= QP2 + QD + 1) || c[part] > 32 * part + KF0 || c[part + 1] < 32 * part + KF0 + 4) T.e[4 * bid] = 0;
    const u32 pitch = part == 0 ? 1024u : (part == 1 ? 8192u : (part == 2 ? 6144u : 2752u));
    T.e[4 * bid + 2] = (part == 0 ? xcd * 512u * pitch : 0u) + blk * (part == 3 ? 64u : 128u);
    T.e[4 * bid + 3] = pitch | (u32)tau2 << 16;
  }
  return T;
}
constexpr int step_range(const Prm& P, bool mx_) {
  int mx = 0, mn = 1 << 20;
  const Tab T = make_tab(P);
  for (int bid = 0; bid < 256; ++bid) { const int n = T.e[4 * bid] >> 7 & 63; mx = n > mx ? n : mx; mn = n < mn ? n : mn; }
  return mx_ ? mx : mn;
}
__constant__ __attribute__((aligned(64))) const Tab TAB = make_tab(PRM0);
static_assert(step_range(PRM0, true) <= NL && step_range(PRM0, false) >= NCMIN / 8, "a wave's key range does not fit its load steps");
static_assert(cons_end(SIDE + WV + 1) <= NCMIN / 8 && cons_end(UPREP) <= NCMIN / 8 && SIDE > MLS + 3, "steps every wave runs");
constexpr int NUP = FF / 32;
constexpr int SPIN_MAX = 1 << 17;

constexpr u32 WS_EPOCH = 0;
constexpr u32 G_TB = 1024;
constexpr u32 G_NT = 2048;
constexpr u32 G_MOD = 4096;
constexpr u32 G_Q = G_MOD + 3072 * 8;
constexpr u32 G_AG = G_Q + 4096 * 8;
constexpr u32 G_ML = G_AG + 4096;
constexpr u32 G_P = 81920;
constexpr u32 G_U = G_P + 65536;
__device__ __forceinline__ u32 dup(u32 o) { return G_U + ((o >> 13) << 14) + (o & 8191u); }
constexpr u32 G_XP = 0;
constexpr u32 G_OP = G_XP + 8 * 512 * 4;
constexpr u32 G_PB = G_U + 2 * (G_OP + 44 * 512 * 8);
constexpr u32 WS_END = G_PB + 32768;
static_assert(G_P % 16384 == 0 && G_PB % 16384 == 0 && G_ML + 4096 <= G_P, "layout");
constexpr u32 SENT = 0xffffffffu;
constexpr u32 OOB = 0x40000000u;
#define WST 0

constexpr int AUX_NT = 2, AUX_SC1 = 16;

__device__ __forceinline__ float lo_f(u32 v) { return __uint_as_float(v << 16); }
__device__ __forceinline__ float hi_f(u32 v) { return __uint_as_float(v & 0xffff0000u); }
__device__ __forceinline__ float rbf(float x) { return (float)(__bf16)x; }
__device__ __forceinline__ u32 pack_bf(float lo, float hi) {
  bf16x2 r; r[0] = (__bf16)lo; r[1] = (__bf16)hi; return __builtin_bit_cast(u32, r);
}
__device__ __forceinline__ float dot2(u32 k, u32 q, float acc) {
  return __builtin_amdgcn_fdot2_f32_bf16(__builtin_bit_cast(bf16x2, k), __builtin_bit_cast(bf16x2, q), acc, false);
}
template <int CTRL> __device__ __forceinline__ float dpp(float x) {
  return __builtin_bit_cast(float, __builtin_amdgcn_mov_dpp(__builtin_bit_cast(int, x), CTRL, 0xf, 0xf, true));
}
constexpr int XOR1 = 0xB1, XOR2 = 0x4E, XOR7 = 0x141, XOR8 = 0x128;
__device__ __forceinline__ float swap32_add(float a, float b) {
  auto t = __builtin_amdgcn_permlane32_swap(__float_as_uint(a), __float_as_uint(b), false, false);
  return __uint_as_float(t[0]) + __uint_as_float(t[1]);
}
__device__ __forceinline__ float swap16_add(float a, float b) {
  auto t = __builtin_amdgcn_permlane16_swap(__float_as_uint(a), __float_as_uint(b), false, false);
  return __uint_as_float(t[0]) + __uint_as_float(t[1]);
}
__device__ __forceinline__ float wave_sum(float x) {
  x += dpp<XOR1>(x); x += dpp<XOR2>(x); x += dpp<XOR7>(x); x += dpp<XOR8>(x);
  x = swap16_add(x, x);
  return swap32_add(x, x);
}
__device__ __forceinline__ float wave_max(float x) {
  x = fmaxf(x, dpp<XOR1>(x)); x = fmaxf(x, dpp<XOR2>(x)); x = fmaxf(x, dpp<XOR7>(x)); x = fmaxf(x, dpp<XOR8>(x));
  auto s = __builtin_amdgcn_permlane16_swap(__float_as_uint(x), __float_as_uint(x), false, false);
  x = fmaxf(__uint_as_float(s[0]), __uint_as_float(s[1]));
  auto t = __builtin_amdgcn_permlane32_swap(__float_as_uint(x), __float_as_uint(x), false, false);
  return fmaxf(__uint_as_float(t[0]), __uint_as_float(t[1]));
}
template <int CTRL> __device__ __forceinline__ float sel_add(float lo, float hi, bool up) {
  const float t1 = lo + dpp<CTRL>(lo), t2 = hi + dpp<CTRL>(hi);
  return up ? t2 : t1;
}
template <int I> __device__ __forceinline__ float bc16(float x) {
  return __builtin_bit_cast(float, __builtin_amdgcn_update_dpp(0, __builtin_bit_cast(int, x), 0x150 + I, 0xf, 0xf, false));
}
__device__ __forceinline__ float bcast16(float x, int i) {
  switch (i) {
    case 0: return bc16<0>(x); case 1: return bc16<1>(x); case 2: return bc16<2>(x); case 3: return bc16<3>(x);
    case 4: return bc16<4>(x); case 5: return bc16<5>(x); case 6: return bc16<6>(x); case 7: return bc16<7>(x);
    case 8: return bc16<8>(x); case 9: return bc16<9>(x); case 10: return bc16<10>(x); case 11: return bc16<11>(x);
    case 12: return bc16<12>(x); case 13: return bc16<13>(x); case 14: return bc16<14>(x); default: return bc16<15>(x);
  }
}
__device__ __forceinline__ float silu_f(float x) { return x * __builtin_amdgcn_rcpf(1.f + __expf(-x)); }

template <int AUX> __device__ __forceinline__ u32x4 ld16(rsrc_t r, u32 voff, u32 soff) {
  return __builtin_amdgcn_raw_buffer_load_b128(r, voff, soff, AUX);
}
template <int AUX> __device__ __forceinline__ u32x2 ld8(rsrc_t r, u32 voff, u32 soff) {
  return __builtin_amdgcn_raw_buffer_load_b64(r, voff, soff, AUX);
}
__device__ __forceinline__ void st8_sc1(rsrc_t r, u32 voff, u32 v, u32 tag) {
  __builtin_amdgcn_raw_buffer_store_b64(u32x2{v, tag}, r, voff, 0, AUX_SC1);
}
__device__ __forceinline__ void st16_sc1(rsrc_t r, u32 voff, u32 v0, u32 v1, u32 tag) {
  __builtin_amdgcn_raw_buffer_store_b128(u32x4{v0, tag, v1, tag}, r, voff, 0, AUX_SC1);
}
__device__ __forceinline__ rsrc_t mk_rsrc(const void* p, u32 bytes) {
  return __builtin_amdgcn_make_buffer_rsrc(const_cast<void*>(p), 0, bytes, 0x00020000);
}
#define WAIT_VM(n) __builtin_amdgcn_s_waitcnt(((n) & 15) | (((n) >> 4) << 14) | (7 << 4) | (15 << 8))
#define SB() __builtin_amdgcn_sched_barrier(0)
#define CFENCE() asm volatile("" ::: "memory")
__device__ __forceinline__ bool all_ok(bool ok) { return __builtin_amdgcn_ballot_w64(!ok) == 0ull; }

template <int N> __device__ __forceinline__ void gather16(rsrc_t ws, u32 voff, u32 stride, u32 tag, u32x4 (&g)[N]) {
#pragma nounroll
  for (int spin = 0; spin < SPIN_MAX; ++spin) {
    CFENCE();
    bool ok = true;
#pragma unroll
    for (int i = 0; i < N; ++i) g[i] = ld16<AUX_SC1>(ws, voff + i * stride, 0);
#pragma unroll
    for (int i = 0; i < N; ++i) ok &= (g[i][1] == tag) & (g[i][3] == tag);
    if (all_ok(ok)) break;
    __builtin_amdgcn_s_sleep(8);
  }
}
template <int N> __device__ __forceinline__ void gather8(rsrc_t ws, u32 voff, u32 stride, u32 tag, u32 (&g)[N], bool act = true) {
#pragma nounroll
  for (int spin = 0; spin < SPIN_MAX; ++spin) {
    CFENCE();
    bool ok = true;
    u32x2 t[N];
#pragma unroll
    for (int i = 0; i < N; ++i) t[i] = ld8<AUX_SC1>(ws, voff + i * stride, 0);
#pragma unroll
    for (int i = 0; i < N; ++i) { ok &= (t[i][1] == tag); g[i] = t[i][0]; }
    if (all_ok(ok | !act)) break;
    __builtin_amdgcn_s_sleep(8);
  }
}

struct Smem {
  float red[8][128];
  float ml[8][2];
  float r2[64 * 20];
  u16 hb[3 * 1024];
  float stat[8][64];
  float red2[8][128];
  u32 dummy[64];
  u32x2 bc;
  u32x2 ctl;
  u32x4 zero[4];
  u32x4 wbuf[8][8][64];
  __attribute__((aligned(256))) u16 w2b[32][512];
  u16 actl[2][32];
};

#define STAMP(k) do {} while (0)
#define WSTAMP(k) do {} while (0)
#define FINISH() return
#define PIN(x) asm volatile("" : "+v"(x))
#define LDS_BARRIER() do { __builtin_amdgcn_s_waitcnt(0xc07f); __builtin_amdgcn_s_barrier(); CFENCE(); } while (0)
#define WAVE_SYNC() do { __builtin_amdgcn_fence(__ATOMIC_RELEASE, "wavefront"); __builtin_amdgcn_wave_barrier(); __builtin_amdgcn_fence(__ATOMIC_ACQUIRE, "wavefront"); } while (0)

__device__ __forceinline__ void dma16(rsrc_t r, u32 voff, u32 soff, u32 lds_dst) {
  u32 keep;
  asm volatile("s_mov_b32 %0, m0\n\ts_mov_b32 m0, %4\n\ts_nop 4\n\tbuffer_load_dwordx4 %1, %2, %3 offen nt lds\n\ts_mov_b32 m0, %0"
               : "=&s"(keep) : "v"(voff), "s"(r), "s"(soff), "s"(lds_dst) : "memory");
}
__device__ __forceinline__ u32 perm_lo(u32 a, u32 b) { return __builtin_amdgcn_perm(b, a, 0x05040100u); }
__device__ __forceinline__ u32 perm_hi(u32 a, u32 b) { return __builtin_amdgcn_perm(b, a, 0x07060302u); }
__device__ __forceinline__ float rdl(float x, int l) { return __int_as_float(__builtin_amdgcn_readlane(__float_as_int(x), l)); }

typedef __attribute__((ext_vector_type(8))) __bf16 bf16x8;
__device__ __forceinline__ f32x4 mfma16(u32x4 a, u32x4 b, f32x4 c) {
  return __builtin_amdgcn_mfma_f32_16x16x32_bf16(__builtin_bit_cast(bf16x8, a), __builtin_bit_cast(bf16x8, b), c, 0, 0, 0);
}
__device__ __forceinline__ float sum8(const float* p) {
  const f32x4 a = *reinterpret_cast<const f32x4*>(p), b = *reinterpret_cast<const f32x4*>(p + 4);
  return ((a[0] + b[0]) + (a[1] + b[1])) + ((a[2] + b[2]) + (a[3] + b[3]));
}
__device__ __forceinline__ float ln_ri(float var) {
  return __builtin_amdgcn_rcpf(rbf(__builtin_amdgcn_sqrtf(rbf(rbf(fmaxf(var, 0.f)) + 1e-5f))));
}
__device__ __forceinline__ void ln_stats2(const float (&x0)[8], const float (&x1)[8], float& mb0, float& ri0, float& mb1, float& ri1) {
  const float k0 = rdl(x0[0], 0), k1 = rdl(x1[0], 0);
  float s0 = 0.f, s1 = 0.f, q0 = 0.f, q1 = 0.f;
#pragma unroll
  for (int k = 0; k < 8; ++k) {
    const float d0 = x0[k] - k0, d1 = x1[k] - k1;
    s0 += d0; s1 += d1; q0 = fmaf(d0, d0, q0); q1 = fmaf(d1, d1, q1);
  }
  float t = swap16_add(swap32_add(s0, s1), swap32_add(q0, q1));
  t += dpp<XOR1>(t); t += dpp<XOR2>(t); t += dpp<XOR7>(t); t += dpp<XOR8>(t);
  auto u = __builtin_amdgcn_permlane16_swap(__float_as_uint(t), __float_as_uint(t), false, false);
  const float ms = __uint_as_float(u[0]) * (1.f / 512.f), mq = __uint_as_float(u[1]) * (1.f / 512.f);
  const float ri = ln_ri(fmaf(-ms, ms, mq));
  mb0 = rbf(k0 + rdl(ms, 0)); mb1 = rbf(k1 + rdl(ms, 32));
  ri0 = rdl(ri, 0); ri1 = rdl(ri, 32);
}
template <int I> struct IC { static constexpr int v = I; };
template <int I, int N, class F> __device__ __forceinline__ void sfor(F&& f) {
  if constexpr (I < N) { if (f(IC<I>{})) sfor<I + 1, N>(f); }
}
typedef __attribute__((ext_vector_type(4))) short s16x4;
typedef __attribute__((address_space(3))) s16x4* lds_s16x4p;
__device__ __forceinline__ u32x2 tr_read(u32 lds_addr) {
  return __builtin_bit_cast(u32x2, __builtin_amdgcn_ds_read_tr16_b64_v4i16((lds_s16x4p)lds_addr));
}
__device__ __forceinline__ u32 next_tag(u32 e) { return ((e | 0x40004000u) + 1u) & ~0x40004000u; }
__device__ __forceinline__ float u2f(u32 v) { return __uint_as_float(v); }
#define LDSP(T, a) ((__attribute__((address_space(3))) T*)(a))
template <class T> __device__ __forceinline__ u32 lds_addr(T* p) { u32 a = (u32)(uintptr_t)p; PIN(a); return a; }

typedef __attribute__((ext_vector_type(16))) unsigned u32x16;
typedef __attribute__((ext_vector_type(8))) unsigned u32x8;
__global__ void __launch_bounds__(512, 2) fused_kernel(const u16* x_, const u16* kc_, const u16* vc_, const u16* wq_, const u16* wo_, const u16* w1_,
                                                       const u16* w3_, const u16* w2_, const u16* cnd_, const u16* wmod_, unsigned char* ws_, u16* out_ PRM_ARG) {
  __shared__ __attribute__((aligned(256))) Smem sm;
  constexpr Prm P = PRM0;
  const int TEND = P.TEND;
  const int bid = blockIdx.x, tid = threadIdx.x;
  u32x4 kr[NL];
  u32x16 ka; u32x8 kb; u32x4 tev; u64 pt0; u32 tch2;
  asm volatile("s_load_dwordx16 %0, %3, 0x0\n\ts_load_dwordx8 %1, %3, 0x40\n\ts_memrealtime %2"
               : "=&s"(ka), "=&s"(kb), "=&s"(pt0) : "s"(__builtin_amdgcn_kernarg_segment_ptr()));
  SB();
  {
    u64 pc; u32 vb;
    asm volatile("s_getpc_b64 %2\n.Lcpf_first:\n\tv_mul_u32_u24 %1, ((.Lend_c-.Lcpf_first)>>10)<<2, %3\n\tglobal_load_dword %0, %1, %2"
                 : "=&v"(tch2), "=&v"(vb), "=&s"(pc) : "v"((bid & ~7) + (tid >> 6)) : "memory");
    asm volatile("global_load_dwordx4 %0, %1, %2" : "=&v"(tev) : "v"(bid << 4), "s"(TABP) : "memory");
  }
  SB();
  const int xcd = bid & 7, idx = bid >> 3, part = idx & 3;
  const int b = idx >> 2 & 1, head = xcd * 4 + (((idx >> 3) & 1) << 1 | idx >> 4);
  const u32 so0 = step_off(P, 32 * part + KF0, b * 32 + head) & ~place(3, P.PM, 2);
  u32 va = (((b * 32 + head) * SK + (SK - WIN)) * DH * 2 + so0) | place(tid >> 6, P.WM, 3) | (tid & 63) << 4;
  const u32 kx1 = place(1, P.PM, 2), kx2 = place(2, P.PM, 2);
  PIN(va);
  u32 va1 = va | kx1, va2 = va | kx2, va3 = va | kx1 | kx2;
  PIN(va1); PIN(va2); PIN(va3);
  int mc = bid & 0xd8;
  asm volatile("" : "+v"(va), "+s"(mc));
  SB();
  asm volatile("s_waitcnt lgkmcnt(0)" : "+s"(ka), "+s"(kb), "+s"(pt0));
  SB();
#define K_FIRST "global_load_dwordx4 %0, %4, %5 nt\n\tglobal_load_dwordx4 %1, %6, %5 nt\n\tglobal_load_dwordx4 %2, %7, %5 nt\n\tglobal_load_dwordx4 %3, %8, %5 nt"
  asm volatile("s_cmp_eq_u32 %9, 16\n\ts_cbranch_scc1 .Lk_later\n\t" K_FIRST "\n.Lk_later:"
               : "=&v"(kr[0]), "=&v"(kr[1]), "=&v"(kr[2]), "=&v"(kr[3]) : "v"(va), "s"((u64)ka[2] | (u64)ka[3] << 32), "v"(va1), "v"(va2), "v"(va3), "s"(mc) : "memory", "scc");
  SB();
  const u32 tka = (u32)wall_clock64(), t0 = tka - P.D0 + (__builtin_amdgcn_readfirstlane(tid >> 6) * 2 - 7) * P.PW / 16;
  auto kap = [&](int k) { return k < 8 ? (u64)ka[2 * k] | (u64)ka[2 * k + 1] << 32 : (u64)kb[2 * k - 16] | (u64)kb[2 * k - 15] << 32; };
  const u16 *x = (const u16*)kap(0), *kc = (const u16*)kap(1), *vc = (const u16*)kap(2), *w2 = (const u16*)kap(7), *cnd = (const u16*)kap(8);
  unsigned char* ws = (unsigned char*)kap(10); u16* out = (u16*)kap(11);
  const int lane = tid & 63, wave = __builtin_amdgcn_readfirstlane(tid >> 6);
  const bool p3 = part == 3;
  const u64 wa = part == 0 ? kap(4) : (part == 1 ? kap(3) : (part == 2 ? kap(9) : ((wave & 1) ? kap(6) : kap(5))));
  const bool modc = mc == 16;
  if (modc) asm volatile("s_waitcnt vmcnt(0)" ::: "memory");
  asm volatile("s_waitcnt vmcnt(4)" : "+v"(tev));
  const u32 te0 = __builtin_amdgcn_readfirstlane(tev[0]), te1 = __builtin_amdgcn_readfirstlane(tev[1]);
  const u32 te3 = __builtin_amdgcn_readfirstlane(tev[3]), pitch = te3 & 0xffff, wsz = pitch << (part == 0 ? 12 : 9);
  const int sh = p3 ? 2 : 3;
  u32 wv0 = (u32)((p3 ? (wave >> 1) * 16 : wave * 64) + (lane >> sh)) * pitch + ((lane & (p3 ? 3 : 7)) ^ (lane >> (p3 ? 5 : 3)) ^ (p3 ? wave >> 2 << 1 : 0)) * 16u;
  const u32 wimg = (u32)(uintptr_t)(&sm.wbuf[0][0][0]);
  u32 wld = wimg + (p3 ? (wave & 1) * 32768 + (wave >> 1) * 1024 : wave * 8192);
  const int bl0 = te0 & 127, nl = te0 >> 7 & 63, blk = te0 >> 13 & 63;
  const bool has_w = te0 >> 19 & 1;
  const rsrc_t rw = mk_rsrc((const void*)wa, wsz);
  const u32 wstep = pitch << (p3 ? 6 : 3), lstep = p3 ? 4096u : 1024u;
  u32 wso = __builtin_amdgcn_readfirstlane(tev[2]);
  int wleft = 8;
  auto piece = [&]() {
    dma16(rw, wv0, wso, wld);
    wso += wstep; wld += lstep;
    --wleft;
  };
  int tau = te1 & 0xffff;
  const int tauw = te1 >> 16, sw = te0 >> 24;
  int tau2 = te3 >> 16;
  int due = P.DS * 256, now = 0;
  auto gate = [&]() {
    while (now < due) { now = (int)(((u32)wall_clock64() - t0) << 8); if (now < due) __builtin_amdgcn_s_sleep(1); }
    due += tau;
  };
  const rsrc_t wsr = mk_rsrc(ws, WS_END);
  const int g = lane >> 4, c = lane & 15;
  const int i8 = idx >> 2, wi = i8 * 8 + xcd;
  const bool is_up = p3 && has_w;
  const size_t base = ((size_t)(b * NH + head) * SK + (SK - WIN)) * DH;
  const rsrc_t rk = mk_rsrc(kc + base, WIN * DH * 2), rv = mk_rsrc(vc + base, WIN * DH * 2);
  u32 sot;
  {
    u32 s0 = bl0 + lane - KF;
    s0 += s0 >= (u32)(32 * part + KF0) ? KF : 0;
    sot = (lane < KF ? so0 | place(lane, P.PM, 2) : step_off(P, s0, b * 32 + head)) | place(wave, P.WM, 3);
    sot = lane < nl ? sot : OOB;
    PIN(sot);
  }
  auto until = [&](int t) { while ((int)(((u32)wall_clock64() - t0) << 8) < t); };
  u32 near = 0;
  int slack = 0;
  const rsrc_t rx = mk_rsrc(part == 2 ? cnd : x, (part == 1 || (part == 2 && has_w)) ? 2048u : 0u);
  u32x2 ep; u32 xs0, xs1, tch1; u32x4 xr0, xr1;
  auto small_ops = [&]() {
    ep = ld8<AUX_SC1>(wsr, wave == 7 ? WS_EPOCH : OOB, 0);
    xs0 = __builtin_amdgcn_raw_buffer_load_b16(rx, tid * 2, 0, 0); xs1 = __builtin_amdgcn_raw_buffer_load_b16(rx, tid * 2, 1024, 0);
  };
  auto small_ops2 = [&]() {
    xr0 = ld16<0>(rx, part == 1 ? lane * 16 : OOB, 0); xr1 = ld16<0>(rx, part == 1 ? lane * 16 : OOB, 1024);
    tch1 = __builtin_amdgcn_raw_buffer_load_b32(mk_rsrc(w2 + (size_t)blk * 32 * 512, is_up ? 64u : 0u), tid == 384 ? 0 : OOB, 0, AUX_NT);
  };
  u32 tag = 0;
  u32x4 bf[8];
  auto wo_prep = [&](int s0 = 0, int s1 = 2) {
    const u32 wi_ = wimg + wave * 8192;
    const int q = (lane >> 2) & 3, pp = lane & 3;
#pragma unroll
    for (int s = s0; s < s1; ++s) {
#pragma unroll
      for (int t = 0; t < 4; ++t) {
        const int r0 = s * 32 + g * 8 + q, r1 = r0 + 4, ch = t * 2 + (pp >> 1);
        const u32x2 lo = tr_read(wi_ + r0 * 128 + ((ch ^ (r0 & 7)) * 16) + (pp & 1) * 8), hi = tr_read(wi_ + r1 * 128 + ((ch ^ (r1 & 7)) * 16) + (pp & 1) * 8);
        bf[s * 4 + t] = u32x4{lo[0], lo[1], hi[0], hi[1]};
      }
    }
  };
  auto gemv_go = [&](u32 hh) {
    sm.hb[tid] = (u16)hh; sm.hb[512 + tid] = (u16)(hh >> 16);
    WAVE_SYNC();
    const u32x4* a = (c & 11) == 0 ? reinterpret_cast<const u32x4*>(&sm.hb[(c >> 2) * 512 + wave * 64 + g * 8]) : &sm.zero[0];
    const u32x4 a0 = a[0], a1 = a[(c & 11) == 0 ? 4 : 0];
    const f32x4 z = {0.f, 0.f, 0.f, 0.f};
    f32x4 d[4];
#pragma unroll
    for (int t = 0; t < 4; ++t) d[t] = mfma16(a0, bf[t], z);
#pragma unroll
    for (int t = 0; t < 4; ++t) d[t] = mfma16(a1, bf[4 + t], d[t]);
    if (lane < 32) {
#pragma unroll
      for (int t = 0; t < 4; ++t) sm.r2[(t * 16 + c) * 20 + g * 8 + wave] = d[t][0];
    }
  };
  auto mod_gemv = [&]() { gemv_go(pack_bf(silu_f(lo_f(xs0)), silu_f(lo_f(xs1)))); };
  auto mod_put = [&]() { st8_sc1(wsr, G_MOD + (blk * 64 + lane) * 8, pack_bf(sum8(&sm.r2[lane * 20]), sum8(&sm.r2[lane * 20 + 8])), tag); };
  auto zero_rows = [&]() { if (lane < 4) sm.zero[lane] = u32x4{0u, 0u, 0u, 0u}; };
  const u32 pbo = G_PB + (wi >> 3) * 16384 + (wi & 7) * 1024;
  u32 l16 = lane * 16;
  asm volatile("" : "+v"(l16), "+v"(wv0) : "s"(rk), "s"(rx), "s"(wsr));
  SB();
  if (has_w) {
    if (__builtin_expect(modc, 0)) {
      now = 1 << 30;
      __builtin_amdgcn_raw_buffer_store_b128(u32x4{SENT, SENT, SENT, SENT}, wsr, tid < 384 ? G_P + wi * 6144 + tid * 16 : pbo + (wave & 1) * 8192 + lane * 16, 0, AUX_SC1);
      __builtin_amdgcn_raw_buffer_store_b128(u32x4{SENT, SENT, SENT, SENT}, wsr, tid < 16 ? G_ML + wi * 256 + tid * 16 : (tid < 24 ? G_NT + wi * 128 + (tid - 16) * 16 : (tid < 32 && wi < 3 ? G_AG + wi * 128 + (tid - 24) * 16 : OOB)), 0, AUX_SC1);
    }
#pragma nounroll
    while (wleft > (int)(te0 >> 20 & 7)) { gate(); due += tauw - tau; piece(); }
    now = 0;
  }
  SB();
  STAMP(23);
  if (__builtin_expect(modc, 0)) {
    small_ops();
    zero_rows();
    {
      until(P.PT * 256);
      const u64 pa = (u64)ws + pbo + wave * 128 + (wave & 1) * 8192;
      u64 c0, c1, c2; u32 d0, d1;
      asm volatile("s_memtime %0\n\ts_waitcnt lgkmcnt(0)\n\ts_load_dword %3, %5, 0x0\n\ts_waitcnt lgkmcnt(0)\n\ts_memtime %1\n\ts_waitcnt lgkmcnt(0)\n\t"
                   "s_load_dword %4, %6, 0x0\n\ts_waitcnt lgkmcnt(0)\n\ts_memtime %2\n\ts_waitcnt lgkmcnt(0)"
                   : "=&s"(c0), "=&s"(c1), "=&s"(c2), "=&s"(d0), "=&s"(d1) : "s"(pa), "s"(pa + 8192 - (wave & 1) * 16384) : "memory");
      const int df = min(max((int)(2 * (u32)c1 - (u32)c0 - (u32)c2), -600), 600);
      if (lane == 0) sm.stat[0][wave] = (float)(((wave ^ (xcd >> 2)) & 1) ? -df : df);
    }
    asm volatile("s_waitcnt vmcnt(0)" ::: "memory");
    tag = next_tag(__builtin_amdgcn_readfirstlane(ep[0]));
    wo_prep();
    mod_gemv();
    LDS_BARRIER();
    if (wave == 7) { mod_put(); if (lane == 0) st8_sc1(wsr, G_AG + (48 + wi) * 8, __float_as_uint(sum8(&sm.stat[0][0])), tag); }
    STAMP(9);
    __builtin_amdgcn_s_barrier();
    asm volatile(K_FIRST :: "v"(kr[0]), "v"(kr[1]), "v"(kr[2]), "v"(kr[3]), "v"(va), "s"((u64)ka[2] | (u64)ka[3] << 32), "v"(va1), "v"(va2), "v"(va3) : "memory");
  }
  SB();
  const int fd = wi * 8 + (lane & 7);
  const int ub = wave >> 2, ud = (wave & 3) * 128 + c * 8 + g * 2;
  u32x4 sg[3]; u32 sx2;
  auto side_loads = [&]() {
    const bool fin = part == 1 && wave == 0;
    sg[0] = ld16<AUX_SC1>(wsr, is_up ? G_MOD + (1024 + ud) * 8 : (fin ? G_MOD + (1024 + fd) * 8 : OOB), 0);
    sg[1] = ld16<AUX_SC1>(wsr, is_up ? G_MOD + (1536 + ud) * 8 : (fin ? G_MOD + (2560 + fd) * 8 : OOB), 0);
    sg[2] = ld16<AUX_SC1>(wsr, is_up ? G_MOD + (2048 + ud) * 8 : OOB, 0);
    sx2 = __builtin_amdgcn_raw_buffer_load_b32(mk_rsrc(x, (is_up || fin) ? 2048u : 0u), is_up ? ub * 1024 + ud * 2 : (g & 1) * 1024 + (fd >> 1) * 4, 0, 0);
  };
  auto side_pin = [&]() { PIN(sg[0]); PIN(sg[1]); PIN(sg[2]); PIN(sx2); };
  const u32 mlo = G_ML + (head * 2 + b) * 64 + (lane & 3) * 16;
  float fmrg = 0.f;
  bool fok = wave != 0;
  auto merge_factor = [&](const u32x4& ml) {
    const float mi = u2f(ml[0]);
    float M = fmaxf(mi, dpp<XOR1>(mi)); M = fmaxf(M, dpp<XOR2>(M));
    const float e = __expf(mi - M);
    float L = e * u2f(ml[2]);
    L += dpp<XOR1>(L); L += dpp<XOR2>(L);
    fmrg = rdl(e * __builtin_amdgcn_rcpf(L), part);
  };
  f32x2 ga = {0.f, 0.f}, xin = {0.f, 0.f}, cc1 = {0.f, 0.f};
  float Af = 0.f, Bf = 0.f;
  const int hpos = ub * 512 + ud;
  auto up_prep = [&]() {
    if (!all_ok((sg[0][1] == tag) & (sg[0][3] == tag) & (sg[1][1] == tag) & (sg[1][3] == tag) & (sg[2][1] == tag) & (sg[2][3] == tag))) gather16<3>(wsr, G_MOD + (1024 + ud) * 8, 512 * 8, tag, sg);
    ga = f32x2{ub ? hi_f(sg[0][0]) : lo_f(sg[0][0]), ub ? hi_f(sg[0][2]) : lo_f(sg[0][2])};
    cc1 = f32x2{rbf(1.f + (ub ? hi_f(sg[2][0]) : lo_f(sg[2][0]))), rbf(1.f + (ub ? hi_f(sg[2][2]) : lo_f(sg[2][2])))};
    xin = f32x2{lo_f(sx2), hi_f(sx2)};
    u32* hp = reinterpret_cast<u32*>(&sm.hb[hpos]);
    hp[512] = pack_bf(cc1[0], cc1[1]);
    hp[1024] = ub ? perm_hi(sg[1][0], sg[1][2]) : perm_lo(sg[1][0], sg[1][2]);
  };
  auto up_frags = [&](int r0, int r1) {
    const int pp = lane & 3;
    const u32 a = wimg + (pp & 1) * 32768 + ((pp >> 1) * 32 + g * 8 + ((lane >> 2) & 3)) * 64 + (((wave >> 1) ^ ((g & 1) | (pp & 2))) * 16) + (wave & 1) * 8;
#pragma unroll
    for (int rb = r0; rb < r1; ++rb) {
      const u32x2 lo = tr_read(a + rb * 4096), hi = tr_read(a + rb * 4096 + 256);
      bf[rb] = u32x4{lo[0], lo[1], hi[0], hi[1]};
    }
  };
  const u32x4* ap = reinterpret_cast<const u32x4*>(&sm.hb[((lane >> 3) & 1) * 512 + (lane & 1) * 32 + g * 8]);
  const bool par = lane & 8;
  auto col_sums = [&](int r0, int r1) {
    const u32x4* a1 = ap + 128 + ((lane >> 1) & 1) * 128;
    f32x4 d0 = {0.f, 0.f, 0.f, 0.f}, d1 = {0.f, 0.f, 0.f, 0.f};
#pragma unroll
    for (int rb = r0; rb < r1; ++rb) { if (rb & 1) d1 = mfma16(a1[rb * 8], bf[rb], d1); else d0 = mfma16(a1[rb * 8], bf[rb], d0); }
    Af += par ? d0[1] + d1[1] : d0[0] + d1[0]; Bf += par ? d0[3] + d1[3] : d0[2] + d1[2];
    if (r1 == 8) { Af += dpp<XOR8>(Af); Bf += dpp<XOR8>(Bf); }
  };
  float l0 = 0.f, l1 = 0.f;
  u32x2 gmd[2], tbg;
  const bool modw = part == 2 && has_w && !modc;
  const int qp = part != 1 ? -99 : ((blk & 7) == 3 ? QP1 : QP2);
  static_assert(QP1 + QD < QP2 && QP1 + QD > WUSE && QP1 + QD > TBU && WUSE + 1 < TBU, "order of the q steps");
  int t1 = 0;
#pragma unroll
  for (int i = NCMIN / 8; i < NL; ++i) kr[i] = u32x4{0u, 0u, 0u, 0u};
  sfor<KF, NL>([&](auto ic) {
    constexpr int i = ic.v;
    if (i >= NCMIN / 8 && i >= nl) return false;
    const u32 so = __builtin_amdgcn_readlane(sot, i);
    if constexpr (i < NCMIN / 8) tau = i == sw ? tau2 : tau;
    gate();
    SB();
    kr[i] = ld16<AUX_NT>(rk, l16, so);
    if constexpr (i == KF) small_ops();
    if constexpr (i == KF + 1) small_ops2();
    if constexpr (i == KF + 2) zero_rows();
    if constexpr (i == TBL) tbg = ld8<AUX_SC1>(wsr, wave == 7 ? G_TB + (lane & 7) * 8 : OOB, 0);
    if constexpr (i == QP1 || i == QP2) { gmd[0] = ld8<AUX_SC1>(wsr, i == qp ? G_MOD + tid * 8 : OOB, 0); gmd[1] = ld8<AUX_SC1>(wsr, i == qp ? G_MOD + tid * 8 : OOB, 4096); }
    SB();
    SB();
    if (i >= WK) { WAIT_VM(win_k(i)); SB(); }
    if (i == KF) STAMP(25);
    if (i == 8) STAMP(12);
    if (i == 20) STAMP(21);
    if (i == 25) STAMP(22);
    if (i == TBP) {
      if (part == 3 && i8 == 0 && wave == 7) { tag = next_tag(__builtin_amdgcn_readfirstlane(ep[0])); if (lane == 0) st8_sc1(wsr, G_TB + xcd * 8, (u32)pt0 + (((tka - (u32)pt0) * P.GA) >> 3) - P.GS, tag); }
      SB();
    }
    if (i == TBU - 1) {
      if (wave == 7) {
        const bool ok = tbg[1] == tag;
        float lf = ok ? (float)min(max((int)(t0 - tbg[0]), -300), 300) : 0.f, nf = ok ? 1.f : 0.f;
        float mn = ok ? lf : 300.f;
        lf += dpp<XOR1>(lf); nf += dpp<XOR1>(nf); mn = fminf(mn, dpp<XOR1>(mn)); lf += dpp<XOR2>(lf); nf += dpp<XOR2>(nf); mn = fminf(mn, dpp<XOR2>(mn));
        lf *= __builtin_amdgcn_rcpf(fmaxf(nf, 1.f));
        const float ot = dpp<XOR7>(lf), av = (lf + ot) * 0.5f;
        mn = fminf(mn, dpp<XOR7>(mn));
        if (lane == 0) sm.bc = u32x2{tag, t0 - (u32)(int)(av - fabsf(lf - ot) * (P.BT / 32.f) + (fminf(mn, av) - av) * (P.GM / 16.f))};
      }
      SB();
    }
    if (i == TBU) {
      LDS_BARRIER();
      const u32x2 bc = sm.bc;
      tag = __builtin_amdgcn_readfirstlane(bc[0]);
      t1 = (__builtin_amdgcn_readfirstlane((int)(bc[1] - t0)) + TEND + P.X1) * 256;
      const int dt = __builtin_amdgcn_readfirstlane((int)((float)(int)(t0 - bc[1]) * 256.f * __builtin_amdgcn_rcpf((float)(2 * nl - TBU - 2))));
      tau -= dt; tau2 -= dt;
      SB();
    }
    if (i == TBU + 1) {
      if (modw) { if (wave == 6) mod_put(); STAMP(9); }
      SB();
    }
    if (i == LNS) {
      tag = next_tag(__builtin_amdgcn_readfirstlane(ep[0]));
      if (part == 1) {
        float x0[8], x1[8], mb0, ri0, mb1, ri1;
#pragma unroll
        for (int k = 0; k < 4; ++k) { x0[2 * k] = lo_f(xr0[k]); x0[2 * k + 1] = hi_f(xr0[k]); x1[2 * k] = lo_f(xr1[k]); x1[2 * k + 1] = hi_f(xr1[k]); }
        ln_stats2(x0, x1, mb0, ri0, mb1, ri1);
        l0 = rbf(rbf(lo_f(xs0) - mb0) * ri0); l1 = rbf(rbf(lo_f(xs1) - mb1) * ri1);
      }
      PIN(l0); PIN(l1);
      SB();
    }
    if (i == WUSE) {
      PIN(tch1); PIN(tch2);
      static_assert(WUSE - WK >= KF, "the weight pieces are older than the steps outside the window");
      CFENCE();
      if (part == 1 || modw) wo_prep();
#pragma unroll
      for (int k = 0; k < 8; ++k) PIN(bf[k]);
      SB();
    }
    if (i == WUSE + 1) {
      if (modw) mod_gemv();
      SB();
    }
    if (i == QP1 + QD || i == QP2 + QD) {
      if (i == qp + QD) {
        u32 gm[2] = {gmd[0][0], gmd[1][0]};
        if (!all_ok((gmd[0][1] == tag) & (gmd[1][1] == tag))) { STAMP(11); gather8<2>(wsr, G_MOD + tid * 8, 512 * 8, tag, gm); }
        STAMP(5);
        gemv_go(pack_bf(rbf(l0 * rbf(1.f + lo_f(gm[1]))) + lo_f(gm[0]), rbf(l1 * rbf(1.f + hi_f(gm[1]))) + hi_f(gm[0])));
      }
      SB();
    }
    if (i == QP1 + QD + 1 || i == QP2 + QD + 1) {
      if (i == qp + QD + 1) {
        LDS_BARRIER();
        if (wave == 0) {
          const int bb = lane >> 5, jq = lane & 31;
          st8_sc1(wsr, G_Q + (bb * 2048 + blk * 32 + jq) * 8, pack_bf(sum8(&sm.r2[2 * jq * 20 + bb * 8]), sum8(&sm.r2[(2 * jq + 1) * 20 + bb * 8])), tag);
        }
        STAMP(6);
      }
      SB();
    }
    return true;
  });
  PIN(sot);
  const u32 qoff = G_Q + ((b * 32 + head) * 64 + c * 4) * 8;
  u32x4 qg[2], vr[NL], fl2[3];
  u32 qq[4];
  constexpr int NB = (NL + 15) / 16;
  float xlo, xs[4], sc[NB], pr[NB], m = 0.f;
  f32x4 acc = {0.f, 0.f, 0.f, 0.f};
  u32 pb[NB], pm[4], cpf;
  u32x2 agr;
  const u32 adst0 = lds_addr(lane < 32 ? &sm.red[wave][c * 8 + g * 4] : &sm.r2[(lane - 32) * 4]);
  u32 pso, po, rdst, rsrc, xo, l2, v2[4];
  rsrc_t r2;
  auto wo_addr = [&]() {
    po = G_P + (xcd & 3) * 16384 + near + (c >> 2 & 1) * 4096 + ((wave >> 1) * 4 + (c & 3)) * 256 + (wave & 1) * 128 + (c >> 3) * 64 + g * 16;
    rdst = lane < 32 ? (u32)(uintptr_t)&sm.red2[wave][0] + lane * 16 : (u32)(uintptr_t)&sm.r2[0] + (lane - 32) * 16;
    rsrc = (u32)(uintptr_t)&sm.red2[0][((wave & 1) * 16 + (lane & 15)) * 4 + (lane >> 4)];
    xo = dup(G_XP + xcd * 2048 + (wave & 1) * 1024 + (blk * 64 + (lane & ~1)) * 2) + (lane & 1) * 8192;
    pso = wave == 0 ? G_P + (xcd & 3) * 16384 + near + b * 4096 + ((head & 3) * 4 + part) * 256 + lane * 4 : OOB;
    PIN(pso); PIN(po); PIN(rdst); PIN(rsrc); PIN(xo);
  };
  auto w2_addr = [&]() {
    r2 = mk_rsrc(w2 + (size_t)(blk * 32 + wave * 4) * 512, 4096u);
    l2 = (u32)(uintptr_t)(&sm.w2b[wave * 4][0]);
#pragma unroll
    for (int n = 0; n < 4; ++n) { v2[n] = (lane ^ (n * 2) ^ ((wave & 2) << 2)) * 16 + n * 1024; PIN(v2[n]); }
    asm volatile("" : "+s"(r2), "+s"(l2));
  };
  const u32 nadd = is_up ? P.LU : (part == 0 && wleft ? P.LW : 0);
  u32 nto = tid == 0 ? G_NT + bid * 8 : OOB;
  PIN(nto);
  auto notices = [&](auto nc_, u32 off, u32x4 (&gn)[nc_.v]) -> int {
    constexpr int N = nc_.v;
#pragma nounroll
    for (int spin = 0; spin < SPIN_MAX; ++spin) {
      bool ok = true;
#pragma unroll
      for (int i = 0; i < N; ++i) ok &= (gn[i][1] == tag) & (gn[i][3] == tag);
      if (all_ok(ok)) break;
      __builtin_amdgcn_s_sleep(10);
      CFENCE();
#pragma unroll
      for (int i = 0; i < N; ++i) gn[i] = ld16<AUX_SC1>(wsr, off, i * 1024);
    }
    int mx = 0;
#pragma unroll
    for (int i = 0; i < N; ++i) mx = max(mx, max((int)(gn[i][0] - t0), (int)(gn[i][2] - t0)));
    return __builtin_amdgcn_readfirstlane((int)wave_max((float)min(mx, 1 << 20)));
  };
  const int side = nl >= SIDE + 13 ? SIDE + 8 : (nl >= SIDE + 9 ? SIDE + 4 : SIDE);
  sfor<0, NL + WV>([&](auto ic) {
    constexpr int v = ic.v;
    if (cons_end(v - 1) >= NCMIN / 8 && cons_end(v - 1) >= nl) return false;
    if constexpr (v < NL) {
      const u32 so = __builtin_amdgcn_readlane(sot, v);
      if constexpr (v >= 12) { if (v == nl - P.NE) tau = (tau * (nadd ? P.FE2 : P.FE)) >> 4; }
      if constexpr (v >= NCMIN / 8) { if (v < nl) gate(); } else gate();
      SB();
      int bk = 0;
      if constexpr (v == AR) { if (part == 1 && wi < 48 && wave == 7) bk = ((int)(((u32)wall_clock64() - t0) << 8) - due + tau + (int)__builtin_amdgcn_s_getreg(7 | (3 << 11)) * tau) >> 8; SB(); }
      vr[v] = ld16<AUX_NT>(rv, l16, so);
      if constexpr (v == AR) {
        SB();
        if (part == 1 && wi < 48 && tid == 448) st8_sc1(wsr, G_AG + wi * 8, (u32)min(max(bk, 0), 250), tag);
      }
      SB();
      SB();
    }
    if constexpr (v == NCMIN / 8 - 1) {
#pragma unroll
      for (int i = NCMIN / 8; i < NL; ++i) asm volatile("" : "=v"(vr[i]));
      asm volatile("" : "=v"(fl2[1]), "=v"(fl2[2]));
    }
    if constexpr (v == VQ - 4) { qg[0] = ld16<AUX_SC1>(wsr, qoff, 0); qg[1] = ld16<AUX_SC1>(wsr, qoff + 16, 0); }
    if constexpr (v == VSIDE) side_loads();
    if constexpr (v == AL) agr = ld8<AUX_SC1>(wsr, wave == 7 ? G_AG + lane * 8 : OOB, 0);
    if constexpr (v == CPF) {
      u64 pc; u32 dlt, len;
      asm volatile("s_getpc_b64 %0\n.Lcpf_here:\n\ts_mov_b32 %1, .Ltail_start-.Lcpf_here\n\ts_mov_b32 %2, .Lend_a-.Ltail_start\n\ts_max_i32 %2, %2, .Lend_b-.Ltail_start\n\ts_max_i32 %2, %2, .Lend_c-.Ltail_start\n\ts_max_i32 %2, %2, .Lend_d-.Ltail_start\n\ts_max_i32 %2, %2, .Lend_e-.Ltail_start" : "=s"(pc), "=s"(dlt), "=s"(len) :: "scc");
      pc += dlt;
      const rsrc_t rc = mk_rsrc((const void*)pc, (part == 3 && i8 == 1) ? len : 0u);
      cpf = __builtin_amdgcn_raw_buffer_load_b32(rc, tid * 64, 0, 0);
    }
    if constexpr (v == CPF + WV + 1) PIN(cpf);
    if constexpr (v == SIDE || v == SIDE + 4 || v == SIDE + 8) fl2[(v - SIDE) / 4] = ld16<AUX_SC1>(wsr, (wave == 0 && v == side) ? mlo : OOB, 0);
    SB();
    if constexpr (v < WV) { WAIT_VM(win_v(v, WK)); SB(); } else if constexpr (v < NL && cons_end(v) < v - WV + 1) { WAIT_VM(win_v(v, WV)); SB(); }
    if constexpr (v == VQ) {
      if (!all_ok((qg[0][1] == tag) & (qg[0][3] == tag) & (qg[1][1] == tag) & (qg[1][3] == tag))) { STAMP(20); gather16<2>(wsr, qoff, 16, tag, qg); }
      STAMP(1); WSTAMP(1);
      qq[0] = qg[0][0]; qq[1] = qg[0][2]; qq[2] = qg[1][0]; qq[3] = qg[1][2];
    }
    if constexpr (v >= VQ && v < VS) {
      constexpr int u0 = (v - VQ) * UPS, u1 = v == VS - 1 ? (NL + 7) / 8 * 8 : u0 + UPS;
      sfor<u0, u1>([&](auto uc) {
        constexpr int u = uc.v, p = u & 7, i = (p & 1) * 4 + (p & 2) + (p >> 2), sl = u >> 3, bt = sl >> 1;
        float d = 0.f;
        if constexpr (sl * 8 + i < NL) {
#pragma unroll
          for (int k = 0; k < 4; ++k) d = dot2(kr[sl * 8 + i][k], qq[k], d);
        }
        if constexpr (p & 1) xs[i - 4] = sel_add<XOR7>(xs[i - 4], d, c & 4); else xs[i] = d;
        if constexpr ((p & 3) == 3) xs[p >> 2] = sel_add<XOR2>(xs[p >> 2], xs[(p >> 2) + 2], c & 2);
        if constexpr (p == 7) {
          xs[0] = sel_add<XOR1>(xs[0], xs[1], c & 1);
          if constexpr ((sl & 1) || u == (NL + 7) / 8 * 8 - 1) {
            const float t = (sl & 1) ? sel_add<XOR8>(xlo, xs[0], c & 8) : xs[0] + dpp<XOR8>(xs[0]);
            const float s = rbf(rbf(t) * 0.08838834764831845f);
            sc[bt] = (bt * 16 + c < nl) ? s : -__builtin_inff();
            PIN(sc[bt]);
          } else {
            xlo = xs[0];
            PIN(xlo);
          }
        }
        return true;
      });
      constexpr int pe = u1 & 7;
      if constexpr (pe >= 1) PIN(xs[0]);
      if constexpr (pe == 3) PIN(xs[2]);
      if constexpr (pe >= 5) PIN(xs[1]);
      if constexpr (pe == 7) PIN(xs[3]);
    }
    if constexpr (v == VS) {
      float mloc = sc[0];
#pragma unroll
      for (int bt = 1; bt < NB; ++bt) mloc = fmaxf(mloc, sc[bt]);
      m = fmaxf(wave_max(mloc), -3.0e38f);
      float ls = 0.f;
#pragma unroll
      for (int bt = 0; bt < NB; ++bt) { pr[bt] = __expf(sc[bt] - m); ls += pr[bt]; }
      const float l = wave_sum(ls);
      if (lane == 0) { sm.ml[wave][0] = m; sm.ml[wave][1] = l; }
      STAMP(2); WSTAMP(2);
    }
    sfor<cons_end(v - 1), cons_end(v)>([&](auto jc) {
      constexpr int i = jc.v;
      if (i < NCMIN / 8 || i < nl) {
        const u32 pv = __float_as_uint(bcast16(u2f(pb[i >> 4]), i & 15));
        acc = mfma16(u32x4{pv & pm[0], pv & pm[1], pv & pm[2], pv & pm[3]}, vr[i], acc);
      }
      PIN(acc);
      return true;
    });
    if constexpr (v == VS - 1) { side_pin(); if (is_up) up_prep(); PIN(ga); PIN(xin); PIN(cc1); }
    if constexpr (v == MLS - 1) {
      if (wave == 7) {
        const float am = wave_sum(lane < 48 ? (agr[1] == tag ? (float)agr[0] : 120.f) : 0.f) * (1.f / 48.f);
        u32 vt[1] = {agr[0]};
        if (!all_ok(lane < 48 || agr[1] == tag)) gather8<1>(wsr, G_AG + lane * 8, 0, tag, vt, lane >= 48);
        float sv = u2f(vt[0]);
        sv += dpp<0x124>(sv); sv += dpp<XOR8>(sv);
        float sa = sv + dpp<XOR1>(sv);
        sv = fmaf(sa + dpp<XOR2>(sa), 0.25f, sv);
        float cf = fabsf(sv);
        cf = fminf(cf, dpp<XOR1>(cf)); cf = fminf(cf, dpp<XOR2>(cf));
        if (lane == 48 + (xcd & 3)) sm.ctl = u32x2{(u32)min(max((int)((am - (float)P.A0) * (float)P.AG * 16.f), 0), P.AC * 256), (((sv > 0.f) ^ (xcd >> 2) ^ P.NR) & 1) * 8192u + (cf < 2500.f ? (u32)P.SL : 0u)};
      }
      PIN(agr);
#pragma unroll
      for (int k = 0; k < 4; ++k) { pm[k] = (c >> 1) == k ? ((c & 1) ? 0xffff0000u : 0x0000ffffu) : 0u; PIN(pm[k]); }
    }
    if constexpr (v == UPREP - 2) w2_addr();
    if constexpr (v == UPREP - 1) wo_addr();
    if constexpr (v == UPREP) {
#pragma unroll
      for (int k = 0; k < 8; ++k) asm volatile("" : "=v"(bf[k]));
      if (part == 0) wo_prep(0, 1); else if (is_up) up_frags(0, 8 - UD);
#pragma unroll
      for (int k = 0; k < 8; ++k) PIN(bf[k]);
    }
    if constexpr (v == UPREP + 1 || v == UPREP + 2) {
      if (is_up) { if constexpr (v == UPREP + 1) col_sums(0, (8 - UD) / 2); else col_sums((8 - UD) / 2, 8 - UD); }
      PIN(Af); PIN(Bf);
    }
    if constexpr (v == SIDE + WV + 1 || v == SIDE + WV + 5 || v == SIDE + WV + 9) {
      if (wave == 0 && v == side + WV + 1) {
        const u32x4 ml = fl2[(v - SIDE - WV - 1) / 4];
        fok = all_ok((ml[1] == tag) & (ml[3] == tag));
        merge_factor(ml);
      }
    }
    if (v % 6 == 4 && v >= 10 && v < 30) STAMP(15 + v / 6);
    if constexpr (v == MLS) {
      LDS_BARRIER();
      float mm[8], ll[8];
#pragma unroll
      for (int w = 0; w < 8; ++w) { const f32x2 t = *reinterpret_cast<const f32x2*>(&sm.ml[w][0]); mm[w] = t[0]; ll[w] = t[1]; }
      float Mwg = mm[0], Lwg = 0.f;
#pragma unroll
      for (int w = 1; w < 8; ++w) Mwg = fmaxf(Mwg, mm[w]);
      if (wave == 0) {
#pragma unroll
        for (int w = 0; w < 8; ++w) Lwg = fmaf(__expf(mm[w] - Mwg), ll[w], Lwg);
        if (lane == 0) st16_sc1(wsr, G_ML + ((head * 2 + b) * 4 + part) * 16, __float_as_uint(Mwg), __float_as_uint(Lwg), tag);
      }
      {
        const u32x2 ct = sm.ctl;
        const int st = __builtin_amdgcn_readfirstlane(ct[0]);
        near = __builtin_amdgcn_readfirstlane(ct[1]);
        slack = (near & 255u) * 256; near &= 8192u;
        t1 += st;
        tau += __builtin_amdgcn_readfirstlane((int)((float)st * __builtin_amdgcn_rcpf((float)(nl - MLS - 3))));
      }
      const float fw = __expf(m - Mwg);
#pragma unroll
      for (int bt = 0; bt < NB; ++bt) { pb[bt] = pack_bf(pr[bt] * fw, pr[bt] * fw); PIN(pb[bt]); }
    }
    SB();
    return true;
  });
  asm volatile(".Ltail_start:");
  *LDSP(f32x4, adst0) = acc;
  STAMP(14); WSTAMP(14);
  if (!fok) {
    u32x4 ml;
#pragma nounroll
    for (int spin = 0; spin < SPIN_MAX; ++spin) {
      CFENCE();
      ml = ld16<AUX_SC1>(wsr, mlo, 0);
      if (all_ok((ml[1] == tag) & (ml[3] == tag))) break;
    }
    merge_factor(ml);
  }
  STAMP(15);
  LDS_BARRIER();
  STAMP(3);
  {
    u32 pw = 0;
    if (wave == 0) {
      f32x2 t[8];
#pragma unroll
      for (int w = 0; w < 8; ++w) t[w] = *reinterpret_cast<const f32x2*>(&sm.red[w][2 * lane]);
      const f32x2 o = ((t[0] + t[1]) + (t[2] + t[3])) + ((t[4] + t[5]) + (t[6] + t[7]));
      pw = min(pack_bf(o[0] * fmrg, o[1] * fmrg), SENT - 1u);
    }
    __builtin_amdgcn_raw_buffer_store_b32(pw, wsr, pso, 0, AUX_SC1);
    st8_sc1(wsr, nto, (u32)wall_clock64() + nadd, tag);
    SB();
#pragma unroll
    for (int i = NCMIN / 8; i < NL; ++i) PIN(vr[i]);
    PIN(fl2[0]); PIN(fl2[1]); PIN(fl2[2]);
  }
  STAMP(4);
  SB();
  if (nadd) {
    asm volatile(".p2align 6\n\ts_barrier" ::: "memory");
#pragma nounroll
    while (wleft > 0) piece();
    if (is_up) {
      asm volatile("s_mov_b32 m0, %5\n\ts_nop 0\n\tbuffer_load_dwordx4 %0, %4, 0 offen nt lds\n\ts_add_u32 m0, m0, 0x400\n\ts_nop 0\n\tbuffer_load_dwordx4 %1, %4, 0 offen nt lds\n\t"
                   "s_add_u32 m0, m0, 0x400\n\ts_nop 0\n\tbuffer_load_dwordx4 %2, %4, 0 offen nt lds\n\ts_add_u32 m0, m0, 0x400\n\ts_nop 0\n\tbuffer_load_dwordx4 %3, %4, 0 offen nt lds"
                   :: "v"(v2[0]), "v"(v2[1]), "v"(v2[2]), "v"(v2[3]), "s"(r2), "s"(l2) : "memory", "scc", "m0");
    }
  }
  SB();

  if (part == 2) FINISH();
  if (part == 0) {
    until(t1);
    STAMP(11);
    CFENCE();
    u32x4 og[2];
    og[0] = ld16<AUX_SC1>(wsr, po, 0);
    SB();
    WAIT_VM(1); CFENCE();
    wo_prep(1, 2);
#pragma unroll
    for (int k = 4; k < 8; ++k) PIN(bf[k]);
    SB();
    f32x4 d[4];
    auto wo_mm = [&]() {
      const f32x4 z = {0.f, 0.f, 0.f, 0.f};
#pragma unroll
      for (int t = 0; t < 4; ++t) { d[t] = mfma16(og[0], bf[t], z); og[1][t] = __float_as_uint(dpp<XOR8>(u2f(og[0][t]))); }
      SB();
#pragma unroll
      for (int t = 0; t < 4; ++t) d[t] = mfma16(og[1], bf[4 + t], d[t]);
    };
    auto p_bad = [&]() { return !all_ok(max(max(og[0][0], og[0][1]), max(og[0][2], og[0][3])) != SENT); };
    wo_mm();
    if (__builtin_expect(p_bad(), 0)) {
#pragma nounroll
      for (int spin = 0; spin < SPIN_MAX; ++spin) {
        __builtin_amdgcn_s_sleep(1);
        CFENCE();
        og[0] = ld16<AUX_SC1>(wsr, po, 0);
        if (!p_bad()) break;
      }
      wo_mm();
      SB();
      asm volatile(".Lend_d:");
    }
    STAMP(5); WSTAMP(5);
    {
      SB();
      f32x4 y; f32x2 h[4];
#pragma unroll
      for (int t = 0; t < 4; ++t) { h[t] = d[t].xy + d[t].zw; PIN(h[t]); }
#pragma unroll
      for (int t = 0; t < 4; ++t) { y[t] = h[t][0] + h[t][1]; PIN(y[t]); }
      *LDSP(f32x4, rdst) = y;
    }
    STAMP(16);
    LDS_BARRIER();
    STAMP(17);
    if (wave < 2) {
      f32x2 v[4];
#pragma unroll
      for (int w = 0; w < 4; ++w) v[w] = f32x2{LDSP(float, rsrc)[w * 256], LDSP(float, rsrc)[w * 256 + 128]};
      const f32x2 h = (v[0] + v[1]) + (v[2] + v[3]);
      const float y = h[0] + h[1];
      __builtin_amdgcn_raw_buffer_store_b32(min(pack_bf(dpp<0xA0>(y), dpp<0xF5>(y)), SENT - 1u), wsr, xo, 0, AUX_SC1);
      asm volatile(".Lend_a:");
    }
    STAMP(6);
    FINISH();
  }
  if (part == 3) {
    if (!has_w) FINISH();
    u32x4 b2[4];
    const u32 tdst = lds_addr(&sm.hb[hpos]);
    const u32 sdst = lds_addr(&sm.stat[wave][lane]);
    const u32 ssrc = lds_addr(&sm.stat[(g >> 1) * 4 + ((lane >> 1) & 3)][(lane & 1) * 16]);
    const u32 adst = lds_addr((c < 4 && (g & 1) == 0) ? &sm.actl[g >> 1][wave * 4 + c] : reinterpret_cast<u16*>(&sm.dummy[lane]));
    const u32 asrc = lds_addr(&sm.actl[lane & 1][g * 8]);
    const u32 apa = lds_addr(&sm.hb[(c >> 3) * 512 + (c & 3) * 64 + ((c >> 2) & 1) * 32 + g * 8]);
    u32 xoff = dup(G_XP) + near + g * 2048 + ub * 1024 + (wave & 3) * 256 + c * 16;
    u32x4 one;
#pragma unroll
    for (int k = 0; k < 4; ++k) one[k] = ((c & 2) == 0 && (c >> 2) == k) ? ((c & 1) ? 0x3f800000u : 0x00003f80u) : 0u;
    PIN(one);
    u32 ooff = dup(G_OP + (blk * 512 + tid) * 8);
    PIN(xoff); PIN(ooff);
    PIN(ga); PIN(xin); PIN(cc1);
    u32 w2a = (u32)(uintptr_t)(&sm.w2b[0][0]) + (g * 8 + ((lane >> 2) & 3)) * 1024 + (wave ^ (g & 1)) * 128 + (lane & 15) * 8;
    PIN(w2a);
    if (wave == 1) {
      due = t1 + P.N1 * 256; gate();
      u32x4 gn[2] = {ld16<AUX_SC1>(wsr, G_NT + lane * 16, 0), ld16<AUX_SC1>(wsr, G_NT + lane * 16, 1024)};
      const int tn = notices(IC<2>{}, G_NT + lane * 16, gn) * 256;
      due = (tn > t1 + 2 * 256 ? max(tn, t1 + P.L0 * 256) + P.L1 * 256 : t1) + P.X2 * 256 + slack;
      until(due);
    }
    asm volatile("s_waitcnt vmcnt(0)" ::: "memory");
    STAMP(9);
    __builtin_amdgcn_s_barrier();
    STAMP(11);
    CFENCE();
    u32x4 xg[2] = {ld16<AUX_SC1>(wsr, xoff, 0), ld16<AUX_SC1>(wsr, xoff, 16384)};
    SB();
    if constexpr (UD > 0) { up_frags(8 - UD, 8); col_sums(8 - UD, 8); }
    PIN(Af); PIN(Bf);
#pragma unroll
    for (int t = 0; t < 4; ++t) {
      const u32x2 lo = tr_read(w2a ^ (t * 32)), hi = tr_read((w2a ^ (t * 32)) + 4096);
      b2[t] = u32x4{lo[0], lo[1], hi[0], hi[1]};
    }
#pragma unroll
    for (int t = 0; t < 4; ++t) PIN(b2[t]);
    SB();
    const f32x4 z4 = {0.f, 0.f, 0.f, 0.f};
    auto xp_bad = [&]() { return !all_ok(max(max(max(xg[0][0], xg[0][1]), max(xg[0][2], xg[0][3])), max(max(xg[1][0], xg[1][1]), max(xg[1][2], xg[1][3]))) != SENT); };
    f32x4 v = mfma16(one, xg[1], mfma16(one, xg[0], z4));
    if (__builtin_expect(xp_bad(), 0)) {
#pragma nounroll
      for (int spin = 0; spin < SPIN_MAX; ++spin) {
        __builtin_amdgcn_s_sleep(1);
        CFENCE();
        xg[0] = ld16<AUX_SC1>(wsr, xoff, 0); xg[1] = ld16<AUX_SC1>(wsr, xoff, 16384);
        if (!xp_bad()) break;
      }
      v = mfma16(one, xg[1], mfma16(one, xg[0], z4));
      SB();
      asm volatile(".Lend_e:");
    }
    STAMP(5); WSTAMP(5);
    {
      const float a0 = v[0], a1 = v[1];
      const float x10 = fmaf(ga[0], a0, xin[0]), x11 = fmaf(ga[1], a1, xin[1]);
      *LDSP(u32, tdst) = pack_bf(x10 * cc1[0], x11 * cc1[1]);
      float u = swap32_add(x10 + x11, fmaf(x10, x10, x11 * x11));
      u += dpp<XOR1>(u); u += dpp<XOR2>(u); u += dpp<XOR7>(u); u += dpp<XOR8>(u);
      *LDSP(float, sdst) = u;
    }
    STAMP(16);
    LDS_BARRIER();
    STAMP(17);
    {
      const f32x4 z = {0.f, 0.f, 0.f, 0.f};
      const u32x4 af0 = LDSP(u32x4, apa)[0], af1 = LDSP(u32x4, apa)[32];
      float s = LDSP(float, ssrc)[0], q = LDSP(float, ssrc)[32];
      SB();
      f32x4 d[4];
#pragma unroll
      for (int j = 0; j < 4; ++j) d[j] = mfma16(af0, bf[j], z);
#pragma unroll
      for (int j = 0; j < 4; ++j) d[j] = mfma16(af1, bf[4 + j], d[j]);
      s += dpp<XOR1>(s); q += dpp<XOR1>(q); s += dpp<XOR2>(s); q += dpp<XOR2>(q); s += dpp<XOR7>(s); q += dpp<XOR7>(q);
      const float mean = s * (1.f / 512.f);
      const float var = fmaf(q, 1.f / 512.f, -mean * mean);
      const float ri = __builtin_amdgcn_rsqf(fmaxf(var, 0.f) + 1e-5f);
      const float cf = fmaf(-ri * mean, Af, Bf);
      float y = (d[0][0] + d[1][1]) + (d[2][2] + d[3][3]);
      y = __builtin_bit_cast(float, __builtin_amdgcn_update_dpp(__builtin_bit_cast(int, y), __builtin_bit_cast(int, y), XOR8, 0xa, 0xf, false));
      y = swap16_add(y, y);
      y = fmaf(y, ri, cf);
      const float up = dpp<0x104>(y);
      *LDSP(u16, adst) = (u16)pack_bf(silu_f(y) * up, 0.f);
    }
    STAMP(18);
    LDS_BARRIER();
    STAMP(10);
    {
      const u32x4 af = *LDSP(u32x4, asrc);
      const f32x4 z = {0.f, 0.f, 0.f, 0.f};
      const f32x4 d0 = mfma16(af, b2[0], z), d1 = mfma16(af, b2[1], z), d2 = mfma16(af, b2[2], z), d3 = mfma16(af, b2[3], z);
      const bool g1 = g & 1, g2 = g & 2;
      const float e0 = g1 ? d1[0] : d0[0], e1 = g1 ? d1[1] : d0[1], f0 = g1 ? d3[0] : d2[0], f1 = g1 ? d3[1] : d2[1];
      const u32x2 ov = {pack_bf(g2 ? f0 : e0, g2 ? f1 : e1), tag};
      __builtin_amdgcn_raw_buffer_store_b64(ov, wsr, ooff, near ^ 8192u, AUX_SC1); __builtin_amdgcn_raw_buffer_store_b64(ov, wsr, ooff, near, AUX_SC1);
    }
    asm volatile(".Lend_b:");
    STAMP(6);
    FINISH();
  }
  if (wave == 0) {
    const int d = fd, ul = lane >> 3;
    u32 gm[2] = {sg[0][0], sg[1][0]};
    if (!all_ok((sg[0][1] == tag) & (sg[1][1] == tag))) gather8<2>(wsr, G_MOD + (1024 + d) * 8, 1536 * 8, tag, gm);
    u32 xa0, xa1;
    {
      due = t1 + P.N2 * 256; gate();
      u32x4 gn[2] = {ld16<AUX_SC1>(wsr, G_NT + lane * 16, 0), ld16<AUX_SC1>(wsr, G_NT + lane * 16, 1024)};
      const int tn = notices(IC<2>{}, G_NT + lane * 16, gn) * 256;
      due = (tn > t1 + 2 * 256 ? max(tn, t1 + P.L0 * 256) + P.L1 * 256 : t1) + P.X3 * 256 + slack;
      until(due);
    }
#pragma nounroll
    for (int spin = 0; spin < SPIN_MAX; ++spin) {
      CFENCE();
      xa0 = __builtin_amdgcn_raw_buffer_load_b32(wsr, dup(G_XP + ul * 2048) + near + (d >> 1) * 4, 0, AUX_SC1);
      xa1 = __builtin_amdgcn_raw_buffer_load_b32(wsr, dup(G_XP + ul * 2048) + near + (d >> 1) * 4, 1024, AUX_SC1);
      if (all_ok(max(xa0, xa1) != SENT)) break;
      __builtin_amdgcn_s_sleep(1);
    }
    float a0 = (d & 1) ? hi_f(xa0) : lo_f(xa0), a1 = (d & 1) ? hi_f(xa1) : lo_f(xa1);
    a0 += dpp<XOR8>(a0); a1 += dpp<XOR8>(a1);
    a0 = swap16_add(a0, a0); a1 = swap16_add(a1, a1);
    a0 = swap32_add(a0, a0); a1 = swap32_add(a1, a1);
    const bool hb = g & 1;
    float xv = rbf(((d & 1) ? hi_f(sx2) : lo_f(sx2)) + rbf((hb ? hi_f(gm[0]) : lo_f(gm[0])) * rbf(hb ? a1 : a0)));
    float gv = hb ? hi_f(gm[1]) : lo_f(gm[1]);
    PIN(xv); PIN(gv);
    if (wi == 0 && lane == 0) __builtin_amdgcn_raw_buffer_store_b32(tag, wsr, WS_EPOCH, 0, AUX_SC1);
    STAMP(10);
    due = max(due + P.X4 * 256 + slack, (int)(((u32)wall_clock64() - t0) << 8) + P.GX * 256);
    const bool last = ul + 40 < NUP;
    u32 o0 = dup(G_OP + (ul * 512 + d) * 8) + near, o5 = last ? o0 : OOB, tg5 = last ? tag : 0u;
    const rsrc_t rout = mk_rsrc(out, 2048u);
    u32 oo = (lane & 40) == 0 ? (g * 512 + d) * 2 : OOB;
    const u32 pk = c == 0 ? 0x00003f80u : (c == 4 ? 0x3f800000u : 0u);
    u32x4 one = {pk, 0u, pk, 0u};
    PIN(o0); PIN(o5); PIN(tg5); PIN(oo); PIN(one);
    __builtin_amdgcn_raw_buffer_store_b16((u16)pack_bf(xv, 0.f), rout, oo, 0, AUX_SC1);
    until(due);
    f32x4 sv;
#pragma nounroll
    for (int spin = 0; spin < SPIN_MAX; ++spin) {
      CFENCE();
      u32x2 og[6];
#pragma unroll
      for (int k = 0; k < 5; ++k) og[k] = ld8<AUX_SC1>(wsr, o0, k * 65536);
      og[5] = ld8<AUX_SC1>(wsr, o5, 5 * 65536);
      sv = f32x4{0.f, 0.f, 0.f, 0.f};
#pragma unroll
      for (int k = 0; k < 3; ++k) sv = mfma16(one, u32x4{og[2 * k][0], og[2 * k][1], og[2 * k + 1][0], og[2 * k + 1][1]}, sv);
      const u32 bad = ((og[0][1] ^ tag) | (og[1][1] ^ tag) | (og[2][1] ^ tag)) | ((og[3][1] ^ tag) | (og[4][1] ^ tag) | (og[5][1] ^ tg5));
      PIN(sv);
      if (all_ok(bad == 0u)) break;
      __builtin_amdgcn_s_sleep(1);
    }
    STAMP(7);
    const float s = sv[0] + dpp<XOR8>(sv[0]);
    __builtin_amdgcn_raw_buffer_store_b16((u16)pack_bf(fmaf(gv, s, xv), 0.f), rout, oo, 0, AUX_SC1);
    asm volatile(".Lend_c:");
  }
  FINISH();
}

extern "C" void kernel_launch(void* const* d_in, const int* in_sizes, int n_in,
                              void* d_out, int out_size, void* d_ws, size_t ws_size,
                              hipStream_t stream) {
  fused_kernel<<<256, 512, 0, stream>>>((const u16*)d_in[0], (const u16*)d_in[1], (const u16*)d_in[2], (const u16*)d_in[3], (const u16*)d_in[4],
                                        (const u16*)d_in[6], (const u16*)d_in[7], (const u16*)d_in[8], (const u16*)d_in[9], (const u16*)d_in[10],
                                        (unsigned char*)d_ws, (u16*)d_out
    );
}
```

```cpp
#include <hip/hip_runtime.h>
#include <stdint.h>

typedef unsigned int u32;
typedef unsigned short u16;
typedef unsigned long long u64;
typedef __attribute__((ext_vector_type(4))) unsigned u32x4;
typedef __attribute__((ext_vector_type(2))) unsigned u32x2;
typedef __attribute__((ext_vector_type(2))) __bf16 bf16x2;
typedef __attribute__((ext_vector_type(4))) float f32x4;
typedef __attribute__((ext_vector_type(2))) float f32x2;
typedef __amdgpu_buffer_rsrc_t rsrc_t;

constexpr int NH = 32, SK = 16384, DH = 128, WIN = 4096, FF = 1376;
struct Prm { int E0, ES, E1, E2, E2C, EU, TEND, DT_UP, L0, L1, X1, X2, X3, X4, NE, FE, M0, M1, M2, M3, M4, GS, GX, N1, N2, BT, WD, DT_WO, LW, LU, EW, PM, WM, FK, FW, FU, EX, TS, DS, A0, AG, AC, PT, SL, TH; };
constexpr Prm PRM0 = {32, 24, 32, 32, 48, 64, 2002, 96, 25, 18, 72, 77, 105, 85, 5, 12, 12, 5, 12, 32, 28, 34, 40, 18, 50, 10, 4, 102, 40, 40, 8, 0x6827941, 0x53, 62, 61, 60, 8, 500, 122, 56, 24, 120, 160, 10, 8};
#define PRM_ARG
#define TABP (&TAB.e[0])
constexpr int NL = 37;
constexpr int NCMIN = 192;
constexpr int WK = 6, WV = 5;
constexpr int WUSE = 13, LNS = 11;
constexpr int QP1 = 12, QP2 = 18, QD = 5;
constexpr int VQ = 6;
constexpr int SLC[] = {0, 6, 9, 15, 21, 27, 33, 40}, NSL = sizeof(SLC) / sizeof(int) - 1;
constexpr int VS = VQ + NSL;
constexpr int MLS = VS + 2;
constexpr int VSIDE = VQ + 1, UPREP = 21;
constexpr int SIDE = NCMIN / 8 - 4;
constexpr int UD = 1;
constexpr int CPF = 18;
constexpr int TBP = 7, TBL = 11, TBU = 16;
constexpr int AR = 0, AL = 9;
constexpr int VL = 0, VU = 5;
constexpr int D0 = 30;
constexpr int KF = 4, KF0 = 8;
constexpr int side_k(int i) { return (i == KF || i == KF + 1) ? 3 : (i == TBL ? 1 : 0) + ((i == QP1 || i == QP2) ? 2 : 0); }
constexpr int side_v(int v) { return v == VQ - 4 ? 2 : (v == VSIDE ? 4 : ((v == AL || v == VL) ? 1 : 0)); }
constexpr int win_k(int i) { int n = WK; for (int j = i - WK; j <= i; ++j) n += side_k(j); return n; }
constexpr int win_v(int v, int w) { int n = w; for (int j = v - w; j <= v; ++j) n += side_v(j); return n; }
constexpr int cons_end(int v) {
  const int a = v - WV + 1, c = (v - VS) * 3;
  const int m = a < c ? a : c;
  return m < 0 ? 0 : (m > NL ? NL : m);
}
struct Tab { u32 e[1024]; };
#define popc __builtin_popcount
__host__ __device__ constexpr u32 place(u32 v, u32 m, int n) {
  u32 r = 0;
  for (int k = 0; k < n; ++k) r |= (v >> k & 1u) << (10 + (m >> (4 * k) & 15));
  return r;
}
__host__ __device__ constexpr u32 step_off(const Prm& P, u32 s0, u32 bh) {
  const u32 src = (s0 >> 5 & 3u) | bh << 2;
  return place(s0 ^ ((popc(src & P.M0) & 1) | (popc(src & P.M1) & 1) << 1 | (popc(src & P.M2) & 1) << 2 | (popc(src & P.M3) & 1) << 3 | (popc(src & P.M4) & 1) << 4), P.PM, 7);
}
constexpr Tab make_tab(const Prm& P) {
  Tab T{};
  for (int bid = 0; bid < 256; ++bid) {
    const int xcd = bid & 7, idx = bid >> 3, part = idx & 3, i8 = idx >> 2, wi = i8 * 8 + xcd;
    const bool has_mod = wi < 48, has_up = wi >= 21;
    const int cb0 = (i8 + 5) & 7, cb1 = (i8 + 4) & 7, ms = wi - 20;
    const int mi = wi < 8 ? wi : (wi < 16 ? 8 + ((xcd + 4) & 7) : (wi < 20 ? (wi - 14) * 8 + 3 : 16 + ms / 7 * 8 + ms % 7 + (ms % 7 >= 3 ? 1 : 0)));
    const int ui = xcd * 5 + (xcd > 5 ? xcd - 5 : 0) + (i8 >= 3 ? (xcd >= 5 ? 1 : 0) + i8 - 3 : 0);
    const int wd = cb0 == 3 ? 0 : P.WD;
    const int e[4] = {P.E0 - 4 * wd + (cb0 == 3 ? P.ES : 0), P.E1 + (cb1 == 3 ? P.ES : 0), has_mod ? (wi < 16 ? P.E2C : P.E2) + ((mi & 7) == 3 ? P.ES : 0) : 0, has_up ? P.EU * (8 - UD) / 8 : 0};
    const int ew = wd ? P.EW : 0, pa = (1024 + e[0] + ew + e[1] + e[2] + e[3] + (has_up ? P.EX : 0)) / 4;
    int c[5] = {0, 0, 0, 0, 128};
    for (int k = 0; k < 3; ++k) c[k + 1] = c[k] + (pa - e[k] - (k == 0 ? ew : 0) + 4) / 8;
    const int nl = c[part + 1] - c[part], wst = e[part] / 4;
    const bool has_w = part < 2 || (part == 2 ? has_mod : has_up);
    const int blk = !has_w ? 0 : (part == 0 ? cb0 : (part == 1 ? xcd * 8 + cb1 : (part == 2 ? mi : ui)));
    const int span = (part == 3 && has_up ? P.TEND - P.DT_UP : (part == 0 && wd ? P.TEND - P.DT_WO : P.TEND)) * 256;
    const int np = part == 0 ? 8 - wd : (part == 3 ? 8 - UD : 8), tau0 = span / (2 * nl + wst);
    const bool up = part == 3 && has_up;
    const int tau = tau0 * (up ? P.FU : (has_w && np == 8 ? P.FK : 64)) / 64, tauw = tau0 * wst / np * (up ? P.FU : P.FW) / 64;
    int sw = KF, at = P.DS * 256 + (has_w ? np * tauw : 0);
    while (sw < NCMIN / 8 - 1 && at < P.TS * 256) { at += tau; ++sw; }
    const int tau2 = (span - at) / (2 * nl - sw);
    T.e[4 * bid] = (u32)(c[part] | nl << 7 | blk << 13 | (has_w ? 1 : 0) << 19 | (8 - np) << 20 | sw << 24);
    T.e[4 * bid + 1] = (u32)(tau | tauw << 16);
    if ((part == 1 && nl <= QP2 + QD + 1) || c[part] > 32 * part + KF0 || c[part + 1] < 32 * part + KF0 + 4) T.e[4 * bid] = 0;
    const u32 pitch = part == 0 ? 1024u : (part == 1 ? 8192u : (part == 2 ? 6144u : 2752u));
    T.e[4 * bid + 2] = (part == 0 ? xcd * 512u * pitch : 0u) + blk * (part == 3 ? 64u : 128u);
    T.e[4 * bid + 3] = pitch | (u32)tau2 << 16;
  }
  return T;
}
constexpr int step_range(const Prm& P, bool mx_) {
  int mx = 0, mn = 1 << 20;
  const Tab T = make_tab(P);
  for (int bid = 0; bid < 256; ++bid) { const int n = T.e[4 * bid] >> 7 & 63; mx = n > mx ? n : mx; mn = n < mn ? n : mn; }
  return mx_ ? mx : mn;
}
__constant__ __attribute__((aligned(64))) const Tab TAB = make_tab(PRM0);
static_assert(step_range(PRM0, true) <= NL && step_range(PRM0, false) >= NCMIN / 8, "a wave's key range does not fit its load steps");
static_assert(cons_end(SIDE + WV + 1) <= NCMIN / 8 && cons_end(UPREP) <= NCMIN / 8 && SIDE > MLS + 3, "steps every wave runs");
constexpr int NUP = FF / 32;
constexpr int SPIN_MAX = 1 << 17;

constexpr u32 WS_EPOCH = 0;
constexpr u32 G_TB = 1024;
constexpr u32 G_NT = 2048;
constexpr u32 G_MOD = 4096;
constexpr u32 G_Q = G_MOD + 3072 * 8;
constexpr u32 G_AG = G_Q + 4096 * 8;
constexpr u32 G_ML = G_AG + 4096;
constexpr u32 G_P = 81920;
constexpr u32 G_U = G_P + 65536;
__device__ __forceinline__ u32 dup(u32 o) { return G_U + ((o >> 13) << 14) + (o & 8191u); }
constexpr u32 G_XP = 0;
constexpr u32 G_OP = G_XP + 8 * 512 * 4;
constexpr u32 G_PB = G_U + 2 * (G_OP + 44 * 512 * 8);
constexpr u32 WS_END = G_PB + 32768;
static_assert(G_P % 16384 == 0 && G_PB % 16384 == 0 && G_ML + 4096 <= G_P, "layout");
constexpr u32 SENT = 0xffffffffu;
constexpr u32 OOB = 0x40000000u;
#define WST 0

constexpr int AUX_NT = 2, AUX_SC1 = 16;

__device__ __forceinline__ float lo_f(u32 v) { return __uint_as_float(v << 16); }
__device__ __forceinline__ float hi_f(u32 v) { return __uint_as_float(v & 0xffff0000u); }
__device__ __forceinline__ float rbf(float x) { return (float)(__bf16)x; }
__device__ __forceinline__ u32 pack_bf(float lo, float hi) {
  bf16x2 r; r[0] = (__bf16)lo; r[1] = (__bf16)hi; return __builtin_bit_cast(u32, r);
}
__device__ __forceinline__ float dot2(u32 k, u32 q, float acc) {
  return __builtin_amdgcn_fdot2_f32_bf16(__builtin_bit_cast(bf16x2, k), __builtin_bit_cast(bf16x2, q), acc, false);
}
template <int CTRL> __device__ __forceinline__ float dpp(float x) {
  return __builtin_bit_cast(float, __builtin_amdgcn_mov_dpp(__builtin_bit_cast(int, x), CTRL, 0xf, 0xf, true));
}
constexpr int XOR1 = 0xB1, XOR2 = 0x4E, XOR7 = 0x141, XOR8 = 0x128;
__device__ __forceinline__ float swap32_add(float a, float b) {
  auto t = __builtin_amdgcn_permlane32_swap(__float_as_uint(a), __float_as_uint(b), false, false);
  return __uint_as_float(t[0]) + __uint_as_float(t[1]);
}
__device__ __forceinline__ float swap16_add(float a, float b) {
  auto t = __builtin_amdgcn_permlane16_swap(__float_as_uint(a), __float_as_uint(b), false, false);
  return __uint_as_float(t[0]) + __uint_as_float(t[1]);
}
__device__ __forceinline__ float wave_sum(float x) {
  x += dpp<XOR1>(x); x += dpp<XOR2>(x); x += dpp<XOR7>(x); x += dpp<XOR8>(x);
  x = swap16_add(x, x);
  return swap32_add(x, x);
}
__device__ __forceinline__ float wave_max(float x) {
  x = fmaxf(x, dpp<XOR1>(x)); x = fmaxf(x, dpp<XOR2>(x)); x = fmaxf(x, dpp<XOR7>(x)); x = fmaxf(x, dpp<XOR8>(x));
  auto s = __builtin_amdgcn_permlane16_swap(__float_as_uint(x), __float_as_uint(x), false, false);
  x = fmaxf(__uint_as_float(s[0]), __uint_as_float(s[1]));
  auto t = __builtin_amdgcn_permlane32_swap(__float_as_uint(x), __float_as_uint(x), false, false);
  return fmaxf(__uint_as_float(t[0]), __uint_as_float(t[1]));
}
template <int CTRL> __device__ __forceinline__ float sel_add(float lo, float hi, bool up) {
  const float t1 = lo + dpp<CTRL>(lo), t2 = hi + dpp<CTRL>(hi);
  return up ? t2 : t1;
}
template <int I> __device__ __forceinline__ float bc16(float x) {
  return __builtin_bit_cast(float, __builtin_amdgcn_update_dpp(0, __builtin_bit_cast(int, x), 0x150 + I, 0xf, 0xf, false));
}
__device__ __forceinline__ float bcast16(float x, int i) {
  switch (i) {
    case 0: return bc16<0>(x); case 1: return bc16<1>(x); case 2: return bc16<2>(x); case 3: return bc16<3>(x);
    case 4: return bc16<4>(x); case 5: return bc16<5>(x); case 6: return bc16<6>(x); case 7: return bc16<7>(x);
    case 8: return bc16<8>(x); case 9: return bc16<9>(x); case 10: return bc16<10>(x); case 11: return bc16<11>(x);
    case 12: return bc16<12>(x); case 13: return bc16<13>(x); case 14: return bc16<14>(x); default: return bc16<15>(x);
  }
}
__device__ __forceinline__ float silu_f(float x) { return x * __builtin_amdgcn_rcpf(1.f + __expf(-x)); }

template <int AUX> __device__ __forceinline__ u32x4 ld16(rsrc_t r, u32 voff, u32 soff) {
  return __builtin_amdgcn_raw_buffer_load_b128(r, voff, soff, AUX);
}
template <int AUX> __device__ __forceinline__ u32x2 ld8(rsrc_t r, u32 voff, u32 soff) {
  return __builtin_amdgcn_raw_buffer_load_b64(r, voff, soff, AUX);
}
__device__ __forceinline__ void st8_sc1(rsrc_t r, u32 voff, u32 v, u32 tag) {
  __builtin_amdgcn_raw_buffer_store_b64(u32x2{v, tag}, r, voff, 0, AUX_SC1);
}
__device__ __forceinline__ void st16_sc1(rsrc_t r, u32 voff, u32 v0, u32 v1, u32 tag) {
  __builtin_amdgcn_raw_buffer_store_b128(u32x4{v0, tag, v1, tag}, r, voff, 0, AUX_SC1);
}
__device__ __forceinline__ rsrc_t mk_rsrc(const void* p, u32 bytes) {
  return __builtin_amdgcn_make_buffer_rsrc(const_cast<void*>(p), 0, bytes, 0x00020000);
}
#define WAIT_VM(n) __builtin_amdgcn_s_waitcnt(((n) & 15) | (((n) >> 4) << 14) | (7 << 4) | (15 << 8))
#define SB() __builtin_amdgcn_sched_barrier(0)
#define CFENCE() asm volatile("" ::: "memory")
__device__ __forceinline__ bool all_ok(bool ok) { return __builtin_amdgcn_ballot_w64(!ok) == 0ull; }

template <int N> __device__ __forceinline__ void gather16(rsrc_t ws, u32 voff, u32 stride, u32 tag, u32x4 (&g)[N]) {
#pragma nounroll
  for (int spin = 0; spin < SPIN_MAX; ++spin) {
    CFENCE();
    bool ok = true;
#pragma unroll
    for (int i = 0; i < N; ++i) g[i] = ld16<AUX_SC1>(ws, voff + i * stride, 0);
#pragma unroll
    for (int i = 0; i < N; ++i) ok &= (g[i][1] == tag) & (g[i][3] == tag);
    if (all_ok(ok)) break;
    __builtin_amdgcn_s_sleep(8);
  }
}
template <int N> __device__ __forceinline__ void gather8(rsrc_t ws, u32 voff, u32 stride, u32 tag, u32 (&g)[N], bool act = true) {
#pragma nounroll
  for (int spin = 0; spin < SPIN_MAX; ++spin) {
    CFENCE();
    bool ok = true;
    u32x2 t[N];
#pragma unroll
    for (int i = 0; i < N; ++i) t[i] = ld8<AUX_SC1>(ws, voff + i * stride, 0);
#pragma unroll
    for (int i = 0; i < N; ++i) { ok &= (t[i][1] == tag); g[i] = t[i][0]; }
    if (all_ok(ok | !act)) break;
    __builtin_amdgcn_s_sleep(8);
  }
}

struct Smem {
  float red[8][128];
  float ml[2][8];
  float r2[64 * 20];
  u16 hb[3 * 1024];
  float stat[8][64];
  float red2[8][128];
  u32 dummy[64];
  u32x2 bc;
  u32x2 ctl;
  u32 nm, pad_[3];
  u32x4 zero[4];
  u32x4 wbuf[8][8][64];
  __attribute__((aligned(256))) u16 w2b[32][512];
  u16 actl[2][32];
};

#define STAMP(k) do {} while (0)
#define WSTAMP(k) do {} while (0)
#define FINISH() return
#define PIN(x) asm volatile("" : "+v"(x))
#define LDS_BARRIER() do { __builtin_amdgcn_s_waitcnt(0xc07f); __builtin_amdgcn_s_barrier(); CFENCE(); } while (0)
#define WAVE_SYNC() do { __builtin_amdgcn_fence(__ATOMIC_RELEASE, "wavefront"); __builtin_amdgcn_wave_barrier(); __builtin_amdgcn_fence(__ATOMIC_ACQUIRE, "wavefront"); } while (0)

__device__ __forceinline__ void dma16(rsrc_t r, u32 voff, u32 soff, u32 lds_dst) {
  u32 keep;
  asm volatile("s_mov_b32 %0, m0\n\ts_mov_b32 m0, %4\n\ts_nop 4\n\tbuffer_load_dwordx4 %1, %2, %3 offen nt lds\n\ts_mov_b32 m0, %0"
               : "=&s"(keep) : "v"(voff), "s"(r), "s"(soff), "s"(lds_dst) : "memory");
}
__device__ __forceinline__ u32 perm_lo(u32 a, u32 b) { return __builtin_amdgcn_perm(b, a, 0x05040100u); }
__device__ __forceinline__ u32 perm_hi(u32 a, u32 b) { return __builtin_amdgcn_perm(b, a, 0x07060302u); }
__device__ __forceinline__ float rdl(float x, int l) { return __int_as_float(__builtin_amdgcn_readlane(__float_as_int(x), l)); }

typedef __attribute__((ext_vector_type(8))) __bf16 bf16x8;
__device__ __forceinline__ f32x4 mfma16(u32x4 a, u32x4 b, f32x4 c) {
  return __builtin_amdgcn_mfma_f32_16x16x32_bf16(__builtin_bit_cast(bf16x8, a), __builtin_bit_cast(bf16x8, b), c, 0, 0, 0);
}
__device__ __forceinline__ float sum8(const float* p) {
  const f32x4 a = *reinterpret_cast<const f32x4*>(p), b = *reinterpret_cast<const f32x4*>(p + 4);
  return ((a[0] + b[0]) + (a[1] + b[1])) + ((a[2] + b[2]) + (a[3] + b[3]));
}
__device__ __forceinline__ float ln_ri(float var) {
  return __builtin_amdgcn_rcpf(rbf(__builtin_amdgcn_sqrtf(rbf(rbf(fmaxf(var, 0.f)) + 1e-5f))));
}
__device__ __forceinline__ void ln_stats2(const float (&x0)[8], const float (&x1)[8], float& mb0, float& ri0, float& mb1, float& ri1) {
  const float k0 = rdl(x0[0], 0), k1 = rdl(x1[0], 0);
  float s0 = 0.f, s1 = 0.f, q0 = 0.f, q1 = 0.f;
#pragma unroll
  for (int k = 0; k < 8; ++k) {
    const float d0 = x0[k] - k0, d1 = x1[k] - k1;
    s0 += d0; s1 += d1; q0 = fmaf(d0, d0, q0); q1 = fmaf(d1, d1, q1);
  }
  float t = swap16_add(swap32_add(s0, s1), swap32_add(q0, q1));
  t += dpp<XOR1>(t); t += dpp<XOR2>(t); t += dpp<XOR7>(t); t += dpp<XOR8>(t);
  auto u = __builtin_amdgcn_permlane16_swap(__float_as_uint(t), __float_as_uint(t), false, false);
  const float ms = __uint_as_float(u[0]) * (1.f / 512.f), mq = __uint_as_float(u[1]) * (1.f / 512.f);
  const float ri = ln_ri(fmaf(-ms, ms, mq));
  mb0 = rbf(k0 + rdl(ms, 0)); mb1 = rbf(k1 + rdl(ms, 32));
  ri0 = rdl(ri, 0); ri1 = rdl(ri, 32);
}
template <int I> struct IC { static constexpr int v = I; };
template <int I, int N, class F> __device__ __forceinline__ void sfor(F&& f) {
  if constexpr (I < N) { if (f(IC<I>{})) sfor<I + 1, N>(f); }
}
typedef __attribute__((ext_vector_type(4))) short s16x4;
typedef __attribute__((address_space(3))) s16x4* lds_s16x4p;
__device__ __forceinline__ u32x2 tr_read(u32 lds_addr) {
  return __builtin_bit_cast(u32x2, __builtin_amdgcn_ds_read_tr16_b64_v4i16((lds_s16x4p)lds_addr));
}
__device__ __forceinline__ u32 next_tag(u32 e) { return ((e | 0x40004000u) + 1u) & ~0x40004000u; }
__device__ __forceinline__ float u2f(u32 v) { return __uint_as_float(v); }
#define LDSP(T, a) ((__attribute__((address_space(3))) T*)(a))
template <class T> __device__ __forceinline__ u32 lds_addr(T* p) { u32 a = (u32)(uintptr_t)p; PIN(a); return a; }

typedef __attribute__((ext_vector_type(16))) unsigned u32x16;
typedef __attribute__((ext_vector_type(8))) unsigned u32x8;
__global__ void __launch_bounds__(512, 2) fused_kernel(const u16* x_, const u16* kc_, const u16* vc_, const u16* wq_, const u16* wo_, const u16* w1_,
                                                       const u16* w3_, const u16* w2_, const u16* cnd_, const u16* wmod_, unsigned char* ws_, u16* out_ PRM_ARG) {
  __shared__ __attribute__((aligned(256))) Smem sm;
  constexpr Prm P = PRM0;
  const int TEND = P.TEND;
  const int bid = blockIdx.x, tid = threadIdx.x;
  u32x4 kr[NL];
  u32x16 ka; u32x8 kb; u32x4 tev; u64 pt0; u32 tch2;
  asm volatile("s_load_dwordx16 %0, %3, 0x0\n\ts_load_dwordx8 %1, %3, 0x40\n\ts_memrealtime %2"
               : "=&s"(ka), "=&s"(kb), "=&s"(pt0) : "s"(__builtin_amdgcn_kernarg_segment_ptr()));
  SB();
  {
    u64 pc; u32 vb;
    asm volatile("s_getpc_b64 %2\n.Lcpf_first:\n\tv_mul_u32_u24 %1, ((.Lend_c-.Lcpf_first)>>10)<<2, %3\n\tglobal_load_dword %0, %1, %2"
                 : "=&v"(tch2), "=&v"(vb), "=&s"(pc) : "v"((bid & ~7) + (tid >> 6)) : "memory");
    asm volatile("global_load_dwordx4 %0, %1, %2" : "=&v"(tev) : "v"(bid << 4), "s"(TABP) : "memory");
  }
  SB();
  const int xcd = bid & 7, idx = bid >> 3, part = idx & 3;
  const int b = idx >> 2 & 1, head = xcd * 4 + (((idx >> 3) & 1) << 1 | idx >> 4);
  const u32 so0 = step_off(P, 32 * part + KF0, b * 32 + head) & ~place(3, P.PM, 2);
  u32 va = (((b * 32 + head) * SK + (SK - WIN)) * DH * 2 + so0) | place(tid >> 6, P.WM, 3) | (tid & 63) << 4;
  const u32 kx1 = place(1, P.PM, 2), kx2 = place(2, P.PM, 2);
  PIN(va);
  u32 va1 = va | kx1, va2 = va | kx2, va3 = va | kx1 | kx2;
  PIN(va1); PIN(va2); PIN(va3);
  int mc = bid & 0xd8;
  asm volatile("" : "+v"(va), "+s"(mc));
  SB();
  asm volatile("s_waitcnt lgkmcnt(0)" : "+s"(ka), "+s"(kb), "+s"(pt0));
  SB();
#define K_FIRST "global_load_dwordx4 %0, %4, %5 nt\n\tglobal_load_dwordx4 %1, %6, %5 nt\n\tglobal_load_dwordx4 %2, %7, %5 nt\n\tglobal_load_dwordx4 %3, %8, %5 nt"
  asm volatile("s_cmp_eq_u32 %9, 16\n\ts_cbranch_scc1 .Lk_later\n\t" K_FIRST "\n.Lk_later:"
               : "=&v"(kr[0]), "=&v"(kr[1]), "=&v"(kr[2]), "=&v"(kr[3]) : "v"(va), "s"((u64)ka[2] | (u64)ka[3] << 32), "v"(va1), "v"(va2), "v"(va3), "s"(mc) : "memory", "scc");
  SB();
  const u32 tka = (u32)wall_clock64(), t0 = tka - D0;
  auto kap = [&](int k) { return k < 8 ? (u64)ka[2 * k] | (u64)ka[2 * k + 1] << 32 : (u64)kb[2 * k - 16] | (u64)kb[2 * k - 15] << 32; };
  const u16 *x = (const u16*)kap(0), *kc = (const u16*)kap(1), *vc = (const u16*)kap(2), *w2 = (const u16*)kap(7), *cnd = (const u16*)kap(8);
  unsigned char* ws = (unsigned char*)kap(10); u16* out = (u16*)kap(11);
  const int lane = tid & 63, wave = __builtin_amdgcn_readfirstlane(tid >> 6);
  const bool p3 = part == 3;
  const u64 wa = part == 0 ? kap(4) : (part == 1 ? kap(3) : (part == 2 ? kap(9) : ((wave & 1) ? kap(6) : kap(5))));
  const bool modc = mc == 16;
  if (modc) asm volatile("s_waitcnt vmcnt(0)" ::: "memory");
  asm volatile("s_waitcnt vmcnt(4)" : "+v"(tev));
  const u32 te0 = __builtin_amdgcn_readfirstlane(tev[0]), te1 = __builtin_amdgcn_readfirstlane(tev[1]);
  const u32 te3 = __builtin_amdgcn_readfirstlane(tev[3]), pitch = te3 & 0xffff, wsz = pitch << (part == 0 ? 12 : 9);
  const int sh = p3 ? 2 : 3;
  u32 wv0 = (u32)((p3 ? (wave >> 1) * 16 : wave * 64) + (lane >> sh)) * pitch + ((lane & (p3 ? 3 : 7)) ^ (lane >> (p3 ? 5 : 3)) ^ (p3 ? wave >> 2 << 1 : 0)) * 16u;
  const u32 wimg = (u32)(uintptr_t)(&sm.wbuf[0][0][0]);
  u32 wld = wimg + (p3 ? (wave & 1) * 32768 + (wave >> 1) * 1024 : wave * 8192);
  const int bl0 = te0 & 127, nl = te0 >> 7 & 63, blk = te0 >> 13 & 63;
  const bool has_w = te0 >> 19 & 1;
  const rsrc_t rw = mk_rsrc((const void*)wa, wsz);
  const u32 wstep = pitch << (p3 ? 6 : 3), lstep = p3 ? 4096u : 1024u;
  u32 wso = __builtin_amdgcn_readfirstlane(tev[2]);
  int wleft = 8;
  auto piece = [&]() {
    dma16(rw, wv0, wso, wld);
    wso += wstep; wld += lstep;
    --wleft;
  };
  int tau = te1 & 0xffff;
  const int tauw = te1 >> 16, sw = te0 >> 24;
  int tau2 = te3 >> 16;
  int due = P.DS * 256, now = 0;
  auto gate = [&]() {
    while (now < due) { now = (int)(((u32)wall_clock64() - t0) << 8); if (now < due) __builtin_amdgcn_s_sleep(1); }
    due += tau;
  };
  const rsrc_t wsr = mk_rsrc(ws, WS_END);
  const int g = lane >> 4, c = lane & 15;
  const int i8 = idx >> 2, wi = i8 * 8 + xcd;
  const bool is_up = p3 && has_w;
  const size_t base = ((size_t)(b * NH + head) * SK + (SK - WIN)) * DH;
  const rsrc_t rk = mk_rsrc(kc + base, WIN * DH * 2), rv = mk_rsrc(vc + base, WIN * DH * 2);
  u32 sot;
  {
    u32 s0 = bl0 + lane - KF;
    s0 += s0 >= (u32)(32 * part + KF0) ? KF : 0;
    sot = (lane < KF ? so0 | place(lane, P.PM, 2) : step_off(P, s0, b * 32 + head)) | place(wave, P.WM, 3);
    sot = lane < nl ? sot : OOB;
    PIN(sot);
  }
  auto until = [&](int t) { while ((int)(((u32)wall_clock64() - t0) << 8) < t); };
  u32 near = 0, nmask = 0;
  int slack = 0;
  const rsrc_t rx = mk_rsrc(part == 2 ? cnd : x, (part == 1 || (part == 2 && has_w)) ? 2048u : 0u);
  u32x2 ep; u32 xs0, xs1, tch1; u32x4 xr0, xr1;
  auto small_ops = [&]() {
    ep = ld8<AUX_SC1>(wsr, wave == 7 ? WS_EPOCH : OOB, 0);
    xs0 = __builtin_amdgcn_raw_buffer_load_b16(rx, tid * 2, 0, 0); xs1 = __builtin_amdgcn_raw_buffer_load_b16(rx, tid * 2, 1024, 0);
  };
  auto small_ops2 = [&]() {
    xr0 = ld16<0>(rx, part == 1 ? lane * 16 : OOB, 0); xr1 = ld16<0>(rx, part == 1 ? lane * 16 : OOB, 1024);
    tch1 = __builtin_amdgcn_raw_buffer_load_b32(mk_rsrc(w2 + (size_t)blk * 32 * 512, is_up ? 64u : 0u), tid == 384 ? 0 : OOB, 0, AUX_NT);
  };
  u32 tag = 0;
  u32x4 bf[8];
  auto wo_prep = [&](int s0 = 0, int s1 = 2) {
    const u32 wi_ = wimg + wave * 8192;
    const int q = (lane >> 2) & 3, pp = lane & 3;
#pragma unroll
    for (int s = s0; s < s1; ++s) {
#pragma unroll
      for (int t = 0; t < 4; ++t) {
        const int r0 = s * 32 + g * 8 + q, r1 = r0 + 4, ch = t * 2 + (pp >> 1);
        const u32x2 lo = tr_read(wi_ + r0 * 128 + ((ch ^ (r0 & 7)) * 16) + (pp & 1) * 8), hi = tr_read(wi_ + r1 * 128 + ((ch ^ (r1 & 7)) * 16) + (pp & 1) * 8);
        bf[s * 4 + t] = u32x4{lo[0], lo[1], hi[0], hi[1]};
      }
    }
  };
  auto gemv_go = [&](u32 hh) {
    sm.hb[tid] = (u16)hh; sm.hb[512 + tid] = (u16)(hh >> 16);
    WAVE_SYNC();
    const u32x4* a = (c & 11) == 0 ? reinterpret_cast<const u32x4*>(&sm.hb[(c >> 2) * 512 + wave * 64 + g * 8]) : &sm.zero[0];
    const u32x4 a0 = a[0], a1 = a[(c & 11) == 0 ? 4 : 0];
    const f32x4 z = {0.f, 0.f, 0.f, 0.f};
    f32x4 d[4];
#pragma unroll
    for (int t = 0; t < 4; ++t) d[t] = mfma16(a0, bf[t], z);
#pragma unroll
    for (int t = 0; t < 4; ++t) d[t] = mfma16(a1, bf[4 + t], d[t]);
    if (lane < 32) {
#pragma unroll
      for (int t = 0; t < 4; ++t) sm.r2[(t * 16 + c) * 20 + g * 8 + wave] = d[t][0];
    }
  };
  auto mod_gemv = [&]() { gemv_go(pack_bf(silu_f(lo_f(xs0)), silu_f(lo_f(xs1)))); };
  auto mod_put = [&]() { st8_sc1(wsr, G_MOD + (blk * 64 + lane) * 8, pack_bf(sum8(&sm.r2[lane * 20]), sum8(&sm.r2[lane * 20 + 8])), tag); };
  auto zero_rows = [&]() { if (lane < 4) sm.zero[lane] = u32x4{0u, 0u, 0u, 0u}; };
  const u32 pbo = G_PB + (wi >> 3) * 16384 + (wi & 7) * 1024;
  u32 l16 = lane * 16;
  asm volatile("" : "+v"(l16), "+v"(wv0) : "s"(rk), "s"(rx), "s"(wsr));
  SB();
  if (has_w) {
    if (__builtin_expect(modc, 0)) {
      now = 1 << 30;
      __builtin_amdgcn_raw_buffer_store_b128(u32x4{SENT, SENT, SENT, SENT}, wsr, tid < 384 ? G_P + wi * 6144 + tid * 16 : pbo + (wave & 1) * 8192 + lane * 16, 0, AUX_SC1);
      __builtin_amdgcn_raw_buffer_store_b128(u32x4{SENT, SENT, SENT, SENT}, wsr, tid < 16 ? G_ML + wi * 256 + tid * 16 : (tid < 24 ? G_NT + wi * 128 + (tid - 16) * 16 : (tid < 32 && wi < 3 ? G_AG + wi * 128 + (tid - 24) * 16 : OOB)), 0, AUX_SC1);
    }
#pragma nounroll
    while (wleft > (int)(te0 >> 20 & 7)) { gate(); due += tauw - tau; piece(); }
    now = 0;
  }
  SB();
  STAMP(23);
  if (__builtin_expect(modc, 0)) {
    small_ops();
    zero_rows();
    {
      until(P.PT * 256);
      const u64 pa = (u64)ws + pbo + wave * 128 + (wave & 1) * 8192;
      u64 c0, c1, c2; u32 d0, d1;
      asm volatile("s_memtime %0\n\ts_waitcnt lgkmcnt(0)\n\ts_load_dword %3, %5, 0x0\n\ts_waitcnt lgkmcnt(0)\n\ts_memtime %1\n\ts_waitcnt lgkmcnt(0)\n\t"
                   "s_load_dword %4, %6, 0x0\n\ts_waitcnt lgkmcnt(0)\n\ts_memtime %2\n\ts_waitcnt lgkmcnt(0)"
                   : "=&s"(c0), "=&s"(c1), "=&s"(c2), "=&s"(d0), "=&s"(d1) : "s"(pa), "s"(pa + 8192 - (wave & 1) * 16384) : "memory");
      const int df = min(max((int)(2 * (u32)c1 - (u32)c0 - (u32)c2), -600), 600);
      if (lane == 0) sm.stat[0][wave] = (float)(((wave ^ (xcd >> 2)) & 1) ? -df : df);
    }
    asm volatile("s_waitcnt vmcnt(0)" ::: "memory");
    tag = next_tag(__builtin_amdgcn_readfirstlane(ep[0]));
    wo_prep();
    mod_gemv();
    LDS_BARRIER();
    if (wave == 7) { mod_put(); if (lane == 0) st8_sc1(wsr, G_AG + (48 + wi) * 8, __float_as_uint(sum8(&sm.stat[0][0])), tag); }
    STAMP(9);
    __builtin_amdgcn_s_barrier();
    asm volatile(K_FIRST :: "v"(kr[0]), "v"(kr[1]), "v"(kr[2]), "v"(kr[3]), "v"(va), "s"((u64)ka[2] | (u64)ka[3] << 32), "v"(va1), "v"(va2), "v"(va3) : "memory");
  }
  SB();
  const bool fin = (part == 1 || part == 2) && wave < 2 && xcd < 2;
  const int fw = wi ^ ((part & 2) * 2) ^ ((wave & 1) * 2), fd = fw * 8 + (lane & 7);
  const int ub = wave >> 2, ud = (wave & 3) * 128 + c * 8 + g * 2;
  u32x4 sg[3]; u32 sx2;
  auto side_loads = [&]() {
    sg[0] = ld16<AUX_SC1>(wsr, is_up ? G_MOD + (1024 + ud) * 8 : (fin ? G_MOD + (1024 + fd) * 8 : OOB), 0);
    sg[1] = ld16<AUX_SC1>(wsr, is_up ? G_MOD + (1536 + ud) * 8 : (fin ? G_MOD + (2560 + fd) * 8 : OOB), 0);
    sg[2] = ld16<AUX_SC1>(wsr, is_up ? G_MOD + (2048 + ud) * 8 : OOB, 0);
    sx2 = __builtin_amdgcn_raw_buffer_load_b32(mk_rsrc(x, (is_up || fin) ? 2048u : 0u), is_up ? ub * 1024 + ud * 2 : (g & 1) * 1024 + (fd >> 1) * 4, 0, 0);
  };
  auto side_pin = [&]() { PIN(sg[0]); PIN(sg[1]); PIN(sg[2]); PIN(sx2); };
  const u32 mlo = G_ML + (head * 2 + b) * 64 + (lane & 3) * 16;
  float fwv = 0.f, fmw[8];
  bool fok = wave != 0;
  auto merge_factor = [&](const u32x4& ml) {
    const float mi = u2f(ml[0]);
    float M = fmaxf(mi, dpp<XOR1>(mi)); M = fmaxf(M, dpp<XOR2>(M));
    const float e = __expf(mi - M);
    float L = e * u2f(ml[2]);
    L += dpp<XOR1>(L); L += dpp<XOR2>(L);
    const float f = fwv * rdl(e * __builtin_amdgcn_rcpf(L), part);
#pragma unroll
    for (int w = 0; w < 8; ++w) fmw[w] = bcast16(f, w);
  };
  f32x2 ga = {0.f, 0.f}, xin = {0.f, 0.f}, cc1 = {0.f, 0.f};
  float Af = 0.f, Bf = 0.f;
  const int hpos = ub * 512 + ud;
  auto up_prep = [&]() {
    if (!all_ok((sg[0][1] == tag) & (sg[0][3] == tag) & (sg[1][1] == tag) & (sg[1][3] == tag) & (sg[2][1] == tag) & (sg[2][3] == tag))) gather16<3>(wsr, G_MOD + (1024 + ud) * 8, 512 * 8, tag, sg);
    ga = f32x2{ub ? hi_f(sg[0][0]) : lo_f(sg[0][0]), ub ? hi_f(sg[0][2]) : lo_f(sg[0][2])};
    cc1 = f32x2{rbf(1.f + (ub ? hi_f(sg[2][0]) : lo_f(sg[2][0]))), rbf(1.f + (ub ? hi_f(sg[2][2]) : lo_f(sg[2][2])))};
    xin = f32x2{lo_f(sx2), hi_f(sx2)};
    u32* hp = reinterpret_cast<u32*>(&sm.hb[hpos]);
    hp[512] = pack_bf(cc1[0], cc1[1]);
    hp[1024] = ub ? perm_hi(sg[1][0], sg[1][2]) : perm_lo(sg[1][0], sg[1][2]);
  };
  auto up_frags = [&](int r0, int r1) {
    const int pp = lane & 3;
    const u32 a = wimg + (pp & 1) * 32768 + ((pp >> 1) * 32 + g * 8 + ((lane >> 2) & 3)) * 64 + (((wave >> 1) ^ ((g & 1) | (pp & 2))) * 16) + (wave & 1) * 8;
#pragma unroll
    for (int rb = r0; rb < r1; ++rb) {
      const u32x2 lo = tr_read(a + rb * 4096), hi = tr_read(a + rb * 4096 + 256);
      bf[rb] = u32x4{lo[0], lo[1], hi[0], hi[1]};
    }
  };
  const u32x4* ap = reinterpret_cast<const u32x4*>(&sm.hb[((lane >> 3) & 1) * 512 + (lane & 1) * 32 + g * 8]);
  const bool par = lane & 8;
  auto col_sums = [&](int r0, int r1) {
    const u32x4* a1 = ap + 128 + ((lane >> 1) & 1) * 128;
    f32x4 d0 = {0.f, 0.f, 0.f, 0.f}, d1 = {0.f, 0.f, 0.f, 0.f};
#pragma unroll
    for (int rb = r0; rb < r1; ++rb) { if (rb & 1) d1 = mfma16(a1[rb * 8], bf[rb], d1); else d0 = mfma16(a1[rb * 8], bf[rb], d0); }
    Af += par ? d0[1] + d1[1] : d0[0] + d1[0]; Bf += par ? d0[3] + d1[3] : d0[2] + d1[2];
    if (r1 == 8) { Af += dpp<XOR8>(Af); Bf += dpp<XOR8>(Bf); }
  };
  float l0 = 0.f, l1 = 0.f;
  u32x2 gmd[2], tbg;
  const bool modw = part == 2 && has_w && !modc;
  const int qp = part != 1 ? -99 : ((blk & 7) == 3 ? QP1 : QP2);
  static_assert(QP1 + QD < QP2 && QP1 + QD > WUSE && QP1 + QD > TBU && WUSE + 1 < TBU, "order of the q steps");
  int t1 = 0;
#pragma unroll
  for (int i = NCMIN / 8; i < NL; ++i) kr[i] = u32x4{0u, 0u, 0u, 0u};
  sfor<KF, NL>([&](auto ic) {
    constexpr int i = ic.v;
    if (i >= NCMIN / 8 && i >= nl) return false;
    const u32 so = __builtin_amdgcn_readlane(sot, i);
    if constexpr (i < NCMIN / 8) tau = i == sw ? tau2 : tau;
    gate();
    SB();
    kr[i] = ld16<AUX_NT>(rk, l16, so);
    if constexpr (i == KF) small_ops();
    if constexpr (i == KF + 1) small_ops2();
    if constexpr (i == KF + 2) zero_rows();
    if constexpr (i == TBL) tbg = ld8<AUX_SC1>(wsr, wave == 7 ? G_TB + (lane & 7) * 8 : OOB, 0);
    if constexpr (i == QP1 || i == QP2) { gmd[0] = ld8<AUX_SC1>(wsr, i == qp ? G_MOD + tid * 8 : OOB, 0); gmd[1] = ld8<AUX_SC1>(wsr, i == qp ? G_MOD + tid * 8 : OOB, 4096); }
    SB();
    SB();
    if (i >= WK) { WAIT_VM(win_k(i)); SB(); }
    if (i == KF) STAMP(25);
    if (i == 8) STAMP(12);
    if (i == 20) STAMP(21);
    if (i == 25) STAMP(22);
    if (i == TBP) {
      if (part == 3 && i8 == 0 && wave == 7) { tag = next_tag(__builtin_amdgcn_readfirstlane(ep[0])); if (lane == 0) st8_sc1(wsr, G_TB + xcd * 8, tka - P.GS, tag); }
      SB();
    }
    if (i == TBU - 1) {
      if (wave == 7) {
        const bool ok = tbg[1] == tag;
        float lf = ok ? (float)min(max((int)(t0 - tbg[0]), -300), 300) : 0.f, nf = ok ? 1.f : 0.f;
        lf += dpp<XOR1>(lf); nf += dpp<XOR1>(nf); lf += dpp<XOR2>(lf); nf += dpp<XOR2>(nf);
        lf *= __builtin_amdgcn_rcpf(fmaxf(nf, 1.f));
        const float ot = dpp<XOR7>(lf);
        if (lane == 0) sm.bc = u32x2{tag, t0 - (u32)(int)((lf + ot) * 0.5f - fabsf(lf - ot) * (P.BT / 32.f))};
      }
      SB();
    }
    if (i == TBU) {
      LDS_BARRIER();
      const u32x2 bc = sm.bc;
      tag = __builtin_amdgcn_readfirstlane(bc[0]);
      t1 = (__builtin_amdgcn_readfirstlane((int)(bc[1] - t0)) + TEND + P.X1) * 256;
      const int dt = __builtin_amdgcn_readfirstlane((int)((float)(int)(t0 - bc[1]) * 256.f * __builtin_amdgcn_rcpf((float)(2 * nl - TBU - 2))));
      tau -= dt; tau2 -= dt;
      SB();
    }
    if (i == TBU + 1) {
      if (modw) { if (wave == 6) mod_put(); STAMP(9); }
      SB();
    }
    if (i == LNS) {
      tag = next_tag(__builtin_amdgcn_readfirstlane(ep[0]));
      if (part == 1) {
        float x0[8], x1[8], mb0, ri0, mb1, ri1;
#pragma unroll
        for (int k = 0; k < 4; ++k) { x0[2 * k] = lo_f(xr0[k]); x0[2 * k + 1] = hi_f(xr0[k]); x1[2 * k] = lo_f(xr1[k]); x1[2 * k + 1] = hi_f(xr1[k]); }
        ln_stats2(x0, x1, mb0, ri0, mb1, ri1);
        l0 = rbf(rbf(lo_f(xs0) - mb0) * ri0); l1 = rbf(rbf(lo_f(xs1) - mb1) * ri1);
      }
      PIN(l0); PIN(l1);
      SB();
    }
    if (i == WUSE) {
      PIN(tch1); PIN(tch2);
      static_assert(WUSE - WK >= KF, "the weight pieces are older than the steps outside the window");
      CFENCE();
      if (part == 1 || modw) wo_prep();
#pragma unroll
      for (int k = 0; k < 8; ++k) PIN(bf[k]);
      SB();
    }
    if (i == WUSE + 1) {
      if (modw) mod_gemv();
      SB();
    }
    if (i == QP1 + QD || i == QP2 + QD) {
      if (i == qp + QD) {
        u32 gm[2] = {gmd[0][0], gmd[1][0]};
        if (!all_ok((gmd[0][1] == tag) & (gmd[1][1] == tag))) { STAMP(11); gather8<2>(wsr, G_MOD + tid * 8, 512 * 8, tag, gm); }
        STAMP(5);
        gemv_go(pack_bf(rbf(l0 * rbf(1.f + lo_f(gm[1]))) + lo_f(gm[0]), rbf(l1 * rbf(1.f + hi_f(gm[1]))) + hi_f(gm[0])));
      }
      SB();
    }
    if (i == QP1 + QD + 1 || i == QP2 + QD + 1) {
      if (i == qp + QD + 1) {
        LDS_BARRIER();
        if (wave == 0) {
          const int bb = lane >> 5, jq = lane & 31;
          st8_sc1(wsr, G_Q + (bb * 2048 + blk * 32 + jq) * 8, pack_bf(sum8(&sm.r2[2 * jq * 20 + bb * 8]), sum8(&sm.r2[(2 * jq + 1) * 20 + bb * 8])), tag);
        }
        STAMP(6);
      }
      SB();
    }
    return true;
  });
  PIN(sot);
  const u32 qoff = G_Q + ((b * 32 + head) * 64 + c * 4) * 8;
  u32x4 qg[2], vr[NL], fl2[3];
  u32 qq[4];
  constexpr int NB = (NL + 15) / 16;
  float xlo, xs[4], sc[NB], pr[NB], m = 0.f;
  f32x4 acc = {0.f, 0.f, 0.f, 0.f};
  u32 pb[NB], pm[4], cpf;
  u32x2 agr;
  const u32 adst0 = lds_addr(lane < 32 ? &sm.red[wave][c * 8 + g * 4] : &sm.r2[(lane - 32) * 4]);
  u32 pso, po, rdst, rsrc, xo, l2, v2[4];
  rsrc_t r2;
  auto wo_addr = [&]() {
    po = G_P + (xcd & 3) * 16384 + near + (c >> 2 & 1) * 4096 + ((wave >> 1) * 4 + (c & 3)) * 256 + (wave & 1) * 128 + (c >> 3) * 64 + g * 16;
    rdst = lane < 32 ? (u32)(uintptr_t)&sm.red2[wave][0] + lane * 16 : (u32)(uintptr_t)&sm.r2[0] + (lane - 32) * 16;
    rsrc = (u32)(uintptr_t)&sm.red2[0][((wave & 1) * 16 + (lane & 15)) * 4 + (lane >> 4)];
    xo = dup(G_XP + xcd * 2048 + (wave & 1) * 1024 + (blk * 64 + (lane & ~1)) * 2) + (lane & 1) * 8192;
    pso = wave == 0 ? G_P + (xcd & 3) * 16384 + near + b * 4096 + ((head & 3) * 4 + part) * 256 + lane * 4 : OOB;
    PIN(pso); PIN(po); PIN(rdst); PIN(rsrc); PIN(xo);
  };
  auto w2_addr = [&]() {
    r2 = mk_rsrc(w2 + (size_t)(blk * 32 + wave * 4) * 512, 4096u);
    l2 = (u32)(uintptr_t)(&sm.w2b[wave * 4][0]);
#pragma unroll
    for (int n = 0; n < 4; ++n) { v2[n] = (lane ^ (n * 2) ^ ((wave & 2) << 2)) * 16 + n * 1024; PIN(v2[n]); }
    asm volatile("" : "+s"(r2), "+s"(l2));
  };
  const u32 nadd = is_up ? P.LU : (part == 0 && wleft ? P.LW : 0);
  u32 nto = tid == 0 ? G_NT + bid * 8 : OOB;
  PIN(nto);
  auto notices = [&](auto nc_, u32 off, u32x4 (&gn)[nc_.v]) -> int {
    constexpr int N = nc_.v;
#pragma nounroll
    for (int spin = 0; spin < SPIN_MAX; ++spin) {
      bool ok = true;
#pragma unroll
      for (int i = 0; i < N; ++i) ok &= (gn[i][1] == tag) & (gn[i][3] == tag);
      if (all_ok(ok)) break;
      __builtin_amdgcn_s_sleep(10);
      CFENCE();
#pragma unroll
      for (int i = 0; i < N; ++i) gn[i] = ld16<AUX_SC1>(wsr, off, i * 1024);
    }
    int mx = 0;
#pragma unroll
    for (int i = 0; i < N; ++i) mx = max(mx, max((int)(gn[i][0] - t0), (int)(gn[i][2] - t0)));
    return __builtin_amdgcn_readfirstlane((int)wave_max((float)min(mx, 1 << 20)));
  };
  const int side = nl >= SIDE + 13 ? SIDE + 8 : (nl >= SIDE + 9 ? SIDE + 4 : SIDE);
  sfor<0, NL + WV>([&](auto ic) {
    constexpr int v = ic.v;
    if (cons_end(v - 1) >= NCMIN / 8 && cons_end(v - 1) >= nl) return false;
    if constexpr (v < NL) {
      const u32 so = __builtin_amdgcn_readlane(sot, v);
      if constexpr (v >= 12) { if (v == nl - P.NE) tau = (tau * (nadd ? 16 : P.FE)) >> 4; }
      if constexpr (v >= NCMIN / 8) { if (v < nl) gate(); } else gate();
      SB();
      int bk = 0;
      if constexpr (v == AR) { if (part == 1 && wi < 48 && wave == 7) bk = ((int)(((u32)wall_clock64() - t0) << 8) - due + tau + (int)__builtin_amdgcn_s_getreg(7 | (3 << 11)) * tau) >> 8; SB(); }
      vr[v] = ld16<AUX_NT>(rv, l16, so);
      if constexpr (v == AR) {
        SB();
        if (part == 1 && wi < 48 && tid == 448) st8_sc1(wsr, G_AG + wi * 8, (u32)min(max(bk, 0), 250), tag);
      }
      SB();
      SB();
    }
    if constexpr (v == NCMIN / 8 - 1) {
#pragma unroll
      for (int i = NCMIN / 8; i < NL; ++i) asm volatile("" : "=v"(vr[i]));
      asm volatile("" : "=v"(fl2[1]), "=v"(fl2[2]));
#pragma unroll
      for (int w = 0; w < 8; ++w) asm volatile("" : "=v"(fmw[w]));
    }
    if constexpr (v == VQ - 4) { qg[0] = ld16<AUX_SC1>(wsr, qoff, 0); qg[1] = ld16<AUX_SC1>(wsr, qoff + 16, 0); }
    if constexpr (v == VSIDE) side_loads();
    if constexpr (v == VL) agr = ld8<AUX_SC1>(wsr, wave == 6 ? G_AG + (48 + c) * 8 : OOB, 0);
    if constexpr (v == AL) agr = ld8<AUX_SC1>(wsr, (wave == 7 && lane < 48) ? G_AG + lane * 8 : OOB, 0);
    if constexpr (v == CPF) {
      u64 pc; u32 dlt, len;
      asm volatile("s_getpc_b64 %0\n.Lcpf_here:\n\ts_mov_b32 %1, .Ltail_start-.Lcpf_here\n\ts_mov_b32 %2, .Lend_a-.Ltail_start\n\ts_max_i32 %2, %2, .Lend_b-.Ltail_start\n\ts_max_i32 %2, %2, .Lend_c-.Ltail_start\n\ts_max_i32 %2, %2, .Lend_d-.Ltail_start\n\ts_max_i32 %2, %2, .Lend_e-.Ltail_start" : "=s"(pc), "=s"(dlt), "=s"(len) :: "scc");
      pc += dlt;
      const rsrc_t rc = mk_rsrc((const void*)pc, (part == 3 && i8 == 1) ? len : 0u);
      cpf = __builtin_amdgcn_raw_buffer_load_b32(rc, tid * 64, 0, 0);
    }
    if constexpr (v == CPF + WV + 1) PIN(cpf);
    if constexpr (v == SIDE || v == SIDE + 4 || v == SIDE + 8) fl2[(v - SIDE) / 4] = ld16<AUX_SC1>(wsr, (wave == 0 && v == side) ? mlo : OOB, 0);
    SB();
    if constexpr (v < WV) { WAIT_VM(win_v(v, WK)); SB(); } else if constexpr (v < NL && cons_end(v) < v - WV + 1) { WAIT_VM(win_v(v, WV)); SB(); }
    if constexpr (v == VU) {
      if (wave == 6) {
        u32 vt[1] = {agr[0]};
        if (!all_ok(agr[1] == tag)) gather8<1>(wsr, G_AG + (48 + c) * 8, 0, tag, vt);
        float sv = u2f(vt[0]);
        sv += dpp<0x124>(sv); sv += dpp<XOR8>(sv);
        float sa = sv + dpp<XOR1>(sv);
        sv = fmaf(sa + dpp<XOR2>(sa), 0.25f, sv);
        float cf = fabsf(sv);
        cf = fminf(cf, dpp<XOR1>(cf)); cf = fminf(cf, dpp<XOR2>(cf));
        const u32 nm = (u32)__builtin_amdgcn_ballot_w64(sv > 0.f) & 15u;
        if (lane == 0) sm.nm = nm << 13 | (cf < 2500.f ? (u32)P.SL : 0u);
      }
      PIN(agr);
    }
    if constexpr (v == VQ) {
      if (!all_ok((qg[0][1] == tag) & (qg[0][3] == tag) & (qg[1][1] == tag) & (qg[1][3] == tag))) { STAMP(20); gather16<2>(wsr, qoff, 16, tag, qg); }
      STAMP(1); WSTAMP(1);
      qq[0] = qg[0][0]; qq[1] = qg[0][2]; qq[2] = qg[1][0]; qq[3] = qg[1][2];
    }
    if constexpr (v >= VQ && v < VS) {
      constexpr int u0 = SLC[v - VQ], u1 = SLC[v - VQ + 1];
      static_assert(SLC[NSL] == (NL + 7) / 8 * 8, "all groups of 8");
      sfor<u0, u1>([&](auto uc) {
        constexpr int u = uc.v, p = u & 7, i = (p & 1) * 4 + (p & 2) + (p >> 2), sl = u >> 3, bt = sl >> 1;
        float d = 0.f;
        if constexpr (sl * 8 + i < NL) {
#pragma unroll
          for (int k = 0; k < 4; ++k) d = dot2(kr[sl * 8 + i][k], qq[k], d);
        }
        if constexpr (p & 1) xs[i - 4] = sel_add<XOR7>(xs[i - 4], d, c & 4); else xs[i] = d;
        if constexpr ((p & 3) == 3) xs[p >> 2] = sel_add<XOR2>(xs[p >> 2], xs[(p >> 2) + 2], c & 2);
        if constexpr (p == 7) {
          xs[0] = sel_add<XOR1>(xs[0], xs[1], c & 1);
          if constexpr ((sl & 1) || u == (NL + 7) / 8 * 8 - 1) {
            const float t = (sl & 1) ? sel_add<XOR8>(xlo, xs[0], c & 8) : xs[0] + dpp<XOR8>(xs[0]);
            const float s = rbf(rbf(t) * 0.08838834764831845f);
            sc[bt] = (bt * 16 + c < nl) ? s : -__builtin_inff();
            PIN(sc[bt]);
          } else {
            xlo = xs[0];
            PIN(xlo);
          }
        }
        return true;
      });
      constexpr int pe = u1 & 7;
      if constexpr (pe >= 1) PIN(xs[0]);
      if constexpr (pe == 3) PIN(xs[2]);
      if constexpr (pe >= 5) PIN(xs[1]);
      if constexpr (pe == 7) PIN(xs[3]);
    }
    if constexpr (v == VS) {
      float mloc = sc[0];
#pragma unroll
      for (int bt = 1; bt < NB; ++bt) mloc = fmaxf(mloc, sc[bt]);
      m = fmaxf(wave_max(mloc), -3.0e38f);
      float ls = 0.f;
#pragma unroll
      for (int bt = 0; bt < NB; ++bt) { pr[bt] = __expf(sc[bt] - m); ls += pr[bt]; }
      const float l = wave_sum(ls);
      if (lane == 0) { sm.ml[0][wave] = m; sm.ml[1][wave] = l; }
#pragma unroll
      for (int bt = 0; bt < NB; ++bt) { pb[bt] = pack_bf(pr[bt], pr[bt]); PIN(pb[bt]); }
#pragma unroll
      for (int k = 0; k < 4; ++k) { pm[k] = (c >> 1) == k ? ((c & 1) ? 0xffff0000u : 0x0000ffffu) : 0u; PIN(pm[k]); }
      STAMP(2); WSTAMP(2);
    }
    sfor<cons_end(v - 1), cons_end(v)>([&](auto jc) {
      constexpr int i = jc.v;
      if (i < NCMIN / 8 || i < nl) {
        const u32 pv = __float_as_uint(bcast16(u2f(pb[i >> 4]), i & 15));
        acc = mfma16(u32x4{pv & pm[0], pv & pm[1], pv & pm[2], pv & pm[3]}, vr[i], acc);
      }
      PIN(acc);
      return true;
    });
    if constexpr (v == VS - 1) { side_pin(); if (is_up) up_prep(); PIN(ga); PIN(xin); PIN(cc1); }
    if constexpr (v == MLS - 1) {
      if (wave == 7) {
        const float am = wave_sum(lane < 48 ? (agr[1] == tag ? (float)agr[0] : 120.f) : 0.f) * (1.f / 48.f);
        const float st = fminf(fmaxf((am - (float)P.A0) * (float)(P.AG * 16), 0.f), (float)(P.AC * 256));
        if (lane == 0) sm.ctl = u32x2{(u32)(int)st, (u32)(int)(st * __builtin_amdgcn_rcpf((float)(nl - MLS - 3)))};
      }
      PIN(agr);
    }
    if constexpr (v == UPREP - 2) {
      nmask = __builtin_amdgcn_readfirstlane(sm.nm);
      slack = (nmask & 255u) * 256; near = ((nmask >> (xcd & 3)) ^ (xcd << 11)) & 8192u;
      w2_addr();
    }
    if constexpr (v == UPREP - 1) wo_addr();
    if constexpr (v == UPREP) {
#pragma unroll
      for (int k = 0; k < 8; ++k) asm volatile("" : "=v"(bf[k]));
      if (part == 0) wo_prep(0, 1); else if (is_up) up_frags(0, 8 - UD);
#pragma unroll
      for (int k = 0; k < 8; ++k) PIN(bf[k]);
    }
    if constexpr (v == UPREP + 1 || v == UPREP + 2) {
      if (is_up) { if constexpr (v == UPREP + 1) col_sums(0, (8 - UD) / 2); else col_sums((8 - UD) / 2, 8 - UD); }
      PIN(Af); PIN(Bf);
    }
    if constexpr (v == SIDE + WV + 1 || v == SIDE + WV + 5 || v == SIDE + WV + 9) {
      if (wave == 0 && v == side + WV + 1) {
        const u32x4 ml = fl2[(v - SIDE - WV - 1) / 4];
        fok = all_ok((ml[1] == tag) & (ml[3] == tag));
        merge_factor(ml);
      }
    }
    if (v % 6 == 4 && v >= 10 && v < 30) STAMP(15 + v / 6);
    if constexpr (v == MLS) {
      LDS_BARRIER();
      const u32x2 ct = sm.ctl;
      if (wave == 0) {
        const float mw = sm.ml[0][lane & 7];
        float Mwg = fmaxf(mw, dpp<XOR1>(mw)); Mwg = fmaxf(Mwg, dpp<XOR2>(Mwg)); Mwg = fmaxf(Mwg, dpp<XOR7>(Mwg));
        fwv = __expf(mw - Mwg);
        float Lwg = fwv * sm.ml[1][lane & 7];
        Lwg += dpp<XOR1>(Lwg); Lwg += dpp<XOR2>(Lwg); Lwg += dpp<XOR7>(Lwg);
        if (lane == 0) st16_sc1(wsr, G_ML + ((head * 2 + b) * 4 + part) * 16, __float_as_uint(Mwg), __float_as_uint(Lwg), tag);
      }
      PIN(fwv);
      t1 += __builtin_amdgcn_readfirstlane(ct[0]);
      tau += __builtin_amdgcn_readfirstlane(ct[1]);
    }
    SB();
    return true;
  });
  asm volatile(".Ltail_start:");
  *LDSP(f32x4, adst0) = acc;
  STAMP(14); WSTAMP(14);
  if (!fok) {
    u32x4 ml;
#pragma nounroll
    for (int spin = 0; spin < SPIN_MAX; ++spin) {
      CFENCE();
      ml = ld16<AUX_SC1>(wsr, mlo, 0);
      if (all_ok((ml[1] == tag) & (ml[3] == tag))) break;
    }
    merge_factor(ml);
  }
  STAMP(15);
  LDS_BARRIER();
  STAMP(3);
  {
    u32 pw = 0;
    if (wave == 0) {
      f32x2 t[8];
#pragma unroll
      for (int w = 0; w < 8; ++w) t[w] = *reinterpret_cast<const f32x2*>(&sm.red[w][2 * lane]);
      f32x2 h[4];
#pragma unroll
      for (int w = 0; w < 4; ++w) h[w] = __builtin_elementwise_fma(t[2 * w + 1], f32x2{fmw[2 * w + 1], fmw[2 * w + 1]}, t[2 * w] * fmw[2 * w]);
      const f32x2 o = (h[0] + h[1]) + (h[2] + h[3]);
      pw = min(pack_bf(o[0], o[1]), SENT - 1u);
    }
    __builtin_amdgcn_raw_buffer_store_b32(pw, wsr, pso, 0, AUX_SC1);
    st8_sc1(wsr, nto, (u32)wall_clock64() + nadd, tag);
    SB();
#pragma unroll
    for (int i = NCMIN / 8; i < NL; ++i) PIN(vr[i]);
    PIN(fl2[0]); PIN(fl2[1]); PIN(fl2[2]);
  }
  STAMP(4);
  SB();
  if (nadd) {
    asm volatile(".p2align 6\n\ts_barrier" ::: "memory");
#pragma nounroll
    while (wleft > 0) piece();
    if (is_up) {
      asm volatile("s_mov_b32 m0, %5\n\ts_nop 0\n\tbuffer_load_dwordx4 %0, %4, 0 offen nt lds\n\ts_add_u32 m0, m0, 0x400\n\ts_nop 0\n\tbuffer_load_dwordx4 %1, %4, 0 offen nt lds\n\t"
                   "s_add_u32 m0, m0, 0x400\n\ts_nop 0\n\tbuffer_load_dwordx4 %2, %4, 0 offen nt lds\n\ts_add_u32 m0, m0, 0x400\n\ts_nop 0\n\tbuffer_load_dwordx4 %3, %4, 0 offen nt lds"
                   :: "v"(v2[0]), "v"(v2[1]), "v"(v2[2]), "v"(v2[3]), "s"(r2), "s"(l2) : "memory", "scc", "m0");
    }
  }
  SB();

  if (part == 0) {
    until(t1);
    STAMP(11);
    CFENCE();
    u32x4 og[2];
    og[0] = ld16<AUX_SC1>(wsr, po, 0);
    SB();
    WAIT_VM(1); CFENCE();
    wo_prep(1, 2);
#pragma unroll
    for (int k = 4; k < 8; ++k) PIN(bf[k]);
    SB();
    f32x4 d[4];
    auto wo_mm = [&]() {
      const f32x4 z = {0.f, 0.f, 0.f, 0.f};
#pragma unroll
      for (int t = 0; t < 4; ++t) { d[t] = mfma16(og[0], bf[t], z); og[1][t] = __float_as_uint(dpp<XOR8>(u2f(og[0][t]))); }
      SB();
#pragma unroll
      for (int t = 0; t < 4; ++t) d[t] = mfma16(og[1], bf[4 + t], d[t]);
    };
    auto p_bad = [&]() { return !all_ok(max(max(og[0][0], og[0][1]), max(og[0][2], og[0][3])) != SENT); };
    wo_mm();
    if (__builtin_expect(p_bad(), 0)) {
#pragma nounroll
      for (int spin = 0; spin < SPIN_MAX; ++spin) {
        __builtin_amdgcn_s_sleep(1);
        CFENCE();
        og[0] = ld16<AUX_SC1>(wsr, po, 0);
        if (!p_bad()) break;
      }
      wo_mm();
      SB();
      asm volatile(".Lend_d:");
    }
    STAMP(5); WSTAMP(5);
    {
      SB();
      f32x4 y; f32x2 h[4];
#pragma unroll
      for (int t = 0; t < 4; ++t) { h[t] = d[t].xy + d[t].zw; PIN(h[t]); }
#pragma unroll
      for (int t = 0; t < 4; ++t) { y[t] = h[t][0] + h[t][1]; PIN(y[t]); }
      *LDSP(f32x4, rdst) = y;
    }
    STAMP(16);
    LDS_BARRIER();
    STAMP(17);
    if (wave < 2) {
      f32x2 v[4];
#pragma unroll
      for (int w = 0; w < 4; ++w) v[w] = f32x2{LDSP(float, rsrc)[w * 256], LDSP(float, rsrc)[w * 256 + 128]};
      const f32x2 h = (v[0] + v[1]) + (v[2] + v[3]);
      const float y = h[0] + h[1];
      __builtin_amdgcn_raw_buffer_store_b32(min(pack_bf(dpp<0xA0>(y), dpp<0xF5>(y)), SENT - 1u), wsr, xo, 0, AUX_SC1);
      asm volatile(".Lend_a:");
    }
    STAMP(6);
    FINISH();
  }
  if (part == 3) {
    if (!has_w) FINISH();
    u32x4 b2[4];
    const u32 tdst = lds_addr(&sm.hb[hpos]);
    const u32 sdst = lds_addr(&sm.stat[wave][lane]);
    const u32 ssrc = lds_addr(&sm.stat[(g >> 1) * 4 + ((lane >> 1) & 3)][(lane & 1) * 16]);
    const u32 adst = lds_addr((c < 4 && (g & 1) == 0) ? &sm.actl[g >> 1][wave * 4 + c] : reinterpret_cast<u16*>(&sm.dummy[lane]));
    const u32 asrc = lds_addr(&sm.actl[lane & 1][g * 8]);
    const u32 apa = lds_addr(&sm.hb[(c >> 3) * 512 + (c & 3) * 64 + ((c >> 2) & 1) * 32 + g * 8]);
    u32 xoff = dup(G_XP) + near + g * 2048 + ub * 1024 + (wave & 3) * 256 + c * 16;
    u32x4 one;
#pragma unroll
    for (int k = 0; k < 4; ++k) one[k] = ((c & 2) == 0 && (c >> 2) == k) ? ((c & 1) ? 0x3f800000u : 0x00003f80u) : 0u;
    PIN(one);
    u32 ooff = dup(G_OP + (blk * 512 + tid) * 8) + ((nmask >> (lane >> 3 & 1)) & 8192u);
    PIN(xoff); PIN(ooff);
    PIN(ga); PIN(xin); PIN(cc1);
    u32 w2a = (u32)(uintptr_t)(&sm.w2b[0][0]) + (g * 8 + ((lane >> 2) & 3)) * 1024 + (wave ^ (g & 1)) * 128 + (lane & 15) * 8;
    PIN(w2a);
    if (wave == 1) {
      due = t1 + P.N1 * 256; gate();
      u32x4 gn[2] = {ld16<AUX_SC1>(wsr, G_NT + lane * 16, 0), ld16<AUX_SC1>(wsr, G_NT + lane * 16, 1024)};
      const int tn = notices(IC<2>{}, G_NT + lane * 16, gn) * 256;
      due = (tn > t1 + P.TH * 256 ? max(tn, t1 + P.L0 * 256) + P.L1 * 256 : t1) + P.X2 * 256 + slack;
      until(due);
    }
    asm volatile("s_waitcnt vmcnt(0)" ::: "memory");
    STAMP(9);
    __builtin_amdgcn_s_barrier();
    STAMP(11);
    CFENCE();
    u32x4 xg[2] = {ld16<AUX_SC1>(wsr, xoff, 0), ld16<AUX_SC1>(wsr, xoff, 16384)};
    SB();
    if constexpr (UD > 0) { up_frags(8 - UD, 8); col_sums(8 - UD, 8); }
    PIN(Af); PIN(Bf);
#pragma unroll
    for (int t = 0; t < 4; ++t) {
      const u32x2 lo = tr_read(w2a ^ (t * 32)), hi = tr_read((w2a ^ (t * 32)) + 4096);
      b2[t] = u32x4{lo[0], lo[1], hi[0], hi[1]};
    }
#pragma unroll
    for (int t = 0; t < 4; ++t) PIN(b2[t]);
    SB();
    const f32x4 z4 = {0.f, 0.f, 0.f, 0.f};
    auto xp_bad = [&]() { return !all_ok(max(max(max(xg[0][0], xg[0][1]), max(xg[0][2], xg[0][3])), max(max(xg[1][0], xg[1][1]), max(xg[1][2], xg[1][3]))) != SENT); };
    f32x4 v = mfma16(one, xg[1], mfma16(one, xg[0], z4));
    if (__builtin_expect(xp_bad(), 0)) {
#pragma nounroll
      for (int spin = 0; spin < SPIN_MAX; ++spin) {
        __builtin_amdgcn_s_sleep(1);
        CFENCE();
        xg[0] = ld16<AUX_SC1>(wsr, xoff, 0); xg[1] = ld16<AUX_SC1>(wsr, xoff, 16384);
        if (!xp_bad()) break;
      }
      v = mfma16(one, xg[1], mfma16(one, xg[0], z4));
      SB();
      asm volatile(".Lend_e:");
    }
    STAMP(5); WSTAMP(5);
    {
      const float a0 = v[0], a1 = v[1];
      const float x10 = fmaf(ga[0], a0, xin[0]), x11 = fmaf(ga[1], a1, xin[1]);
      *LDSP(u32, tdst) = pack_bf(x10 * cc1[0], x11 * cc1[1]);
      float u = swap32_add(x10 + x11, fmaf(x10, x10, x11 * x11));
      u += dpp<XOR1>(u); u += dpp<XOR2>(u); u += dpp<XOR7>(u); u += dpp<XOR8>(u);
      *LDSP(float, sdst) = u;
    }
    STAMP(16);
    LDS_BARRIER();
    STAMP(17);
    {
      const f32x4 z = {0.f, 0.f, 0.f, 0.f};
      const u32x4 af0 = LDSP(u32x4, apa)[0], af1 = LDSP(u32x4, apa)[32];
      float s = LDSP(float, ssrc)[0], q = LDSP(float, ssrc)[32];
      SB();
      f32x4 d[4];
#pragma unroll
      for (int j = 0; j < 4; ++j) d[j] = mfma16(af0, bf[j], z);
#pragma unroll
      for (int j = 0; j < 4; ++j) d[j] = mfma16(af1, bf[4 + j], d[j]);
      s += dpp<XOR1>(s); q += dpp<XOR1>(q); s += dpp<XOR2>(s); q += dpp<XOR2>(q); s += dpp<XOR7>(s); q += dpp<XOR7>(q);
      const float mean = s * (1.f / 512.f);
      const float var = fmaf(q, 1.f / 512.f, -mean * mean);
      const float ri = __builtin_amdgcn_rsqf(fmaxf(var, 0.f) + 1e-5f);
      const float cf = fmaf(-ri * mean, Af, Bf);
      float y = (d[0][0] + d[1][1]) + (d[2][2] + d[3][3]);
      y = __builtin_bit_cast(float, __builtin_amdgcn_update_dpp(__builtin_bit_cast(int, y), __builtin_bit_cast(int, y), XOR8, 0xa, 0xf, false));
      y = swap16_add(y, y);
      y = fmaf(y, ri, cf);
      const float up = dpp<0x104>(y);
      *LDSP(u16, adst) = (u16)pack_bf(silu_f(y) * up, 0.f);
    }
    STAMP(18);
    LDS_BARRIER();
    STAMP(10);
    {
      const u32x4 af = *LDSP(u32x4, asrc);
      const f32x4 z = {0.f, 0.f, 0.f, 0.f};
      const f32x4 d0 = mfma16(af, b2[0], z), d1 = mfma16(af, b2[1], z), d2 = mfma16(af, b2[2], z), d3 = mfma16(af, b2[3], z);
      const bool g1 = g & 1, g2 = g & 2;
      const float e0 = g1 ? d1[0] : d0[0], e1 = g1 ? d1[1] : d0[1], f0 = g1 ? d3[0] : d2[0], f1 = g1 ? d3[1] : d2[1];
      st8_sc1(wsr, ooff, pack_bf(g2 ? f0 : e0, g2 ? f1 : e1), tag);
    }
    asm volatile(".Lend_b:");
    STAMP(6);
    FINISH();
  }
  if (fin) {
    const int d = fd, ul = lane >> 3;
    u32 gm[2] = {sg[0][0], sg[1][0]};
    if (!all_ok((sg[0][1] == tag) & (sg[1][1] == tag))) gather8<2>(wsr, G_MOD + (1024 + d) * 8, 1536 * 8, tag, gm);
    u32 xa0, xa1;
    {
      due = t1 + P.N2 * 256; gate();
      u32x4 gn[2] = {ld16<AUX_SC1>(wsr, G_NT + lane * 16, 0), ld16<AUX_SC1>(wsr, G_NT + lane * 16, 1024)};
      const int tn = notices(IC<2>{}, G_NT + lane * 16, gn) * 256;
      due = (tn > t1 + P.TH * 256 ? max(tn, t1 + P.L0 * 256) + P.L1 * 256 : t1) + P.X3 * 256 + slack;
      until(due);
    }
#pragma nounroll
    for (int spin = 0; spin < SPIN_MAX; ++spin) {
      CFENCE();
      xa0 = __builtin_amdgcn_raw_buffer_load_b32(wsr, dup(G_XP + ul * 2048) + near + (d >> 1) * 4, 0, AUX_SC1);
      xa1 = __builtin_amdgcn_raw_buffer_load_b32(wsr, dup(G_XP + ul * 2048) + near + (d >> 1) * 4, 1024, AUX_SC1);
      if (all_ok(max(xa0, xa1) != SENT)) break;
      __builtin_amdgcn_s_sleep(1);
    }
    float a0 = (d & 1) ? hi_f(xa0) : lo_f(xa0), a1 = (d & 1) ? hi_f(xa1) : lo_f(xa1);
    a0 += dpp<XOR8>(a0); a1 += dpp<XOR8>(a1);
    a0 = swap16_add(a0, a0); a1 = swap16_add(a1, a1);
    a0 = swap32_add(a0, a0); a1 = swap32_add(a1, a1);
    const bool hb = g & 1;
    float xv = rbf(((d & 1) ? hi_f(sx2) : lo_f(sx2)) + rbf((hb ? hi_f(gm[0]) : lo_f(gm[0])) * rbf(hb ? a1 : a0)));
    float gv = hb ? hi_f(gm[1]) : lo_f(gm[1]);
    PIN(xv); PIN(gv);
    if (fw == 0 && lane == 0) __builtin_amdgcn_raw_buffer_store_b32(tag, wsr, WS_EPOCH, 0, AUX_SC1);
    STAMP(10);
    due = max(due + P.X4 * 256 + slack, (int)(((u32)wall_clock64() - t0) << 8) + P.GX * 256);
    const bool last = ul + 40 < NUP;
    u32 o0 = dup(G_OP + (ul * 512 + d) * 8) + near, o5 = last ? o0 : OOB, tg5 = last ? tag : 0u;
    const rsrc_t rout = mk_rsrc(out, 2048u);
    u32 oo = (lane & 40) == 0 ? (g * 512 + d) * 2 : OOB;
    const u32 pk = c == 0 ? 0x00003f80u : (c == 4 ? 0x3f800000u : 0u);
    u32x4 one = {pk, 0u, pk, 0u};
    PIN(o0); PIN(o5); PIN(tg5); PIN(oo); PIN(one);
    __builtin_amdgcn_raw_buffer_store_b16((u16)pack_bf(xv, 0.f), rout, oo, 0, AUX_SC1);
    until(due);
    f32x4 sv;
#pragma nounroll
    for (int spin = 0; spin < SPIN_MAX; ++spin) {
      CFENCE();
      u32x2 og[6];
#pragma unroll
      for (int k = 0; k < 5; ++k) og[k] = ld8<AUX_SC1>(wsr, o0, k * 65536);
      og[5] = ld8<AUX_SC1>(wsr, o5, 5 * 65536);
      sv = f32x4{0.f, 0.f, 0.f, 0.f};
#pragma unroll
      for (int k = 0; k < 3; ++k) sv = mfma16(one, u32x4{og[2 * k][0], og[2 * k][1], og[2 * k + 1][0], og[2 * k + 1][1]}, sv);
      const u32 bad = ((og[0][1] ^ tag) | (og[1][1] ^ tag) | (og[2][1] ^ tag)) | ((og[3][1] ^ tag) | (og[4][1] ^ tag) | (og[5][1] ^ tg5));
      PIN(sv);
      if (all_ok(bad == 0u)) break;
      __builtin_amdgcn_s_sleep(1);
    }
    STAMP(7);
    const float s = sv[0] + dpp<XOR8>(sv[0]);
    __builtin_amdgcn_raw_buffer_store_b16((u16)pack_bf(fmaf(gv, s, xv), 0.f), rout, oo, 0, AUX_SC1);
    asm volatile(".Lend_c:");
  }
  FINISH();
}

extern "C" void kernel_launch(void* const* d_in, const int* in_sizes, int n_in,
                              void* d_out, int out_size, void* d_ws, size_t ws_size,
                              hipStream_t stream) {
  fused_kernel<<<256, 512, 0, stream>>>((const u16*)d_in[0], (const u16*)d_in[1], (const u16*)d_in[2], (const u16*)d_in[3], (const u16*)d_in[4],
                                        (const u16*)d_in[6], (const u16*)d_in[7], (const u16*)d_in[8], (const u16*)d_in[9], (const u16*)d_in[10],
                                        (unsigned char*)d_ws, (u16*)d_out
    );
}
```

```cpp
#include <hip/hip_runtime.h>
#include <stdint.h>

typedef unsigned int u32;
typedef unsigned short u16;
typedef unsigned long long u64;
typedef __attribute__((ext_vector_type(4))) unsigned u32x4;
typedef __attribute__((ext_vector_type(2))) unsigned u32x2;
typedef __attribute__((ext_vector_type(2))) __bf16 bf16x2;
typedef __attribute__((ext_vector_type(4))) float f32x4;
typedef __attribute__((ext_vector_type(2))) float f32x2;
typedef __amdgpu_buffer_rsrc_t rsrc_t;

constexpr int NH = 32, SK = 16384, DH = 128, WIN = 4096, FF = 1376;
struct Prm { int E0, ES, E1, E2, E2C, EU, TEND, DT_UP, L0, L1, X1, X2, X3, X4, NE, FE, M0, M1, M2, M3, M4, GS, GX, N1, N2, BT, WD, DT_WO, LW, LU, EW, PM, WM, FK, FW, FU, EX, TS, DS, A0, AG, AC, PT, SL, TH, DT_0, L3; };
constexpr Prm PRM0 = {32, 24, 32, 32, 48, 64, 2008, 96, 25, 18, 72, 78, 98, 86, 5, 12, 12, 5, 12, 32, 28, 34, 34, 18, 45, 10, 4, 103, 40, 40, 8, 0x6827941, 0x53, 62, 61, 60, 8, 500, 122, 56, 24, 120, 160, 10, 6, 25, 15};
#define PRM_ARG
#define TABP (&TAB.e[0])
constexpr int NL = 37;
constexpr int NCMIN = 192;
constexpr int WK = 6, WV = 5;
constexpr int WUSE = 13, LNS = 11;
constexpr int QP1 = 12, QP2 = 18, QD = 5;
constexpr int VQ = 6;
constexpr int SLC[] = {0, 6, 9, 15, 21, 27, 33, 40}, NSL = sizeof(SLC) / sizeof(int) - 1;
constexpr int VS = VQ + NSL;
constexpr int MLS = VS + 2;
constexpr int VSIDE = VQ + 1, UPREP = 21;
constexpr int SIDE = NCMIN / 8 - 4;
constexpr int UD = 1;
constexpr int CPF = 18;
constexpr int TBP = 7, TBL = 11, TBU = 16;
constexpr int AR = 0, AL = 10;
constexpr int VL = 0, VU = 5;
constexpr int D0 = 30;
constexpr int KF = 4, KF0 = 8;
constexpr int side_k(int i) { return (i == KF || i == KF + 1) ? 3 : (i == TBL ? 1 : 0) + ((i == QP1 || i == QP2) ? 2 : 0); }
constexpr int side_v(int v) { return v == VQ - 4 ? 2 : (v == VSIDE ? 4 : ((v == AL || v == VL) ? 1 : 0)); }
constexpr int win_k(int i) { int n = WK; for (int j = i - WK; j <= i; ++j) n += side_k(j); return n; }
constexpr int win_v(int v, int w) { int n = w; for (int j = v - w; j <= v; ++j) n += side_v(j); return n; }
constexpr int cons_end(int v) {
  const int a = v - WV + 1, c = (v - VS) * 3;
  const int m = a < c ? a : c;
  return m < 0 ? 0 : (m > NL ? NL : m);
}
struct Tab { u32 e[1024]; };
#define popc __builtin_popcount
__host__ __device__ constexpr u32 place(u32 v, u32 m, int n) {
  u32 r = 0;
  for (int k = 0; k < n; ++k) r |= (v >> k & 1u) << (10 + (m >> (4 * k) & 15));
  return r;
}
__host__ __device__ constexpr u32 step_off(const Prm& P, u32 s0, u32 bh) {
  const u32 src = (s0 >> 5 & 3u) | bh << 2;
  return place(s0 ^ ((popc(src & P.M0) & 1) | (popc(src & P.M1) & 1) << 1 | (popc(src & P.M2) & 1) << 2 | (popc(src & P.M3) & 1) << 3 | (popc(src & P.M4) & 1) << 4), P.PM, 7);
}
constexpr Tab make_tab(const Prm& P) {
  Tab T{};
  for (int bid = 0; bid < 256; ++bid) {
    const int xcd = bid & 7, idx = bid >> 3, part = idx & 3, i8 = idx >> 2, wi = i8 * 8 + xcd;
    const bool has_mod = wi < 48, has_up = wi >= 21;
    const int cb0 = (i8 + 5) & 7, cb1 = (i8 + 4) & 7, ms = wi - 20;
    const int mi = wi < 8 ? wi : (wi < 16 ? 8 + ((xcd + 4) & 7) : (wi < 20 ? (wi - 14) * 8 + 3 : 16 + ms / 7 * 8 + ms % 7 + (ms % 7 >= 3 ? 1 : 0)));
    const int ui = xcd * 5 + (xcd > 5 ? xcd - 5 : 0) + (i8 >= 3 ? (xcd >= 5 ? 1 : 0) + i8 - 3 : 0);
    const int wd = cb0 == 3 ? 0 : P.WD;
    const int e[4] = {P.E0 - 4 * wd + (cb0 == 3 ? P.ES : 0), P.E1 + (cb1 == 3 ? P.ES : 0), has_mod ? (wi < 16 ? P.E2C : P.E2) + ((mi & 7) == 3 ? P.ES : 0) : 0, has_up ? P.EU * (8 - UD) / 8 : 0};
    const int ew = wd ? P.EW : 0, pa = (1024 + e[0] + ew + e[1] + e[2] + e[3] + (has_up ? P.EX : 0)) / 4;
    int c[5] = {0, 0, 0, 0, 128};
    for (int k = 0; k < 3; ++k) c[k + 1] = c[k] + (pa - e[k] - (k == 0 ? ew : 0) + 4) / 8;
    const int nl = c[part + 1] - c[part], wst = e[part] / 4;
    const bool has_w = part < 2 || (part == 2 ? has_mod : has_up);
    const int blk = !has_w ? 0 : (part == 0 ? cb0 : (part == 1 ? xcd * 8 + cb1 : (part == 2 ? mi : ui)));
    const int span = (part == 3 && has_up ? P.TEND - P.DT_UP : (part == 0 ? P.TEND - (wd ? P.DT_WO : P.DT_0) : P.TEND)) * 256;
    const int np = part == 0 ? 8 - wd : (part == 3 ? 8 - UD : 8), tau0 = span / (2 * nl + wst);
    const bool up = part == 3 && has_up;
    const int tau = tau0 * (up ? P.FU : (has_w && np == 8 ? P.FK : 64)) / 64, tauw = tau0 * wst / np * (up ? P.FU : P.FW) / 64;
    int sw = KF, at = P.DS * 256 + (has_w ? np * tauw : 0);
    while (sw < NCMIN / 8 - 1 && at < P.TS * 256) { at += tau; ++sw; }
    const int tau2 = (span - at) / (2 * nl - sw);
    T.e[4 * bid] = (u32)(c[part] | nl << 7 | blk << 13 | (has_w ? 1 : 0) << 19 | (8 - np) << 20 | sw << 24);
    T.e[4 * bid + 1] = (u32)(tau | tauw << 16);
    if ((part == 1 && nl <= QP2 + QD + 1) || c[part] > 32 * part + KF0 || c[part + 1] < 32 * part + KF0 + 4) T.e[4 * bid] = 0;
    const u32 pitch = part == 0 ? 1024u : (part == 1 ? 8192u : (part == 2 ? 6144u : 2752u));
    T.e[4 * bid + 2] = (part == 0 ? xcd * 512u * pitch : 0u) + blk * (part == 3 ? 64u : 128u);
    T.e[4 * bid + 3] = pitch | (u32)tau2 << 16;
  }
  return T;
}
constexpr int step_range(const Prm& P, bool mx_) {
  int mx = 0, mn = 1 << 20;
  const Tab T = make_tab(P);
  for (int bid = 0; bid < 256; ++bid) { const int n = T.e[4 * bid] >> 7 & 63; mx = n > mx ? n : mx; mn = n < mn ? n : mn; }
  return mx_ ? mx : mn;
}
__constant__ __attribute__((aligned(64))) const Tab TAB = make_tab(PRM0);
static_assert(step_range(PRM0, true) <= NL && step_range(PRM0, false) >= NCMIN / 8, "a wave's key range does not fit its load steps");
static_assert(cons_end(SIDE + WV + 1) <= NCMIN / 8 && cons_end(UPREP) <= NCMIN / 8 && SIDE > MLS + 3, "steps every wave runs");
constexpr int NUP = FF / 32;
constexpr int SPIN_MAX = 1 << 17;

constexpr u32 WS_EPOCH = 0;
constexpr u32 G_TB = 1024;
constexpr u32 G_NT = 2048;
constexpr u32 G_MOD = 4096;
constexpr u32 G_Q = G_MOD + 3072 * 8;
constexpr u32 G_AG = G_Q + 4096 * 8;
constexpr u32 G_ML = G_AG + 4096;
constexpr u32 G_P = 81920;
constexpr u32 G_U = G_P + 65536;
__device__ __forceinline__ u32 dup(u32 o) { return G_U + ((o >> 13) << 14) + (o & 8191u); }
constexpr u32 G_XP = 0;
constexpr u32 G_OP = G_XP + 8 * 512 * 4;
constexpr u32 G_PB = G_U + 2 * (G_OP + 44 * 512 * 8);
constexpr u32 WS_END = G_PB + 32768;
static_assert(G_P % 16384 == 0 && G_PB % 16384 == 0 && G_ML + 4096 <= G_P, "layout");
constexpr u32 SENT = 0xffffffffu;
constexpr u32 OOB = 0x40000000u;
#define WST 0

constexpr int AUX_NT = 2, AUX_SC1 = 16;

__device__ __forceinline__ float lo_f(u32 v) { return __uint_as_float(v << 16); }
__device__ __forceinline__ float hi_f(u32 v) { return __uint_as_float(v & 0xffff0000u); }
__device__ __forceinline__ float rbf(float x) { return (float)(__bf16)x; }
__device__ __forceinline__ u32 pack_bf(float lo, float hi) {
  bf16x2 r; r[0] = (__bf16)lo; r[1] = (__bf16)hi; return __builtin_bit_cast(u32, r);
}
__device__ __forceinline__ float dot2(u32 k, u32 q, float acc) {
  return __builtin_amdgcn_fdot2_f32_bf16(__builtin_bit_cast(bf16x2, k), __builtin_bit_cast(bf16x2, q), acc, false);
}
template <int CTRL> __device__ __forceinline__ float dpp(float x) {
  return __builtin_bit_cast(float, __builtin_amdgcn_mov_dpp(__builtin_bit_cast(int, x), CTRL, 0xf, 0xf, true));
}
constexpr int XOR1 = 0xB1, XOR2 = 0x4E, XOR7 = 0x141, XOR8 = 0x128;
__device__ __forceinline__ float swap32_add(float a, float b) {
  auto t = __builtin_amdgcn_permlane32_swap(__float_as_uint(a), __float_as_uint(b), false, false);
  return __uint_as_float(t[0]) + __uint_as_float(t[1]);
}
__device__ __forceinline__ float swap16_add(float a, float b) {
  auto t = __builtin_amdgcn_permlane16_swap(__float_as_uint(a), __float_as_uint(b), false, false);
  return __uint_as_float(t[0]) + __uint_as_float(t[1]);
}
__device__ __forceinline__ float wave_sum(float x) {
  x += dpp<XOR1>(x); x += dpp<XOR2>(x); x += dpp<XOR7>(x); x += dpp<XOR8>(x);
  x = swap16_add(x, x);
  return swap32_add(x, x);
}
__device__ __forceinline__ float wave_max(float x) {
  x = fmaxf(x, dpp<XOR1>(x)); x = fmaxf(x, dpp<XOR2>(x)); x = fmaxf(x, dpp<XOR7>(x)); x = fmaxf(x, dpp<XOR8>(x));
  auto s = __builtin_amdgcn_permlane16_swap(__float_as_uint(x), __float_as_uint(x), false, false);
  x = fmaxf(__uint_as_float(s[0]), __uint_as_float(s[1]));
  auto t = __builtin_amdgcn_permlane32_swap(__float_as_uint(x), __float_as_uint(x), false, false);
  return fmaxf(__uint_as_float(t[0]), __uint_as_float(t[1]));
}
template <int CTRL> __device__ __forceinline__ float sel_add(float lo, float hi, bool up) {
  const float t1 = lo + dpp<CTRL>(lo), t2 = hi + dpp<CTRL>(hi);
  return up ? t2 : t1;
}
template <int I> __device__ __forceinline__ float bc16(float x) {
  return __builtin_bit_cast(float, __builtin_amdgcn_update_dpp(0, __builtin_bit_cast(int, x), 0x150 + I, 0xf, 0xf, false));
}
__device__ __forceinline__ float bcast16(float x, int i) {
  switch (i) {
    case 0: return bc16<0>(x); case 1: return bc16<1>(x); case 2: return bc16<2>(x); case 3: return bc16<3>(x);
    case 4: return bc16<4>(x); case 5: return bc16<5>(x); case 6: return bc16<6>(x); case 7: return bc16<7>(x);
    case 8: return bc16<8>(x); case 9: return bc16<9>(x); case 10: return bc16<10>(x); case 11: return bc16<11>(x);
    case 12: return bc16<12>(x); case 13: return bc16<13>(x); case 14: return bc16<14>(x); default: return bc16<15>(x);
  }
}
__device__ __forceinline__ float silu_f(float x) { return x * __builtin_amdgcn_rcpf(1.f + __expf(-x)); }

template <int AUX> __device__ __forceinline__ u32x4 ld16(rsrc_t r, u32 voff, u32 soff) {
  return __builtin_amdgcn_raw_buffer_load_b128(r, voff, soff, AUX);
}
template <int AUX> __device__ __forceinline__ u32x2 ld8(rsrc_t r, u32 voff, u32 soff) {
  return __builtin_amdgcn_raw_buffer_load_b64(r, voff, soff, AUX);
}
__device__ __forceinline__ void st8_sc1(rsrc_t r, u32 voff, u32 v, u32 tag) {
  __builtin_amdgcn_raw_buffer_store_b64(u32x2{v, tag}, r, voff, 0, AUX_SC1);
}
__device__ __forceinline__ void st16_sc1(rsrc_t r, u32 voff, u32 v0, u32 v1, u32 tag) {
  __builtin_amdgcn_raw_buffer_store_b128(u32x4{v0, tag, v1, tag}, r, voff, 0, AUX_SC1);
}
__device__ __forceinline__ rsrc_t mk_rsrc(const void* p, u32 bytes) {
  return __builtin_amdgcn_make_buffer_rsrc(const_cast<void*>(p), 0, bytes, 0x00020000);
}
#define WAIT_VM(n) __builtin_amdgcn_s_waitcnt(((n) & 15) | (((n) >> 4) << 14) | (7 << 4) | (15 << 8))
#define SB() __builtin_amdgcn_sched_barrier(0)
#define CFENCE() asm volatile("" ::: "memory")
__device__ __forceinline__ bool all_ok(bool ok) { return __builtin_amdgcn_ballot_w64(!ok) == 0ull; }

template <int N> __device__ __forceinline__ void gather16(rsrc_t ws, u32 voff, u32 stride, u32 tag, u32x4 (&g)[N]) {
#pragma nounroll
  for (int spin = 0; spin < SPIN_MAX; ++spin) {
    CFENCE();
    bool ok = true;
#pragma unroll
    for (int i = 0; i < N; ++i) g[i] = ld16<AUX_SC1>(ws, voff + i * stride, 0);
#pragma unroll
    for (int i = 0; i < N; ++i) ok &= (g[i][1] == tag) & (g[i][3] == tag);
    if (all_ok(ok)) break;
    __builtin_amdgcn_s_sleep(8);
  }
}
template <int N> __device__ __forceinline__ void gather8(rsrc_t ws, u32 voff, u32 stride, u32 tag, u32 (&g)[N], bool act = true) {
#pragma nounroll
  for (int spin = 0; spin < SPIN_MAX; ++spin) {
    CFENCE();
    bool ok = true;
    u32x2 t[N];
#pragma unroll
    for (int i = 0; i < N; ++i) t[i] = ld8<AUX_SC1>(ws, voff + i * stride, 0);
#pragma unroll
    for (int i = 0; i < N; ++i) { ok &= (t[i][1] == tag); g[i] = t[i][0]; }
    if (all_ok(ok | !act)) break;
    __builtin_amdgcn_s_sleep(8);
  }
}

struct Smem {
  float red[8][128];
  float ml[2][8];
  float r2[64 * 20];
  u16 hb[3 * 1024];
  float stat[8][64];
  float red2[8][128];
  u32 dummy[64];
  u32x2 bc;
  u32x2 ctl;
  u32 nm, pad_[3];
  u32x4 zero[4];
  u32x4 wbuf[8][8][64];
  __attribute__((aligned(256))) u16 w2b[32][512];
  u16 actl[2][32];
};

#define STAMP(k) do {} while (0)
#define WSTAMP(k) do {} while (0)
#define FINISH() return
#define PIN(x) asm volatile("" : "+v"(x))
#define LDS_BARRIER() do { __builtin_amdgcn_s_waitcnt(0xc07f); __builtin_amdgcn_s_barrier(); CFENCE(); } while (0)
#define WAVE_SYNC() do { __builtin_amdgcn_fence(__ATOMIC_RELEASE, "wavefront"); __builtin_amdgcn_wave_barrier(); __builtin_amdgcn_fence(__ATOMIC_ACQUIRE, "wavefront"); } while (0)

__device__ __forceinline__ void dma16(rsrc_t r, u32 voff, u32 soff, u32 lds_dst) {
  u32 keep;
  asm volatile("s_mov_b32 %0, m0\n\ts_mov_b32 m0, %4\n\ts_nop 4\n\tbuffer_load_dwordx4 %1, %2, %3 offen nt lds\n\ts_mov_b32 m0, %0"
               : "=&s"(keep) : "v"(voff), "s"(r), "s"(soff), "s"(lds_dst) : "memory");
}
__device__ __forceinline__ u32 perm_lo(u32 a, u32 b) { return __builtin_amdgcn_perm(b, a, 0x05040100u); }
__device__ __forceinline__ u32 perm_hi(u32 a, u32 b) { return __builtin_amdgcn_perm(b, a, 0x07060302u); }
__device__ __forceinline__ float rdl(float x, int l) { return __int_as_float(__builtin_amdgcn_readlane(__float_as_int(x), l)); }

typedef __attribute__((ext_vector_type(8))) __bf16 bf16x8;
__device__ __forceinline__ f32x4 mfma16(u32x4 a, u32x4 b, f32x4 c) {
  return __builtin_amdgcn_mfma_f32_16x16x32_bf16(__builtin_bit_cast(bf16x8, a), __builtin_bit_cast(bf16x8, b), c, 0, 0, 0);
}
__device__ __forceinline__ float sum8(const float* p) {
  const f32x4 a = *reinterpret_cast<const f32x4*>(p), b = *reinterpret_cast<const f32x4*>(p + 4);
  return ((a[0] + b[0]) + (a[1] + b[1])) + ((a[2] + b[2]) + (a[3] + b[3]));
}
__device__ __forceinline__ float ln_ri(float var) {
  return __builtin_amdgcn_rcpf(rbf(__builtin_amdgcn_sqrtf(rbf(rbf(fmaxf(var, 0.f)) + 1e-5f))));
}
__device__ __forceinline__ void ln_stats2(const float (&x0)[8], const float (&x1)[8], float& mb0, float& ri0, float& mb1, float& ri1) {
  const float k0 = rdl(x0[0], 0), k1 = rdl(x1[0], 0);
  float s0 = 0.f, s1 = 0.f, q0 = 0.f, q1 = 0.f;
#pragma unroll
  for (int k = 0; k < 8; ++k) {
    const float d0 = x0[k] - k0, d1 = x1[k] - k1;
    s0 += d0; s1 += d1; q0 = fmaf(d0, d0, q0); q1 = fmaf(d1, d1, q1);
  }
  float t = swap16_add(swap32_add(s0, s1), swap32_add(q0, q1));
  t += dpp<XOR1>(t); t += dpp<XOR2>(t); t += dpp<XOR7>(t); t += dpp<XOR8>(t);
  auto u = __builtin_amdgcn_permlane16_swap(__float_as_uint(t), __float_as_uint(t), false, false);
  const float ms = __uint_as_float(u[0]) * (1.f / 512.f), mq = __uint_as_float(u[1]) * (1.f / 512.f);
  const float ri = ln_ri(fmaf(-ms, ms, mq));
  mb0 = rbf(k0 + rdl(ms, 0)); mb1 = rbf(k1 + rdl(ms, 32));
  ri0 = rdl(ri, 0); ri1 = rdl(ri, 32);
}
template <int I> struct IC { static constexpr int v = I; };
template <int I, int N, class F> __device__ __forceinline__ void sfor(F&& f) {
  if constexpr (I < N) { if (f(IC<I>{})) sfor<I + 1, N>(f); }
}
typedef __attribute__((ext_vector_type(4))) short s16x4;
typedef __attribute__((address_space(3))) s16x4* lds_s16x4p;
__device__ __forceinline__ u32x2 tr_read(u32 lds_addr) {
  return __builtin_bit_cast(u32x2, __builtin_amdgcn_ds_read_tr16_b64_v4i16((lds_s16x4p)lds_addr));
}
__device__ __forceinline__ u32 next_tag(u32 e) { return ((e | 0x40004000u) + 1u) & ~0x40004000u; }
__device__ __forceinline__ float u2f(u32 v) { return __uint_as_float(v); }
#define LDSP(T, a) ((__attribute__((address_space(3))) T*)(a))
template <class T> __device__ __forceinline__ u32 lds_addr(T* p) { u32 a = (u32)(uintptr_t)p; PIN(a); return a; }

typedef __attribute__((ext_vector_type(16))) unsigned u32x16;
typedef __attribute__((ext_vector_type(8))) unsigned u32x8;
__global__ void __launch_bounds__(512, 2) fused_kernel(const u16* x_, const u16* kc_, const u16* vc_, const u16* wq_, const u16* wo_, const u16* w1_,
                                                       const u16* w3_, const u16* w2_, const u16* cnd_, const u16* wmod_, unsigned char* ws_, u16* out_ PRM_ARG) {
  __shared__ __attribute__((aligned(256))) Smem sm;
  constexpr Prm P = PRM0;
  const int TEND = P.TEND;
  const int bid = blockIdx.x, tid = threadIdx.x;
  u32x4 kr[NL];
  u32x16 ka; u32x8 kb; u32x4 tev; u64 pt0; u32 tch2;
  asm volatile("s_load_dwordx16 %0, %3, 0x0\n\ts_load_dwordx8 %1, %3, 0x40\n\ts_memrealtime %2"
               : "=&s"(ka), "=&s"(kb), "=&s"(pt0) : "s"(__builtin_amdgcn_kernarg_segment_ptr()));
  SB();
  {
    u64 pc; u32 vb;
    asm volatile("s_getpc_b64 %2\n.Lcpf_first:\n\tv_mul_u32_u24 %1, ((.Lend_c-.Lcpf_first)>>10)<<2, %3\n\tglobal_load_dword %0, %1, %2"
                 : "=&v"(tch2), "=&v"(vb), "=&s"(pc) : "v"((bid & ~7) + (tid >> 6)) : "memory");
    asm volatile("global_load_dwordx4 %0, %1, %2" : "=&v"(tev) : "v"(bid << 4), "s"(TABP) : "memory");
  }
  SB();
  const int xcd = bid & 7, idx = bid >> 3, part = idx & 3;
  const int b = idx >> 2 & 1, head = xcd * 4 + (((idx >> 3) & 1) << 1 | idx >> 4);
  const u32 so0 = step_off(P, 32 * part + KF0, b * 32 + head) & ~place(3, P.PM, 2);
  u32 va = (((b * 32 + head) * SK + (SK - WIN)) * DH * 2 + so0) | place(tid >> 6, P.WM, 3) | (tid & 63) << 4;
  const u32 kx1 = place(1, P.PM, 2), kx2 = place(2, P.PM, 2);
  PIN(va);
  u32 va1 = va | kx1, va2 = va | kx2, va3 = va | kx1 | kx2;
  PIN(va1); PIN(va2); PIN(va3);
  int mc = bid & 0xd8;
  asm volatile("" : "+v"(va), "+s"(mc));
  SB();
  asm volatile("s_waitcnt lgkmcnt(0)" : "+s"(ka), "+s"(kb), "+s"(pt0));
  SB();
#define K_FIRST "global_load_dwordx4 %0, %4, %5 nt\n\tglobal_load_dwordx4 %1, %6, %5 nt\n\tglobal_load_dwordx4 %2, %7, %5 nt\n\tglobal_load_dwordx4 %3, %8, %5 nt"
  asm volatile("s_cmp_eq_u32 %9, 16\n\ts_cbranch_scc1 .Lk_later\n\t" K_FIRST "\n.Lk_later:"
               : "=&v"(kr[0]), "=&v"(kr[1]), "=&v"(kr[2]), "=&v"(kr[3]) : "v"(va), "s"((u64)ka[2] | (u64)ka[3] << 32), "v"(va1), "v"(va2), "v"(va3), "s"(mc) : "memory", "scc");
  SB();
  const u32 tka = (u32)wall_clock64(), t0 = tka - D0;
  auto kap = [&](int k) { return k < 8 ? (u64)ka[2 * k] | (u64)ka[2 * k + 1] << 32 : (u64)kb[2 * k - 16] | (u64)kb[2 * k - 15] << 32; };
  const u16 *x = (const u16*)kap(0), *kc = (const u16*)kap(1), *vc = (const u16*)kap(2), *w2 = (const u16*)kap(7), *cnd = (const u16*)kap(8);
  unsigned char* ws = (unsigned char*)kap(10); u16* out = (u16*)kap(11);
  const int lane = tid & 63, wave = __builtin_amdgcn_readfirstlane(tid >> 6);
  const bool p3 = part == 3;
  const u64 wa = part == 0 ? kap(4) : (part == 1 ? kap(3) : (part == 2 ? kap(9) : ((wave & 1) ? kap(6) : kap(5))));
  const bool modc = mc == 16;
  if (modc) asm volatile("s_waitcnt vmcnt(0)" ::: "memory");
  asm volatile("s_waitcnt vmcnt(4)" : "+v"(tev));
  const u32 te0 = __builtin_amdgcn_readfirstlane(tev[0]), te1 = __builtin_amdgcn_readfirstlane(tev[1]);
  const u32 te3 = __builtin_amdgcn_readfirstlane(tev[3]), pitch = te3 & 0xffff, wsz = pitch << (part == 0 ? 12 : 9);
  const int sh = p3 ? 2 : 3;
  u32 wv0 = (u32)((p3 ? (wave >> 1) * 16 : wave * 64) + (lane >> sh)) * pitch + ((lane & (p3 ? 3 : 7)) ^ (lane >> (p3 ? 5 : 3)) ^ (p3 ? wave >> 2 << 1 : 0)) * 16u;
  const u32 wimg = (u32)(uintptr_t)(&sm.wbuf[0][0][0]);
  u32 wld = wimg + (p3 ? (wave & 1) * 32768 + (wave >> 1) * 1024 : wave * 8192);
  const int bl0 = te0 & 127, nl = te0 >> 7 & 63, blk = te0 >> 13 & 63;
  const bool has_w = te0 >> 19 & 1;
  const rsrc_t rw = mk_rsrc((const void*)wa, wsz);
  const u32 wstep = pitch << (p3 ? 6 : 3), lstep = p3 ? 4096u : 1024u;
  u32 wso = __builtin_amdgcn_readfirstlane(tev[2]);
  int wleft = 8;
  auto piece = [&]() {
    dma16(rw, wv0, wso, wld);
    wso += wstep; wld += lstep;
    --wleft;
  };
  int tau = te1 & 0xffff;
  const int tauw = te1 >> 16, sw = te0 >> 24;
  int tau2 = te3 >> 16;
  int due = P.DS * 256, now = 0;
  auto gate = [&]() {
    while (now < due) { now = (int)(((u32)wall_clock64() - t0) << 8); if (now < due) __builtin_amdgcn_s_sleep(1); }
    due += tau;
  };
  const rsrc_t wsr = mk_rsrc(ws, WS_END);
  const int g = lane >> 4, c = lane & 15;
  const int i8 = idx >> 2, wi = i8 * 8 + xcd;
  const bool is_up = p3 && has_w;
  const size_t base = ((size_t)(b * NH + head) * SK + (SK - WIN)) * DH;
  const rsrc_t rk = mk_rsrc(kc + base, WIN * DH * 2), rv = mk_rsrc(vc + base, WIN * DH * 2);
  u32 sot;
  {
    u32 s0 = bl0 + lane - KF;
    s0 += s0 >= (u32)(32 * part + KF0) ? KF : 0;
    sot = (lane < KF ? so0 | place(lane, P.PM, 2) : step_off(P, s0, b * 32 + head)) | place(wave, P.WM, 3);
    sot = lane < nl ? sot : OOB;
    PIN(sot);
  }
  auto until = [&](int t) { while ((int)(((u32)wall_clock64() - t0) << 8) < t); };
  u32 near = 0, nmask = 0;
  int slack = 0;
  const rsrc_t rx = mk_rsrc(part == 2 ? cnd : x, (part == 1 || (part == 2 && has_w)) ? 2048u : 0u);
  u32x2 ep; u32 xs0, xs1, tch1; u32x4 xr0, xr1;
  auto small_ops = [&]() {
    ep = ld8<AUX_SC1>(wsr, wave == 7 ? WS_EPOCH : OOB, 0);
    xs0 = __builtin_amdgcn_raw_buffer_load_b16(rx, tid * 2, 0, 0); xs1 = __builtin_amdgcn_raw_buffer_load_b16(rx, tid * 2, 1024, 0);
  };
  auto small_ops2 = [&]() {
    xr0 = ld16<0>(rx, part == 1 ? lane * 16 : OOB, 0); xr1 = ld16<0>(rx, part == 1 ? lane * 16 : OOB, 1024);
    tch1 = __builtin_amdgcn_raw_buffer_load_b32(mk_rsrc(w2 + (size_t)blk * 32 * 512, is_up ? 64u : 0u), tid == 384 ? 0 : OOB, 0, AUX_NT);
  };
  u32 tag = 0;
  u32x4 bf[8];
  auto wo_prep = [&](int s0 = 0, int s1 = 2) {
    const u32 wi_ = wimg + wave * 8192;
    const int q = (lane >> 2) & 3, pp = lane & 3;
#pragma unroll
    for (int s = s0; s < s1; ++s) {
#pragma unroll
      for (int t = 0; t < 4; ++t) {
        const int r0 = s * 32 + g * 8 + q, r1 = r0 + 4, ch = t * 2 + (pp >> 1);
        const u32x2 lo = tr_read(wi_ + r0 * 128 + ((ch ^ (r0 & 7)) * 16) + (pp & 1) * 8), hi = tr_read(wi_ + r1 * 128 + ((ch ^ (r1 & 7)) * 16) + (pp & 1) * 8);
        bf[s * 4 + t] = u32x4{lo[0], lo[1], hi[0], hi[1]};
      }
    }
  };
  auto gemv_go = [&](u32 hh) {
    sm.hb[tid] = (u16)hh; sm.hb[512 + tid] = (u16)(hh >> 16);
    WAVE_SYNC();
    const u32x4* a = (c & 11) == 0 ? reinterpret_cast<const u32x4*>(&sm.hb[(c >> 2) * 512 + wave * 64 + g * 8]) : &sm.zero[0];
    const u32x4 a0 = a[0], a1 = a[(c & 11) == 0 ? 4 : 0];
    const f32x4 z = {0.f, 0.f, 0.f, 0.f};
    f32x4 d[4];
#pragma unroll
    for (int t = 0; t < 4; ++t) d[t] = mfma16(a0, bf[t], z);
#pragma unroll
    for (int t = 0; t < 4; ++t) d[t] = mfma16(a1, bf[4 + t], d[t]);
    if (lane < 32) {
#pragma unroll
      for (int t = 0; t < 4; ++t) sm.r2[(t * 16 + c) * 20 + g * 8 + wave] = d[t][0];
    }
  };
  auto mod_gemv = [&]() { gemv_go(pack_bf(silu_f(lo_f(xs0)), silu_f(lo_f(xs1)))); };
  auto mod_put = [&]() { st8_sc1(wsr, G_MOD + (blk * 64 + lane) * 8, pack_bf(sum8(&sm.r2[lane * 20]), sum8(&sm.r2[lane * 20 + 8])), tag); };
  auto zero_rows = [&]() { if (lane < 4) sm.zero[lane] = u32x4{0u, 0u, 0u, 0u}; };
  const u32 pbo = G_PB + (wi >> 3) * 16384 + (wi & 7) * 1024;
  u32 l16 = lane * 16;
  asm volatile("" : "+v"(l16), "+v"(wv0) : "s"(rk), "s"(rx), "s"(wsr));
  SB();
  if (has_w) {
    if (__builtin_expect(modc, 0)) {
      now = 1 << 30;
      __builtin_amdgcn_raw_buffer_store_b128(u32x4{SENT, SENT, SENT, SENT}, wsr, tid < 384 ? G_P + wi * 6144 + tid * 16 : pbo + (wave & 1) * 8192 + lane * 16, 0, AUX_SC1);
      __builtin_amdgcn_raw_buffer_store_b128(u32x4{SENT, SENT, SENT, SENT}, wsr, tid < 16 ? G_ML + wi * 256 + tid * 16 : (tid < 24 ? G_NT + wi * 128 + (tid - 16) * 16 : (tid < 32 && wi < 3 ? G_AG + wi * 128 + (tid - 24) * 16 : OOB)), 0, AUX_SC1);
    }
#pragma nounroll
    while (wleft > (int)(te0 >> 20 & 7)) { gate(); due += tauw - tau; piece(); }
    now = 0;
  }
  SB();
  STAMP(23);
  if (__builtin_expect(modc, 0)) {
    small_ops();
    zero_rows();
    {
      until(P.PT * 256);
      const u64 pa = (u64)ws + pbo + wave * 128 + (wave & 1) * 8192;
      u64 c0, c1, c2; u32 d0, d1;
      asm volatile("s_memtime %0\n\ts_waitcnt lgkmcnt(0)\n\ts_load_dword %3, %5, 0x0\n\ts_waitcnt lgkmcnt(0)\n\ts_memtime %1\n\ts_waitcnt lgkmcnt(0)\n\t"
                   "s_load_dword %4, %6, 0x0\n\ts_waitcnt lgkmcnt(0)\n\ts_memtime %2\n\ts_waitcnt lgkmcnt(0)"
                   : "=&s"(c0), "=&s"(c1), "=&s"(c2), "=&s"(d0), "=&s"(d1) : "s"(pa), "s"(pa + 8192 - (wave & 1) * 16384) : "memory");
      const int df = min(max((int)(2 * (u32)c1 - (u32)c0 - (u32)c2), -600), 600);
      if (lane == 0) sm.stat[0][wave] = (float)(((wave ^ (xcd >> 2)) & 1) ? -df : df);
    }
    asm volatile("s_waitcnt vmcnt(0)" ::: "memory");
    tag = next_tag(__builtin_amdgcn_readfirstlane(ep[0]));
    wo_prep();
    mod_gemv();
    LDS_BARRIER();
    if (wave == 7) { mod_put(); if (lane == 0) st8_sc1(wsr, G_AG + (48 + wi) * 8, __float_as_uint(sum8(&sm.stat[0][0])), tag); }
    STAMP(9);
    __builtin_amdgcn_s_barrier();
    asm volatile(K_FIRST :: "v"(kr[0]), "v"(kr[1]), "v"(kr[2]), "v"(kr[3]), "v"(va), "s"((u64)ka[2] | (u64)ka[3] << 32), "v"(va1), "v"(va2), "v"(va3) : "memory");
  }
  SB();
  const bool fin = (part == 1 || part == 2) && wave == 0 && xcd < 2;
  const int fw = i8 * 4 + (part & 2) + xcd, fd = fw * 16 + (lane & 7) * 2;
  const int ub = wave >> 2, ud = (wave & 3) * 128 + c * 8 + g * 2;
  u32x4 sg[3]; u32 sx2;
  auto side_loads = [&]() {
    sg[0] = ld16<AUX_SC1>(wsr, is_up ? G_MOD + (1024 + ud) * 8 : (fin ? G_MOD + (1024 + fd) * 8 : OOB), 0);
    sg[1] = ld16<AUX_SC1>(wsr, is_up ? G_MOD + (1536 + ud) * 8 : (fin ? G_MOD + (2560 + fd) * 8 : OOB), 0);
    sg[2] = ld16<AUX_SC1>(wsr, is_up ? G_MOD + (2048 + ud) * 8 : OOB, 0);
    sx2 = __builtin_amdgcn_raw_buffer_load_b32(mk_rsrc(x, (is_up || fin) ? 2048u : 0u), is_up ? ub * 1024 + ud * 2 : (g & 1) * 1024 + fd * 2, 0, 0);
  };
  auto side_pin = [&]() { PIN(sg[0]); PIN(sg[1]); PIN(sg[2]); PIN(sx2); };
  const u32 mlo = G_ML + (head * 2 + b) * 64 + (lane & 3) * 16;
  float fwv = 0.f, fmw[8];
  bool fok = wave != 0;
  auto merge_factor = [&](const u32x4& ml) {
    const float mi = u2f(ml[0]);
    float M = fmaxf(mi, dpp<XOR1>(mi)); M = fmaxf(M, dpp<XOR2>(M));
    const float e = __expf(mi - M);
    float L = e * u2f(ml[2]);
    L += dpp<XOR1>(L); L += dpp<XOR2>(L);
    const float f = fwv * rdl(e * __builtin_amdgcn_rcpf(L), part);
#pragma unroll
    for (int w = 0; w < 8; ++w) fmw[w] = bcast16(f, w);
  };
  f32x2 ga = {0.f, 0.f}, xin = {0.f, 0.f}, cc1 = {0.f, 0.f};
  float Af = 0.f, Bf = 0.f;
  const int hpos = ub * 512 + ud;
  auto up_prep = [&]() {
    if (!all_ok((sg[0][1] == tag) & (sg[0][3] == tag) & (sg[1][1] == tag) & (sg[1][3] == tag) & (sg[2][1] == tag) & (sg[2][3] == tag))) gather16<3>(wsr, G_MOD + (1024 + ud) * 8, 512 * 8, tag, sg);
    ga = f32x2{ub ? hi_f(sg[0][0]) : lo_f(sg[0][0]), ub ? hi_f(sg[0][2]) : lo_f(sg[0][2])};
    cc1 = f32x2{rbf(1.f + (ub ? hi_f(sg[2][0]) : lo_f(sg[2][0]))), rbf(1.f + (ub ? hi_f(sg[2][2]) : lo_f(sg[2][2])))};
    xin = f32x2{lo_f(sx2), hi_f(sx2)};
    u32* hp = reinterpret_cast<u32*>(&sm.hb[hpos]);
    hp[512] = pack_bf(cc1[0], cc1[1]);
    hp[1024] = ub ? perm_hi(sg[1][0], sg[1][2]) : perm_lo(sg[1][0], sg[1][2]);
  };
  auto up_addr = [&]() {
    const int pp = lane & 3;
    return wimg + (pp & 1) * 32768 + ((pp >> 1) * 32 + g * 8 + ((lane >> 2) & 3)) * 64 + (((wave >> 1) ^ ((g & 1) | (pp & 2))) * 16) + (wave & 1) * 8;
  };
  auto up_frags = [&](int r0, int r1) {
    const u32 a = up_addr();
#pragma unroll
    for (int rb = r0; rb < r1; ++rb) {
      const u32x2 lo = tr_read(a + rb * 4096), hi = tr_read(a + rb * 4096 + 256);
      bf[rb] = u32x4{lo[0], lo[1], hi[0], hi[1]};
    }
  };
  const u32x4* ap = reinterpret_cast<const u32x4*>(&sm.hb[((lane >> 3) & 1) * 512 + (lane & 1) * 32 + g * 8]);
  const bool par = lane & 8;
  auto col_sums = [&](int r0, int r1) {
    const u32x4* a1 = ap + 128 + ((lane >> 1) & 1) * 128;
    f32x4 d0 = {0.f, 0.f, 0.f, 0.f}, d1 = {0.f, 0.f, 0.f, 0.f};
#pragma unroll
    for (int rb = r0; rb < r1; ++rb) { if (rb & 1) d1 = mfma16(a1[rb * 8], bf[rb], d1); else d0 = mfma16(a1[rb * 8], bf[rb], d0); }
    Af += par ? d0[1] + d1[1] : d0[0] + d1[0]; Bf += par ? d0[3] + d1[3] : d0[2] + d1[2];
  };
  float l0 = 0.f, l1 = 0.f;
  u32x2 gmd[2], tbg;
  const bool modw = part == 2 && has_w && !modc;
  const int qp = part != 1 ? -99 : ((blk & 7) == 3 ? QP1 : QP2);
  static_assert(QP1 + QD < QP2 && QP1 + QD > WUSE && QP1 + QD > TBU && WUSE + 1 < TBU, "order of the q steps");
  int t1 = 0;
#pragma unroll
  for (int i = NCMIN / 8; i < NL; ++i) kr[i] = u32x4{0u, 0u, 0u, 0u};
  sfor<KF, NL>([&](auto ic) {
    constexpr int i = ic.v;
    if (i >= NCMIN / 8 && i >= nl) return false;
    const u32 so = __builtin_amdgcn_readlane(sot, i);
    if constexpr (i < NCMIN / 8) tau = i == sw ? tau2 : tau;
    gate();
    SB();
    kr[i] = ld16<AUX_NT>(rk, l16, so);
    if constexpr (i == KF) small_ops();
    if constexpr (i == KF + 1) small_ops2();
    if constexpr (i == KF + 2) zero_rows();
    if constexpr (i == TBL) tbg = ld8<AUX_SC1>(wsr, wave == 7 ? G_TB + (lane & 7) * 8 : OOB, 0);
    if constexpr (i == QP1 || i == QP2) { gmd[0] = ld8<AUX_SC1>(wsr, i == qp ? G_MOD + tid * 8 : OOB, 0); gmd[1] = ld8<AUX_SC1>(wsr, i == qp ? G_MOD + tid * 8 : OOB, 4096); }
    SB();
    SB();
    if (i >= WK) { WAIT_VM(win_k(i)); SB(); }
    if (i == KF) STAMP(25);
    if (i == 8) STAMP(12);
    if (i == 20) STAMP(21);
    if (i == 25) STAMP(22);
    if (i == TBP) {
      if (part == 3 && i8 == 0 && wave == 7) { tag = next_tag(__builtin_amdgcn_readfirstlane(ep[0])); if (lane == 0) st8_sc1(wsr, G_TB + xcd * 8, tka - P.GS, tag); }
      SB();
    }
    if (i == TBU - 1) {
      if (wave == 7) {
        const bool ok = tbg[1] == tag;
        float lf = ok ? (float)min(max((int)(t0 - tbg[0]), -300), 300) : 0.f, nf = ok ? 1.f : 0.f;
        lf += dpp<XOR1>(lf); nf += dpp<XOR1>(nf); lf += dpp<XOR2>(lf); nf += dpp<XOR2>(nf);
        lf *= __builtin_amdgcn_rcpf(fmaxf(nf, 1.f));
        const float ot = dpp<XOR7>(lf);
        if (lane == 0) sm.bc = u32x2{tag, t0 - (u32)(int)((lf + ot) * 0.5f - fabsf(lf - ot) * (P.BT / 32.f))};
      }
      SB();
    }
    if (i == TBU) {
      LDS_BARRIER();
      const u32x2 bc = sm.bc;
      tag = __builtin_amdgcn_readfirstlane(bc[0]);
      t1 = (__builtin_amdgcn_readfirstlane((int)(bc[1] - t0)) + TEND + P.X1) * 256;
      const int dt = __builtin_amdgcn_readfirstlane((int)((float)(int)(t0 - bc[1]) * 256.f * __builtin_amdgcn_rcpf((float)(2 * nl - TBU - 2))));
      tau -= dt; tau2 -= dt;
      SB();
    }
    if (i == TBU + 1) {
      if (modw) { if (wave == 6) mod_put(); STAMP(9); }
      SB();
    }
    if (i == LNS) {
      tag = next_tag(__builtin_amdgcn_readfirstlane(ep[0]));
      if (part == 1) {
        float x0[8], x1[8], mb0, ri0, mb1, ri1;
#pragma unroll
        for (int k = 0; k < 4; ++k) { x0[2 * k] = lo_f(xr0[k]); x0[2 * k + 1] = hi_f(xr0[k]); x1[2 * k] = lo_f(xr1[k]); x1[2 * k + 1] = hi_f(xr1[k]); }
        ln_stats2(x0, x1, mb0, ri0, mb1, ri1);
        l0 = rbf(rbf(lo_f(xs0) - mb0) * ri0); l1 = rbf(rbf(lo_f(xs1) - mb1) * ri1);
      }
      PIN(l0); PIN(l1);
      SB();
    }
    if (i == WUSE) {
      PIN(tch1); PIN(tch2);
      static_assert(WUSE - WK >= KF, "the weight pieces are older than the steps outside the window");
      CFENCE();
      if (part == 1 || modw) wo_prep();
#pragma unroll
      for (int k = 0; k < 8; ++k) PIN(bf[k]);
      SB();
    }
    if (i == WUSE + 1) {
      if (modw) mod_gemv();
      SB();
    }
    if (i == QP1 + QD || i == QP2 + QD) {
      if (i == qp + QD) {
        u32 gm[2] = {gmd[0][0], gmd[1][0]};
        if (!all_ok((gmd[0][1] == tag) & (gmd[1][1] == tag))) { STAMP(11); gather8<2>(wsr, G_MOD + tid * 8, 512 * 8, tag, gm); }
        STAMP(5);
        gemv_go(pack_bf(rbf(l0 * rbf(1.f + lo_f(gm[1]))) + lo_f(gm[0]), rbf(l1 * rbf(1.f + hi_f(gm[1]))) + hi_f(gm[0])));
      }
      SB();
    }
    if (i == QP1 + QD + 1 || i == QP2 + QD + 1) {
      if (i == qp + QD + 1) {
        LDS_BARRIER();
        if (wave == 0) {
          const int bb = lane >> 5, jq = lane & 31;
          st8_sc1(wsr, G_Q + (bb * 2048 + blk * 32 + jq) * 8, pack_bf(sum8(&sm.r2[2 * jq * 20 + bb * 8]), sum8(&sm.r2[(2 * jq + 1) * 20 + bb * 8])), tag);
        }
        STAMP(6);
      }
      SB();
    }
    return true;
  });
  PIN(sot);
  const u32 qoff = G_Q + ((b * 32 + head) * 64 + c * 4) * 8;
  u32x4 qg[2], vr[NL], fl2[3];
  u32 qq[4];
  constexpr int NB = (NL + 15) / 16;
  float xlo, xs[4], sc[NB], pr[NB], m = 0.f;
  f32x4 acc = {0.f, 0.f, 0.f, 0.f};
  u32 pb[NB], pm[4], cpf;
  u32x2 agr;
  const u32 adst0 = lds_addr(lane < 32 ? &sm.red[wave][c * 8 + g * 4] : &sm.r2[(lane - 32) * 4]);
  u32 pso, po, rdst, rsrc, xo, l2, v2[4];
  rsrc_t r2;
  auto wo_addr = [&]() {
    po = G_P + (xcd & 3) * 16384 + near + (c >> 2 & 1) * 4096 + ((wave >> 1) * 4 + (c & 3)) * 256 + (wave & 1) * 128 + (c >> 3) * 64 + g * 16;
    rdst = lane < 32 ? (u32)(uintptr_t)&sm.red2[wave][0] + lane * 16 : (u32)(uintptr_t)&sm.r2[0] + (lane - 32) * 16;
    rsrc = (u32)(uintptr_t)&sm.red2[0][((wave & 1) * 16 + (lane & 15)) * 4 + (lane >> 4)];
    xo = dup(G_XP + xcd * 2048 + (wave & 1) * 1024 + (blk * 64 + (lane & ~1)) * 2) + (lane & 1) * 8192;
    pso = wave == 0 ? G_P + (xcd & 3) * 16384 + near + b * 4096 + ((head & 3) * 4 + part) * 256 + lane * 4 : OOB;
    PIN(pso); PIN(po); PIN(rdst); PIN(rsrc); PIN(xo);
  };
  auto w2_addr = [&]() {
    r2 = mk_rsrc(w2 + (size_t)(blk * 32 + wave * 4) * 512, 4096u);
    l2 = (u32)(uintptr_t)(&sm.w2b[wave * 4][0]);
#pragma unroll
    for (int n = 0; n < 4; ++n) { v2[n] = (lane ^ (n * 2) ^ ((wave & 2) << 2)) * 16 + n * 1024; PIN(v2[n]); }
    asm volatile("" : "+s"(r2), "+s"(l2));
  };
  const bool late = is_up || (part == 0 && wleft);
  const u32 nadd = is_up ? P.LU : (part == 0 ? (wleft ? P.LW : P.L3) : 0);
  u32 nto = tid == 0 ? G_NT + bid * 8 : OOB;
  PIN(nto);
  auto notices = [&](auto nc_, u32 off, u32x4 (&gn)[nc_.v]) -> int {
    constexpr int N = nc_.v;
#pragma nounroll
    for (int spin = 0; spin < SPIN_MAX; ++spin) {
      bool ok = true;
#pragma unroll
      for (int i = 0; i < N; ++i) ok &= (gn[i][1] == tag) & (gn[i][3] == tag);
      if (all_ok(ok)) break;
      __builtin_amdgcn_s_sleep(10);
      CFENCE();
#pragma unroll
      for (int i = 0; i < N; ++i) gn[i] = ld16<AUX_SC1>(wsr, off, i * 1024);
    }
    int mx = 0;
#pragma unroll
    for (int i = 0; i < N; ++i) mx = max(mx, max((int)(gn[i][0] - t0), (int)(gn[i][2] - t0)));
    return __builtin_amdgcn_readfirstlane((int)wave_max((float)min(mx, 1 << 20)));
  };
  const int side = nl >= SIDE + 13 ? SIDE + 8 : (nl >= SIDE + 9 ? SIDE + 4 : SIDE);
  sfor<0, NL + WV>([&](auto ic) {
    constexpr int v = ic.v;
    if (cons_end(v - 1) >= NCMIN / 8 && cons_end(v - 1) >= nl) return false;
    if constexpr (v < NL) {
      const u32 so = __builtin_amdgcn_readlane(sot, v);
      if constexpr (v >= 12) { if (v == nl - P.NE) tau = (tau * (late ? 16 : P.FE)) >> 4; }
      if constexpr (v >= NCMIN / 8) { if (v < nl) gate(); } else gate();
      SB();
      int bk = 0;
      if constexpr (v == AR) { if (part == 1 && wi < 48 && wave == 7) bk = ((int)(((u32)wall_clock64() - t0) << 8) - due + tau + (int)__builtin_amdgcn_s_getreg(7 | (3 << 11)) * tau) >> 8; SB(); }
      vr[v] = ld16<AUX_NT>(rv, l16, so);
      if constexpr (v == AR) {
        SB();
        if (part == 1 && wi < 48 && tid == 448) st8_sc1(wsr, G_AG + wi * 8, (u32)min(max(bk, 0), 250), tag);
      }
      SB();
      SB();
    }
    if constexpr (v == NCMIN / 8 - 1) {
#pragma unroll
      for (int i = NCMIN / 8; i < NL; ++i) asm volatile("" : "=v"(vr[i]));
      asm volatile("" : "=v"(fl2[1]), "=v"(fl2[2]));
#pragma unroll
      for (int w = 0; w < 8; ++w) asm volatile("" : "=v"(fmw[w]));
    }
    if constexpr (v == VQ - 4) { qg[0] = ld16<AUX_SC1>(wsr, qoff, 0); qg[1] = ld16<AUX_SC1>(wsr, qoff + 16, 0); }
    if constexpr (v == VSIDE) side_loads();
    if constexpr (v == VL) agr = ld8<AUX_SC1>(wsr, wave == 6 ? G_AG + (48 + c) * 8 : OOB, 0);
    if constexpr (v == AL) agr = ld8<AUX_SC1>(wsr, (wave == 7 && lane < 48) ? G_AG + lane * 8 : OOB, 0);
    if constexpr (v == CPF) {
      u64 pc; u32 dlt, len;
      asm volatile("s_getpc_b64 %0\n.Lcpf_here:\n\ts_mov_b32 %1, .Ltail_start-.Lcpf_here\n\ts_mov_b32 %2, .Lend_a-.Ltail_start\n\ts_max_i32 %2, %2, .Lend_b-.Ltail_start\n\ts_max_i32 %2, %2, .Lend_c-.Ltail_start\n\ts_max_i32 %2, %2, .Lend_d-.Ltail_start\n\ts_max_i32 %2, %2, .Lend_e-.Ltail_start" : "=s"(pc), "=s"(dlt), "=s"(len) :: "scc");
      pc += dlt;
      const rsrc_t rc = mk_rsrc((const void*)pc, (part == 3 && i8 == 1) ? len : 0u);
      cpf = __builtin_amdgcn_raw_buffer_load_b32(rc, tid * 64, 0, 0);
    }
    if constexpr (v == CPF + WV + 1) PIN(cpf);
    if constexpr (v == SIDE || v == SIDE + 4 || v == SIDE + 8) fl2[(v - SIDE) / 4] = ld16<AUX_SC1>(wsr, (wave == 0 && v == side) ? mlo : OOB, 0);
    SB();
    if constexpr (v < WV) { WAIT_VM(win_v(v, WK)); SB(); } else if constexpr (v < NL && cons_end(v) < v - WV + 1) { WAIT_VM(win_v(v, WV)); SB(); }
    if constexpr (v == VU) {
      if (wave == 6) {
        u32 vt[1] = {agr[0]};
        if (!all_ok(agr[1] == tag)) gather8<1>(wsr, G_AG + (48 + c) * 8, 0, tag, vt);
        float sv = u2f(vt[0]);
        sv += dpp<0x124>(sv); sv += dpp<XOR8>(sv);
        float sa = sv + dpp<XOR1>(sv);
        sv = fmaf(sa + dpp<XOR2>(sa), 0.25f, sv);
        float cf = fabsf(sv);
        cf = fminf(cf, dpp<XOR1>(cf)); cf = fminf(cf, dpp<XOR2>(cf));
        const u32 nm = (u32)__builtin_amdgcn_ballot_w64(sv > 0.f) & 15u;
        if (lane == 0) sm.nm = nm << 13 | (cf < 2500.f ? (u32)P.SL : 0u);
      }
      PIN(agr);
    }
    if constexpr (v == VQ) {
      if (!all_ok((qg[0][1] == tag) & (qg[0][3] == tag) & (qg[1][1] == tag) & (qg[1][3] == tag))) { STAMP(20); gather16<2>(wsr, qoff, 16, tag, qg); }
      STAMP(1); WSTAMP(1);
      qq[0] = qg[0][0]; qq[1] = qg[0][2]; qq[2] = qg[1][0]; qq[3] = qg[1][2];
    }
    if constexpr (v >= VQ && v < VS) {
      constexpr int u0 = SLC[v - VQ], u1 = SLC[v - VQ + 1];
      static_assert(SLC[NSL] == (NL + 7) / 8 * 8, "all groups of 8");
      sfor<u0, u1>([&](auto uc) {
        constexpr int u = uc.v, p = u & 7, i = (p & 1) * 4 + (p & 2) + (p >> 2), sl = u >> 3, bt = sl >> 1;
        float d = 0.f;
        if constexpr (sl * 8 + i < NL) {
#pragma unroll
          for (int k = 0; k < 4; ++k) d = dot2(kr[sl * 8 + i][k], qq[k], d);
        }
        if constexpr (p & 1) xs[i - 4] = sel_add<XOR7>(xs[i - 4], d, c & 4); else xs[i] = d;
        if constexpr ((p & 3) == 3) xs[p >> 2] = sel_add<XOR2>(xs[p >> 2], xs[(p >> 2) + 2], c & 2);
        if constexpr (p == 7) {
          xs[0] = sel_add<XOR1>(xs[0], xs[1], c & 1);
          if constexpr ((sl & 1) || u == (NL + 7) / 8 * 8 - 1) {
            const float t = (sl & 1) ? sel_add<XOR8>(xlo, xs[0], c & 8) : xs[0] + dpp<XOR8>(xs[0]);
            const float s = rbf(rbf(t) * 0.08838834764831845f);
            sc[bt] = (bt * 16 + c < nl) ? s : -__builtin_inff();
            PIN(sc[bt]);
          } else {
            xlo = xs[0];
            PIN(xlo);
          }
        }
        return true;
      });
      constexpr int pe = u1 & 7;
      if constexpr (pe >= 1) PIN(xs[0]);
      if constexpr (pe == 3) PIN(xs[2]);
      if constexpr (pe >= 5) PIN(xs[1]);
      if constexpr (pe == 7) PIN(xs[3]);
    }
    if constexpr (v == VS) {
      float mloc = sc[0];
#pragma unroll
      for (int bt = 1; bt < NB; ++bt) mloc = fmaxf(mloc, sc[bt]);
      m = fmaxf(wave_max(mloc), -3.0e38f);
      float ls = 0.f;
#pragma unroll
      for (int bt = 0; bt < NB; ++bt) { pr[bt] = __expf(sc[bt] - m); ls += pr[bt]; }
      const float l = wave_sum(ls);
      if (lane == 0) { sm.ml[0][wave] = m; sm.ml[1][wave] = l; }
#pragma unroll
      for (int bt = 0; bt < NB; ++bt) { pb[bt] = pack_bf(pr[bt], pr[bt]); PIN(pb[bt]); }
#pragma unroll
      for (int k = 0; k < 4; ++k) { pm[k] = (c >> 1) == k ? ((c & 1) ? 0xffff0000u : 0x0000ffffu) : 0u; PIN(pm[k]); }
      STAMP(2); WSTAMP(2);
    }
    sfor<cons_end(v - 1), cons_end(v)>([&](auto jc) {
      constexpr int i = jc.v;
      if (i < NCMIN / 8 || i < nl) {
        const u32 pv = __float_as_uint(bcast16(u2f(pb[i >> 4]), i & 15));
        acc = mfma16(u32x4{pv & pm[0], pv & pm[1], pv & pm[2], pv & pm[3]}, vr[i], acc);
      }
      PIN(acc);
      return true;
    });
    if constexpr (v == VS - 1) { side_pin(); if (is_up) up_prep(); PIN(ga); PIN(xin); PIN(cc1); }
    if constexpr (v == MLS - 1) {
      if (wave == 7) {
        const float am = wave_sum(lane < 48 ? (agr[1] == tag ? (float)agr[0] : 120.f) : 0.f) * (1.f / 48.f);
        const float st = fminf(fmaxf((am - (float)P.A0) * (float)(P.AG * 16), 0.f), (float)(P.AC * 256));
        if (lane == 0) sm.ctl = u32x2{(u32)(int)st, (u32)(int)(st * __builtin_amdgcn_rcpf((float)(nl - MLS - 3)))};
      }
      PIN(agr);
    }
    if constexpr (v == UPREP - 2) {
      nmask = __builtin_amdgcn_readfirstlane(sm.nm);
      slack = (nmask & 255u) * 256; near = ((nmask >> (xcd & 3)) ^ (xcd << 11)) & 8192u;
      w2_addr();
    }
    if constexpr (v == UPREP - 1) wo_addr();
    if constexpr (v == UPREP) {
#pragma unroll
      for (int k = 0; k < 8; ++k) asm volatile("" : "=v"(bf[k]));
      if (part == 0) wo_prep(0, 1); else if (is_up) up_frags(0, 8 - UD);
#pragma unroll
      for (int k = 0; k < 8; ++k) PIN(bf[k]);
    }
    if constexpr (v == UPREP + 1 || v == UPREP + 2) {
      if (is_up) { if constexpr (v == UPREP + 1) col_sums(0, (8 - UD) / 2); else col_sums((8 - UD) / 2, 8 - UD); }
      PIN(Af); PIN(Bf);
    }
    if constexpr (v == SIDE + WV + 1 || v == SIDE + WV + 5 || v == SIDE + WV + 9) {
      if (wave == 0 && v == side + WV + 1) {
        const u32x4 ml = fl2[(v - SIDE - WV - 1) / 4];
        fok = all_ok((ml[1] == tag) & (ml[3] == tag));
        merge_factor(ml);
      }
    }
    if (v % 6 == 4 && v >= 10 && v < 30) STAMP(15 + v / 6);
    if constexpr (v == MLS) {
      LDS_BARRIER();
      const u32x2 ct = sm.ctl;
      if (wave == 0) {
        const float mw = sm.ml[0][lane & 7];
        float Mwg = fmaxf(mw, dpp<XOR1>(mw)); Mwg = fmaxf(Mwg, dpp<XOR2>(Mwg)); Mwg = fmaxf(Mwg, dpp<XOR7>(Mwg));
        fwv = __expf(mw - Mwg);
        float Lwg = fwv * sm.ml[1][lane & 7];
        Lwg += dpp<XOR1>(Lwg); Lwg += dpp<XOR2>(Lwg); Lwg += dpp<XOR7>(Lwg);
        if (lane == 0) st16_sc1(wsr, G_ML + ((head * 2 + b) * 4 + part) * 16, __float_as_uint(Mwg), __float_as_uint(Lwg), tag);
      }
      PIN(fwv);
      t1 += __builtin_amdgcn_readfirstlane(ct[0]);
      tau += __builtin_amdgcn_readfirstlane(ct[1]);
    }
    SB();
    return true;
  });
  asm volatile(".Ltail_start:");
  *LDSP(f32x4, adst0) = acc;
  STAMP(14); WSTAMP(14);
  if (!fok) {
    u32x4 ml;
#pragma nounroll
    for (int spin = 0; spin < SPIN_MAX; ++spin) {
      CFENCE();
      ml = ld16<AUX_SC1>(wsr, mlo, 0);
      if (all_ok((ml[1] == tag) & (ml[3] == tag))) break;
    }
    merge_factor(ml);
  }
  STAMP(15);
  LDS_BARRIER();
  STAMP(3);
  {
    u32 pw = 0;
    if (wave == 0) {
      f32x2 t[8];
#pragma unroll
      for (int w = 0; w < 8; ++w) t[w] = *reinterpret_cast<const f32x2*>(&sm.red[w][2 * lane]);
      f32x2 h[4];
#pragma unroll
      for (int w = 0; w < 4; ++w) h[w] = __builtin_elementwise_fma(t[2 * w + 1], f32x2{fmw[2 * w + 1], fmw[2 * w + 1]}, t[2 * w] * fmw[2 * w]);
      const f32x2 o = (h[0] + h[1]) + (h[2] + h[3]);
      pw = min(pack_bf(o[0], o[1]), SENT - 1u);
    }
    __builtin_amdgcn_raw_buffer_store_b32(pw, wsr, pso, 0, AUX_SC1);
    st8_sc1(wsr, nto, (u32)wall_clock64() + nadd, tag);
    SB();
#pragma unroll
    for (int i = NCMIN / 8; i < NL; ++i) PIN(vr[i]);
    PIN(fl2[0]); PIN(fl2[1]); PIN(fl2[2]);
  }
  STAMP(4);
  SB();
  if (late) {
    asm volatile(".p2align 6\n\ts_barrier" ::: "memory");
#pragma nounroll
    while (wleft > 0) piece();
    if (is_up) {
      asm volatile("s_mov_b32 m0, %5\n\ts_nop 0\n\tbuffer_load_dwordx4 %0, %4, 0 offen nt lds\n\ts_add_u32 m0, m0, 0x400\n\ts_nop 0\n\tbuffer_load_dwordx4 %1, %4, 0 offen nt lds\n\t"
                   "s_add_u32 m0, m0, 0x400\n\ts_nop 0\n\tbuffer_load_dwordx4 %2, %4, 0 offen nt lds\n\ts_add_u32 m0, m0, 0x400\n\ts_nop 0\n\tbuffer_load_dwordx4 %3, %4, 0 offen nt lds"
                   :: "v"(v2[0]), "v"(v2[1]), "v"(v2[2]), "v"(v2[3]), "s"(r2), "s"(l2) : "memory", "scc", "m0");
    }
  }
  SB();

  if (part == 0) {
    u32 wa2[8];
#pragma unroll
    for (int k = 0; k < 8; ++k) {
      const int r = 32 + g * 8 + ((lane >> 2) & 3) + (k & 1) * 4, ch = (k & 6) + ((lane >> 1) & 1);
      wa2[k] = wimg + wave * 8192 + r * 128 + ((ch ^ (r & 7)) * 16) + (lane & 1) * 8;
      PIN(wa2[k]);
    }
    rsrc_t wr2 = wsr;
    asm volatile("" : "+s"(wr2));
    until(t1);
    STAMP(11);
    CFENCE();
    u32x4 og[2];
    og[0] = ld16<AUX_SC1>(wr2, po, 0);
    SB();
    WAIT_VM(1); CFENCE();
#pragma unroll
    for (int t = 0; t < 4; ++t) {
      const u32x2 lo = tr_read(wa2[2 * t]), hi = tr_read(wa2[2 * t + 1]);
      bf[4 + t] = u32x4{lo[0], lo[1], hi[0], hi[1]};
    }
    SB();
    f32x4 d[4];
    auto wo_mm = [&]() {
      const f32x4 z = {0.f, 0.f, 0.f, 0.f};
#pragma unroll
      for (int t = 0; t < 4; ++t) { d[t] = mfma16(og[0], bf[t], z); og[1][t] = __float_as_uint(dpp<XOR8>(u2f(og[0][t]))); }
      SB();
#pragma unroll
      for (int t = 0; t < 4; ++t) d[t] = mfma16(og[1], bf[4 + t], d[t]);
    };
    auto p_bad = [&]() { return !all_ok(max(max(og[0][0], og[0][1]), max(og[0][2], og[0][3])) != SENT); };
    wo_mm();
    if (__builtin_expect(p_bad(), 0)) {
#pragma nounroll
      for (int spin = 0; spin < SPIN_MAX; ++spin) {
        __builtin_amdgcn_s_sleep(1);
        CFENCE();
        og[0] = ld16<AUX_SC1>(wsr, po, 0);
        if (!p_bad()) break;
      }
      wo_mm();
      SB();
      asm volatile(".Lend_d:");
    }
    STAMP(5); WSTAMP(5);
    {
      SB();
      f32x4 y; f32x2 h[4];
#pragma unroll
      for (int t = 0; t < 4; ++t) { h[t] = d[t].xy + d[t].zw; PIN(h[t]); }
#pragma unroll
      for (int t = 0; t < 4; ++t) { y[t] = h[t][0] + h[t][1]; PIN(y[t]); }
      *LDSP(f32x4, rdst) = y;
    }
    STAMP(16);
    LDS_BARRIER();
    STAMP(17);
    if (wave < 2) {
      f32x2 v[4];
#pragma unroll
      for (int w = 0; w < 4; ++w) v[w] = f32x2{LDSP(float, rsrc)[w * 256], LDSP(float, rsrc)[w * 256 + 128]};
      const f32x2 h = (v[0] + v[1]) + (v[2] + v[3]);
      const float y = h[0] + h[1];
      __builtin_amdgcn_raw_buffer_store_b32(min(pack_bf(dpp<0xA0>(y), dpp<0xF5>(y)), SENT - 1u), wr2, xo, 0, AUX_SC1);
      asm volatile(".Lend_a:");
    }
    STAMP(6);
    FINISH();
  }
  if (part == 3) {
    if (!has_w) FINISH();
    u32x4 b2[4];
    const u32 tdst = lds_addr(&sm.hb[hpos ^ ((wave & 1) * 16)]);
    const u32 sdst = lds_addr(&sm.stat[wave][lane]);
    const u32 ssrc = lds_addr(&sm.stat[(g >> 1) * 4 + (c >> 2)][(g & 1) * 32 + (c & 3) * 8]);
    const u32 adst = lds_addr((c < 4 && (g & 1) == 0) ? &sm.actl[g >> 1][wave * 4 + c] : reinterpret_cast<u16*>(&sm.dummy[lane]));
    const u32 asrc = lds_addr(&sm.actl[lane & 1][g * 8]);
    const u32 apa = lds_addr(&sm.hb[(c >> 3) * 512 + (c & 3) * 64 + ((c >> 2) & 1) * 32 + (g ^ (c & 2)) * 8]);
    u32 xoff = dup(G_XP) + near + g * 2048 + ub * 1024 + (wave & 3) * 256 + c * 16;
    u32x4 one;
#pragma unroll
    for (int k = 0; k < 4; ++k) one[k] = ((c & 2) == 0 && (c >> 2) == k) ? ((c & 1) ? 0x3f800000u : 0x00003f80u) : 0u;
    PIN(one);
    u32 ooff = dup(G_OP + (blk * 512 + tid) * 8) + ((nmask >> (g & 1)) & 8192u);
    PIN(xoff); PIN(ooff);
    PIN(ga); PIN(xin); PIN(cc1);
    u32 w2a = (u32)(uintptr_t)(&sm.w2b[0][0]) + (g * 8 + ((lane >> 2) & 3)) * 1024 + (wave ^ (g & 1)) * 128 + (lane & 15) * 8;
    u32 w2t[4];
#pragma unroll
    for (int t = 0; t < 4; ++t) { w2t[t] = w2a ^ (t * 32); PIN(w2t[t]); }
    static_assert(UD == 1, "one late fragment");
    u32 ufa = up_addr() + 7 * 4096;
    const u32 a7a = lds_addr(ap + 128 + ((lane >> 1) & 1) * 128 + 7 * 8);
    PIN(ufa);
    u64 gm1 = __builtin_amdgcn_ballot_w64(g == 1), gm2 = __builtin_amdgcn_ballot_w64(g == 2), gm3 = __builtin_amdgcn_ballot_w64(g == 3);
    asm volatile("" : "+s"(gm1), "+s"(gm2), "+s"(gm3));
    const bool g1 = __builtin_amdgcn_inverse_ballot_w64(gm1), g2 = __builtin_amdgcn_inverse_ballot_w64(gm2), g3 = __builtin_amdgcn_inverse_ballot_w64(gm3);
    if (wave == 1) {
      due = t1 + P.N1 * 256; gate();
      u32x4 gn[2] = {ld16<AUX_SC1>(wsr, G_NT + lane * 16, 0), ld16<AUX_SC1>(wsr, G_NT + lane * 16, 1024)};
      const int tn = notices(IC<2>{}, G_NT + lane * 16, gn) * 256;
      due = (tn > t1 + P.TH * 256 ? max(tn, t1 + P.L0 * 256) + P.L1 * 256 : t1) + P.X2 * 256 + slack;
      until(due);
    }
    asm volatile("s_waitcnt vmcnt(0)" ::: "memory");
    STAMP(9);
    __builtin_amdgcn_s_barrier();
    STAMP(11);
    CFENCE();
    u32x4 xg[2] = {ld16<AUX_SC1>(wsr, xoff, 0), ld16<AUX_SC1>(wsr, xoff, 16384)};
    SB();
    {
      const u32x4 a7 = *LDSP(u32x4, a7a);
      const u32x2 l7 = tr_read(ufa), h7 = tr_read(ufa + 256);
      bf[7] = u32x4{l7[0], l7[1], h7[0], h7[1]};
#pragma unroll
      for (int t = 0; t < 4; ++t) {
        const u32x2 lo = tr_read(w2t[t]), hi = tr_read(w2t[t] + 4096);
        b2[t] = u32x4{lo[0], lo[1], hi[0], hi[1]};
      }
      SB();
      const f32x4 d0 = mfma16(a7, bf[7], f32x4{0.f, 0.f, 0.f, 0.f});
      Af += par ? d0[1] : d0[0]; Bf += par ? d0[3] : d0[2];
      Af += dpp<XOR8>(Af); Bf += dpp<XOR8>(Bf);
    }
    PIN(Af); PIN(Bf);
#pragma unroll
    for (int t = 0; t < 4; ++t) PIN(b2[t]);
    SB();
    const f32x4 z4 = {0.f, 0.f, 0.f, 0.f};
    auto xp_bad = [&]() { return !all_ok(max(max(max(xg[0][0], xg[0][1]), max(xg[0][2], xg[0][3])), max(max(xg[1][0], xg[1][1]), max(xg[1][2], xg[1][3]))) != SENT); };
    f32x4 v = mfma16(one, xg[1], mfma16(one, xg[0], z4));
    if (__builtin_expect(xp_bad(), 0)) {
#pragma nounroll
      for (int spin = 0; spin < SPIN_MAX; ++spin) {
        __builtin_amdgcn_s_sleep(1);
        CFENCE();
        xg[0] = ld16<AUX_SC1>(wsr, xoff, 0); xg[1] = ld16<AUX_SC1>(wsr, xoff, 16384);
        if (!xp_bad()) break;
      }
      v = mfma16(one, xg[1], mfma16(one, xg[0], z4));
      SB();
      asm volatile(".Lend_e:");
    }
    STAMP(5); WSTAMP(5);
    {
      const float a0 = v[0], a1 = v[1];
      const float x10 = fmaf(ga[0], a0, xin[0]), x11 = fmaf(ga[1], a1, xin[1]);
      *LDSP(u32, tdst) = pack_bf(x10 * cc1[0], x11 * cc1[1]);
      float u = swap32_add(x10 + x11, fmaf(x10, x10, x11 * x11));
      u += dpp<XOR1>(u); u += dpp<XOR2>(u); u += dpp<XOR7>(u);
      *LDSP(float, sdst) = u;
    }
    STAMP(16);
    LDS_BARRIER();
    STAMP(17);
    {
      const f32x4 z = {0.f, 0.f, 0.f, 0.f};
      const u32x4 af0 = LDSP(u32x4, apa)[0], af1 = LDSP(u32x4, apa)[32];
      float s = LDSP(float, ssrc)[0];
      SB();
      f32x4 d[4];
      d[0] = mfma16(af0, bf[0], z); d[1] = mfma16(af0, bf[1], z);
      SB();
      s += dpp<XOR1>(s); s += dpp<XOR2>(s);
      PIN(s);
      d[2] = mfma16(af0, bf[2], z);
      SB();
      s += dpp<XOR7>(s); s += dpp<XOR8>(s);
      PIN(s);
      d[3] = mfma16(af0, bf[3], z);
      SB();
      const auto sw2 = __builtin_amdgcn_permlane16_swap(__float_as_uint(s), __float_as_uint(s), false, false);
      float mean = u2f(sw2[0]) * (1.f / 512.f), qm = fmaf(u2f(sw2[1]), 1.f / 512.f, 1e-5f);
      PIN(mean); PIN(qm);
      d[0] = mfma16(af1, bf[4], d[0]);
      SB();
      float ve = fmaxf(fmaf(-mean, mean, qm), 1e-5f), ma = mean * Af;
      PIN(ve); PIN(ma);
      d[1] = mfma16(af1, bf[5], d[1]);
      SB();
      float ri = __builtin_amdgcn_rsqf(ve);
      PIN(ri);
      d[2] = mfma16(af1, bf[6], d[2]);
      d[3] = mfma16(af1, bf[7], d[3]);
      SB();
      float cf = fmaf(-ri, ma, Bf), rl = ri * -1.4426950408889634f, cl = cf * -1.4426950408889634f;
      PIN(cf); PIN(rl); PIN(cl);
      PIN(d[0]); PIN(d[1]);
      float y0 = d[0][0] + d[1][1];
      PIN(d[2]);
      y0 += d[2][2];
      PIN(y0); PIN(d[3]);
      const float y1 = d[3][3];
      float y = y0 + y1;
      y = __builtin_bit_cast(float, __builtin_amdgcn_update_dpp(__builtin_bit_cast(int, y), __builtin_bit_cast(int, y), XOR8, 0xa, 0xf, false));
      y = swap16_add(y, y);
      const float e = __builtin_amdgcn_exp2f(fmaf(y, rl, cl));
      y = fmaf(y, ri, cf);
      float gu = y * dpp<0x104>(y);
      PIN(gu);
      *LDSP(u16, adst) = (u16)pack_bf(gu * __builtin_amdgcn_rcpf(1.f + e), 0.f);
    }
    STAMP(18);
    LDS_BARRIER();
    STAMP(10);
    {
      const u32x4 af = *LDSP(u32x4, asrc);
      const f32x4 z = {0.f, 0.f, 0.f, 0.f};
      f32x4 d[4];
#pragma unroll
      for (int t = 0; t < 4; ++t) d[t] = mfma16(af, b2[t], z);
      SB();
      PIN(d[0]); PIN(d[1]);
      float e0 = g1 ? d[1][0] : d[0][0], e1 = g1 ? d[1][1] : d[0][1];
      PIN(d[2]);
      e0 = g2 ? d[2][0] : e0; e1 = g2 ? d[2][1] : e1;
      PIN(e0); PIN(e1); PIN(d[3]);
      st8_sc1(wsr, ooff, pack_bf(g3 ? d[3][0] : e0, g3 ? d[3][1] : e1), tag);
    }
    asm volatile(".Lend_b:");
    STAMP(6);
    FINISH();
  }
  if (fin) {
    const int d = fd, ul = lane >> 3;
    u32x4 gm[2] = {sg[0], sg[1]};
    if (!all_ok((sg[0][1] == tag) & (sg[0][3] == tag) & (sg[1][1] == tag) & (sg[1][3] == tag))) gather16<2>(wsr, G_MOD + (1024 + d) * 8, 1536 * 8, tag, gm);
    u32 xa0, xa1;
    {
      due = t1 + P.N2 * 256; gate();
      u32x4 gn[2] = {ld16<AUX_SC1>(wsr, G_NT + lane * 16, 0), ld16<AUX_SC1>(wsr, G_NT + lane * 16, 1024)};
      const int tn = notices(IC<2>{}, G_NT + lane * 16, gn) * 256;
      due = (tn > t1 + P.TH * 256 ? max(tn, t1 + P.L0 * 256) + P.L1 * 256 : t1) + P.X3 * 256 + slack;
      until(due);
    }
#pragma nounroll
    for (int spin = 0; spin < SPIN_MAX; ++spin) {
      CFENCE();
      xa0 = __builtin_amdgcn_raw_buffer_load_b32(wsr, dup(G_XP + ul * 2048) + near + d * 2, 0, AUX_SC1);
      xa1 = __builtin_amdgcn_raw_buffer_load_b32(wsr, dup(G_XP + ul * 2048) + near + d * 2, 1024, AUX_SC1);
      if (all_ok(max(xa0, xa1) != SENT)) break;
      __builtin_amdgcn_s_sleep(1);
    }
    float a0 = swap16_add(lo_f(xa0), lo_f(xa1)), a1 = swap16_add(hi_f(xa0), hi_f(xa1));
    a0 += dpp<XOR8>(a0); a1 += dpp<XOR8>(a1);
    a0 = swap32_add(a0, a0); a1 = swap32_add(a1, a1);
    const bool hb = g & 1;
    f32x2 xv = {rbf(lo_f(sx2) + rbf((hb ? hi_f(gm[0][0]) : lo_f(gm[0][0])) * rbf(a0))), rbf(hi_f(sx2) + rbf((hb ? hi_f(gm[0][2]) : lo_f(gm[0][2])) * rbf(a1)))};
    f32x2 gv = {hb ? hi_f(gm[1][0]) : lo_f(gm[1][0]), hb ? hi_f(gm[1][2]) : lo_f(gm[1][2])};
    PIN(xv); PIN(gv);
    if (fw == 0 && lane == 0) __builtin_amdgcn_raw_buffer_store_b32(tag, wsr, WS_EPOCH, 0, AUX_SC1);
    STAMP(10);
    due = max(due + P.X4 * 256 + slack, (int)(((u32)wall_clock64() - t0) << 8) + P.GX * 256);
    const bool last = ul + 40 < NUP;
    u32 o0 = dup(G_OP + (ul * 512 + d) * 8) + near, o5 = last ? o0 : OOB, tg5 = last ? tag : 0u;
    const rsrc_t rout = mk_rsrc(out, 2048u);
    u32 oo = (lane & 40) == 0 ? (g * 512 + d) * 2 : OOB;
    const u32 pk = (c & 10) ? 0u : ((c & 4) ? 0x3f800000u : 0x00003f80u);
    u32x4 one = {(c & 1) ? 0u : pk, 0u, (c & 1) ? pk : 0u, 0u};
    PIN(o0); PIN(o5); PIN(tg5); PIN(oo); PIN(one);
    u32 so[6];
#pragma unroll
    for (int k = 0; k < 6; ++k) { so[k] = k * 65536; asm volatile("" : "+s"(so[k])); }
    __builtin_amdgcn_raw_buffer_store_b32(pack_bf(xv[0], xv[1]), rout, oo, 0, AUX_SC1);
    until(due);
#pragma nounroll
    for (int spin = 0; spin < SPIN_MAX; ++spin) {
      CFENCE();
      u32x4 og[6];
      og[0] = ld16<AUX_SC1>(wsr, o5, so[5]);
#pragma unroll
      for (int k = 1; k < 6; ++k) { SB(); og[k] = ld16<AUX_SC1>(wsr, o0, so[k - 1]); }
      SB();
      f32x4 sv = {0.f, 0.f, 0.f, 0.f};
#pragma unroll
      for (int k = 0; k < 6; ++k) sv = mfma16(one, og[k], sv);
      const float s0 = sv[0] + dpp<XOR8>(sv[0]), s1 = sv[1] + dpp<XOR8>(sv[1]);
      __builtin_amdgcn_raw_buffer_store_b32(pack_bf(fmaf(gv[0], s0, xv[0]), fmaf(gv[1], s1, xv[1])), rout, oo, 0, AUX_SC1);
      SB();
      u32 bad = (og[0][1] ^ tg5) | (og[0][3] ^ tg5);
#pragma unroll
      for (int k = 1; k < 6; ++k) { bad |= (og[k][1] ^ tag) | (og[k][3] ^ tag); PIN(bad); }
      if (all_ok(bad == 0u)) break;
      __builtin_amdgcn_s_sleep(1);
    }
    STAMP(7);
    asm volatile(".Lend_c:");
  }
  FINISH();
}

extern "C" void kernel_launch(void* const* d_in, const int* in_sizes, int n_in,
                              void* d_out, int out_size, void* d_ws, size_t ws_size,
                              hipStream_t stream) {
  fused_kernel<<<256, 512, 0, stream>>>((const u16*)d_in[0], (const u16*)d_in[1], (const u16*)d_in[2], (const u16*)d_in[3], (const u16*)d_in[4],
                                        (const u16*)d_in[6], (const u16*)d_in[7], (const u16*)d_in[8], (const u16*)d_in[9], (const u16*)d_in[10],
                                        (unsigned char*)d_ws, (u16*)d_out
    );
}
```

```cpp
#include <hip/hip_runtime.h>
#include <stdint.h>

typedef unsigned int u32;
typedef unsigned short u16;
typedef unsigned long long u64;
typedef __attribute__((ext_vector_type(4))) unsigned u32x4;
typedef __attribute__((ext_vector_type(2))) unsigned u32x2;
typedef __attribute__((ext_vector_type(2))) __bf16 bf16x2;
typedef __attribute__((ext_vector_type(4))) float f32x4;
typedef __attribute__((ext_vector_type(2))) float f32x2;
typedef __amdgpu_buffer_rsrc_t rsrc_t;

constexpr int NH = 32, SK = 16384, DH = 128, WIN = 4096, FF = 1376;
struct Prm { int E0, ES, E1, E2, E2C, EU, TEND, DT_UP, L0, L1, X1, X2, X3, X4, NE, FE, M0, M1, M2, M3, M4, GS, GX, N1, N2, BT, WD, DT_WO, LW, LU, EW, PM, WM, FK, FW, FU, EX, TS, DS, A0, AG, AC, PT, SL, TH, DT_0, L3; };
constexpr Prm PRM0 = {32, 24, 32, 32, 48, 64, 2008, 96, 25, 18, 72, 78, 98, 86, 5, 12, 12, 5, 12, 32, 28, 34, 34, 18, 45, 10, 4, 103, 40, 40, 8, 0x6827941, 0x53, 62, 61, 60, 8, 500, 122, 56, 24, 120, 160, 10, 6, 25, 15};
#define PRM_ARG
#define TABP (&TAB.e[0])
constexpr int NL = 37;
constexpr int NCMIN = 192;
constexpr int WK = 6, WV = 5;
constexpr int WUSE = 13, LNS = 11;
constexpr int QP1 = 12, QP2 = 18, QD = 5;
constexpr int VQ = 6;
constexpr int SLC[] = {0, 6, 9, 15, 21, 27, 33, 40}, NSL = sizeof(SLC) / sizeof(int) - 1;
constexpr int VS = VQ + NSL;
constexpr int MLS = VS + 2;
constexpr int VSIDE = VQ + 1, UPREP = 21;
constexpr int SIDE = NCMIN / 8 - 4;
constexpr int UD = 1;
constexpr int CPF = 18;
constexpr int TBP = 7, TBL = 11, TBU = 16;
constexpr int AR = 0, AL = 10;
constexpr int VL = 0, VU = 5;
constexpr int D0 = 30;
constexpr int KF = 4, KF0 = 8;
constexpr int side_k(int i) { return (i == KF || i == KF + 1) ? 3 : (i == TBL ? 1 : 0) + ((i == QP1 || i == QP2) ? 2 : 0); }
constexpr int side_v(int v) { return v == VQ - 4 ? 2 : (v == VSIDE ? 4 : ((v == AL || v == VL) ? 1 : 0)); }
constexpr int win_k(int i) { int n = WK; for (int j = i - WK; j <= i; ++j) n += side_k(j); return n; }
constexpr int win_v(int v, int w) { int n = w; for (int j = v - w; j <= v; ++j) n += side_v(j); return n; }
constexpr int cons_end(int v) {
  const int a = v - WV + 1, c = (v - VS) * 3;
  const int m = a < c ? a : c;
  return m < 0 ? 0 : (m > NL ? NL : m);
}
struct Tab { u32 e[1024]; };
#define popc __builtin_popcount
__host__ __device__ constexpr u32 place(u32 v, u32 m, int n) {
  u32 r = 0;
  for (int k = 0; k < n; ++k) r |= (v >> k & 1u) << (10 + (m >> (4 * k) & 15));
  return r;
}
__host__ __device__ constexpr u32 step_off(const Prm& P, u32 s0, u32 bh) {
  const u32 src = (s0 >> 5 & 3u) | bh << 2;
  return place(s0 ^ ((popc(src & P.M0) & 1) | (popc(src & P.M1) & 1) << 1 | (popc(src & P.M2) & 1) << 2 | (popc(src & P.M3) & 1) << 3 | (popc(src & P.M4) & 1) << 4), P.PM, 7);
}
constexpr Tab make_tab(const Prm& P) {
  Tab T{};
  for (int bid = 0; bid < 256; ++bid) {
    const int xcd = bid & 7, idx = bid >> 3, part = idx & 3, i8 = idx >> 2, wi = i8 * 8 + xcd;
    const bool has_mod = wi < 48, has_up = wi >= 21;
    const int cb0 = (i8 + 5) & 7, cb1 = (i8 + 4) & 7, ms = wi - 20;
    const int mi = wi < 8 ? wi : (wi < 16 ? 8 + ((xcd + 4) & 7) : (wi < 20 ? (wi - 14) * 8 + 3 : 16 + ms / 7 * 8 + ms % 7 + (ms % 7 >= 3 ? 1 : 0)));
    const int ui = xcd * 5 + (xcd > 5 ? xcd - 5 : 0) + (i8 >= 3 ? (xcd >= 5 ? 1 : 0) + i8 - 3 : 0);
    const int wd = cb0 == 3 ? 0 : P.WD;
    const int e[4] = {P.E0 - 4 * wd + (cb0 == 3 ? P.ES : 0), P.E1 + (cb1 == 3 ? P.ES : 0), has_mod ? (wi < 16 ? P.E2C : P.E2) + ((mi & 7) == 3 ? P.ES : 0) : 0, has_up ? P.EU * (8 - UD) / 8 : 0};
    const int ew = wd ? P.EW : 0, pa = (1024 + e[0] + ew + e[1] + e[2] + e[3] + (has_up ? P.EX : 0)) / 4;
    int c[5] = {0, 0, 0, 0, 128};
    for (int k = 0; k < 3; ++k) c[k + 1] = c[k] + (pa - e[k] - (k == 0 ? ew : 0) + 4) / 8;
    const int nl = c[part + 1] - c[part], wst = e[part] / 4;
    const bool has_w = part < 2 || (part == 2 ? has_mod : has_up);
    const int blk = !has_w ? 0 : (part == 0 ? cb0 : (part == 1 ? xcd * 8 + cb1 : (part == 2 ? mi : ui)));
    const int span = (part == 3 && has_up ? P.TEND - P.DT_UP : (part == 0 ? P.TEND - (wd ? P.DT_WO : P.DT_0) : P.TEND)) * 256;
    const int np = part == 0 ? 8 - wd : (part == 3 ? 8 - UD : 8), tau0 = span / (2 * nl + wst);
    const bool up = part == 3 && has_up;
    const int tau = tau0 * (up ? P.FU : (has_w && np == 8 ? P.FK : 64)) / 64, tauw = tau0 * wst / np * (up ? P.FU : P.FW) / 64;
    int sw = KF, at = P.DS * 256 + (has_w ? np * tauw : 0);
    while (sw < NCMIN / 8 - 1 && at < P.TS * 256) { at += tau; ++sw; }
    const int tau2 = (span - at) / (2 * nl - sw);
    T.e[4 * bid] = (u32)(c[part] | nl << 7 | blk << 13 | (has_w ? 1 : 0) << 19 | (8 - np) << 20 | sw << 24);
    T.e[4 * bid + 1] = (u32)(tau | tauw << 16);
    if ((part == 1 && nl <= QP2 + QD + 1) || c[part] > 32 * part + KF0 || c[part + 1] < 32 * part + KF0 + 4) T.e[4 * bid] = 0;
    const u32 pitch = part == 0 ? 1024u : (part == 1 ? 8192u : (part == 2 ? 6144u : 2752u));
    T.e[4 * bid + 2] = (part == 0 ? xcd * 512u * pitch : 0u) + blk * (part == 3 ? 64u : 128u);
    T.e[4 * bid + 3] = pitch | (u32)tau2 << 16;
  }
  return T;
}
constexpr int step_range(const Prm& P, bool mx_) {
  int mx = 0, mn = 1 << 20;
  const Tab T = make_tab(P);
  for (int bid = 0; bid < 256; ++bid) { const int n = T.e[4 * bid] >> 7 & 63; mx = n > mx ? n : mx; mn = n < mn ? n : mn; }
  return mx_ ? mx : mn;
}
__constant__ __attribute__((aligned(64))) const Tab TAB = make_tab(PRM0);
static_assert(step_range(PRM0, true) <= NL && step_range(PRM0, false) >= NCMIN / 8, "a wave's key range does not fit its load steps");
static_assert(cons_end(SIDE + WV + 1) <= NCMIN / 8 && cons_end(UPREP) <= NCMIN / 8 && SIDE > MLS + 3, "steps every wave runs");
constexpr int NUP = FF / 32;
constexpr int SPIN_MAX = 1 << 17;

constexpr u32 WS_EPOCH = 0;
constexpr u32 G_TB = 1024;
constexpr u32 G_NT = 2048;
constexpr u32 G_MOD = 4096;
constexpr u32 G_Q = G_MOD + 3072 * 8;
constexpr u32 G_AG = G_Q + 4096 * 8;
constexpr u32 G_ML = G_AG + 4096;
constexpr u32 G_P = 81920;
constexpr u32 G_U = G_P + 65536;
__device__ __forceinline__ u32 dup(u32 o) { return G_U + ((o >> 13) << 14) + (o & 8191u); }
constexpr u32 G_XP = 0;
constexpr u32 G_OP = G_XP + 8 * 512 * 4;
constexpr u32 G_PB = G_U + 2 * (G_OP + 44 * 512 * 8);
constexpr u32 WS_END = G_PB + 32768;
static_assert(G_P % 16384 == 0 && G_PB % 16384 == 0 && G_ML + 4096 <= G_P, "layout");
constexpr u32 SENT = 0xffffffffu;
constexpr u32 OOB = 0x40000000u;
#define WST 0

constexpr int AUX_NT = 2, AUX_SC1 = 16;

__device__ __forceinline__ float lo_f(u32 v) { return __uint_as_float(v << 16); }
__device__ __forceinline__ float hi_f(u32 v) { return __uint_as_float(v & 0xffff0000u); }
__device__ __forceinline__ float rbf(float x) { return (float)(__bf16)x; }
__device__ __forceinline__ u32 pack_bf(float lo, float hi) {
  bf16x2 r; r[0] = (__bf16)lo; r[1] = (__bf16)hi; return __builtin_bit_cast(u32, r);
}
__device__ __forceinline__ float dot2(u32 k, u32 q, float acc) {
  return __builtin_amdgcn_fdot2_f32_bf16(__builtin_bit_cast(bf16x2, k), __builtin_bit_cast(bf16x2, q), acc, false);
}
template <int CTRL> __device__ __forceinline__ float dpp(float x) {
  return __builtin_bit_cast(float, __builtin_amdgcn_mov_dpp(__builtin_bit_cast(int, x), CTRL, 0xf, 0xf, true));
}
constexpr int XOR1 = 0xB1, XOR2 = 0x4E, XOR7 = 0x141, XOR8 = 0x128;
__device__ __forceinline__ float swap32_add(float a, float b) {
  auto t = __builtin_amdgcn_permlane32_swap(__float_as_uint(a), __float_as_uint(b), false, false);
  return __uint_as_float(t[0]) + __uint_as_float(t[1]);
}
__device__ __forceinline__ float swap16_add(float a, float b) {
  auto t = __builtin_amdgcn_permlane16_swap(__float_as_uint(a), __float_as_uint(b), false, false);
  return __uint_as_float(t[0]) + __uint_as_float(t[1]);
}
__device__ __forceinline__ float wave_sum(float x) {
  x += dpp<XOR1>(x); x += dpp<XOR2>(x); x += dpp<XOR7>(x); x += dpp<XOR8>(x);
  x = swap16_add(x, x);
  return swap32_add(x, x);
}
__device__ __forceinline__ float wave_max(float x) {
  x = fmaxf(x, dpp<XOR1>(x)); x = fmaxf(x, dpp<XOR2>(x)); x = fmaxf(x, dpp<XOR7>(x)); x = fmaxf(x, dpp<XOR8>(x));
  auto s = __builtin_amdgcn_permlane16_swap(__float_as_uint(x), __float_as_uint(x), false, false);
  x = fmaxf(__uint_as_float(s[0]), __uint_as_float(s[1]));
  auto t = __builtin_amdgcn_permlane32_swap(__float_as_uint(x), __float_as_uint(x), false, false);
  return fmaxf(__uint_as_float(t[0]), __uint_as_float(t[1]));
}
template <int CTRL> __device__ __forceinline__ float sel_add(float lo, float hi, bool up) {
  const float t1 = lo + dpp<CTRL>(lo), t2 = hi + dpp<CTRL>(hi);
  return up ? t2 : t1;
}
template <int I> __device__ __forceinline__ float bc16(float x) {
  return __builtin_bit_cast(float, __builtin_amdgcn_update_dpp(0, __builtin_bit_cast(int, x), 0x150 + I, 0xf, 0xf, false));
}
__device__ __forceinline__ float bcast16(float x, int i) {
  switch (i) {
    case 0: return bc16<0>(x); case 1: return bc16<1>(x); case 2: return bc16<2>(x); case 3: return bc16<3>(x);
    case 4: return bc16<4>(x); case 5: return bc16<5>(x); case 6: return bc16<6>(x); case 7: return bc16<7>(x);
    case 8: return bc16<8>(x); case 9: return bc16<9>(x); case 10: return bc16<10>(x); case 11: return bc16<11>(x);
    case 12: return bc16<12>(x); case 13: return bc16<13>(x); case 14: return bc16<14>(x); default: return bc16<15>(x);
  }
}
__device__ __forceinline__ float silu_f(float x) { return x * __builtin_amdgcn_rcpf(1.f + __expf(-x)); }

template <int AUX> __device__ __forceinline__ u32x4 ld16(rsrc_t r, u32 voff, u32 soff) {
  return __builtin_amdgcn_raw_buffer_load_b128(r, voff, soff, AUX);
}
template <int AUX> __device__ __forceinline__ u32x2 ld8(rsrc_t r, u32 voff, u32 soff) {
  return __builtin_amdgcn_raw_buffer_load_b64(r, voff, soff, AUX);
}
__device__ __forceinline__ void st8_sc1(rsrc_t r, u32 voff, u32 v, u32 tag) {
  __builtin_amdgcn_raw_buffer_store_b64(u32x2{v, tag}, r, voff, 0, AUX_SC1);
}
__device__ __forceinline__ void st16_sc1(rsrc_t r, u32 voff, u32 v0, u32 v1, u32 tag) {
  __builtin_amdgcn_raw_buffer_store_b128(u32x4{v0, tag, v1, tag}, r, voff, 0, AUX_SC1);
}
__device__ __forceinline__ rsrc_t mk_rsrc(const void* p, u32 bytes) {
  return __builtin_amdgcn_make_buffer_rsrc(const_cast<void*>(p), 0, bytes, 0x00020000);
}
#define WAIT_VM(n) __builtin_amdgcn_s_waitcnt(((n) & 15) | (((n) >> 4) << 14) | (7 << 4) | (15 << 8))
#define SB() __builtin_amdgcn_sched_barrier(0)
#define CFENCE() asm volatile("" ::: "memory")
__device__ __forceinline__ bool all_ok(bool ok) { return __builtin_amdgcn_ballot_w64(!ok) == 0ull; }

template <int N> __device__ __forceinline__ void gather16(rsrc_t ws, u32 voff, u32 stride, u32 tag, u32x4 (&g)[N]) {
#pragma nounroll
  for (int spin = 0; spin < SPIN_MAX; ++spin) {
    CFENCE();
    bool ok = true;
#pragma unroll
    for (int i = 0; i < N; ++i) g[i] = ld16<AUX_SC1>(ws, voff + i * stride, 0);
#pragma unroll
    for (int i = 0; i < N; ++i) ok &= (g[i][1] == tag) & (g[i][3] == tag);
    if (all_ok(ok)) break;
    __builtin_amdgcn_s_sleep(8);
  }
}
template <int N> __device__ __forceinline__ void gather8(rsrc_t ws, u32 voff, u32 stride, u32 tag, u32 (&g)[N], bool act = true) {
#pragma nounroll
  for (int spin = 0; spin < SPIN_MAX; ++spin) {
    CFENCE();
    bool ok = true;
    u32x2 t[N];
#pragma unroll
    for (int i = 0; i < N; ++i) t[i] = ld8<AUX_SC1>(ws, voff + i * stride, 0);
#pragma unroll
    for (int i = 0; i < N; ++i) { ok &= (t[i][1] == tag); g[i] = t[i][0]; }
    if (all_ok(ok | !act)) break;
    __builtin_amdgcn_s_sleep(8);
  }
}

struct Smem {
  float red[8][128];
  float ml[2][8];
  float r2[64 * 20];
  u16 hb[3 * 1024];
  float stat[8][64];
  float red2[8][128];
  u32 dummy[64];
  u32x2 bc;
  u32x2 ctl;
  u32 nm, pad_[3];
  u32x4 zero[4];
  u32x4 wbuf[8][8][64];
  __attribute__((aligned(256))) u16 w2b[32][512];
  u16 actl[2][32];
};

#define STAMP(k) do {} while (0)
#define WSTAMP(k) do {} while (0)
#define FINISH() return
#define PIN(x) asm volatile("" : "+v"(x))
#define LDS_BARRIER() do { __builtin_amdgcn_s_waitcnt(0xc07f); __builtin_amdgcn_s_barrier(); CFENCE(); } while (0)
#define WAVE_SYNC() do { __builtin_amdgcn_fence(__ATOMIC_RELEASE, "wavefront"); __builtin_amdgcn_wave_barrier(); __builtin_amdgcn_fence(__ATOMIC_ACQUIRE, "wavefront"); } while (0)

__device__ __forceinline__ void dma16(rsrc_t r, u32 voff, u32 soff, u32 lds_dst) {
  u32 keep;
  asm volatile("s_mov_b32 %0, m0\n\ts_mov_b32 m0, %4\n\ts_nop 4\n\tbuffer_load_dwordx4 %1, %2, %3 offen nt lds\n\ts_mov_b32 m0, %0"
               : "=&s"(keep) : "v"(voff), "s"(r), "s"(soff), "s"(lds_dst) : "memory");
}
__device__ __forceinline__ u32 perm_lo(u32 a, u32 b) { return __builtin_amdgcn_perm(b, a, 0x05040100u); }
__device__ __forceinline__ u32 perm_hi(u32 a, u32 b) { return __builtin_amdgcn_perm(b, a, 0x07060302u); }
__device__ __forceinline__ float rdl(float x, int l) { return __int_as_float(__builtin_amdgcn_readlane(__float_as_int(x), l)); }

typedef __attribute__((ext_vector_type(8))) __bf16 bf16x8;
__device__ __forceinline__ f32x4 mfma16(u32x4 a, u32x4 b, f32x4 c) {
  return __builtin_amdgcn_mfma_f32_16x16x32_bf16(__builtin_bit_cast(bf16x8, a), __builtin_bit_cast(bf16x8, b), c, 0, 0, 0);
}
__device__ __forceinline__ float sum8(const float* p) {
  const f32x4 a = *reinterpret_cast<const f32x4*>(p), b = *reinterpret_cast<const f32x4*>(p + 4);
  return ((a[0] + b[0]) + (a[1] + b[1])) + ((a[2] + b[2]) + (a[3] + b[3]));
}
__device__ __forceinline__ float ln_ri(float var) {
  return __builtin_amdgcn_rcpf(rbf(__builtin_amdgcn_sqrtf(rbf(rbf(fmaxf(var, 0.f)) + 1e-5f))));
}
__device__ __forceinline__ void ln_stats2(const float (&x0)[8], const float (&x1)[8], float& mb0, float& ri0, float& mb1, float& ri1) {
  const float k0 = rdl(x0[0], 0), k1 = rdl(x1[0], 0);
  float s0 = 0.f, s1 = 0.f, q0 = 0.f, q1 = 0.f;
#pragma unroll
  for (int k = 0; k < 8; ++k) {
    const float d0 = x0[k] - k0, d1 = x1[k] - k1;
    s0 += d0; s1 += d1; q0 = fmaf(d0, d0, q0); q1 = fmaf(d1, d1, q1);
  }
  float t = swap16_add(swap32_add(s0, s1), swap32_add(q0, q1));
  t += dpp<XOR1>(t); t += dpp<XOR2>(t); t += dpp<XOR7>(t); t += dpp<XOR8>(t);
  auto u = __builtin_amdgcn_permlane16_swap(__float_as_uint(t), __float_as_uint(t), false, false);
  const float ms = __uint_as_float(u[0]) * (1.f / 512.f), mq = __uint_as_float(u[1]) * (1.f / 512.f);
  const float ri = ln_ri(fmaf(-ms, ms, mq));
  mb0 = rbf(k0 + rdl(ms, 0)); mb1 = rbf(k1 + rdl(ms, 32));
  ri0 = rdl(ri, 0); ri1 = rdl(ri, 32);
}
template <int I> struct IC { static constexpr int v = I; };
template <int I, int N, class F> __device__ __forceinline__ void sfor(F&& f) {
  if constexpr (I < N) { if (f(IC<I>{})) sfor<I + 1, N>(f); }
}
typedef __attribute__((ext_vector_type(4))) short s16x4;
typedef __attribute__((address_space(3))) s16x4* lds_s16x4p;
__device__ __forceinline__ u32x2 tr_read(u32 lds_addr) {
  return __builtin_bit_cast(u32x2, __builtin_amdgcn_ds_read_tr16_b64_v4i16((lds_s16x4p)lds_addr));
}
__device__ __forceinline__ u32 next_tag(u32 e) { return ((e | 0x40004000u) + 1u) & ~0x40004000u; }
__device__ __forceinline__ float u2f(u32 v) { return __uint_as_float(v); }
#define LDSP(T, a) ((__attribute__((address_space(3))) T*)(a))
template <class T> __device__ __forceinline__ u32 lds_addr(T* p) { u32 a = (u32)(uintptr_t)p; PIN(a); return a; }

typedef __attribute__((ext_vector_type(16))) unsigned u32x16;
typedef __attribute__((ext_vector_type(8))) unsigned u32x8;
__global__ void __launch_bounds__(512, 2) fused_kernel(const u16* x_, const u16* kc_, const u16* vc_, const u16* wq_, const u16* wo_, const u16* w1_,
                                                       const u16* w3_, const u16* w2_, const u16* cnd_, const u16* wmod_, unsigned char* ws_, u16* out_ PRM_ARG) {
  __shared__ __attribute__((aligned(256))) Smem sm;
  constexpr Prm P = PRM0;
  const int TEND = P.TEND;
  const int bid = blockIdx.x, tid = threadIdx.x;
  u32x4 kr[NL];
  u32x16 ka; u32x8 kb; u32x4 tev; u64 pt0; u32 tch2;
  asm volatile("s_load_dwordx16 %0, %3, 0x0\n\ts_load_dwordx8 %1, %3, 0x40\n\ts_memrealtime %2"
               : "=&s"(ka), "=&s"(kb), "=&s"(pt0) : "s"(__builtin_amdgcn_kernarg_segment_ptr()));
  SB();
  {
    u64 pc; u32 vb;
    asm volatile("s_getpc_b64 %2\n.Lcpf_first:\n\tv_mul_u32_u24 %1, ((.Lend_c-.Lcpf_first)>>10)<<2, %3\n\tglobal_load_dword %0, %1, %2"
                 : "=&v"(tch2), "=&v"(vb), "=&s"(pc) : "v"((bid & ~7) + (tid >> 6)) : "memory");
    asm volatile("global_load_dwordx4 %0, %1, %2" : "=&v"(tev) : "v"(bid << 4), "s"(TABP) : "memory");
  }
  SB();
  const int xcd = bid & 7, idx = bid >> 3, part = idx & 3;
  const int b = idx >> 2 & 1, head = xcd * 4 + (((idx >> 3) & 1) << 1 | idx >> 4);
  const u32 so0 = step_off(P, 32 * part + KF0, b * 32 + head) & ~place(3, P.PM, 2);
  u32 va = (((b * 32 + head) * SK + (SK - WIN)) * DH * 2 + so0) | place(tid >> 6, P.WM, 3) | (tid & 63) << 4;
  const u32 kx1 = place(1, P.PM, 2), kx2 = place(2, P.PM, 2);
  PIN(va);
  u32 va1 = va | kx1, va2 = va | kx2, va3 = va | kx1 | kx2;
  PIN(va1); PIN(va2); PIN(va3);
  int mc = bid & 0xd8;
  asm volatile("" : "+v"(va), "+s"(mc));
  SB();
  asm volatile("s_waitcnt lgkmcnt(0)" : "+s"(ka), "+s"(kb), "+s"(pt0));
  SB();
#define K_FIRST "global_load_dwordx4 %0, %4, %5 nt\n\tglobal_load_dwordx4 %1, %6, %5 nt\n\tglobal_load_dwordx4 %2, %7, %5 nt\n\tglobal_load_dwordx4 %3, %8, %5 nt"
  asm volatile("s_cmp_eq_u32 %9, 16\n\ts_cbranch_scc1 .Lk_later\n\t" K_FIRST "\n.Lk_later:"
               : "=&v"(kr[0]), "=&v"(kr[1]), "=&v"(kr[2]), "=&v"(kr[3]) : "v"(va), "s"((u64)ka[2] | (u64)ka[3] << 32), "v"(va1), "v"(va2), "v"(va3), "s"(mc) : "memory", "scc");
  SB();
  const u32 tka = (u32)wall_clock64(), t0 = tka - D0;
  auto kap = [&](int k) { return k < 8 ? (u64)ka[2 * k] | (u64)ka[2 * k + 1] << 32 : (u64)kb[2 * k - 16] | (u64)kb[2 * k - 15] << 32; };
  const u16 *x = (const u16*)kap(0), *kc = (const u16*)kap(1), *vc = (const u16*)kap(2), *w2 = (const u16*)kap(7), *cnd = (const u16*)kap(8);
  unsigned char* ws = (unsigned char*)kap(10); u16* out = (u16*)kap(11);
  const int lane = tid & 63, wave = __builtin_amdgcn_readfirstlane(tid >> 6);
  const bool p3 = part == 3;
  const u64 wa = part == 0 ? kap(4) : (part == 1 ? kap(3) : (part == 2 ? kap(9) : ((wave & 1) ? kap(6) : kap(5))));
  const bool modc = mc == 16;
  if (modc) asm volatile("s_waitcnt vmcnt(0)" ::: "memory");
  asm volatile("s_waitcnt vmcnt(4)" : "+v"(tev));
  const u32 te0 = __builtin_amdgcn_readfirstlane(tev[0]), te1 = __builtin_amdgcn_readfirstlane(tev[1]);
  const u32 te3 = __builtin_amdgcn_readfirstlane(tev[3]), pitch = te3 & 0xffff, wsz = pitch << (part == 0 ? 12 : 9);
  const int sh = p3 ? 2 : 3;
  u32 wv0 = (u32)((p3 ? (wave >> 1) * 16 : wave * 64) + (lane >> sh)) * pitch + ((lane & (p3 ? 3 : 7)) ^ (lane >> (p3 ? 5 : 3)) ^ (p3 ? wave >> 2 << 1 : 0)) * 16u;
  const u32 wimg = (u32)(uintptr_t)(&sm.wbuf[0][0][0]);
  u32 wld = wimg + (p3 ? (wave & 1) * 32768 + (wave >> 1) * 1024 : wave * 8192);
  const int bl0 = te0 & 127, nl = te0 >> 7 & 63, blk = te0 >> 13 & 63;
  const bool has_w = te0 >> 19 & 1;
  const rsrc_t rw = mk_rsrc((const void*)wa, wsz);
  const u32 wstep = pitch << (p3 ? 6 : 3), lstep = p3 ? 4096u : 1024u;
  u32 wso = __builtin_amdgcn_readfirstlane(tev[2]);
  int wleft = 8;
  auto piece = [&]() {
    dma16(rw, wv0, wso, wld);
    wso += wstep; wld += lstep;
    --wleft;
  };
  int tau = te1 & 0xffff;
  const int tauw = te1 >> 16, sw = te0 >> 24;
  int tau2 = te3 >> 16;
  int due = P.DS * 256, now = 0;
  auto gate = [&]() {
    while (now < due) { now = (int)(((u32)wall_clock64() - t0) << 8); if (now < due) __builtin_amdgcn_s_sleep(1); }
    due += tau;
  };
  const rsrc_t wsr = mk_rsrc(ws, WS_END);
  const int g = lane >> 4, c = lane & 15;
  const int i8 = idx >> 2, wi = i8 * 8 + xcd;
  const bool is_up = p3 && has_w;
  const size_t base = ((size_t)(b * NH + head) * SK + (SK - WIN)) * DH;
  const rsrc_t rk = mk_rsrc(kc + base, WIN * DH * 2), rv = mk_rsrc(vc + base, WIN * DH * 2);
  u32 sot;
  {
    u32 s0 = bl0 + lane - KF;
    s0 += s0 >= (u32)(32 * part + KF0) ? KF : 0;
    sot = (lane < KF ? so0 | place(lane, P.PM, 2) : step_off(P, s0, b * 32 + head)) | place(wave, P.WM, 3);
    sot = lane < nl ? sot : OOB;
    PIN(sot);
  }
  auto until = [&](int t) { while ((int)(((u32)wall_clock64() - t0) << 8) < t); };
  u32 near = 0, nmask = 0;
  int slack = 0, stv = 0;
  const rsrc_t rx = mk_rsrc(part == 2 ? cnd : x, (part == 1 || (part == 2 && has_w)) ? 2048u : 0u);
  u32x2 ep; u32 xs0, xs1, tch1; u32x4 xr0, xr1;
  auto small_ops = [&]() {
    ep = ld8<AUX_SC1>(wsr, wave == 7 ? WS_EPOCH : OOB, 0);
    xs0 = __builtin_amdgcn_raw_buffer_load_b16(rx, tid * 2, 0, 0); xs1 = __builtin_amdgcn_raw_buffer_load_b16(rx, tid * 2, 1024, 0);
  };
  auto small_ops2 = [&]() {
    xr0 = ld16<0>(rx, part == 1 ? lane * 16 : OOB, 0); xr1 = ld16<0>(rx, part == 1 ? lane * 16 : OOB, 1024);
    tch1 = __builtin_amdgcn_raw_buffer_load_b32(mk_rsrc(w2 + (size_t)blk * 32 * 512, is_up ? 64u : 0u), tid == 384 ? 0 : OOB, 0, AUX_NT);
  };
  u32 tag = 0;
  u32x4 bf[8];
  auto wo_prep = [&](int s0 = 0, int s1 = 2) {
    const u32 wi_ = wimg + wave * 8192;
    const int q = (lane >> 2) & 3, pp = lane & 3;
#pragma unroll
    for (int s = s0; s < s1; ++s) {
#pragma unroll
      for (int t = 0; t < 4; ++t) {
        const int r0 = s * 32 + g * 8 + q, r1 = r0 + 4, ch = t * 2 + (pp >> 1);
        const u32x2 lo = tr_read(wi_ + r0 * 128 + ((ch ^ (r0 & 7)) * 16) + (pp & 1) * 8), hi = tr_read(wi_ + r1 * 128 + ((ch ^ (r1 & 7)) * 16) + (pp & 1) * 8);
        bf[s * 4 + t] = u32x4{lo[0], lo[1], hi[0], hi[1]};
      }
    }
  };
  auto gemv_go = [&](u32 hh) {
    sm.hb[tid] = (u16)hh; sm.hb[512 + tid] = (u16)(hh >> 16);
    WAVE_SYNC();
    const u32x4* a = (c & 11) == 0 ? reinterpret_cast<const u32x4*>(&sm.hb[(c >> 2) * 512 + wave * 64 + g * 8]) : &sm.zero[0];
    const u32x4 a0 = a[0], a1 = a[(c & 11) == 0 ? 4 : 0];
    const f32x4 z = {0.f, 0.f, 0.f, 0.f};
    f32x4 d[4];
#pragma unroll
    for (int t = 0; t < 4; ++t) d[t] = mfma16(a0, bf[t], z);
#pragma unroll
    for (int t = 0; t < 4; ++t) d[t] = mfma16(a1, bf[4 + t], d[t]);
    if (lane < 32) {
#pragma unroll
      for (int t = 0; t < 4; ++t) sm.r2[(t * 16 + c) * 20 + g * 8 + wave] = d[t][0];
    }
  };
  auto mod_gemv = [&]() { gemv_go(pack_bf(silu_f(lo_f(xs0)), silu_f(lo_f(xs1)))); };
  auto mod_put = [&]() { st8_sc1(wsr, G_MOD + (blk * 64 + lane) * 8, pack_bf(sum8(&sm.r2[lane * 20]), sum8(&sm.r2[lane * 20 + 8])), tag); };
  auto zero_rows = [&]() { if (lane < 4) sm.zero[lane] = u32x4{0u, 0u, 0u, 0u}; };
  const u32 pbo = G_PB + (wi >> 3) * 16384 + (wi & 7) * 1024;
  u32 l16 = lane * 16;
  asm volatile("" : "+v"(l16), "+v"(wv0) : "s"(rk), "s"(rx), "s"(wsr));
  SB();
  if (has_w) {
    if (__builtin_expect(modc, 0)) {
      now = 1 << 30;
      __builtin_amdgcn_raw_buffer_store_b128(u32x4{SENT, SENT, SENT, SENT}, wsr, tid < 384 ? G_P + wi * 6144 + tid * 16 : pbo + (wave & 1) * 8192 + lane * 16, 0, AUX_SC1);
      __builtin_amdgcn_raw_buffer_store_b128(u32x4{SENT, SENT, SENT, SENT}, wsr, tid < 16 ? G_ML + wi * 256 + tid * 16 : (tid < 24 ? G_NT + wi * 128 + (tid - 16) * 16 : (tid < 32 && wi < 3 ? G_AG + wi * 128 + (tid - 24) * 16 : OOB)), 0, AUX_SC1);
    }
#pragma nounroll
    while (wleft > (int)(te0 >> 20 & 7)) { gate(); due += tauw - tau; piece(); }
    now = 0;
  }
  SB();
  STAMP(23);
  if (__builtin_expect(modc, 0)) {
    small_ops();
    zero_rows();
    {
      until(P.PT * 256);
      const u64 pa = (u64)ws + pbo + wave * 128 + (wave & 1) * 8192;
      u64 c0, c1, c2; u32 d0, d1;
      asm volatile("s_memtime %0\n\ts_waitcnt lgkmcnt(0)\n\ts_load_dword %3, %5, 0x0\n\ts_waitcnt lgkmcnt(0)\n\ts_memtime %1\n\ts_waitcnt lgkmcnt(0)\n\t"
                   "s_load_dword %4, %6, 0x0\n\ts_waitcnt lgkmcnt(0)\n\ts_memtime %2\n\ts_waitcnt lgkmcnt(0)"
                   : "=&s"(c0), "=&s"(c1), "=&s"(c2), "=&s"(d0), "=&s"(d1) : "s"(pa), "s"(pa + 8192 - (wave & 1) * 16384) : "memory");
      const int df = min(max((int)(2 * (u32)c1 - (u32)c0 - (u32)c2), -600), 600);
      if (lane == 0) sm.stat[0][wave] = (float)(((wave ^ (xcd >> 2)) & 1) ? -df : df);
    }
    asm volatile("s_waitcnt vmcnt(0)" ::: "memory");
    tag = next_tag(__builtin_amdgcn_readfirstlane(ep[0]));
    wo_prep();
    mod_gemv();
    LDS_BARRIER();
    if (wave == 7) { mod_put(); if (lane == 0) st8_sc1(wsr, G_AG + (48 + wi) * 8, __float_as_uint(sum8(&sm.stat[0][0])), tag); }
    STAMP(9);
    __builtin_amdgcn_s_barrier();
    asm volatile(K_FIRST :: "v"(kr[0]), "v"(kr[1]), "v"(kr[2]), "v"(kr[3]), "v"(va), "s"((u64)ka[2] | (u64)ka[3] << 32), "v"(va1), "v"(va2), "v"(va3) : "memory");
  }
  SB();
  const bool fin = (part == 1 || part == 2) && wave == 0 && xcd < 2;
  const int fw = i8 * 4 + (part & 2) + xcd, fd = fw * 16 + (lane & 7) * 2;
  const int ub = wave >> 2, ud = (wave & 3) * 128 + c * 8 + g * 2;
  u32x4 sg[3]; u32 sx2;
  auto side_loads = [&]() {
    sg[0] = ld16<AUX_SC1>(wsr, is_up ? G_MOD + (1024 + ud) * 8 : (fin ? G_MOD + (1024 + fd) * 8 : OOB), 0);
    sg[1] = ld16<AUX_SC1>(wsr, is_up ? G_MOD + (1536 + ud) * 8 : (fin ? G_MOD + (2560 + fd) * 8 : OOB), 0);
    sg[2] = ld16<AUX_SC1>(wsr, is_up ? G_MOD + (2048 + ud) * 8 : OOB, 0);
    sx2 = __builtin_amdgcn_raw_buffer_load_b32(mk_rsrc(x, (is_up || fin) ? 2048u : 0u), is_up ? ub * 1024 + ud * 2 : (g & 1) * 1024 + fd * 2, 0, 0);
  };
  auto side_pin = [&]() { PIN(sg[0]); PIN(sg[1]); PIN(sg[2]); PIN(sx2); };
  const u32 mlo = G_ML + (head * 2 + b) * 64 + (lane & 3) * 16;
  float fwv = 0.f, fmw[8];
  bool fok = wave != 0;
  auto merge_factor = [&](const u32x4& ml) {
    const float mi = u2f(ml[0]);
    float M = fmaxf(mi, dpp<XOR1>(mi)); M = fmaxf(M, dpp<XOR2>(M));
    const float e = __expf(mi - M);
    float L = e * u2f(ml[2]);
    L += dpp<XOR1>(L); L += dpp<XOR2>(L);
    const float f = fwv * rdl(e * __builtin_amdgcn_rcpf(L), part);
#pragma unroll
    for (int w = 0; w < 8; ++w) fmw[w] = bcast16(f, w);
  };
  f32x2 ga = {0.f, 0.f}, xin = {0.f, 0.f}, cc1 = {0.f, 0.f};
  float Af = 0.f, Bf = 0.f;
  const int hpos = ub * 512 + ud;
  auto up_prep = [&]() {
    if (!all_ok((sg[0][1] == tag) & (sg[0][3] == tag) & (sg[1][1] == tag) & (sg[1][3] == tag) & (sg[2][1] == tag) & (sg[2][3] == tag))) gather16<3>(wsr, G_MOD + (1024 + ud) * 8, 512 * 8, tag, sg);
    ga = f32x2{ub ? hi_f(sg[0][0]) : lo_f(sg[0][0]), ub ? hi_f(sg[0][2]) : lo_f(sg[0][2])};
    cc1 = f32x2{rbf(1.f + (ub ? hi_f(sg[2][0]) : lo_f(sg[2][0]))), rbf(1.f + (ub ? hi_f(sg[2][2]) : lo_f(sg[2][2])))};
    xin = f32x2{lo_f(sx2), hi_f(sx2)};
    u32* hp = reinterpret_cast<u32*>(&sm.hb[hpos]);
    hp[512] = pack_bf(cc1[0], cc1[1]);
    hp[1024] = ub ? perm_hi(sg[1][0], sg[1][2]) : perm_lo(sg[1][0], sg[1][2]);
  };
  auto up_addr = [&]() {
    const int pp = lane & 3;
    return wimg + (pp & 1) * 32768 + ((pp >> 1) * 32 + g * 8 + ((lane >> 2) & 3)) * 64 + (((wave >> 1) ^ ((g & 1) | (pp & 2))) * 16) + (wave & 1) * 8;
  };
  auto up_frags = [&](int r0, int r1) {
    const u32 a = up_addr();
#pragma unroll
    for (int rb = r0; rb < r1; ++rb) {
      const u32x2 lo = tr_read(a + rb * 4096), hi = tr_read(a + rb * 4096 + 256);
      bf[rb] = u32x4{lo[0], lo[1], hi[0], hi[1]};
    }
  };
  const u32x4* ap = reinterpret_cast<const u32x4*>(&sm.hb[((lane >> 3) & 1) * 512 + (lane & 1) * 32 + g * 8]);
  const bool par = lane & 8;
  auto col_sums = [&](int r0, int r1) {
    const u32x4* a1 = ap + 128 + ((lane >> 1) & 1) * 128;
    f32x4 d0 = {0.f, 0.f, 0.f, 0.f}, d1 = {0.f, 0.f, 0.f, 0.f};
#pragma unroll
    for (int rb = r0; rb < r1; ++rb) { if (rb & 1) d1 = mfma16(a1[rb * 8], bf[rb], d1); else d0 = mfma16(a1[rb * 8], bf[rb], d0); }
    Af += par ? d0[1] + d1[1] : d0[0] + d1[0]; Bf += par ? d0[3] + d1[3] : d0[2] + d1[2];
  };
  float l0 = 0.f, l1 = 0.f;
  u32x2 gmd[2], tbg;
  const bool modw = part == 2 && has_w && !modc;
  const int qp = part != 1 ? -99 : ((blk & 7) == 3 ? QP1 : QP2);
  static_assert(QP1 + QD < QP2 && QP1 + QD > WUSE && QP1 + QD > TBU && WUSE + 1 < TBU, "order of the q steps");
  int t1 = 0;
#pragma unroll
  for (int i = NCMIN / 8; i < NL; ++i) kr[i] = u32x4{0u, 0u, 0u, 0u};
  sfor<KF, NL>([&](auto ic) {
    constexpr int i = ic.v;
    if (i >= NCMIN / 8 && i >= nl) return false;
    const u32 so = __builtin_amdgcn_readlane(sot, i);
    if constexpr (i < NCMIN / 8) tau = i == sw ? tau2 : tau;
    gate();
    SB();
    kr[i] = ld16<AUX_NT>(rk, l16, so);
    if constexpr (i == KF) small_ops();
    if constexpr (i == KF + 1) small_ops2();
    if constexpr (i == KF + 2) zero_rows();
    if constexpr (i == TBL) tbg = ld8<AUX_SC1>(wsr, wave == 7 ? G_TB + (lane & 7) * 8 : OOB, 0);
    if constexpr (i == QP1 || i == QP2) { gmd[0] = ld8<AUX_SC1>(wsr, i == qp ? G_MOD + tid * 8 : OOB, 0); gmd[1] = ld8<AUX_SC1>(wsr, i == qp ? G_MOD + tid * 8 : OOB, 4096); }
    SB();
    SB();
    if (i >= WK) { WAIT_VM(win_k(i)); SB(); }
    if (i == KF) STAMP(25);
    if (i == 8) STAMP(12);
    if (i == 20) STAMP(21);
    if (i == 25) STAMP(22);
    if (i == TBP) {
      if (part == 3 && i8 == 0 && wave == 7) { tag = next_tag(__builtin_amdgcn_readfirstlane(ep[0])); if (lane == 0) st8_sc1(wsr, G_TB + xcd * 8, tka - P.GS, tag); }
      SB();
    }
    if (i == TBU - 1) {
      if (wave == 7) {
        const bool ok = tbg[1] == tag;
        float lf = ok ? (float)min(max((int)(t0 - tbg[0]), -300), 300) : 0.f, nf = ok ? 1.f : 0.f;
        lf += dpp<XOR1>(lf); nf += dpp<XOR1>(nf); lf += dpp<XOR2>(lf); nf += dpp<XOR2>(nf);
        lf *= __builtin_amdgcn_rcpf(fmaxf(nf, 1.f));
        const float ot = dpp<XOR7>(lf);
        if (lane == 0) sm.bc = u32x2{tag, t0 - (u32)(int)((lf + ot) * 0.5f - fabsf(lf - ot) * (P.BT / 32.f))};
      }
      SB();
    }
    if (i == TBU) {
      LDS_BARRIER();
      const u32x2 bc = sm.bc;
      tag = __builtin_amdgcn_readfirstlane(bc[0]);
      t1 = (__builtin_amdgcn_readfirstlane((int)(bc[1] - t0)) + TEND + P.X1) * 256;
      const int dt = __builtin_amdgcn_readfirstlane((int)((float)(int)(t0 - bc[1]) * 256.f * __builtin_amdgcn_rcpf((float)(2 * nl - TBU - 2))));
      tau -= dt; tau2 -= dt;
      SB();
    }
    if (i == TBU + 1) {
      if (modw) { if (wave == 6) mod_put(); STAMP(9); }
      SB();
    }
    if (i == LNS) {
      tag = next_tag(__builtin_amdgcn_readfirstlane(ep[0]));
      if (part == 1) {
        float x0[8], x1[8], mb0, ri0, mb1, ri1;
#pragma unroll
        for (int k = 0; k < 4; ++k) { x0[2 * k] = lo_f(xr0[k]); x0[2 * k + 1] = hi_f(xr0[k]); x1[2 * k] = lo_f(xr1[k]); x1[2 * k + 1] = hi_f(xr1[k]); }
        ln_stats2(x0, x1, mb0, ri0, mb1, ri1);
        l0 = rbf(rbf(lo_f(xs0) - mb0) * ri0); l1 = rbf(rbf(lo_f(xs1) - mb1) * ri1);
      }
      PIN(l0); PIN(l1);
      SB();
    }
    if (i == WUSE) {
      PIN(tch1); PIN(tch2);
      static_assert(WUSE - WK >= KF, "the weight pieces are older than the steps outside the window");
      CFENCE();
      if (part == 1 || modw) wo_prep();
#pragma unroll
      for (int k = 0; k < 8; ++k) PIN(bf[k]);
      SB();
    }
    if (i == WUSE + 1) {
      if (modw) mod_gemv();
      SB();
    }
    if (i == QP1 + QD || i == QP2 + QD) {
      if (i == qp + QD) {
        u32 gm[2] = {gmd[0][0], gmd[1][0]};
        if (!all_ok((gmd[0][1] == tag) & (gmd[1][1] == tag))) { STAMP(11); gather8<2>(wsr, G_MOD + tid * 8, 512 * 8, tag, gm); }
        STAMP(5);
        gemv_go(pack_bf(rbf(l0 * rbf(1.f + lo_f(gm[1]))) + lo_f(gm[0]), rbf(l1 * rbf(1.f + hi_f(gm[1]))) + hi_f(gm[0])));
      }
      SB();
    }
    if (i == QP1 + QD + 1 || i == QP2 + QD + 1) {
      if (i == qp + QD + 1) {
        LDS_BARRIER();
        if (wave == 0) {
          const int bb = lane >> 5, jq = lane & 31;
          st8_sc1(wsr, G_Q + (bb * 2048 + blk * 32 + jq) * 8, pack_bf(sum8(&sm.r2[2 * jq * 20 + bb * 8]), sum8(&sm.r2[(2 * jq + 1) * 20 + bb * 8])), tag);
        }
        STAMP(6);
      }
      SB();
    }
    return true;
  });
  PIN(sot);
  const u32 qoff = G_Q + ((b * 32 + head) * 64 + c * 4) * 8;
  u32x4 qg[2], vr[NL], fl2[3];
  u32 qq[4];
  constexpr int NB = (NL + 15) / 16;
  float xlo, xs[4], sc[NB], pr[NB], m = 0.f;
  f32x4 acc = {0.f, 0.f, 0.f, 0.f};
  u32 pb[NB], pm[4], cpf;
  u32x2 agr;
  const u32 adst0 = lds_addr(lane < 32 ? &sm.red[wave][c * 8 + g * 4] : &sm.r2[(lane - 32) * 4]);
  u32 pso, po, rdst, rsrc, xo, l2, v2[4];
  rsrc_t r2;
  auto wo_addr = [&]() {
    po = G_P + (xcd & 3) * 16384 + near + (c >> 2 & 1) * 4096 + ((wave >> 1) * 4 + (c & 3)) * 256 + (wave & 1) * 128 + (c >> 3) * 64 + g * 16;
    rdst = lane < 32 ? (u32)(uintptr_t)&sm.red2[wave][0] + lane * 16 : (u32)(uintptr_t)&sm.r2[0] + (lane - 32) * 16;
    rsrc = (u32)(uintptr_t)&sm.red2[0][((wave & 1) * 16 + (lane & 15)) * 4 + (lane >> 4)];
    xo = dup(G_XP + xcd * 2048 + (wave & 1) * 1024 + (blk * 64 + (lane & ~1)) * 2) + (lane & 1) * 8192;
    pso = wave == 0 ? G_P + (xcd & 3) * 16384 + near + b * 4096 + ((head & 3) * 4 + part) * 256 + lane * 4 : OOB;
    PIN(pso); PIN(po); PIN(rdst); PIN(rsrc); PIN(xo);
  };
  auto w2_addr = [&]() {
    r2 = mk_rsrc(w2 + (size_t)(blk * 32 + wave * 4) * 512, 4096u);
    l2 = (u32)(uintptr_t)(&sm.w2b[wave * 4][0]);
#pragma unroll
    for (int n = 0; n < 4; ++n) { v2[n] = (lane ^ (n * 2) ^ ((wave & 2) << 2)) * 16 + n * 1024; PIN(v2[n]); }
    asm volatile("" : "+s"(r2), "+s"(l2));
  };
  const bool late = is_up || (part == 0 && wleft);
  const u32 nadd = is_up ? P.LU : (part == 0 ? (wleft ? P.LW : P.L3) : 0);
  u32 nto = tid == 0 ? G_NT + bid * 8 : OOB;
  PIN(nto);
  auto notices = [&](auto nc_, u32 off, u32x4 (&gn)[nc_.v]) -> int {
    constexpr int N = nc_.v;
#pragma nounroll
    for (int spin = 0; spin < SPIN_MAX; ++spin) {
      bool ok = true;
#pragma unroll
      for (int i = 0; i < N; ++i) ok &= (gn[i][1] == tag) & (gn[i][3] == tag);
      if (all_ok(ok)) break;
      __builtin_amdgcn_s_sleep(10);
      CFENCE();
#pragma unroll
      for (int i = 0; i < N; ++i) gn[i] = ld16<AUX_SC1>(wsr, off, i * 1024);
    }
    int mx = 0;
#pragma unroll
    for (int i = 0; i < N; ++i) mx = max(mx, max((int)(gn[i][0] - t0), (int)(gn[i][2] - t0)));
    return __builtin_amdgcn_readfirstlane((int)wave_max((float)min(mx, 1 << 20)));
  };
  const int side = nl >= SIDE + 13 ? SIDE + 8 : (nl >= SIDE + 9 ? SIDE + 4 : SIDE);
  sfor<0, NL + WV>([&](auto ic) {
    constexpr int v = ic.v;
    if (cons_end(v - 1) >= NCMIN / 8 && cons_end(v - 1) >= nl) return false;
    if constexpr (v < NL) {
      const u32 so = __builtin_amdgcn_readlane(sot, v);
      if constexpr (v >= 12) { if (v == nl - P.NE) tau = (tau * (late ? 16 : P.FE)) >> 4; }
      if constexpr (v >= NCMIN / 8) { if (v < nl) gate(); } else gate();
      SB();
      int bk = 0;
      if constexpr (v == AR) { if (part == 1 && wi < 48 && wave == 7) bk = ((int)(((u32)wall_clock64() - t0) << 8) - due + tau + (int)__builtin_amdgcn_s_getreg(7 | (3 << 11)) * tau) >> 8; SB(); }
      vr[v] = ld16<AUX_NT>(rv, l16, so);
      if constexpr (v == AR) {
        SB();
        if (part == 1 && wi < 48 && tid == 448) st8_sc1(wsr, G_AG + wi * 8, (u32)min(max(bk, 0), 250), tag);
      }
      SB();
      SB();
    }
    if constexpr (v == NCMIN / 8 - 1) {
#pragma unroll
      for (int i = NCMIN / 8; i < NL; ++i) asm volatile("" : "=v"(vr[i]));
      asm volatile("" : "=v"(fl2[1]), "=v"(fl2[2]));
#pragma unroll
      for (int w = 0; w < 8; ++w) asm volatile("" : "=v"(fmw[w]));
    }
    if constexpr (v == VQ - 4) { qg[0] = ld16<AUX_SC1>(wsr, qoff, 0); qg[1] = ld16<AUX_SC1>(wsr, qoff + 16, 0); }
    if constexpr (v == VSIDE) side_loads();
    if constexpr (v == VL) agr = ld8<AUX_SC1>(wsr, wave == 6 ? G_AG + (48 + c) * 8 : OOB, 0);
    if constexpr (v == AL) agr = ld8<AUX_SC1>(wsr, (wave == 7 && lane < 48) ? G_AG + lane * 8 : OOB, 0);
    if constexpr (v == CPF) {
      u64 pc; u32 dlt, len;
      asm volatile("s_getpc_b64 %0\n.Lcpf_here:\n\ts_mov_b32 %1, .Ltail_start-.Lcpf_here\n\ts_mov_b32 %2, .Lend_a-.Ltail_start\n\ts_max_i32 %2, %2, .Lend_b-.Ltail_start\n\ts_max_i32 %2, %2, .Lend_c-.Ltail_start\n\ts_max_i32 %2, %2, .Lend_d-.Ltail_start\n\ts_max_i32 %2, %2, .Lend_e-.Ltail_start" : "=s"(pc), "=s"(dlt), "=s"(len) :: "scc");
      pc += dlt;
      const rsrc_t rc = mk_rsrc((const void*)pc, (part == 3 && i8 == 1) ? len : 0u);
      cpf = __builtin_amdgcn_raw_buffer_load_b32(rc, tid * 64, 0, 0);
    }
    if constexpr (v == CPF + WV + 1) PIN(cpf);
    if constexpr (v == SIDE || v == SIDE + 4 || v == SIDE + 8) fl2[(v - SIDE) / 4] = ld16<AUX_SC1>(wsr, (wave == 0 && v == side) ? mlo : OOB, 0);
    SB();
    if constexpr (v < WV) { WAIT_VM(win_v(v, WK)); SB(); } else if constexpr (v < NL && cons_end(v) < v - WV + 1) { WAIT_VM(win_v(v, WV)); SB(); }
    if constexpr (v == VU) {
      if (wave == 6) {
        u32 vt[1] = {agr[0]};
        if (!all_ok(agr[1] == tag)) gather8<1>(wsr, G_AG + (48 + c) * 8, 0, tag, vt);
        float sv = u2f(vt[0]);
        sv += dpp<0x124>(sv); sv += dpp<XOR8>(sv);
        float sa = sv + dpp<XOR1>(sv);
        sv = fmaf(sa + dpp<XOR2>(sa), 0.25f, sv);
        float cf = fabsf(sv);
        cf = fminf(cf, dpp<XOR1>(cf)); cf = fminf(cf, dpp<XOR2>(cf));
        const u32 nm = (u32)__builtin_amdgcn_ballot_w64(sv > 0.f) & 15u;
        if (lane == 0) sm.nm = nm << 13 | (cf < 2500.f ? (u32)P.SL : 0u);
      }
      PIN(agr);
    }
    if constexpr (v == VQ) {
      if (!all_ok((qg[0][1] == tag) & (qg[0][3] == tag) & (qg[1][1] == tag) & (qg[1][3] == tag))) { STAMP(20); gather16<2>(wsr, qoff, 16, tag, qg); }
      STAMP(1); WSTAMP(1);
      qq[0] = qg[0][0]; qq[1] = qg[0][2]; qq[2] = qg[1][0]; qq[3] = qg[1][2];
    }
    if constexpr (v >= VQ && v < VS) {
      constexpr int u0 = SLC[v - VQ], u1 = SLC[v - VQ + 1];
      static_assert(SLC[NSL] == (NL + 7) / 8 * 8, "all groups of 8");
      sfor<u0, u1>([&](auto uc) {
        constexpr int u = uc.v, p = u & 7, i = (p & 1) * 4 + (p & 2) + (p >> 2), sl = u >> 3, bt = sl >> 1;
        float d = 0.f;
        if constexpr (sl * 8 + i < NL) {
#pragma unroll
          for (int k = 0; k < 4; ++k) d = dot2(kr[sl * 8 + i][k], qq[k], d);
        }
        if constexpr (p & 1) xs[i - 4] = sel_add<XOR7>(xs[i - 4], d, c & 4); else xs[i] = d;
        if constexpr ((p & 3) == 3) xs[p >> 2] = sel_add<XOR2>(xs[p >> 2], xs[(p >> 2) + 2], c & 2);
        if constexpr (p == 7) {
          xs[0] = sel_add<XOR1>(xs[0], xs[1], c & 1);
          if constexpr ((sl & 1) || u == (NL + 7) / 8 * 8 - 1) {
            const float t = (sl & 1) ? sel_add<XOR8>(xlo, xs[0], c & 8) : xs[0] + dpp<XOR8>(xs[0]);
            const float s = rbf(rbf(t) * 0.08838834764831845f);
            sc[bt] = (bt * 16 + c < nl) ? s : -__builtin_inff();
            PIN(sc[bt]);
          } else {
            xlo = xs[0];
            PIN(xlo);
          }
        }
        return true;
      });
      constexpr int pe = u1 & 7;
      if constexpr (pe >= 1) PIN(xs[0]);
      if constexpr (pe == 3) PIN(xs[2]);
      if constexpr (pe >= 5) PIN(xs[1]);
      if constexpr (pe == 7) PIN(xs[3]);
    }
    if constexpr (v == VS) {
      float mloc = sc[0];
#pragma unroll
      for (int bt = 1; bt < NB; ++bt) mloc = fmaxf(mloc, sc[bt]);
      m = fmaxf(wave_max(mloc), -3.0e38f);
      float ls = 0.f;
#pragma unroll
      for (int bt = 0; bt < NB; ++bt) { pr[bt] = __expf(sc[bt] - m); ls += pr[bt]; }
      const float l = wave_sum(ls);
      if (lane == 0) { sm.ml[0][wave] = m; sm.ml[1][wave] = l; }
#pragma unroll
      for (int bt = 0; bt < NB; ++bt) { pb[bt] = pack_bf(pr[bt], pr[bt]); PIN(pb[bt]); }
#pragma unroll
      for (int k = 0; k < 4; ++k) { pm[k] = (c >> 1) == k ? ((c & 1) ? 0xffff0000u : 0x0000ffffu) : 0u; PIN(pm[k]); }
      STAMP(2); WSTAMP(2);
    }
    sfor<cons_end(v - 1), cons_end(v)>([&](auto jc) {
      constexpr int i = jc.v;
      if (i < NCMIN / 8 || i < nl) {
        const u32 pv = __float_as_uint(bcast16(u2f(pb[i >> 4]), i & 15));
        acc = mfma16(u32x4{pv & pm[0], pv & pm[1], pv & pm[2], pv & pm[3]}, vr[i], acc);
      }
      PIN(acc);
      return true;
    });
    if constexpr (v == VS - 1) { side_pin(); if (is_up) up_prep(); PIN(ga); PIN(xin); PIN(cc1); }
    if constexpr (v == MLS - 1) {
      if (wave == 7) {
        const float am = wave_sum(lane < 48 ? (agr[1] == tag ? (float)agr[0] : 120.f) : 0.f) * (1.f / 48.f);
        const float st = fminf(fmaxf((am - (float)P.A0) * (float)(P.AG * 16), 0.f), (float)(P.AC * 256));
        if (lane == 0) sm.ctl = u32x2{(u32)(int)st, (u32)(int)(st * __builtin_amdgcn_rcpf((float)(nl - MLS - 3)))};
      }
      PIN(agr);
    }
    if constexpr (v == UPREP - 2) {
      nmask = __builtin_amdgcn_readfirstlane(sm.nm);
      slack = (nmask & 255u) * 256; near = ((nmask >> (xcd & 3)) ^ (xcd << 11)) & 8192u;
      w2_addr();
    }
    if constexpr (v == UPREP - 1) wo_addr();
    if constexpr (v == UPREP) {
#pragma unroll
      for (int k = 0; k < 8; ++k) asm volatile("" : "=v"(bf[k]));
      if (part == 0) wo_prep(0, 1); else if (is_up) up_frags(0, 8 - UD);
#pragma unroll
      for (int k = 0; k < 8; ++k) PIN(bf[k]);
    }
    if constexpr (v == UPREP + 1 || v == UPREP + 2) {
      if (is_up) { if constexpr (v == UPREP + 1) col_sums(0, (8 - UD) / 2); else col_sums((8 - UD) / 2, 8 - UD); }
      PIN(Af); PIN(Bf);
    }
    if constexpr (v == SIDE + WV + 1 || v == SIDE + WV + 5 || v == SIDE + WV + 9) {
      if (wave == 0 && v == side + WV + 1) {
        const u32x4 ml = fl2[(v - SIDE - WV - 1) / 4];
        fok = all_ok((ml[1] == tag) & (ml[3] == tag));
        merge_factor(ml);
      }
    }
    if (v % 6 == 4 && v >= 10 && v < 30) STAMP(15 + v / 6);
    if constexpr (v == MLS) {
      LDS_BARRIER();
      const u32x2 ct = sm.ctl;
      if (wave == 0) {
        const float mw = sm.ml[0][lane & 7];
        float Mwg = fmaxf(mw, dpp<XOR1>(mw)); Mwg = fmaxf(Mwg, dpp<XOR2>(Mwg)); Mwg = fmaxf(Mwg, dpp<XOR7>(Mwg));
        fwv = __expf(mw - Mwg);
        float Lwg = fwv * sm.ml[1][lane & 7];
        Lwg += dpp<XOR1>(Lwg); Lwg += dpp<XOR2>(Lwg); Lwg += dpp<XOR7>(Lwg);
        if (lane == 0) st16_sc1(wsr, G_ML + ((head * 2 + b) * 4 + part) * 16, __float_as_uint(Mwg), __float_as_uint(Lwg), tag);
      }
      PIN(fwv);
      stv = __builtin_amdgcn_readfirstlane(ct[0]); t1 += stv / 2;
      tau += __builtin_amdgcn_readfirstlane(ct[1]) / 2;
    }
    SB();
    return true;
  });
  asm volatile(".Ltail_start:");
  *LDSP(f32x4, adst0) = acc;
  STAMP(14); WSTAMP(14);
  if (!fok) {
    u32x4 ml;
#pragma nounroll
    for (int spin = 0; spin < SPIN_MAX; ++spin) {
      CFENCE();
      ml = ld16<AUX_SC1>(wsr, mlo, 0);
      if (all_ok((ml[1] == tag) & (ml[3] == tag))) break;
    }
    merge_factor(ml);
  }
  STAMP(15);
  LDS_BARRIER();
  STAMP(3);
  {
    u32 pw = 0;
    if (wave == 0) {
      f32x2 t[8];
#pragma unroll
      for (int w = 0; w < 8; ++w) t[w] = *reinterpret_cast<const f32x2*>(&sm.red[w][2 * lane]);
      f32x2 h[4];
#pragma unroll
      for (int w = 0; w < 4; ++w) h[w] = __builtin_elementwise_fma(t[2 * w + 1], f32x2{fmw[2 * w + 1], fmw[2 * w + 1]}, t[2 * w] * fmw[2 * w]);
      const f32x2 o = (h[0] + h[1]) + (h[2] + h[3]);
      pw = min(pack_bf(o[0], o[1]), SENT - 1u);
    }
    __builtin_amdgcn_raw_buffer_store_b32(pw, wsr, pso, 0, AUX_SC1);
    st8_sc1(wsr, nto, (u32)wall_clock64() + nadd, tag);
    SB();
#pragma unroll
    for (int i = NCMIN / 8; i < NL; ++i) PIN(vr[i]);
    PIN(fl2[0]); PIN(fl2[1]); PIN(fl2[2]);
  }
  STAMP(4);
  SB();
  if (late) {
    asm volatile(".p2align 6\n\ts_barrier" ::: "memory");
#pragma nounroll
    while (wleft > 0) piece();
    if (is_up) {
      asm volatile("s_mov_b32 m0, %5\n\ts_nop 0\n\tbuffer_load_dwordx4 %0, %4, 0 offen nt lds\n\ts_add_u32 m0, m0, 0x400\n\ts_nop 0\n\tbuffer_load_dwordx4 %1, %4, 0 offen nt lds\n\t"
                   "s_add_u32 m0, m0, 0x400\n\ts_nop 0\n\tbuffer_load_dwordx4 %2, %4, 0 offen nt lds\n\ts_add_u32 m0, m0, 0x400\n\ts_nop 0\n\tbuffer_load_dwordx4 %3, %4, 0 offen nt lds"
                   :: "v"(v2[0]), "v"(v2[1]), "v"(v2[2]), "v"(v2[3]), "s"(r2), "s"(l2) : "memory", "scc", "m0");
    }
  }
  SB();

  if (part == 0) {
    u32 wa2[8];
#pragma unroll
    for (int k = 0; k < 8; ++k) {
      const int r = 32 + g * 8 + ((lane >> 2) & 3) + (k & 1) * 4, ch = (k & 6) + ((lane >> 1) & 1);
      wa2[k] = wimg + wave * 8192 + r * 128 + ((ch ^ (r & 7)) * 16) + (lane & 1) * 8;
      PIN(wa2[k]);
    }
    rsrc_t wr2 = wsr;
    asm volatile("" : "+s"(wr2));
    until(t1);
    STAMP(11);
    CFENCE();
    u32x4 og[2];
    og[0] = ld16<AUX_SC1>(wr2, po, 0);
    SB();
    WAIT_VM(1); CFENCE();
#pragma unroll
    for (int t = 0; t < 4; ++t) {
      const u32x2 lo = tr_read(wa2[2 * t]), hi = tr_read(wa2[2 * t + 1]);
      bf[4 + t] = u32x4{lo[0], lo[1], hi[0], hi[1]};
    }
    SB();
    f32x4 d[4];
    auto wo_mm = [&]() {
      const f32x4 z = {0.f, 0.f, 0.f, 0.f};
#pragma unroll
      for (int t = 0; t < 4; ++t) { d[t] = mfma16(og[0], bf[t], z); og[1][t] = __float_as_uint(dpp<XOR8>(u2f(og[0][t]))); }
      SB();
#pragma unroll
      for (int t = 0; t < 4; ++t) d[t] = mfma16(og[1], bf[4 + t], d[t]);
    };
    auto p_bad = [&]() { return !all_ok(max(max(og[0][0], og[0][1]), max(og[0][2], og[0][3])) != SENT); };
    wo_mm();
    if (__builtin_expect(p_bad(), 0)) {
#pragma nounroll
      for (int spin = 0; spin < SPIN_MAX; ++spin) {
        __builtin_amdgcn_s_sleep(1);
        CFENCE();
        og[0] = ld16<AUX_SC1>(wsr, po, 0);
        if (!p_bad()) break;
      }
      wo_mm();
      SB();
      asm volatile(".Lend_d:");
    }
    STAMP(5); WSTAMP(5);
    {
      SB();
      f32x4 y; f32x2 h[4];
#pragma unroll
      for (int t = 0; t < 4; ++t) { h[t] = d[t].xy + d[t].zw; PIN(h[t]); }
#pragma unroll
      for (int t = 0; t < 4; ++t) { y[t] = h[t][0] + h[t][1]; PIN(y[t]); }
      *LDSP(f32x4, rdst) = y;
    }
    STAMP(16);
    LDS_BARRIER();
    STAMP(17);
    if (wave < 2) {
      f32x2 v[4];
#pragma unroll
      for (int w = 0; w < 4; ++w) v[w] = f32x2{LDSP(float, rsrc)[w * 256], LDSP(float, rsrc)[w * 256 + 128]};
      const f32x2 h = (v[0] + v[1]) + (v[2] + v[3]);
      const float y = h[0] + h[1];
      __builtin_amdgcn_raw_buffer_store_b32(min(pack_bf(dpp<0xA0>(y), dpp<0xF5>(y)), SENT - 1u), wr2, xo, 0, AUX_SC1);
      asm volatile(".Lend_a:");
    }
    STAMP(6);
    FINISH();
  }
  if (part == 3) {
    if (!has_w) FINISH();
    u32x4 b2[4];
    const u32 tdst = lds_addr(&sm.hb[hpos ^ ((wave & 1) * 16)]);
    const u32 sdst = lds_addr(&sm.stat[wave][lane]);
    const u32 ssrc = lds_addr(&sm.stat[(g >> 1) * 4 + (c >> 2)][(g & 1) * 32 + (c & 3) * 8]);
    const u32 adst = lds_addr((c < 4 && (g & 1) == 0) ? &sm.actl[g >> 1][wave * 4 + c] : reinterpret_cast<u16*>(&sm.dummy[lane]));
    const u32 asrc = lds_addr(&sm.actl[lane & 1][g * 8]);
    const u32 apa = lds_addr(&sm.hb[(c >> 3) * 512 + (c & 3) * 64 + ((c >> 2) & 1) * 32 + (g ^ (c & 2)) * 8]);
    u32 xoff = dup(G_XP) + near + g * 2048 + ub * 1024 + (wave & 3) * 256 + c * 16;
    u32x4 one;
#pragma unroll
    for (int k = 0; k < 4; ++k) one[k] = ((c & 2) == 0 && (c >> 2) == k) ? ((c & 1) ? 0x3f800000u : 0x00003f80u) : 0u;
    PIN(one);
    u32 ooff = dup(G_OP + (blk * 512 + tid) * 8) + ((nmask >> (g & 1)) & 8192u);
    PIN(xoff); PIN(ooff);
    PIN(ga); PIN(xin); PIN(cc1);
    u32 w2a = (u32)(uintptr_t)(&sm.w2b[0][0]) + (g * 8 + ((lane >> 2) & 3)) * 1024 + (wave ^ (g & 1)) * 128 + (lane & 15) * 8;
    u32 w2t[4];
#pragma unroll
    for (int t = 0; t < 4; ++t) { w2t[t] = w2a ^ (t * 32); PIN(w2t[t]); }
    static_assert(UD == 1, "one late fragment");
    u32 ufa = up_addr() + 7 * 4096;
    const u32 a7a = lds_addr(ap + 128 + ((lane >> 1) & 1) * 128 + 7 * 8);
    PIN(ufa);
    u64 gm1 = __builtin_amdgcn_ballot_w64(g == 1), gm2 = __builtin_amdgcn_ballot_w64(g == 2), gm3 = __builtin_amdgcn_ballot_w64(g == 3);
    asm volatile("" : "+s"(gm1), "+s"(gm2), "+s"(gm3));
    const bool g1 = __builtin_amdgcn_inverse_ballot_w64(gm1), g2 = __builtin_amdgcn_inverse_ballot_w64(gm2), g3 = __builtin_amdgcn_inverse_ballot_w64(gm3);
    if (wave == 1) {
      due = t1 + P.N1 * 256; gate();
      u32x4 gn[2] = {ld16<AUX_SC1>(wsr, G_NT + lane * 16, 0), ld16<AUX_SC1>(wsr, G_NT + lane * 16, 1024)};
      const int tn = notices(IC<2>{}, G_NT + lane * 16, gn) * 256;
      due = (tn > t1 + P.TH * 256 ? max(tn, t1 + P.L0 * 256) + P.L1 * 256 : t1) + P.X2 * 256 + slack;
      until(due);
    }
    asm volatile("s_waitcnt vmcnt(0)" ::: "memory");
    STAMP(9);
    __builtin_amdgcn_s_barrier();
    STAMP(11);
    CFENCE();
    u32x4 xg[2] = {ld16<AUX_SC1>(wsr, xoff, 0), ld16<AUX_SC1>(wsr, xoff, 16384)};
    SB();
    {
      const u32x4 a7 = *LDSP(u32x4, a7a);
      const u32x2 l7 = tr_read(ufa), h7 = tr_read(ufa + 256);
      bf[7] = u32x4{l7[0], l7[1], h7[0], h7[1]};
#pragma unroll
      for (int t = 0; t < 4; ++t) {
        const u32x2 lo = tr_read(w2t[t]), hi = tr_read(w2t[t] + 4096);
        b2[t] = u32x4{lo[0], lo[1], hi[0], hi[1]};
      }
      SB();
      const f32x4 d0 = mfma16(a7, bf[7], f32x4{0.f, 0.f, 0.f, 0.f});
      Af += par ? d0[1] : d0[0]; Bf += par ? d0[3] : d0[2];
      Af += dpp<XOR8>(Af); Bf += dpp<XOR8>(Bf);
    }
    PIN(Af); PIN(Bf);
#pragma unroll
    for (int t = 0; t < 4; ++t) PIN(b2[t]);
    SB();
    const f32x4 z4 = {0.f, 0.f, 0.f, 0.f};
    auto xp_bad = [&]() { return !all_ok(max(max(max(xg[0][0], xg[0][1]), max(xg[0][2], xg[0][3])), max(max(xg[1][0], xg[1][1]), max(xg[1][2], xg[1][3]))) != SENT); };
    f32x4 v = mfma16(one, xg[1], mfma16(one, xg[0], z4));
    if (__builtin_expect(xp_bad(), 0)) {
#pragma nounroll
      for (int spin = 0; spin < SPIN_MAX; ++spin) {
        __builtin_amdgcn_s_sleep(1);
        CFENCE();
        xg[0] = ld16<AUX_SC1>(wsr, xoff, 0); xg[1] = ld16<AUX_SC1>(wsr, xoff, 16384);
        if (!xp_bad()) break;
      }
      v = mfma16(one, xg[1], mfma16(one, xg[0], z4));
      SB();
      asm volatile(".Lend_e:");
    }
    STAMP(5); WSTAMP(5);
    {
      const float a0 = v[0], a1 = v[1];
      const float x10 = fmaf(ga[0], a0, xin[0]), x11 = fmaf(ga[1], a1, xin[1]);
      *LDSP(u32, tdst) = pack_bf(x10 * cc1[0], x11 * cc1[1]);
      float u = swap32_add(x10 + x11, fmaf(x10, x10, x11 * x11));
      u += dpp<XOR1>(u); u += dpp<XOR2>(u); u += dpp<XOR7>(u);
      *LDSP(float, sdst) = u;
    }
    STAMP(16);
    LDS_BARRIER();
    STAMP(17);
    {
      const f32x4 z = {0.f, 0.f, 0.f, 0.f};
      const u32x4 af0 = LDSP(u32x4, apa)[0], af1 = LDSP(u32x4, apa)[32];
      float s = LDSP(float, ssrc)[0];
      SB();
      f32x4 d[4];
      d[0] = mfma16(af0, bf[0], z); d[1] = mfma16(af0, bf[1], z);
      SB();
      s += dpp<XOR1>(s); s += dpp<XOR2>(s);
      PIN(s);
      d[2] = mfma16(af0, bf[2], z);
      SB();
      s += dpp<XOR7>(s); s += dpp<XOR8>(s);
      PIN(s);
      d[3] = mfma16(af0, bf[3], z);
      SB();
      const auto sw2 = __builtin_amdgcn_permlane16_swap(__float_as_uint(s), __float_as_uint(s), false, false);
      float mean = u2f(sw2[0]) * (1.f / 512.f), qm = fmaf(u2f(sw2[1]), 1.f / 512.f, 1e-5f);
      PIN(mean); PIN(qm);
      d[0] = mfma16(af1, bf[4], d[0]);
      SB();
      float ve = fmaxf(fmaf(-mean, mean, qm), 1e-5f), ma = mean * Af;
      PIN(ve); PIN(ma);
      d[1] = mfma16(af1, bf[5], d[1]);
      SB();
      float ri = __builtin_amdgcn_rsqf(ve);
      PIN(ri);
      d[2] = mfma16(af1, bf[6], d[2]);
      d[3] = mfma16(af1, bf[7], d[3]);
      SB();
      float cf = fmaf(-ri, ma, Bf), rl = ri * -1.4426950408889634f, cl = cf * -1.4426950408889634f;
      PIN(cf); PIN(rl); PIN(cl);
      PIN(d[0]); PIN(d[1]);
      float y0 = d[0][0] + d[1][1];
      PIN(d[2]);
      y0 += d[2][2];
      PIN(y0); PIN(d[3]);
      const float y1 = d[3][3];
      float y = y0 + y1;
      y = __builtin_bit_cast(float, __builtin_amdgcn_update_dpp(__builtin_bit_cast(int, y), __builtin_bit_cast(int, y), XOR8, 0xa, 0xf, false));
      y = swap16_add(y, y);
      const float e = __builtin_amdgcn_exp2f(fmaf(y, rl, cl));
      y = fmaf(y, ri, cf);
      float gu = y * dpp<0x104>(y);
      PIN(gu);
      *LDSP(u16, adst) = (u16)pack_bf(gu * __builtin_amdgcn_rcpf(1.f + e), 0.f);
    }
    STAMP(18);
    LDS_BARRIER();
    STAMP(10);
    {
      const u32x4 af = *LDSP(u32x4, asrc);
      const f32x4 z = {0.f, 0.f, 0.f, 0.f};
      f32x4 d[4];
#pragma unroll
      for (int t = 0; t < 4; ++t) d[t] = mfma16(af, b2[t], z);
      SB();
      PIN(d[0]); PIN(d[1]);
      float e0 = g1 ? d[1][0] : d[0][0], e1 = g1 ? d[1][1] : d[0][1];
      PIN(d[2]);
      e0 = g2 ? d[2][0] : e0; e1 = g2 ? d[2][1] : e1;
      PIN(e0); PIN(e1); PIN(d[3]);
      st8_sc1(wsr, ooff, pack_bf(g3 ? d[3][0] : e0, g3 ? d[3][1] : e1), tag);
    }
    asm volatile(".Lend_b:");
    STAMP(6);
    FINISH();
  }
  if (fin) {
    const int d = fd, ul = lane >> 3;
    u32x4 gm[2] = {sg[0], sg[1]};
    if (!all_ok((sg[0][1] == tag) & (sg[0][3] == tag) & (sg[1][1] == tag) & (sg[1][3] == tag))) gather16<2>(wsr, G_MOD + (1024 + d) * 8, 1536 * 8, tag, gm);
    u32 xa0, xa1; int tnf;
    {
      due = t1 + P.N2 * 256; gate();
      u32x4 gn[2] = {ld16<AUX_SC1>(wsr, G_NT + lane * 16, 0), ld16<AUX_SC1>(wsr, G_NT + lane * 16, 1024)};
      const int tn = notices(IC<2>{}, G_NT + lane * 16, gn) * 256;
      due = (tn > t1 + P.TH * 256 ? max(tn, t1 + P.L0 * 256) + P.L1 * 256 : t1) + P.X3 * 256 + slack;
      tnf = tn;
      until(due);
    }
#pragma nounroll
    for (int spin = 0; spin < SPIN_MAX; ++spin) {
      CFENCE();
      xa0 = __builtin_amdgcn_raw_buffer_load_b32(wsr, dup(G_XP + ul * 2048) + near + d * 2, 0, AUX_SC1);
      xa1 = __builtin_amdgcn_raw_buffer_load_b32(wsr, dup(G_XP + ul * 2048) + near + d * 2, 1024, AUX_SC1);
      if (all_ok(max(xa0, xa1) != SENT)) break;
      __builtin_amdgcn_s_sleep(1);
    }
    float a0 = swap16_add(lo_f(xa0), lo_f(xa1)), a1 = swap16_add(hi_f(xa0), hi_f(xa1));
    a0 += dpp<XOR8>(a0); a1 += dpp<XOR8>(a1);
    a0 = swap32_add(a0, a0); a1 = swap32_add(a1, a1);
    const bool hb = g & 1;
    f32x2 xv = {rbf(lo_f(sx2) + rbf((hb ? hi_f(gm[0][0]) : lo_f(gm[0][0])) * rbf(a0))), rbf(hi_f(sx2) + rbf((hb ? hi_f(gm[0][2]) : lo_f(gm[0][2])) * rbf(a1)))};
    f32x2 gv = {hb ? hi_f(gm[1][0]) : lo_f(gm[1][0]), hb ? hi_f(gm[1][2]) : lo_f(gm[1][2])};
    PIN(xv); PIN(gv);
    if (fw == 0 && lane == 0) __builtin_amdgcn_raw_buffer_store_b32(tag, wsr, WS_EPOCH, 0, AUX_SC1);
    STAMP(10);
    due = max(due + P.X4 * 256 + slack, (int)(((u32)wall_clock64() - t0) << 8) + P.GX * 256);
    const bool last = ul + 40 < NUP;
    u32 o0 = dup(G_OP + (ul * 512 + d) * 8) + near, o5 = last ? o0 : OOB, tg5 = last ? tag : 0u;
    const rsrc_t rout = mk_rsrc(out, 2048u);
    u32 oo = (lane & 40) == 0 ? (g * 512 + d) * 2 : OOB;
    const u32 pk = (c & 10) ? 0u : ((c & 4) ? 0x3f800000u : 0x00003f80u);
    u32x4 one = {(c & 1) ? 0u : pk, 0u, (c & 1) ? pk : 0u, 0u};
    PIN(o0); PIN(o5); PIN(tg5); PIN(oo); PIN(one);
    u32 so[6];
#pragma unroll
    for (int k = 0; k < 6; ++k) { so[k] = k * 65536; asm volatile("" : "+s"(so[k])); }
    __builtin_amdgcn_raw_buffer_store_b32(pack_bf(xv[0], xv[1]), rout, oo, 0, AUX_SC1);
    until(due);
#pragma nounroll
    for (int spin = 0; spin < SPIN_MAX; ++spin) {
      CFENCE();
      u32x4 og[6];
      og[0] = ld16<AUX_SC1>(wsr, o5, so[5]);
#pragma unroll
      for (int k = 1; k < 6; ++k) { SB(); og[k] = ld16<AUX_SC1>(wsr, o0, so[k - 1]); }
      SB();
      f32x4 sv = {0.f, 0.f, 0.f, 0.f};
#pragma unroll
      for (int k = 0; k < 6; ++k) sv = mfma16(one, og[k], sv);
      const float s0 = sv[0] + dpp<XOR8>(sv[0]), s1 = sv[1] + dpp<XOR8>(sv[1]);
      __builtin_amdgcn_raw_buffer_store_b32(pack_bf(fmaf(gv[0], s0, xv[0]), fmaf(gv[1], s1, xv[1])), rout, oo, 0, AUX_SC1);
      SB();
      u32 bad = (og[0][1] ^ tg5) | (og[0][3] ^ tg5);
#pragma unroll
      for (int k = 1; k < 6; ++k) { bad |= (og[k][1] ^ tag) | (og[k][3] ^ tag); PIN(bad); }
      if (all_ok(bad == 0u)) break;
      __builtin_amdgcn_s_sleep(1);
    }
    STAMP(7);
    asm volatile(".Lend_c:");
    until(t1 - stv / 2 + (3000 + (stv / 1280) * 1000 + 450) * 256 + 10 * min(max(tnf - t1, -45 * 256), 50 * 256));
  }
  FINISH();
}

extern "C" void kernel_launch(void* const* d_in, const int* in_sizes, int n_in,
                              void* d_out, int out_size, void* d_ws, size_t ws_size,
                              hipStream_t stream) {
  fused_kernel<<<256, 512, 0, stream>>>((const u16*)d_in[0], (const u16*)d_in[1], (const u16*)d_in[2], (const u16*)d_in[3], (const u16*)d_in[4],
                                        (const u16*)d_in[6], (const u16*)d_in[7], (const u16*)d_in[8], (const u16*)d_in[9], (const u16*)d_in[10],
                                        (unsigned char*)d_ws, (u16*)d_out
    );
}
```

```cpp
#include <hip/hip_runtime.h>
#include <stdint.h>

typedef unsigned int u32;
typedef unsigned short u16;
typedef unsigned long long u64;
typedef __attribute__((ext_vector_type(4))) unsigned u32x4;
typedef __attribute__((ext_vector_type(2))) unsigned u32x2;
typedef __attribute__((ext_vector_type(2))) __bf16 bf16x2;
typedef __attribute__((ext_vector_type(4))) float f32x4;
typedef __attribute__((ext_vector_type(2))) float f32x2;
typedef __amdgpu_buffer_rsrc_t rsrc_t;

constexpr int NH = 32, SK = 16384, DH = 128, WIN = 4096, FF = 1376;
struct Prm { int E0, ES, E1, E2, E2C, EU, TEND, DT_UP, L0, L1, X1, X2, X3, X4, NE, FE, M0, M1, M2, M3, M4, GS, GX, N1, N2, BT, WD, DT_WO, LW, LU, EW, PM, WM, FK, FW, FU, EX, TS, DS, A0, AG, AC, PT, SL, TH, DT_0, L3; };
constexpr Prm PRM0 = {32, 24, 32, 32, 48, 64, 2001, 96, 20, 14, 72, 78, 98, 86, 5, 12, 12, 5, 12, 32, 28, 34, 34, 18, 45, 10, 4, 103, 40, 40, 8, 0x6827941, 0x53, 62, 61, 60, 8, 500, 122, 55, 26, 120, 160, 10, 6, 25, 15};
#define PRM_ARG
#define TABP (&TAB.e[0])
constexpr int NL = 37;
constexpr int NCMIN = 192;
constexpr int WK = 6, WV = 5;
constexpr int WUSE = 13, LNS = 11;
constexpr int QP1 = 12, QP2 = 18, QD = 5;
constexpr int VQ = 6;
constexpr int SLC[] = {0, 6, 9, 15, 21, 27, 33, 40}, NSL = sizeof(SLC) / sizeof(int) - 1;
constexpr int VS = VQ + NSL;
constexpr int MLS = VS + 2;
constexpr int VSIDE = VQ + 1, UPREP = 21;
constexpr int SIDE = NCMIN / 8 - 4;
constexpr int UD = 1;
constexpr int CPF = 18;
constexpr int TBP = 7, TBL = 11, TBU = 16;
constexpr int AR = 0, AL = 10;
constexpr int VL = 0, VU = 5;
constexpr int D0 = 30;
constexpr int KF = 4, KF0 = 8;
constexpr int side_k(int i) { return (i == KF || i == KF + 1) ? 3 : (i == TBL ? 1 : 0) + ((i == QP1 || i == QP2) ? 2 : 0); }
constexpr int side_v(int v) { return v == VQ - 4 ? 2 : (v == VSIDE ? 4 : ((v == AL || v == VL) ? 1 : 0)); }
constexpr int win_k(int i) { int n = WK; for (int j = i - WK; j <= i; ++j) n += side_k(j); return n; }
constexpr int win_v(int v, int w) { int n = w; for (int j = v - w; j <= v; ++j) n += side_v(j); return n; }
constexpr int cons_end(int v) {
  const int a = v - WV + 1, c = (v - VS) * 3;
  const int m = a < c ? a : c;
  return m < 0 ? 0 : (m > NL ? NL : m);
}
struct Tab { u32 e[1024]; };
#define popc __builtin_popcount
__host__ __device__ constexpr u32 place(u32 v, u32 m, int n) {
  u32 r = 0;
  for (int k = 0; k < n; ++k) r |= (v >> k & 1u) << (10 + (m >> (4 * k) & 15));
  return r;
}
__host__ __device__ constexpr u32 step_off(const Prm& P, u32 s0, u32 bh) {
  const u32 src = (s0 >> 5 & 3u) | bh << 2;
  return place(s0 ^ ((popc(src & P.M0) & 1) | (popc(src & P.M1) & 1) << 1 | (popc(src & P.M2) & 1) << 2 | (popc(src & P.M3) & 1) << 3 | (popc(src & P.M4) & 1) << 4), P.PM, 7);
}
constexpr Tab make_tab(const Prm& P) {
  Tab T{};
  for (int bid = 0; bid < 256; ++bid) {
    const int xcd = bid & 7, idx = bid >> 3, part = idx & 3, i8 = idx >> 2, wi = i8 * 8 + xcd;
    const bool has_mod = wi < 48, has_up = wi >= 21;
    const int cb0 = (i8 + 5) & 7, cb1 = (i8 + 4) & 7, ms = wi - 20;
    const int mi = wi < 8 ? wi : (wi < 16 ? 8 + ((xcd + 4) & 7) : (wi < 20 ? (wi - 14) * 8 + 3 : 16 + ms / 7 * 8 + ms % 7 + (ms % 7 >= 3 ? 1 : 0)));
    const int ui = xcd * 5 + (xcd > 5 ? xcd - 5 : 0) + (i8 >= 3 ? (xcd >= 5 ? 1 : 0) + i8 - 3 : 0);
    const int wd = cb0 == 3 ? 0 : P.WD;
    const int e[4] = {P.E0 - 4 * wd + (cb0 == 3 ? P.ES : 0), P.E1 + (cb1 == 3 ? P.ES : 0), has_mod ? (wi < 16 ? P.E2C : P.E2) + ((mi & 7) == 3 ? P.ES : 0) : 0, has_up ? P.EU * (8 - UD) / 8 : 0};
    const int ew = wd ? P.EW : 0, pa = (1024 + e[0] + ew + e[1] + e[2] + e[3] + (has_up ? P.EX : 0)) / 4;
    int c[5] = {0, 0, 0, 0, 128};
    for (int k = 0; k < 3; ++k) c[k + 1] = c[k] + (pa - e[k] - (k == 0 ? ew : 0) + 4) / 8;
    const int nl = c[part + 1] - c[part], wst = e[part] / 4;
    const bool has_w = part < 2 || (part == 2 ? has_mod : has_up);
    const int blk = !has_w ? 0 : (part == 0 ? cb0 : (part == 1 ? xcd * 8 + cb1 : (part == 2 ? mi : ui)));
    const int span = (part == 3 && has_up ? P.TEND - P.DT_UP : (part == 0 ? P.TEND - (wd ? P.DT_WO : P.DT_0) : P.TEND)) * 256;
    const int np = part == 0 ? 8 - wd : (part == 3 ? 8 - UD : 8), tau0 = span / (2 * nl + wst);
    const bool up = part == 3 && has_up;
    const int tau = tau0 * (up ? P.FU : (has_w && np == 8 ? P.FK : 64)) / 64, tauw = tau0 * wst / np * (up ? P.FU : P.FW) / 64;
    int sw = KF, at = P.DS * 256 + (has_w ? np * tauw : 0);
    while (sw < NCMIN / 8 - 1 && at < P.TS * 256) { at += tau; ++sw; }
    const int tau2 = (span - at) / (2 * nl - sw);
    T.e[4 * bid] = (u32)(c[part] | nl << 7 | blk << 13 | (has_w ? 1 : 0) << 19 | (8 - np) << 20 | sw << 24);
    T.e[4 * bid + 1] = (u32)(tau | tauw << 16);
    if ((part == 1 && nl <= QP2 + QD + 1) || c[part] > 32 * part + KF0 || c[part + 1] < 32 * part + KF0 + 4) T.e[4 * bid] = 0;
    const u32 pitch = part == 0 ? 1024u : (part == 1 ? 8192u : (part == 2 ? 6144u : 2752u));
    T.e[4 * bid + 2] = (part == 0 ? xcd * 512u * pitch : 0u) + blk * (part == 3 ? 64u : 128u);
    T.e[4 * bid + 3] = pitch | (u32)tau2 << 16;
  }
  return T;
}
constexpr int step_range(const Prm& P, bool mx_) {
  int mx = 0, mn = 1 << 20;
  const Tab T = make_tab(P);
  for (int bid = 0; bid < 256; ++bid) { const int n = T.e[4 * bid] >> 7 & 63; mx = n > mx ? n : mx; mn = n < mn ? n : mn; }
  return mx_ ? mx : mn;
}
__constant__ __attribute__((aligned(64))) const Tab TAB = make_tab(PRM0);
static_assert(step_range(PRM0, true) <= NL && step_range(PRM0, false) >= NCMIN / 8, "a wave's key range does not fit its load steps");
static_assert(cons_end(SIDE + WV + 1) <= NCMIN / 8 && cons_end(UPREP) <= NCMIN / 8 && SIDE > MLS + 3, "steps every wave runs");
constexpr int NUP = FF / 32;
constexpr int SPIN_MAX = 1 << 17;

constexpr u32 WS_EPOCH = 0;
constexpr u32 G_TB = 1024;
constexpr u32 G_NT = 2048;
constexpr u32 G_MOD = 4096;
constexpr u32 G_Q = G_MOD + 3072 * 8;
constexpr u32 G_AG = G_Q + 4096 * 8;
constexpr u32 G_ML = G_AG + 4096;
constexpr u32 G_P = 81920;
constexpr u32 G_U = G_P + 65536;
__device__ __forceinline__ u32 dup(u32 o) { return G_U + ((o >> 13) << 14) + (o & 8191u); }
constexpr u32 G_XP = 0;
constexpr u32 G_OP = G_XP + 8 * 512 * 4;
constexpr u32 G_PB = G_U + 2 * (G_OP + 44 * 512 * 8);
constexpr u32 WS_END = G_PB + 32768;
static_assert(G_P % 16384 == 0 && G_PB % 16384 == 0 && G_ML + 4096 <= G_P, "layout");
constexpr u32 SENT = 0xffffffffu;
constexpr u32 OOB = 0x40000000u;
#define WST 0

constexpr int AUX_NT = 2, AUX_SC1 = 16;

__device__ __forceinline__ float lo_f(u32 v) { return __uint_as_float(v << 16); }
__device__ __forceinline__ float hi_f(u32 v) { return __uint_as_float(v & 0xffff0000u); }
__device__ __forceinline__ float rbf(float x) { return (float)(__bf16)x; }
__device__ __forceinline__ u32 pack_bf(float lo, float hi) {
  bf16x2 r; r[0] = (__bf16)lo; r[1] = (__bf16)hi; return __builtin_bit_cast(u32, r);
}
__device__ __forceinline__ float dot2(u32 k, u32 q, float acc) {
  return __builtin_amdgcn_fdot2_f32_bf16(__builtin_bit_cast(bf16x2, k), __builtin_bit_cast(bf16x2, q), acc, false);
}
template <int CTRL> __device__ __forceinline__ float dpp(float x) {
  return __builtin_bit_cast(float, __builtin_amdgcn_mov_dpp(__builtin_bit_cast(int, x), CTRL, 0xf, 0xf, true));
}
constexpr int XOR1 = 0xB1, XOR2 = 0x4E, XOR7 = 0x141, XOR8 = 0x128;
__device__ __forceinline__ float swap32_add(float a, float b) {
  auto t = __builtin_amdgcn_permlane32_swap(__float_as_uint(a), __float_as_uint(b), false, false);
  return __uint_as_float(t[0]) + __uint_as_float(t[1]);
}
__device__ __forceinline__ float swap16_add(float a, float b) {
  auto t = __builtin_amdgcn_permlane16_swap(__float_as_uint(a), __float_as_uint(b), false, false);
  return __uint_as_float(t[0]) + __uint_as_float(t[1]);
}
__device__ __forceinline__ float wave_sum(float x) {
  x += dpp<XOR1>(x); x += dpp<XOR2>(x); x += dpp<XOR7>(x); x += dpp<XOR8>(x);
  x = swap16_add(x, x);
  return swap32_add(x, x);
}
__device__ __forceinline__ float wave_max(float x) {
  x = fmaxf(x, dpp<XOR1>(x)); x = fmaxf(x, dpp<XOR2>(x)); x = fmaxf(x, dpp<XOR7>(x)); x = fmaxf(x, dpp<XOR8>(x));
  auto s = __builtin_amdgcn_permlane16_swap(__float_as_uint(x), __float_as_uint(x), false, false);
  x = fmaxf(__uint_as_float(s[0]), __uint_as_float(s[1]));
  auto t = __builtin_amdgcn_permlane32_swap(__float_as_uint(x), __float_as_uint(x), false, false);
  return fmaxf(__uint_as_float(t[0]), __uint_as_float(t[1]));
}
template <int CTRL> __device__ __forceinline__ float sel_add(float lo, float hi, bool up) {
  const float t1 = lo + dpp<CTRL>(lo), t2 = hi + dpp<CTRL>(hi);
  return up ? t2 : t1;
}
template <int I> __device__ __forceinline__ float bc16(float x) {
  return __builtin_bit_cast(float, __builtin_amdgcn_update_dpp(0, __builtin_bit_cast(int, x), 0x150 + I, 0xf, 0xf, false));
}
__device__ __forceinline__ float bcast16(float x, int i) {
  switch (i) {
    case 0: return bc16<0>(x); case 1: return bc16<1>(x); case 2: return bc16<2>(x); case 3: return bc16<3>(x);
    case 4: return bc16<4>(x); case 5: return bc16<5>(x); case 6: return bc16<6>(x); case 7: return bc16<7>(x);
    case 8: return bc16<8>(x); case 9: return bc16<9>(x); case 10: return bc16<10>(x); case 11: return bc16<11>(x);
    case 12: return bc16<12>(x); case 13: return bc16<13>(x); case 14: return bc16<14>(x); default: return bc16<15>(x);
  }
}
__device__ __forceinline__ float silu_f(float x) { return x * __builtin_amdgcn_rcpf(1.f + __expf(-x)); }

template <int AUX> __device__ __forceinline__ u32x4 ld16(rsrc_t r, u32 voff, u32 soff) {
  return __builtin_amdgcn_raw_buffer_load_b128(r, voff, soff, AUX);
}
template <int AUX> __device__ __forceinline__ u32x2 ld8(rsrc_t r, u32 voff, u32 soff) {
  return __builtin_amdgcn_raw_buffer_load_b64(r, voff, soff, AUX);
}
__device__ __forceinline__ void st8_sc1(rsrc_t r, u32 voff, u32 v, u32 tag) {
  __builtin_amdgcn_raw_buffer_store_b64(u32x2{v, tag}, r, voff, 0, AUX_SC1);
}
__device__ __forceinline__ void st16_sc1(rsrc_t r, u32 voff, u32 v0, u32 v1, u32 tag) {
  __builtin_amdgcn_raw_buffer_store_b128(u32x4{v0, tag, v1, tag}, r, voff, 0, AUX_SC1);
}
__device__ __forceinline__ rsrc_t mk_rsrc(const void* p, u32 bytes) {
  return __builtin_amdgcn_make_buffer_rsrc(const_cast<void*>(p), 0, bytes, 0x00020000);
}
#define WAIT_VM(n) __builtin_amdgcn_s_waitcnt(((n) & 15) | (((n) >> 4) << 14) | (7 << 4) | (15 << 8))
#define SB() __builtin_amdgcn_sched_barrier(0)
#define CFENCE() asm volatile("" ::: "memory")
__device__ __forceinline__ bool all_ok(bool ok) { return __builtin_amdgcn_ballot_w64(!ok) == 0ull; }

template <int N> __device__ __forceinline__ void gather16(rsrc_t ws, u32 voff, u32 stride, u32 tag, u32x4 (&g)[N]) {
#pragma nounroll
  for (int spin = 0; spin < SPIN_MAX; ++spin) {
    CFENCE();
    bool ok = true;
#pragma unroll
    for (int i = 0; i < N; ++i) g[i] = ld16<AUX_SC1>(ws, voff + i * stride, 0);
#pragma unroll
    for (int i = 0; i < N; ++i) ok &= (g[i][1] == tag) & (g[i][3] == tag);
    if (all_ok(ok)) break;
    __builtin_amdgcn_s_sleep(8);
  }
}
template <int N> __device__ __forceinline__ void gather8(rsrc_t ws, u32 voff, u32 stride, u32 tag, u32 (&g)[N], bool act = true) {
#pragma nounroll
  for (int spin = 0; spin < SPIN_MAX; ++spin) {
    CFENCE();
    bool ok = true;
    u32x2 t[N];
#pragma unroll
    for (int i = 0; i < N; ++i) t[i] = ld8<AUX_SC1>(ws, voff + i * stride, 0);
#pragma unroll
    for (int i = 0; i < N; ++i) { ok &= (t[i][1] == tag); g[i] = t[i][0]; }
    if (all_ok(ok | !act)) break;
    __builtin_amdgcn_s_sleep(8);
  }
}

struct Smem {
  float red[8][128];
  float ml[2][8];
  float r2[64 * 20];
  u16 hb[3 * 1024];
  float stat[8][64];
  float red2[8][128];
  u32 dummy[64];
  u32x2 bc;
  u32x2 ctl;
  u32 nm, pad_[3];
  u32x4 zero[4];
  u32x4 wbuf[8][8][64];
  __attribute__((aligned(256))) u16 w2b[32][512];
  u16 actl[2][32];
};

#define STAMP(k) do {} while (0)
#define WSTAMP(k) do {} while (0)
#define FINISH() return
#define PIN(x) asm volatile("" : "+v"(x))
#define LDS_BARRIER() do { __builtin_amdgcn_s_waitcnt(0xc07f); __builtin_amdgcn_s_barrier(); CFENCE(); } while (0)
#define WAVE_SYNC() do { __builtin_amdgcn_fence(__ATOMIC_RELEASE, "wavefront"); __builtin_amdgcn_wave_barrier(); __builtin_amdgcn_fence(__ATOMIC_ACQUIRE, "wavefront"); } while (0)

__device__ __forceinline__ void dma16(rsrc_t r, u32 voff, u32 soff, u32 lds_dst) {
  u32 keep;
  asm volatile("s_mov_b32 %0, m0\n\ts_mov_b32 m0, %4\n\ts_nop 4\n\tbuffer_load_dwordx4 %1, %2, %3 offen nt lds\n\ts_mov_b32 m0, %0"
               : "=&s"(keep) : "v"(voff), "s"(r), "s"(soff), "s"(lds_dst) : "memory");
}
__device__ __forceinline__ u32 perm_lo(u32 a, u32 b) { return __builtin_amdgcn_perm(b, a, 0x05040100u); }
__device__ __forceinline__ u32 perm_hi(u32 a, u32 b) { return __builtin_amdgcn_perm(b, a, 0x07060302u); }
__device__ __forceinline__ float rdl(float x, int l) { return __int_as_float(__builtin_amdgcn_readlane(__float_as_int(x), l)); }

typedef __attribute__((ext_vector_type(8))) __bf16 bf16x8;
__device__ __forceinline__ f32x4 mfma16(u32x4 a, u32x4 b, f32x4 c) {
  return __builtin_amdgcn_mfma_f32_16x16x32_bf16(__builtin_bit_cast(bf16x8, a), __builtin_bit_cast(bf16x8, b), c, 0, 0, 0);
}
__device__ __forceinline__ float sum8(const float* p) {
  const f32x4 a = *reinterpret_cast<const f32x4*>(p), b = *reinterpret_cast<const f32x4*>(p + 4);
  return ((a[0] + b[0]) + (a[1] + b[1])) + ((a[2] + b[2]) + (a[3] + b[3]));
}
__device__ __forceinline__ float ln_ri(float var) {
  return __builtin_amdgcn_rcpf(rbf(__builtin_amdgcn_sqrtf(rbf(rbf(fmaxf(var, 0.f)) + 1e-5f))));
}
__device__ __forceinline__ void ln_stats2(const float (&x0)[8], const float (&x1)[8], float& mb0, float& ri0, float& mb1, float& ri1) {
  const float k0 = rdl(x0[0], 0), k1 = rdl(x1[0], 0);
  float s0 = 0.f, s1 = 0.f, q0 = 0.f, q1 = 0.f;
#pragma unroll
  for (int k = 0; k < 8; ++k) {
    const float d0 = x0[k] - k0, d1 = x1[k] - k1;
    s0 += d0; s1 += d1; q0 = fmaf(d0, d0, q0); q1 = fmaf(d1, d1, q1);
  }
  float t = swap16_add(swap32_add(s0, s1), swap32_add(q0, q1));
  t += dpp<XOR1>(t); t += dpp<XOR2>(t); t += dpp<XOR7>(t); t += dpp<XOR8>(t);
  auto u = __builtin_amdgcn_permlane16_swap(__float_as_uint(t), __float_as_uint(t), false, false);
  const float ms = __uint_as_float(u[0]) * (1.f / 512.f), mq = __uint_as_float(u[1]) * (1.f / 512.f);
  const float ri = ln_ri(fmaf(-ms, ms, mq));
  mb0 = rbf(k0 + rdl(ms, 0)); mb1 = rbf(k1 + rdl(ms, 32));
  ri0 = rdl(ri, 0); ri1 = rdl(ri, 32);
}
template <int I> struct IC { static constexpr int v = I; };
template <int I, int N, class F> __device__ __forceinline__ void sfor(F&& f) {
  if constexpr (I < N) { if (f(IC<I>{})) sfor<I + 1, N>(f); }
}
typedef __attribute__((ext_vector_type(4))) short s16x4;
typedef __attribute__((address_space(3))) s16x4* lds_s16x4p;
__device__ __forceinline__ u32x2 tr_read(u32 lds_addr) {
  return __builtin_bit_cast(u32x2, __builtin_amdgcn_ds_read_tr16_b64_v4i16((lds_s16x4p)lds_addr));
}
__device__ __forceinline__ u32 next_tag(u32 e) { return ((e | 0x40004000u) + 1u) & ~0x40004000u; }
__device__ __forceinline__ float u2f(u32 v) { return __uint_as_float(v); }
#define LDSP(T, a) ((__attribute__((address_space(3))) T*)(a))
template <class T> __device__ __forceinline__ u32 lds_addr(T* p) { u32 a = (u32)(uintptr_t)p; PIN(a); return a; }

typedef __attribute__((ext_vector_type(16))) unsigned u32x16;
typedef __attribute__((ext_vector_type(8))) unsigned u32x8;
__global__ void __launch_bounds__(512, 2) fused_kernel(const u16* x_, const u16* kc_, const u16* vc_, const u16* wq_, const u16* wo_, const u16* w1_,
                                                       const u16* w3_, const u16* w2_, const u16* cnd_, const u16* wmod_, unsigned char* ws_, u16* out_ PRM_ARG) {
  __shared__ __attribute__((aligned(256))) Smem sm;
  constexpr Prm P = PRM0;
  const int TEND = P.TEND;
  const int bid = blockIdx.x, tid = threadIdx.x;
  u32x4 kr[NL];
  u32x16 ka; u32x8 kb; u32x4 tev; u64 pt0; u32 tch2;
  asm volatile("s_load_dwordx16 %0, %3, 0x0\n\ts_load_dwordx8 %1, %3, 0x40\n\ts_memrealtime %2"
               : "=&s"(ka), "=&s"(kb), "=&s"(pt0) : "s"(__builtin_amdgcn_kernarg_segment_ptr()));
  SB();
  {
    u64 pc; u32 vb;
    asm volatile("s_getpc_b64 %2\n.Lcpf_first:\n\tv_mul_u32_u24 %1, ((.Lend_c-.Lcpf_first)>>10)<<2, %3\n\tglobal_load_dword %0, %1, %2"
                 : "=&v"(tch2), "=&v"(vb), "=&s"(pc) : "v"((bid & ~7) + (tid >> 6)) : "memory");
    asm volatile("global_load_dwordx4 %0, %1, %2" : "=&v"(tev) : "v"(bid << 4), "s"(TABP) : "memory");
  }
  SB();
  const int xcd = bid & 7, idx = bid >> 3, part = idx & 3;
  const int b = idx >> 2 & 1, head = xcd * 4 + (((idx >> 3) & 1) << 1 | idx >> 4);
  const u32 so0 = step_off(P, 32 * part + KF0, b * 32 + head) & ~place(3, P.PM, 2);
  u32 va = (((b * 32 + head) * SK + (SK - WIN)) * DH * 2 + so0) | place(tid >> 6, P.WM, 3) | (tid & 63) << 4;
  const u32 kx1 = place(1, P.PM, 2), kx2 = place(2, P.PM, 2);
  PIN(va);
  u32 va1 = va | kx1, va2 = va | kx2, va3 = va | kx1 | kx2;
  PIN(va1); PIN(va2); PIN(va3);
  int mc = bid & 0xd8;
  asm volatile("" : "+v"(va), "+s"(mc));
  SB();
  asm volatile("s_waitcnt lgkmcnt(0)" : "+s"(ka), "+s"(kb), "+s"(pt0));
  SB();
#define K_FIRST "global_load_dwordx4 %0, %4, %5 nt\n\tglobal_load_dwordx4 %1, %6, %5 nt\n\tglobal_load_dwordx4 %2, %7, %5 nt\n\tglobal_load_dwordx4 %3, %8, %5 nt"
  asm volatile("s_cmp_eq_u32 %9, 16\n\ts_cbranch_scc1 .Lk_later\n\t" K_FIRST "\n.Lk_later:"
               : "=&v"(kr[0]), "=&v"(kr[1]), "=&v"(kr[2]), "=&v"(kr[3]) : "v"(va), "s"((u64)ka[2] | (u64)ka[3] << 32), "v"(va1), "v"(va2), "v"(va3), "s"(mc) : "memory", "scc");
  SB();
  const u32 tka = (u32)wall_clock64(), t0 = tka - D0;
  auto kap = [&](int k) { return k < 8 ? (u64)ka[2 * k] | (u64)ka[2 * k + 1] << 32 : (u64)kb[2 * k - 16] | (u64)kb[2 * k - 15] << 32; };
  const u16 *x = (const u16*)kap(0), *kc = (const u16*)kap(1), *vc = (const u16*)kap(2), *w2 = (const u16*)kap(7), *cnd = (const u16*)kap(8);
  unsigned char* ws = (unsigned char*)kap(10); u16* out = (u16*)kap(11);
  const int lane = tid & 63, wave = __builtin_amdgcn_readfirstlane(tid >> 6);
  const bool p3 = part == 3;
  const u64 wa = part == 0 ? kap(4) : (part == 1 ? kap(3) : (part == 2 ? kap(9) : ((wave & 1) ? kap(6) : kap(5))));
  const bool modc = mc == 16;
  if (modc) asm volatile("s_waitcnt vmcnt(0)" ::: "memory");
  asm volatile("s_waitcnt vmcnt(4)" : "+v"(tev));
  const u32 te0 = __builtin_amdgcn_readfirstlane(tev[0]), te1 = __builtin_amdgcn_readfirstlane(tev[1]);
  const u32 te3 = __builtin_amdgcn_readfirstlane(tev[3]), pitch = te3 & 0xffff, wsz = pitch << (part == 0 ? 12 : 9);
  const int sh = p3 ? 2 : 3;
  u32 wv0 = (u32)((p3 ? (wave >> 1) * 16 : wave * 64) + (lane >> sh)) * pitch + ((lane & (p3 ? 3 : 7)) ^ (lane >> (p3 ? 5 : 3)) ^ (p3 ? wave >> 2 << 1 : 0)) * 16u;
  const u32 wimg = (u32)(uintptr_t)(&sm.wbuf[0][0][0]);
  u32 wld = wimg + (p3 ? (wave & 1) * 32768 + (wave >> 1) * 1024 : wave * 8192);
  const int bl0 = te0 & 127, nl = te0 >> 7 & 63, blk = te0 >> 13 & 63;
  const bool has_w = te0 >> 19 & 1;
  const rsrc_t rw = mk_rsrc((const void*)wa, wsz);
  const u32 wstep = pitch << (p3 ? 6 : 3), lstep = p3 ? 4096u : 1024u;
  u32 wso = __builtin_amdgcn_readfirstlane(tev[2]);
  int wleft = 8;
  auto piece = [&]() {
    dma16(rw, wv0, wso, wld);
    wso += wstep; wld += lstep;
    --wleft;
  };
  int tau = te1 & 0xffff;
  const int tauw = te1 >> 16, sw = te0 >> 24;
  int tau2 = te3 >> 16;
  int due = P.DS * 256, now = 0;
  auto gate = [&]() {
    while (now < due) { now = (int)(((u32)wall_clock64() - t0) << 8); if (now < due) __builtin_amdgcn_s_sleep(1); }
    due += tau;
  };
  const rsrc_t wsr = mk_rsrc(ws, WS_END);
  const int g = lane >> 4, c = lane & 15;
  const int i8 = idx >> 2, wi = i8 * 8 + xcd;
  const bool is_up = p3 && has_w;
  const size_t base = ((size_t)(b * NH + head) * SK + (SK - WIN)) * DH;
  const rsrc_t rk = mk_rsrc(kc + base, WIN * DH * 2), rv = mk_rsrc(vc + base, WIN * DH * 2);
  u32 sot;
  {
    u32 s0 = bl0 + lane - KF;
    s0 += s0 >= (u32)(32 * part + KF0) ? KF : 0;
    sot = (lane < KF ? so0 | place(lane, P.PM, 2) : step_off(P, s0, b * 32 + head)) | place(wave, P.WM, 3);
    sot = lane < nl ? sot : OOB;
    PIN(sot);
  }
  auto until = [&](int t) { while ((int)(((u32)wall_clock64() - t0) << 8) < t); };
  u32 near = 0, nmask = 0;
  int slack = 0;
  const rsrc_t rx = mk_rsrc(part == 2 ? cnd : x, (part == 1 || (part == 2 && has_w)) ? 2048u : 0u);
  u32x2 ep; u32 xs0, xs1, tch1; u32x4 xr0, xr1;
  auto small_ops = [&]() {
    ep = ld8<AUX_SC1>(wsr, wave == 7 ? WS_EPOCH : OOB, 0);
    xs0 = __builtin_amdgcn_raw_buffer_load_b16(rx, tid * 2, 0, 0); xs1 = __builtin_amdgcn_raw_buffer_load_b16(rx, tid * 2, 1024, 0);
  };
  auto small_ops2 = [&]() {
    xr0 = ld16<0>(rx, part == 1 ? lane * 16 : OOB, 0); xr1 = ld16<0>(rx, part == 1 ? lane * 16 : OOB, 1024);
    tch1 = __builtin_amdgcn_raw_buffer_load_b32(mk_rsrc(w2 + (size_t)blk * 32 * 512, is_up ? 64u : 0u), tid == 384 ? 0 : OOB, 0, AUX_NT);
  };
  u32 tag = 0;
  u32x4 bf[8];
  auto wo_prep = [&](int s0 = 0, int s1 = 2) {
    const u32 wi_ = wimg + wave * 8192;
    const int q = (lane >> 2) & 3, pp = lane & 3;
#pragma unroll
    for (int s = s0; s < s1; ++s) {
#pragma unroll
      for (int t = 0; t < 4; ++t) {
        const int r0 = s * 32 + g * 8 + q, r1 = r0 + 4, ch = t * 2 + (pp >> 1);
        const u32x2 lo = tr_read(wi_ + r0 * 128 + ((ch ^ (r0 & 7)) * 16) + (pp & 1) * 8), hi = tr_read(wi_ + r1 * 128 + ((ch ^ (r1 & 7)) * 16) + (pp & 1) * 8);
        bf[s * 4 + t] = u32x4{lo[0], lo[1], hi[0], hi[1]};
      }
    }
  };
  auto gemv_go = [&](u32 hh) {
    sm.hb[tid] = (u16)hh; sm.hb[512 + tid] = (u16)(hh >> 16);
    WAVE_SYNC();
    const u32x4* a = (c & 11) == 0 ? reinterpret_cast<const u32x4*>(&sm.hb[(c >> 2) * 512 + wave * 64 + g * 8]) : &sm.zero[0];
    const u32x4 a0 = a[0], a1 = a[(c & 11) == 0 ? 4 : 0];
    const f32x4 z = {0.f, 0.f, 0.f, 0.f};
    f32x4 d[4];
#pragma unroll
    for (int t = 0; t < 4; ++t) d[t] = mfma16(a0, bf[t], z);
#pragma unroll
    for (int t = 0; t < 4; ++t) d[t] = mfma16(a1, bf[4 + t], d[t]);
    if (lane < 32) {
#pragma unroll
      for (int t = 0; t < 4; ++t) sm.r2[(t * 16 + c) * 20 + g * 8 + wave] = d[t][0];
    }
  };
  auto mod_gemv = [&]() { gemv_go(pack_bf(silu_f(lo_f(xs0)), silu_f(lo_f(xs1)))); };
  auto mod_put = [&]() { st8_sc1(wsr, G_MOD + (blk * 64 + lane) * 8, pack_bf(sum8(&sm.r2[lane * 20]), sum8(&sm.r2[lane * 20 + 8])), tag); };
  auto zero_rows = [&]() { if (lane < 4) sm.zero[lane] = u32x4{0u, 0u, 0u, 0u}; };
  const u32 pbo = G_PB + (wi >> 3) * 16384 + (wi & 7) * 1024;
  u32 l16 = lane * 16;
  asm volatile("" : "+v"(l16), "+v"(wv0) : "s"(rk), "s"(rx), "s"(wsr));
  SB();
  if (has_w) {
    if (__builtin_expect(modc, 0)) {
      now = 1 << 30;
      __builtin_amdgcn_raw_buffer_store_b128(u32x4{SENT, SENT, SENT, SENT}, wsr, tid < 384 ? G_P + wi * 6144 + tid * 16 : pbo + (wave & 1) * 8192 + lane * 16, 0, AUX_SC1);
      __builtin_amdgcn_raw_buffer_store_b128(u32x4{SENT, SENT, SENT, SENT}, wsr, tid < 16 ? G_ML + wi * 256 + tid * 16 : (tid < 24 ? G_NT + wi * 128 + (tid - 16) * 16 : (tid < 32 && wi < 3 ? G_AG + wi * 128 + (tid - 24) * 16 : OOB)), 0, AUX_SC1);
    }
#pragma nounroll
    while (wleft > (int)(te0 >> 20 & 7)) { gate(); due += tauw - tau; piece(); }
    now = 0;
  }
  SB();
  STAMP(23);
  if (__builtin_expect(modc, 0)) {
    small_ops();
    zero_rows();
    {
      until(P.PT * 256);
      const u64 pa = (u64)ws + pbo + wave * 128 + (wave & 1) * 8192;
      u64 c0, c1, c2; u32 d0, d1;
      asm volatile("s_memtime %0\n\ts_waitcnt lgkmcnt(0)\n\ts_load_dword %3, %5, 0x0\n\ts_waitcnt lgkmcnt(0)\n\ts_memtime %1\n\ts_waitcnt lgkmcnt(0)\n\t"
                   "s_load_dword %4, %6, 0x0\n\ts_waitcnt lgkmcnt(0)\n\ts_memtime %2\n\ts_waitcnt lgkmcnt(0)"
                   : "=&s"(c0), "=&s"(c1), "=&s"(c2), "=&s"(d0), "=&s"(d1) : "s"(pa), "s"(pa + 8192 - (wave & 1) * 16384) : "memory");
      const int df = min(max((int)(2 * (u32)c1 - (u32)c0 - (u32)c2), -600), 600);
      if (lane == 0) sm.stat[0][wave] = (float)(((wave ^ (xcd >> 2)) & 1) ? -df : df);
    }
    asm volatile("s_waitcnt vmcnt(0)" ::: "memory");
    tag = next_tag(__builtin_amdgcn_readfirstlane(ep[0]));
    wo_prep();
    mod_gemv();
    LDS_BARRIER();
    if (wave == 7) { mod_put(); if (lane == 0) st8_sc1(wsr, G_AG + (48 + wi) * 8, __float_as_uint(sum8(&sm.stat[0][0])), tag); }
    STAMP(9);
    __builtin_amdgcn_s_barrier();
    asm volatile(K_FIRST :: "v"(kr[0]), "v"(kr[1]), "v"(kr[2]), "v"(kr[3]), "v"(va), "s"((u64)ka[2] | (u64)ka[3] << 32), "v"(va1), "v"(va2), "v"(va3) : "memory");
  }
  SB();
  const bool fin = (part == 1 || part == 2) && wave == 0 && xcd < 2;
  const int fw = i8 * 4 + (part & 2) + xcd, fd = fw * 16 + (lane & 7) * 2;
  const int ub = wave >> 2, ud = (wave & 3) * 128 + c * 8 + g * 2;
  u32x4 sg[3]; u32 sx2;
  auto side_loads = [&]() {
    sg[0] = ld16<AUX_SC1>(wsr, is_up ? G_MOD + (1024 + ud) * 8 : (fin ? G_MOD + (1024 + fd) * 8 : OOB), 0);
    sg[1] = ld16<AUX_SC1>(wsr, is_up ? G_MOD + (1536 + ud) * 8 : (fin ? G_MOD + (2560 + fd) * 8 : OOB), 0);
    sg[2] = ld16<AUX_SC1>(wsr, is_up ? G_MOD + (2048 + ud) * 8 : OOB, 0);
    sx2 = __builtin_amdgcn_raw_buffer_load_b32(mk_rsrc(x, (is_up || fin) ? 2048u : 0u), is_up ? ub * 1024 + ud * 2 : (g & 1) * 1024 + fd * 2, 0, 0);
  };
  auto side_pin = [&]() { PIN(sg[0]); PIN(sg[1]); PIN(sg[2]); PIN(sx2); };
  const u32 mlo = G_ML + (head * 2 + b) * 64 + (lane & 3) * 16;
  float fwv = 0.f, fmw[8];
  bool fok = wave != 0;
  auto merge_factor = [&](const u32x4& ml) {
    const float mi = u2f(ml[0]);
    float M = fmaxf(mi, dpp<XOR1>(mi)); M = fmaxf(M, dpp<XOR2>(M));
    const float e = __expf(mi - M);
    float L = e * u2f(ml[2]);
    L += dpp<XOR1>(L); L += dpp<XOR2>(L);
    const float f = fwv * rdl(e * __builtin_amdgcn_rcpf(L), part);
#pragma unroll
    for (int w = 0; w < 8; ++w) fmw[w] = bcast16(f, w);
  };
  f32x2 ga = {0.f, 0.f}, xin = {0.f, 0.f}, cc1 = {0.f, 0.f};
  float Af = 0.f, Bf = 0.f;
  const int hpos = ub * 512 + ud;
  auto up_prep = [&]() {
    if (!all_ok((sg[0][1] == tag) & (sg[0][3] == tag) & (sg[1][1] == tag) & (sg[1][3] == tag) & (sg[2][1] == tag) & (sg[2][3] == tag))) gather16<3>(wsr, G_MOD + (1024 + ud) * 8, 512 * 8, tag, sg);
    ga = f32x2{ub ? hi_f(sg[0][0]) : lo_f(sg[0][0]), ub ? hi_f(sg[0][2]) : lo_f(sg[0][2])};
    cc1 = f32x2{rbf(1.f + (ub ? hi_f(sg[2][0]) : lo_f(sg[2][0]))), rbf(1.f + (ub ? hi_f(sg[2][2]) : lo_f(sg[2][2])))};
    xin = f32x2{lo_f(sx2), hi_f(sx2)};
    u32* hp = reinterpret_cast<u32*>(&sm.hb[hpos]);
    hp[512] = pack_bf(cc1[0], cc1[1]);
    hp[1024] = ub ? perm_hi(sg[1][0], sg[1][2]) : perm_lo(sg[1][0], sg[1][2]);
  };
  auto up_addr = [&]() {
    const int pp = lane & 3;
    return wimg + (pp & 1) * 32768 + ((pp >> 1) * 32 + g * 8 + ((lane >> 2) & 3)) * 64 + (((wave >> 1) ^ ((g & 1) | (pp & 2))) * 16) + (wave & 1) * 8;
  };
  auto up_frags = [&](int r0, int r1) {
    const u32 a = up_addr();
#pragma unroll
    for (int rb = r0; rb < r1; ++rb) {
      const u32x2 lo = tr_read(a + rb * 4096), hi = tr_read(a + rb * 4096 + 256);
      bf[rb] = u32x4{lo[0], lo[1], hi[0], hi[1]};
    }
  };
  const u32x4* ap = reinterpret_cast<const u32x4*>(&sm.hb[((lane >> 3) & 1) * 512 + (lane & 1) * 32 + g * 8]);
  const bool par = lane & 8;
  auto col_sums = [&](int r0, int r1) {
    const u32x4* a1 = ap + 128 + ((lane >> 1) & 1) * 128;
    f32x4 d0 = {0.f, 0.f, 0.f, 0.f}, d1 = {0.f, 0.f, 0.f, 0.f};
#pragma unroll
    for (int rb = r0; rb < r1; ++rb) { if (rb & 1) d1 = mfma16(a1[rb * 8], bf[rb], d1); else d0 = mfma16(a1[rb * 8], bf[rb], d0); }
    Af += par ? d0[1] + d1[1] : d0[0] + d1[0]; Bf += par ? d0[3] + d1[3] : d0[2] + d1[2];
  };
  float l0 = 0.f, l1 = 0.f;
  u32x2 gmd[2], tbg;
  const bool modw = part == 2 && has_w && !modc;
  const int qp = part != 1 ? -99 : ((blk & 7) == 3 ? QP1 : QP2);
  static_assert(QP1 + QD < QP2 && QP1 + QD > WUSE && QP1 + QD > TBU && WUSE + 1 < TBU, "order of the q steps");
  int t1 = 0;
#pragma unroll
  for (int i = NCMIN / 8; i < NL; ++i) kr[i] = u32x4{0u, 0u, 0u, 0u};
  sfor<KF, NL>([&](auto ic) {
    constexpr int i = ic.v;
    if (i >= NCMIN / 8 && i >= nl) return false;
    const u32 so = __builtin_amdgcn_readlane(sot, i);
    if constexpr (i < NCMIN / 8) tau = i == sw ? tau2 : tau;
    gate();
    SB();
    kr[i] = ld16<AUX_NT>(rk, l16, so);
    if constexpr (i == KF) small_ops();
    if constexpr (i == KF + 1) small_ops2();
    if constexpr (i == KF + 2) zero_rows();
    if constexpr (i == TBL) tbg = ld8<AUX_SC1>(wsr, wave == 7 ? G_TB + (lane & 7) * 8 : OOB, 0);
    if constexpr (i == QP1 || i == QP2) { gmd[0] = ld8<AUX_SC1>(wsr, i == qp ? G_MOD + tid * 8 : OOB, 0); gmd[1] = ld8<AUX_SC1>(wsr, i == qp ? G_MOD + tid * 8 : OOB, 4096); }
    SB();
    SB();
    if (i >= WK) { WAIT_VM(win_k(i)); SB(); }
    if (i == KF) STAMP(25);
    if (i == 8) STAMP(12);
    if (i == 20) STAMP(21);
    if (i == 25) STAMP(22);
    if (i == TBP) {
      if (part == 3 && i8 == 0 && wave == 7) { tag = next_tag(__builtin_amdgcn_readfirstlane(ep[0])); if (lane == 0) st8_sc1(wsr, G_TB + xcd * 8, tka - P.GS, tag); }
      SB();
    }
    if (i == TBU - 1) {
      if (wave == 7) {
        const bool ok = tbg[1] == tag;
        float lf = ok ? (float)min(max((int)(t0 - tbg[0]), -300), 300) : 0.f, nf = ok ? 1.f : 0.f;
        lf += dpp<XOR1>(lf); nf += dpp<XOR1>(nf); lf += dpp<XOR2>(lf); nf += dpp<XOR2>(nf);
        lf *= __builtin_amdgcn_rcpf(fmaxf(nf, 1.f));
        const float ot = dpp<XOR7>(lf);
        if (lane == 0) sm.bc = u32x2{tag, t0 - (u32)(int)((lf + ot) * 0.5f - fabsf(lf - ot) * (P.BT / 32.f))};
      }
      SB();
    }
    if (i == TBU) {
      LDS_BARRIER();
      const u32x2 bc = sm.bc;
      tag = __builtin_amdgcn_readfirstlane(bc[0]);
      t1 = (__builtin_amdgcn_readfirstlane((int)(bc[1] - t0)) + TEND + P.X1) * 256;
      const int dt = __builtin_amdgcn_readfirstlane((int)((float)(int)(t0 - bc[1]) * 256.f * __builtin_amdgcn_rcpf((float)(2 * nl - TBU - 2))));
      tau -= dt; tau2 -= dt;
      SB();
    }
    if (i == TBU + 1) {
      if (modw) { if (wave == 6) mod_put(); STAMP(9); }
      SB();
    }
    if (i == LNS) {
      tag = next_tag(__builtin_amdgcn_readfirstlane(ep[0]));
      if (part == 1) {
        float x0[8], x1[8], mb0, ri0, mb1, ri1;
#pragma unroll
        for (int k = 0; k < 4; ++k) { x0[2 * k] = lo_f(xr0[k]); x0[2 * k + 1] = hi_f(xr0[k]); x1[2 * k] = lo_f(xr1[k]); x1[2 * k + 1] = hi_f(xr1[k]); }
        ln_stats2(x0, x1, mb0, ri0, mb1, ri1);
        l0 = rbf(rbf(lo_f(xs0) - mb0) * ri0); l1 = rbf(rbf(lo_f(xs1) - mb1) * ri1);
      }
      PIN(l0); PIN(l1);
      SB();
    }
    if (i == WUSE) {
      PIN(tch1); PIN(tch2);
      static_assert(WUSE - WK >= KF, "the weight pieces are older than the steps outside the window");
      CFENCE();
      if (part == 1 || modw) wo_prep();
#pragma unroll
      for (int k = 0; k < 8; ++k) PIN(bf[k]);
      SB();
    }
    if (i == WUSE + 1) {
      if (modw) mod_gemv();
      SB();
    }
    if (i == QP1 + QD || i == QP2 + QD) {
      if (i == qp + QD) {
        u32 gm[2] = {gmd[0][0], gmd[1][0]};
        if (!all_ok((gmd[0][1] == tag) & (gmd[1][1] == tag))) { STAMP(11); gather8<2>(wsr, G_MOD + tid * 8, 512 * 8, tag, gm); }
        STAMP(5);
        gemv_go(pack_bf(rbf(l0 * rbf(1.f + lo_f(gm[1]))) + lo_f(gm[0]), rbf(l1 * rbf(1.f + hi_f(gm[1]))) + hi_f(gm[0])));
      }
      SB();
    }
    if (i == QP1 + QD + 1 || i == QP2 + QD + 1) {
      if (i == qp + QD + 1) {
        LDS_BARRIER();
        if (wave == 0) {
          const int bb = lane >> 5, jq = lane & 31;
          st8_sc1(wsr, G_Q + (bb * 2048 + blk * 32 + jq) * 8, pack_bf(sum8(&sm.r2[2 * jq * 20 + bb * 8]), sum8(&sm.r2[(2 * jq + 1) * 20 + bb * 8])), tag);
        }
        STAMP(6);
      }
      SB();
    }
    return true;
  });
  PIN(sot);
  const u32 qoff = G_Q + ((b * 32 + head) * 64 + c * 4) * 8;
  u32x4 qg[2], vr[NL], fl2[3];
  u32 qq[4];
  constexpr int NB = (NL + 15) / 16;
  float xlo, xs[4], sc[NB], pr[NB], m = 0.f;
  f32x4 acc = {0.f, 0.f, 0.f, 0.f};
  u32 pb[NB], pm[4], cpf;
  u32x2 agr;
  const u32 adst0 = lds_addr(lane < 32 ? &sm.red[wave][c * 8 + g * 4] : &sm.r2[(lane - 32) * 4]);
  u32 pso, po, rdst, rsrc, xo, l2, v2[4];
  rsrc_t r2;
  auto wo_addr = [&]() {
    po = G_P + (xcd & 3) * 16384 + near + (c >> 2 & 1) * 4096 + ((wave >> 1) * 4 + (c & 3)) * 256 + (wave & 1) * 128 + (c >> 3) * 64 + g * 16;
    rdst = lane < 32 ? (u32)(uintptr_t)&sm.red2[wave][0] + lane * 16 : (u32)(uintptr_t)&sm.r2[0] + (lane - 32) * 16;
    rsrc = (u32)(uintptr_t)&sm.red2[0][((wave & 1) * 16 + (lane & 15)) * 4 + (lane >> 4)];
    xo = dup(G_XP + xcd * 2048 + (wave & 1) * 1024 + (blk * 64 + (lane & ~1)) * 2) + (lane & 1) * 8192;
    pso = wave == 0 ? G_P + (xcd & 3) * 16384 + near + b * 4096 + ((head & 3) * 4 + part) * 256 + lane * 4 : OOB;
    PIN(pso); PIN(po); PIN(rdst); PIN(rsrc); PIN(xo);
  };
  auto w2_addr = [&]() {
    r2 = mk_rsrc(w2 + (size_t)(blk * 32 + wave * 4) * 512, 4096u);
    l2 = (u32)(uintptr_t)(&sm.w2b[wave * 4][0]);
#pragma unroll
    for (int n = 0; n < 4; ++n) { v2[n] = (lane ^ (n * 2) ^ ((wave & 2) << 2)) * 16 + n * 1024; PIN(v2[n]); }
    asm volatile("" : "+s"(r2), "+s"(l2));
  };
  const bool late = is_up || (part == 0 && wleft);
  const u32 nadd = is_up ? P.LU : (part == 0 ? (wleft ? P.LW : P.L3) : 0);
  u32 nto = tid == 0 ? G_NT + bid * 8 : OOB;
  PIN(nto);
  auto notices = [&](auto nc_, u32 off, u32x4 (&gn)[nc_.v]) -> int {
    constexpr int N = nc_.v;
#pragma nounroll
    for (int spin = 0; spin < SPIN_MAX; ++spin) {
      bool ok = true;
#pragma unroll
      for (int i = 0; i < N; ++i) ok &= (gn[i][1] == tag) & (gn[i][3] == tag);
      if (all_ok(ok)) break;
      __builtin_amdgcn_s_sleep(10);
      CFENCE();
#pragma unroll
      for (int i = 0; i < N; ++i) gn[i] = ld16<AUX_SC1>(wsr, off, i * 1024);
    }
    int mx = 0;
#pragma unroll
    for (int i = 0; i < N; ++i) mx = max(mx, max((int)(gn[i][0] - t0), (int)(gn[i][2] - t0)));
    return __builtin_amdgcn_readfirstlane((int)wave_max((float)min(mx, 1 << 20)));
  };
  const int side = nl >= SIDE + 13 ? SIDE + 8 : (nl >= SIDE + 9 ? SIDE + 4 : SIDE);
  sfor<0, NL + WV>([&](auto ic) {
    constexpr int v = ic.v;
    if (cons_end(v - 1) >= NCMIN / 8 && cons_end(v - 1) >= nl) return false;
    if constexpr (v < NL) {
      const u32 so = __builtin_amdgcn_readlane(sot, v);
      if constexpr (v >= 12) { if (v == nl - P.NE) tau = (tau * (late ? 16 : P.FE)) >> 4; }
      if constexpr (v >= NCMIN / 8) { if (v < nl) gate(); } else gate();
      SB();
      int bk = 0;
      if constexpr (v == AR) { if (part == 1 && wi < 48 && wave == 7) bk = ((int)(((u32)wall_clock64() - t0) << 8) - due + tau + (int)__builtin_amdgcn_s_getreg(7 | (3 << 11)) * tau) >> 8; SB(); }
      vr[v] = ld16<AUX_NT>(rv, l16, so);
      if constexpr (v == AR) {
        SB();
        if (part == 1 && wi < 48 && tid == 448) st8_sc1(wsr, G_AG + wi * 8, (u32)min(max(bk, 0), 250), tag);
      }
      SB();
      SB();
    }
    if constexpr (v == NCMIN / 8 - 1) {
#pragma unroll
      for (int i = NCMIN / 8; i < NL; ++i) asm volatile("" : "=v"(vr[i]));
      asm volatile("" : "=v"(fl2[1]), "=v"(fl2[2]));
#pragma unroll
      for (int w = 0; w < 8; ++w) asm volatile("" : "=v"(fmw[w]));
    }
    if constexpr (v == VQ - 4) { qg[0] = ld16<AUX_SC1>(wsr, qoff, 0); qg[1] = ld16<AUX_SC1>(wsr, qoff + 16, 0); }
    if constexpr (v == VSIDE) side_loads();
    if constexpr (v == VL) agr = ld8<AUX_SC1>(wsr, wave == 6 ? G_AG + (48 + c) * 8 : OOB, 0);
    if constexpr (v == AL) agr = ld8<AUX_SC1>(wsr, (wave == 7 && lane < 48) ? G_AG + lane * 8 : OOB, 0);
    if constexpr (v == CPF) {
      u64 pc; u32 dlt, len;
      asm volatile("s_getpc_b64 %0\n.Lcpf_here:\n\ts_mov_b32 %1, .Ltail_start-.Lcpf_here\n\ts_mov_b32 %2, .Lend_a-.Ltail_start\n\ts_max_i32 %2, %2, .Lend_b-.Ltail_start\n\ts_max_i32 %2, %2, .Lend_c-.Ltail_start\n\ts_max_i32 %2, %2, .Lend_d-.Ltail_start\n\ts_max_i32 %2, %2, .Lend_e-.Ltail_start" : "=s"(pc), "=s"(dlt), "=s"(len) :: "scc");
      pc += dlt;
      const rsrc_t rc = mk_rsrc((const void*)pc, (part == 3 && i8 == 1) ? len : 0u);
      cpf = __builtin_amdgcn_raw_buffer_load_b32(rc, tid * 64, 0, 0);
    }
    if constexpr (v == CPF + WV + 1) PIN(cpf);
    if constexpr (v == SIDE || v == SIDE + 4 || v == SIDE + 8) fl2[(v - SIDE) / 4] = ld16<AUX_SC1>(wsr, (wave == 0 && v == side) ? mlo : OOB, 0);
    SB();
    if constexpr (v < WV) { WAIT_VM(win_v(v, WK)); SB(); } else if constexpr (v < NL && cons_end(v) < v - WV + 1) { WAIT_VM(win_v(v, WV)); SB(); }
    if constexpr (v == VU) {
      if (wave == 6) {
        u32 vt[1] = {agr[0]};
        if (!all_ok(agr[1] == tag)) gather8<1>(wsr, G_AG + (48 + c) * 8, 0, tag, vt);
        float sv = u2f(vt[0]);
        sv += dpp<0x124>(sv); sv += dpp<XOR8>(sv);
        float sa = sv + dpp<XOR1>(sv);
        sv = fmaf(sa + dpp<XOR2>(sa), 0.25f, sv);
        float cf = fabsf(sv);
        cf = fminf(cf, dpp<XOR1>(cf)); cf = fminf(cf, dpp<XOR2>(cf));
        const u32 nm = (u32)__builtin_amdgcn_ballot_w64(sv > 0.f) & 15u;
        if (lane == 0) sm.nm = nm << 13 | (cf < 2500.f ? (u32)P.SL : 0u);
      }
      PIN(agr);
    }
    if constexpr (v == VQ) {
      if (!all_ok((qg[0][1] == tag) & (qg[0][3] == tag) & (qg[1][1] == tag) & (qg[1][3] == tag))) { STAMP(20); gather16<2>(wsr, qoff, 16, tag, qg); }
      STAMP(1); WSTAMP(1);
      qq[0] = qg[0][0]; qq[1] = qg[0][2]; qq[2] = qg[1][0]; qq[3] = qg[1][2];
    }
    if constexpr (v >= VQ && v < VS) {
      constexpr int u0 = SLC[v - VQ], u1 = SLC[v - VQ + 1];
      static_assert(SLC[NSL] == (NL + 7) / 8 * 8, "all groups of 8");
      sfor<u0, u1>([&](auto uc) {
        constexpr int u = uc.v, p = u & 7, i = (p & 1) * 4 + (p & 2) + (p >> 2), sl = u >> 3, bt = sl >> 1;
        float d = 0.f;
        if constexpr (sl * 8 + i < NL) {
#pragma unroll
          for (int k = 0; k < 4; ++k) d = dot2(kr[sl * 8 + i][k], qq[k], d);
        }
        if constexpr (p & 1) xs[i - 4] = sel_add<XOR7>(xs[i - 4], d, c & 4); else xs[i] = d;
        if constexpr ((p & 3) == 3) xs[p >> 2] = sel_add<XOR2>(xs[p >> 2], xs[(p >> 2) + 2], c & 2);
        if constexpr (p == 7) {
          xs[0] = sel_add<XOR1>(xs[0], xs[1], c & 1);
          if constexpr ((sl & 1) || u == (NL + 7) / 8 * 8 - 1) {
            const float t = (sl & 1) ? sel_add<XOR8>(xlo, xs[0], c & 8) : xs[0] + dpp<XOR8>(xs[0]);
            const float s = rbf(rbf(t) * 0.08838834764831845f);
            sc[bt] = (bt * 16 + c < nl) ? s : -__builtin_inff();
            PIN(sc[bt]);
          } else {
            xlo = xs[0];
            PIN(xlo);
          }
        }
        return true;
      });
      constexpr int pe = u1 & 7;
      if constexpr (pe >= 1) PIN(xs[0]);
      if constexpr (pe == 3) PIN(xs[2]);
      if constexpr (pe >= 5) PIN(xs[1]);
      if constexpr (pe == 7) PIN(xs[3]);
    }
    if constexpr (v == VS) {
      float mloc = sc[0];
#pragma unroll
      for (int bt = 1; bt < NB; ++bt) mloc = fmaxf(mloc, sc[bt]);
      m = fmaxf(wave_max(mloc), -3.0e38f);
      float ls = 0.f;
#pragma unroll
      for (int bt = 0; bt < NB; ++bt) { pr[bt] = __expf(sc[bt] - m); ls += pr[bt]; }
      const float l = wave_sum(ls);
      if (lane == 0) { sm.ml[0][wave] = m; sm.ml[1][wave] = l; }
#pragma unroll
      for (int bt = 0; bt < NB; ++bt) { pb[bt] = pack_bf(pr[bt], pr[bt]); PIN(pb[bt]); }
#pragma unroll
      for (int k = 0; k < 4; ++k) { pm[k] = (c >> 1) == k ? ((c & 1) ? 0xffff0000u : 0x0000ffffu) : 0u; PIN(pm[k]); }
      STAMP(2); WSTAMP(2);
    }
    sfor<cons_end(v - 1), cons_end(v)>([&](auto jc) {
      constexpr int i = jc.v;
      if (i < NCMIN / 8 || i < nl) {
        const u32 pv = __float_as_uint(bcast16(u2f(pb[i >> 4]), i & 15));
        acc = mfma16(u32x4{pv & pm[0], pv & pm[1], pv & pm[2], pv & pm[3]}, vr[i], acc);
      }
      PIN(acc);
      return true;
    });
    if constexpr (v == VS - 1) { side_pin(); if (is_up) up_prep(); PIN(ga); PIN(xin); PIN(cc1); }
    if constexpr (v == MLS - 1) {
      if (wave == 7) {
        const float am = wave_sum(lane < 48 ? (agr[1] == tag ? (float)agr[0] : 120.f) : 0.f) * (1.f / 48.f);
        const float st = fminf(fmaxf((am - (float)P.A0) * (float)(P.AG * 16), 0.f), (float)(P.AC * 256));
        if (lane == 0) sm.ctl = u32x2{(u32)(int)st, (u32)(int)(st * __builtin_amdgcn_rcpf((float)(nl - MLS - 3)))};
      }
      PIN(agr);
    }
    if constexpr (v == UPREP - 2) {
      nmask = __builtin_amdgcn_readfirstlane(sm.nm);
      slack = (nmask & 255u) * 256; near = ((nmask >> (xcd & 3)) ^ (xcd << 11)) & 8192u;
      w2_addr();
    }
    if constexpr (v == UPREP - 1) wo_addr();
    if constexpr (v == UPREP) {
#pragma unroll
      for (int k = 0; k < 8; ++k) asm volatile("" : "=v"(bf[k]));
      if (part == 0) wo_prep(0, 1); else if (is_up) up_frags(0, 8 - UD);
#pragma unroll
      for (int k = 0; k < 8; ++k) PIN(bf[k]);
    }
    if constexpr (v == UPREP + 1 || v == UPREP + 2) {
      if (is_up) { if constexpr (v == UPREP + 1) col_sums(0, (8 - UD) / 2); else col_sums((8 - UD) / 2, 8 - UD); }
      PIN(Af); PIN(Bf);
    }
    if constexpr (v == SIDE + WV + 1 || v == SIDE + WV + 5 || v == SIDE + WV + 9) {
      if (wave == 0 && v == side + WV + 1) {
        const u32x4 ml = fl2[(v - SIDE - WV - 1) / 4];
        fok = all_ok((ml[1] == tag) & (ml[3] == tag));
        merge_factor(ml);
      }
    }
    if (v % 6 == 4 && v >= 10 && v < 30) STAMP(15 + v / 6);
    if constexpr (v == MLS) {
      LDS_BARRIER();
      const u32x2 ct = sm.ctl;
      if (wave == 0) {
        const float mw = sm.ml[0][lane & 7];
        float Mwg = fmaxf(mw, dpp<XOR1>(mw)); Mwg = fmaxf(Mwg, dpp<XOR2>(Mwg)); Mwg = fmaxf(Mwg, dpp<XOR7>(Mwg));
        fwv = __expf(mw - Mwg);
        float Lwg = fwv * sm.ml[1][lane & 7];
        Lwg += dpp<XOR1>(Lwg); Lwg += dpp<XOR2>(Lwg); Lwg += dpp<XOR7>(Lwg);
        if (lane == 0) st16_sc1(wsr, G_ML + ((head * 2 + b) * 4 + part) * 16, __float_as_uint(Mwg), __float_as_uint(Lwg), tag);
      }
      PIN(fwv);
      t1 += __builtin_amdgcn_readfirstlane(ct[0]);
      tau += __builtin_amdgcn_readfirstlane(ct[1]);
    }
    SB();
    return true;
  });
  asm volatile(".Ltail_start:");
  *LDSP(f32x4, adst0) = acc;
  STAMP(14); WSTAMP(14);
  if (!fok) {
    u32x4 ml;
#pragma nounroll
    for (int spin = 0; spin < SPIN_MAX; ++spin) {
      CFENCE();
      ml = ld16<AUX_SC1>(wsr, mlo, 0);
      if (all_ok((ml[1] == tag) & (ml[3] == tag))) break;
    }
    merge_factor(ml);
  }
  STAMP(15);
  LDS_BARRIER();
  STAMP(3);
  {
    u32 pw = 0;
    if (wave == 0) {
      f32x2 t[8];
#pragma unroll
      for (int w = 0; w < 8; ++w) t[w] = *reinterpret_cast<const f32x2*>(&sm.red[w][2 * lane]);
      f32x2 h[4];
#pragma unroll
      for (int w = 0; w < 4; ++w) h[w] = __builtin_elementwise_fma(t[2 * w + 1], f32x2{fmw[2 * w + 1], fmw[2 * w + 1]}, t[2 * w] * fmw[2 * w]);
      const f32x2 o = (h[0] + h[1]) + (h[2] + h[3]);
      pw = min(pack_bf(o[0], o[1]), SENT - 1u);
    }
    __builtin_amdgcn_raw_buffer_store_b32(pw, wsr, pso, 0, AUX_SC1);
    st8_sc1(wsr, nto, (u32)wall_clock64() + nadd, tag);
    SB();
#pragma unroll
    for (int i = NCMIN / 8; i < NL; ++i) PIN(vr[i]);
    PIN(fl2[0]); PIN(fl2[1]); PIN(fl2[2]);
  }
  STAMP(4);
  SB();
  if (late) {
    asm volatile(".p2align 6\n\ts_barrier" ::: "memory");
#pragma nounroll
    while (wleft > 0) piece();
    if (is_up) {
      asm volatile("s_mov_b32 m0, %5\n\ts_nop 0\n\tbuffer_load_dwordx4 %0, %4, 0 offen nt lds\n\ts_add_u32 m0, m0, 0x400\n\ts_nop 0\n\tbuffer_load_dwordx4 %1, %4, 0 offen nt lds\n\t"
                   "s_add_u32 m0, m0, 0x400\n\ts_nop 0\n\tbuffer_load_dwordx4 %2, %4, 0 offen nt lds\n\ts_add_u32 m0, m0, 0x400\n\ts_nop 0\n\tbuffer_load_dwordx4 %3, %4, 0 offen nt lds"
                   :: "v"(v2[0]), "v"(v2[1]), "v"(v2[2]), "v"(v2[3]), "s"(r2), "s"(l2) : "memory", "scc", "m0");
    }
  }
  SB();

  if (part == 0) {
    u32 wa2[8];
#pragma unroll
    for (int k = 0; k < 8; ++k) {
      const int r = 32 + g * 8 + ((lane >> 2) & 3) + (k & 1) * 4, ch = (k & 6) + ((lane >> 1) & 1);
      wa2[k] = wimg + wave * 8192 + r * 128 + ((ch ^ (r & 7)) * 16) + (lane & 1) * 8;
      PIN(wa2[k]);
    }
    rsrc_t wr2 = wsr;
    asm volatile("" : "+s"(wr2));
    until(t1);
    STAMP(11);
    CFENCE();
    u32x4 og[2];
    og[0] = ld16<AUX_SC1>(wr2, po, 0);
    SB();
    WAIT_VM(1); CFENCE();
#pragma unroll
    for (int t = 0; t < 4; ++t) {
      const u32x2 lo = tr_read(wa2[2 * t]), hi = tr_read(wa2[2 * t + 1]);
      bf[4 + t] = u32x4{lo[0], lo[1], hi[0], hi[1]};
    }
    SB();
    f32x4 d[4];
    auto wo_mm = [&]() {
      const f32x4 z = {0.f, 0.f, 0.f, 0.f};
#pragma unroll
      for (int t = 0; t < 4; ++t) { d[t] = mfma16(og[0], bf[t], z); og[1][t] = __float_as_uint(dpp<XOR8>(u2f(og[0][t]))); }
      SB();
#pragma unroll
      for (int t = 0; t < 4; ++t) d[t] = mfma16(og[1], bf[4 + t], d[t]);
    };
    auto p_bad = [&]() { return !all_ok(max(max(og[0][0], og[0][1]), max(og[0][2], og[0][3])) != SENT); };
    wo_mm();
    if (__builtin_expect(p_bad(), 0)) {
#pragma nounroll
      for (int spin = 0; spin < SPIN_MAX; ++spin) {
        __builtin_amdgcn_s_sleep(1);
        CFENCE();
        og[0] = ld16<AUX_SC1>(wsr, po, 0);
        if (!p_bad()) break;
      }
      wo_mm();
      SB();
      asm volatile(".Lend_d:");
    }
    STAMP(5); WSTAMP(5);
    {
      SB();
      f32x4 y; f32x2 h[4];
#pragma unroll
      for (int t = 0; t < 4; ++t) { h[t] = d[t].xy + d[t].zw; PIN(h[t]); }
#pragma unroll
      for (int t = 0; t < 4; ++t) { y[t] = h[t][0] + h[t][1]; PIN(y[t]); }
      *LDSP(f32x4, rdst) = y;
    }
    STAMP(16);
    LDS_BARRIER();
    STAMP(17);
    if (wave < 2) {
      f32x2 v[4];
#pragma unroll
      for (int w = 0; w < 4; ++w) v[w] = f32x2{LDSP(float, rsrc)[w * 256], LDSP(float, rsrc)[w * 256 + 128]};
      const f32x2 h = (v[0] + v[1]) + (v[2] + v[3]);
      const float y = h[0] + h[1];
      __builtin_amdgcn_raw_buffer_store_b32(min(pack_bf(dpp<0xA0>(y), dpp<0xF5>(y)), SENT - 1u), wr2, xo, 0, AUX_SC1);
      asm volatile(".Lend_a:");
    }
    STAMP(6);
    FINISH();
  }
  if (part == 3) {
    if (!has_w) FINISH();
    u32x4 b2[4];
    const u32 tdst = lds_addr(&sm.hb[hpos ^ ((wave & 1) * 16)]);
    const u32 sdst = lds_addr(&sm.stat[wave][lane]);
    const u32 ssrc = lds_addr(&sm.stat[(g >> 1) * 4 + (c >> 2)][(g & 1) * 32 + (c & 3) * 8]);
    const u32 adst = lds_addr((c < 4 && (g & 1) == 0) ? &sm.actl[g >> 1][wave * 4 + c] : reinterpret_cast<u16*>(&sm.dummy[lane]));
    const u32 asrc = lds_addr(&sm.actl[lane & 1][g * 8]);
    const u32 apa = lds_addr(&sm.hb[(c >> 3) * 512 + (c & 3) * 64 + ((c >> 2) & 1) * 32 + (g ^ (c & 2)) * 8]);
    u32 xoff = dup(G_XP) + near + g * 2048 + ub * 1024 + (wave & 3) * 256 + c * 16;
    u32x4 one;
#pragma unroll
    for (int k = 0; k < 4; ++k) one[k] = ((c & 2) == 0 && (c >> 2) == k) ? ((c & 1) ? 0x3f800000u : 0x00003f80u) : 0u;
    PIN(one);
    u32 ooff = dup(G_OP + (blk * 512 + tid) * 8) + ((nmask >> (g & 1)) & 8192u);
    PIN(xoff); PIN(ooff);
    PIN(ga); PIN(xin); PIN(cc1);
    u32 w2a = (u32)(uintptr_t)(&sm.w2b[0][0]) + (g * 8 + ((lane >> 2) & 3)) * 1024 + (wave ^ (g & 1)) * 128 + (lane & 15) * 8;
    u32 w2t[4];
#pragma unroll
    for (int t = 0; t < 4; ++t) { w2t[t] = w2a ^ (t * 32); PIN(w2t[t]); }
    static_assert(UD == 1, "one late fragment");
    u32 ufa = up_addr() + 7 * 4096;
    const u32 a7a = lds_addr(ap + 128 + ((lane >> 1) & 1) * 128 + 7 * 8);
    PIN(ufa);
    u64 gm1 = __builtin_amdgcn_ballot_w64(g == 1), gm2 = __builtin_amdgcn_ballot_w64(g == 2), gm3 = __builtin_amdgcn_ballot_w64(g == 3);
    asm volatile("" : "+s"(gm1), "+s"(gm2), "+s"(gm3));
    const bool g1 = __builtin_amdgcn_inverse_ballot_w64(gm1), g2 = __builtin_amdgcn_inverse_ballot_w64(gm2), g3 = __builtin_amdgcn_inverse_ballot_w64(gm3);
    if (wave == 1) {
      due = t1 + P.N1 * 256; gate();
      u32x4 gn[2] = {ld16<AUX_SC1>(wsr, G_NT + lane * 16, 0), ld16<AUX_SC1>(wsr, G_NT + lane * 16, 1024)};
      const int tn = notices(IC<2>{}, G_NT + lane * 16, gn) * 256;
      due = (tn > t1 + P.TH * 256 ? max(tn, t1 + P.L0 * 256) + P.L1 * 256 : t1) + P.X2 * 256 + slack;
      until(due);
    }
    asm volatile("s_waitcnt vmcnt(0)" ::: "memory");
    STAMP(9);
    __builtin_amdgcn_s_barrier();
    STAMP(11);
    CFENCE();
    u32x4 xg[2] = {ld16<AUX_SC1>(wsr, xoff, 0), ld16<AUX_SC1>(wsr, xoff, 16384)};
    SB();
    {
      const u32x4 a7 = *LDSP(u32x4, a7a);
      const u32x2 l7 = tr_read(ufa), h7 = tr_read(ufa + 256);
      bf[7] = u32x4{l7[0], l7[1], h7[0], h7[1]};
#pragma unroll
      for (int t = 0; t < 4; ++t) {
        const u32x2 lo = tr_read(w2t[t]), hi = tr_read(w2t[t] + 4096);
        b2[t] = u32x4{lo[0], lo[1], hi[0], hi[1]};
      }
      SB();
      const f32x4 d0 = mfma16(a7, bf[7], f32x4{0.f, 0.f, 0.f, 0.f});
      Af += par ? d0[1] : d0[0]; Bf += par ? d0[3] : d0[2];
      Af += dpp<XOR8>(Af); Bf += dpp<XOR8>(Bf);
    }
    PIN(Af); PIN(Bf);
#pragma unroll
    for (int t = 0; t < 4; ++t) PIN(b2[t]);
    SB();
    const f32x4 z4 = {0.f, 0.f, 0.f, 0.f};
    auto xp_bad = [&]() { return !all_ok(max(max(max(xg[0][0], xg[0][1]), max(xg[0][2], xg[0][3])), max(max(xg[1][0], xg[1][1]), max(xg[1][2], xg[1][3]))) != SENT); };
    f32x4 v = mfma16(one, xg[1], mfma16(one, xg[0], z4));
    if (__builtin_expect(xp_bad(), 0)) {
#pragma nounroll
      for (int spin = 0; spin < SPIN_MAX; ++spin) {
        __builtin_amdgcn_s_sleep(1);
        CFENCE();
        xg[0] = ld16<AUX_SC1>(wsr, xoff, 0); xg[1] = ld16<AUX_SC1>(wsr, xoff, 16384);
        if (!xp_bad()) break;
      }
      v = mfma16(one, xg[1], mfma16(one, xg[0], z4));
      SB();
      asm volatile(".Lend_e:");
    }
    STAMP(5); WSTAMP(5);
    {
      const float a0 = v[0], a1 = v[1];
      const float x10 = fmaf(ga[0], a0, xin[0]), x11 = fmaf(ga[1], a1, xin[1]);
      *LDSP(u32, tdst) = pack_bf(x10 * cc1[0], x11 * cc1[1]);
      float u = swap32_add(x10 + x11, fmaf(x10, x10, x11 * x11));
      u += dpp<XOR1>(u); u += dpp<XOR2>(u); u += dpp<XOR7>(u);
      *LDSP(float, sdst) = u;
    }
    STAMP(16);
    LDS_BARRIER();
    STAMP(17);
    {
      const f32x4 z = {0.f, 0.f, 0.f, 0.f};
      const u32x4 af0 = LDSP(u32x4, apa)[0], af1 = LDSP(u32x4, apa)[32];
      float s = LDSP(float, ssrc)[0];
      SB();
      f32x4 d[4];
      d[0] = mfma16(af0, bf[0], z); d[1] = mfma16(af0, bf[1], z);
      SB();
      s += dpp<XOR1>(s); s += dpp<XOR2>(s);
      PIN(s);
      d[2] = mfma16(af0, bf[2], z);
      SB();
      s += dpp<XOR7>(s); s += dpp<XOR8>(s);
      PIN(s);
      d[3] = mfma16(af0, bf[3], z);
      SB();
      const auto sw2 = __builtin_amdgcn_permlane16_swap(__float_as_uint(s), __float_as_uint(s), false, false);
      float mean = u2f(sw2[0]) * (1.f / 512.f), qm = fmaf(u2f(sw2[1]), 1.f / 512.f, 1e-5f);
      PIN(mean); PIN(qm);
      d[0] = mfma16(af1, bf[4], d[0]);
      SB();
      float ve = fmaxf(fmaf(-mean, mean, qm), 1e-5f), ma = mean * Af;
      PIN(ve); PIN(ma);
      d[1] = mfma16(af1, bf[5], d[1]);
      SB();
      float ri = __builtin_amdgcn_rsqf(ve);
      PIN(ri);
      d[2] = mfma16(af1, bf[6], d[2]);
      d[3] = mfma16(af1, bf[7], d[3]);
      SB();
      float cf = fmaf(-ri, ma, Bf), rl = ri * -1.4426950408889634f, cl = cf * -1.4426950408889634f;
      PIN(cf); PIN(rl); PIN(cl);
      PIN(d[0]); PIN(d[1]);
      float y0 = d[0][0] + d[1][1];
      PIN(d[2]);
      y0 += d[2][2];
      PIN(y0); PIN(d[3]);
      const float y1 = d[3][3];
      float y = y0 + y1;
      y = __builtin_bit_cast(float, __builtin_amdgcn_update_dpp(__builtin_bit_cast(int, y), __builtin_bit_cast(int, y), XOR8, 0xa, 0xf, false));
      y = swap16_add(y, y);
      const float e = __builtin_amdgcn_exp2f(fmaf(y, rl, cl));
      y = fmaf(y, ri, cf);
      float gu = y * dpp<0x104>(y);
      PIN(gu);
      *LDSP(u16, adst) = (u16)pack_bf(gu * __builtin_amdgcn_rcpf(1.f + e), 0.f);
    }
    STAMP(18);
    LDS_BARRIER();
    STAMP(10);
    {
      const u32x4 af = *LDSP(u32x4, asrc);
      const f32x4 z = {0.f, 0.f, 0.f, 0.f};
      f32x4 d[4];
#pragma unroll
      for (int t = 0; t < 4; ++t) d[t] = mfma16(af, b2[t], z);
      SB();
      PIN(d[0]); PIN(d[1]);
      float e0 = g1 ? d[1][0] : d[0][0], e1 = g1 ? d[1][1] : d[0][1];
      PIN(d[2]);
      e0 = g2 ? d[2][0] : e0; e1 = g2 ? d[2][1] : e1;
      PIN(e0); PIN(e1); PIN(d[3]);
      st8_sc1(wsr, ooff, pack_bf(g3 ? d[3][0] : e0, g3 ? d[3][1] : e1), tag);
    }
    asm volatile(".Lend_b:");
    STAMP(6);
    FINISH();
  }
  if (fin) {
    const int d = fd, ul = lane >> 3;
    u32x4 gm[2] = {sg[0], sg[1]};
    if (!all_ok((sg[0][1] == tag) & (sg[0][3] == tag) & (sg[1][1] == tag) & (sg[1][3] == tag))) gather16<2>(wsr, G_MOD + (1024 + d) * 8, 1536 * 8, tag, gm);
    u32 xa0, xa1;
    {
      due = t1 + P.N2 * 256; gate();
      u32x4 gn[2] = {ld16<AUX_SC1>(wsr, G_NT + lane * 16, 0), ld16<AUX_SC1>(wsr, G_NT + lane * 16, 1024)};
      const int tn = notices(IC<2>{}, G_NT + lane * 16, gn) * 256;
      due = (tn > t1 + P.TH * 256 ? max(tn, t1 + P.L0 * 256) + P.L1 * 256 : t1) + P.X3 * 256 + slack;
      until(due);
    }
#pragma nounroll
    for (int spin = 0; spin < SPIN_MAX; ++spin) {
      CFENCE();
      xa0 = __builtin_amdgcn_raw_buffer_load_b32(wsr, dup(G_XP + ul * 2048) + near + d * 2, 0, AUX_SC1);
      xa1 = __builtin_amdgcn_raw_buffer_load_b32(wsr, dup(G_XP + ul * 2048) + near + d * 2, 1024, AUX_SC1);
      if (all_ok(max(xa0, xa1) != SENT)) break;
      __builtin_amdgcn_s_sleep(1);
    }
    float a0 = swap16_add(lo_f(xa0), lo_f(xa1)), a1 = swap16_add(hi_f(xa0), hi_f(xa1));
    a0 += dpp<XOR8>(a0); a1 += dpp<XOR8>(a1);
    a0 = swap32_add(a0, a0); a1 = swap32_add(a1, a1);
    const bool hb = g & 1;
    f32x2 xv = {rbf(lo_f(sx2) + rbf((hb ? hi_f(gm[0][0]) : lo_f(gm[0][0])) * rbf(a0))), rbf(hi_f(sx2) + rbf((hb ? hi_f(gm[0][2]) : lo_f(gm[0][2])) * rbf(a1)))};
    f32x2 gv = {hb ? hi_f(gm[1][0]) : lo_f(gm[1][0]), hb ? hi_f(gm[1][2]) : lo_f(gm[1][2])};
    PIN(xv); PIN(gv);
    if (fw == 0 && lane == 0) __builtin_amdgcn_raw_buffer_store_b32(tag, wsr, WS_EPOCH, 0, AUX_SC1);
    STAMP(10);
    due = max(due + P.X4 * 256 + slack, (int)(((u32)wall_clock64() - t0) << 8) + P.GX * 256);
    const bool last = ul + 40 < NUP;
    u32 o0 = dup(G_OP + (ul * 512 + d) * 8) + near, o5 = last ? o0 : OOB, tg5 = last ? tag : 0u;
    const rsrc_t rout = mk_rsrc(out, 2048u);
    u32 oo = (lane & 40) == 0 ? (g * 512 + d) * 2 : OOB;
    const u32 pk = (c & 10) ? 0u : ((c & 4) ? 0x3f800000u : 0x00003f80u);
    u32x4 one = {(c & 1) ? 0u : pk, 0u, (c & 1) ? pk : 0u, 0u};
    PIN(o0); PIN(o5); PIN(tg5); PIN(oo); PIN(one);
    u32 so[6];
#pragma unroll
    for (int k = 0; k < 6; ++k) { so[k] = k * 65536; asm volatile("" : "+s"(so[k])); }
    __builtin_amdgcn_raw_buffer_store_b32(pack_bf(xv[0], xv[1]), rout, oo, 0, AUX_SC1);
    until(due);
#pragma nounroll
    for (int spin = 0; spin < SPIN_MAX; ++spin) {
      CFENCE();
      u32x4 og[6];
      og[0] = ld16<AUX_SC1>(wsr, o5, so[5]);
#pragma unroll
      for (int k = 1; k < 6; ++k) { SB(); og[k] = ld16<AUX_SC1>(wsr, o0, so[k - 1]); }
      SB();
      f32x4 sv = {0.f, 0.f, 0.f, 0.f};
#pragma unroll
      for (int k = 0; k < 6; ++k) sv = mfma16(one, og[k], sv);
      const float s0 = sv[0] + dpp<XOR8>(sv[0]), s1 = sv[1] + dpp<XOR8>(sv[1]);
      __builtin_amdgcn_raw_buffer_store_b32(pack_bf(fmaf(gv[0], s0, xv[0]), fmaf(gv[1], s1, xv[1])), rout, oo, 0, AUX_SC1);
      SB();
      u32 bad = (og[0][1] ^ tg5) | (og[0][3] ^ tg5);
#pragma unroll
      for (int k = 1; k < 6; ++k) { bad |= (og[k][1] ^ tag) | (og[k][3] ^ tag); PIN(bad); }
      if (all_ok(bad == 0u)) break;
      __builtin_amdgcn_s_sleep(1);
    }
    STAMP(7);
    asm volatile(".Lend_c:");
  }
  FINISH();
}

extern "C" void kernel_launch(void* const* d_in, const int* in_sizes, int n_in,
                              void* d_out, int out_size, void* d_ws, size_t ws_size,
                              hipStream_t stream) {
  fused_kernel<<<256, 512, 0, stream>>>((const u16*)d_in[0], (const u16*)d_in[1], (const u16*)d_in[2], (const u16*)d_in[3], (const u16*)d_in[4],
                                        (const u16*)d_in[6], (const u16*)d_in[7], (const u16*)d_in[8], (const u16*)d_in[9], (const u16*)d_in[10],
                                        (unsigned char*)d_ws, (u16*)d_out
    );
}
```

```cpp
#include <hip/hip_runtime.h>
#include <stdint.h>

typedef unsigned int u32;
typedef unsigned short u16;
typedef unsigned long long u64;
typedef __attribute__((ext_vector_type(4))) unsigned u32x4;
typedef __attribute__((ext_vector_type(2))) unsigned u32x2;
typedef __attribute__((ext_vector_type(2))) __bf16 bf16x2;
typedef __attribute__((ext_vector_type(4))) float f32x4;
typedef __attribute__((ext_vector_type(2))) float f32x2;
typedef __amdgpu_buffer_rsrc_t rsrc_t;

constexpr int NH = 32, SK = 16384, DH = 128, WIN = 4096, FF = 1376;
struct Prm { int E0, ES, E1, E2, E2C, EU, TEND, DT_UP, L0, L1, X1, X2, X3, X4, NE, FE, M0, M1, M2, M3, M4, GS, GX, N1, N2, BT, WD, DT_WO, LW, LU, EW, PM, WM, FK, FW, FU, EX, TS, DS, A0, AG, AC, PT, SL, TH, DT_0, L3; };
constexpr Prm PRM0 = {32, 24, 32, 32, 48, 64, 2000, 96, 20, 14, 72, 78, 98, 86, 5, 12, 12, 5, 12, 32, 28, 34, 34, 18, 45, 10, 4, 103, 40, 40, 8, 0x6827941, 0x53, 62, 61, 60, 8, 500, 122, 55, 26, 120, 160, 10, 6, 25, 15};
#define PRM_ARG
#define TABP (&TAB.e[0])
constexpr int NL = 37;
constexpr int NCMIN = 192;
constexpr int WK = 6, WV = 5;
constexpr int WUSE = 13, LNS = 11;
constexpr int QP1 = 12, QP2 = 18, QD = 5;
constexpr int VQ = 6;
constexpr int SLC[] = {0, 6, 9, 15, 21, 27, 33, 40}, NSL = sizeof(SLC) / sizeof(int) - 1;
constexpr int VS = VQ + NSL;
constexpr int MLS = VS + 2;
constexpr int VSIDE = VQ + 1, UPREP = 21;
constexpr int SIDE = NCMIN / 8 - 4;
constexpr int UD = 1;
constexpr int CPF = 18;
constexpr int TBP = 7, TBL = 11, TBU = 16;
constexpr int AR = 0, AL = 10;
constexpr int VL = 0, VU = 5;
constexpr int D0 = 30;
constexpr int KF = 4, KF0 = 8;
constexpr int side_k(int i) { return (i == KF || i == KF + 1) ? 3 : (i == TBL ? 1 : 0) + ((i == QP1 || i == QP2) ? 2 : 0); }
constexpr int side_v(int v) { return v == VQ - 4 ? 2 : (v == VSIDE ? 4 : ((v == AL || v == VL) ? 1 : 0)); }
constexpr int win_k(int i) { int n = WK; for (int j = i - WK; j <= i; ++j) n += side_k(j); return n; }
constexpr int win_v(int v, int w) { int n = w; for (int j = v - w; j <= v; ++j) n += side_v(j); return n; }
constexpr int cons_end(int v) {
  const int a = v - WV + 1, c = (v - VS) * 3;
  const int m = a < c ? a : c;
  return m < 0 ? 0 : (m > NL ? NL : m);
}
struct Tab { u32 e[1024]; };
#define popc __builtin_popcount
__host__ __device__ constexpr u32 place(u32 v, u32 m, int n) {
  u32 r = 0;
  for (int k = 0; k < n; ++k) r |= (v >> k & 1u) << (10 + (m >> (4 * k) & 15));
  return r;
}
__host__ __device__ constexpr u32 step_off(const Prm& P, u32 s0, u32 bh) {
  const u32 src = (s0 >> 5 & 3u) | bh << 2;
  return place(s0 ^ ((popc(src & P.M0) & 1) | (popc(src & P.M1) & 1) << 1 | (popc(src & P.M2) & 1) << 2 | (popc(src & P.M3) & 1) << 3 | (popc(src & P.M4) & 1) << 4), P.PM, 7);
}
constexpr Tab make_tab(const Prm& P) {
  Tab T{};
  for (int bid = 0; bid < 256; ++bid) {
    const int xcd = bid & 7, idx = bid >> 3, part = idx & 3, i8 = idx >> 2, wi = i8 * 8 + xcd;
    const bool has_mod = wi < 48, has_up = wi >= 21;
    const int cb0 = (i8 + 5) & 7, cb1 = (i8 + 4) & 7, ms = wi - 20;
    const int mi = wi < 8 ? wi : (wi < 16 ? 8 + ((xcd + 4) & 7) : (wi < 20 ? (wi - 14) * 8 + 3 : 16 + ms / 7 * 8 + ms % 7 + (ms % 7 >= 3 ? 1 : 0)));
    const int ui = xcd * 5 + (xcd > 5 ? xcd - 5 : 0) + (i8 >= 3 ? (xcd >= 5 ? 1 : 0) + i8 - 3 : 0);
    const int wd = cb0 == 3 ? 0 : P.WD;
    const int e[4] = {P.E0 - 4 * wd + (cb0 == 3 ? P.ES : 0), P.E1 + (cb1 == 3 ? P.ES : 0), has_mod ? (wi < 16 ? P.E2C : P.E2) + ((mi & 7) == 3 ? P.ES : 0) : 0, has_up ? P.EU * (8 - UD) / 8 : 0};
    const int ew = wd ? P.EW : 0, pa = (1024 + e[0] + ew + e[1] + e[2] + e[3] + (has_up ? P.EX : 0)) / 4;
    int c[5] = {0, 0, 0, 0, 128};
    for (int k = 0; k < 3; ++k) c[k + 1] = c[k] + (pa - e[k] - (k == 0 ? ew : 0) + 4) / 8;
    const int nl = c[part + 1] - c[part], wst = e[part] / 4;
    const bool has_w = part < 2 || (part == 2 ? has_mod : has_up);
    const int blk = !has_w ? 0 : (part == 0 ? cb0 : (part == 1 ? xcd * 8 + cb1 : (part == 2 ? mi : ui)));
    const int span = (part == 3 && has_up ? P.TEND - P.DT_UP : (part == 0 ? P.TEND - (wd ? P.DT_WO : P.DT_0) : P.TEND)) * 256;
    const int np = part == 0 ? 8 - wd : (part == 3 ? 8 - UD : 8), tau0 = span / (2 * nl + wst);
    const bool up = part == 3 && has_up;
    const int tau = tau0 * (up ? P.FU : (has_w && np == 8 ? P.FK : 64)) / 64, tauw = tau0 * wst / np * (up ? P.FU : P.FW) / 64;
    int sw = KF, at = P.DS * 256 + (has_w ? np * tauw : 0);
    while (sw < NCMIN / 8 - 1 && at < P.TS * 256) { at += tau; ++sw; }
    const int tau2 = (span - at) / (2 * nl - sw);
    T.e[4 * bid] = (u32)(c[part] | nl << 7 | blk << 13 | (has_w ? 1 : 0) << 19 | (8 - np) << 20 | sw << 24);
    T.e[4 * bid + 1] = (u32)(tau | tauw << 16);
    if ((part == 1 && nl <= QP2 + QD + 1) || c[part] > 32 * part + KF0 || c[part + 1] < 32 * part + KF0 + 4) T.e[4 * bid] = 0;
    const u32 pitch = part == 0 ? 1024u : (part == 1 ? 8192u : (part == 2 ? 6144u : 2752u));
    T.e[4 * bid + 2] = (part == 0 ? xcd * 512u * pitch : 0u) + blk * (part == 3 ? 64u : 128u);
    T.e[4 * bid + 3] = pitch | (u32)tau2 << 16;
  }
  return T;
}
constexpr int step_range(const Prm& P, bool mx_) {
  int mx = 0, mn = 1 << 20;
  const Tab T = make_tab(P);
  for (int bid = 0; bid < 256; ++bid) { const int n = T.e[4 * bid] >> 7 & 63; mx = n > mx ? n : mx; mn = n < mn ? n : mn; }
  return mx_ ? mx : mn;
}
__constant__ __attribute__((aligned(64))) const Tab TAB = make_tab(PRM0);
static_assert(step_range(PRM0, true) <= NL && step_range(PRM0, false) >= NCMIN / 8, "a wave's key range does not fit its load steps");
static_assert(cons_end(SIDE + WV + 1) <= NCMIN / 8 && cons_end(UPREP) <= NCMIN / 8 && SIDE > MLS + 3, "steps every wave runs");
constexpr int NUP = FF / 32;
constexpr int SPIN_MAX = 1 << 17;

constexpr u32 WS_EPOCH = 0;
constexpr u32 G_TB = 1024;
constexpr u32 G_NT = 2048;
constexpr u32 G_MOD = 4096;
constexpr u32 G_Q = G_MOD + 3072 * 8;
constexpr u32 G_AG = G_Q + 4096 * 8;
constexpr u32 G_ML = G_AG + 4096;
constexpr u32 G_P = 81920;
constexpr u32 G_U = G_P + 65536;
__device__ __forceinline__ u32 dup(u32 o) { return G_U + ((o >> 13) << 14) + (o & 8191u); }
constexpr u32 G_XP = 0;
constexpr u32 G_OP = G_XP + 8 * 512 * 4;
constexpr u32 G_PB = G_U + 2 * (G_OP + 44 * 512 * 8);
constexpr u32 WS_END = G_PB + 16384;
static_assert(G_P % 16384 == 0 && G_PB % 16384 == 0 && G_ML + 4096 <= G_P, "layout");
constexpr u32 SENT = 0xffffffffu;
constexpr u32 OOB = 0x40000000u;
#define WST 0

constexpr int AUX_NT = 2, AUX_SC1 = 16;

__device__ __forceinline__ float lo_f(u32 v) { return __uint_as_float(v << 16); }
__device__ __forceinline__ float hi_f(u32 v) { return __uint_as_float(v & 0xffff0000u); }
__device__ __forceinline__ float rbf(float x) { return (float)(__bf16)x; }
__device__ __forceinline__ u32 pack_bf(float lo, float hi) {
  bf16x2 r; r[0] = (__bf16)lo; r[1] = (__bf16)hi; return __builtin_bit_cast(u32, r);
}
__device__ __forceinline__ float dot2(u32 k, u32 q, float acc) {
  return __builtin_amdgcn_fdot2_f32_bf16(__builtin_bit_cast(bf16x2, k), __builtin_bit_cast(bf16x2, q), acc, false);
}
template <int CTRL> __device__ __forceinline__ float dpp(float x) {
  return __builtin_bit_cast(float, __builtin_amdgcn_mov_dpp(__builtin_bit_cast(int, x), CTRL, 0xf, 0xf, true));
}
constexpr int XOR1 = 0xB1, XOR2 = 0x4E, XOR7 = 0x141, XOR8 = 0x128;
__device__ __forceinline__ float swap32_add(float a, float b) {
  auto t = __builtin_amdgcn_permlane32_swap(__float_as_uint(a), __float_as_uint(b), false, false);
  return __uint_as_float(t[0]) + __uint_as_float(t[1]);
}
__device__ __forceinline__ float swap16_add(float a, float b) {
  auto t = __builtin_amdgcn_permlane16_swap(__float_as_uint(a), __float_as_uint(b), false, false);
  return __uint_as_float(t[0]) + __uint_as_float(t[1]);
}
__device__ __forceinline__ float wave_sum(float x) {
  x += dpp<XOR1>(x); x += dpp<XOR2>(x); x += dpp<XOR7>(x); x += dpp<XOR8>(x);
  x = swap16_add(x, x);
  return swap32_add(x, x);
}
__device__ __forceinline__ float wave_max(float x) {
  x = fmaxf(x, dpp<XOR1>(x)); x = fmaxf(x, dpp<XOR2>(x)); x = fmaxf(x, dpp<XOR7>(x)); x = fmaxf(x, dpp<XOR8>(x));
  auto s = __builtin_amdgcn_permlane16_swap(__float_as_uint(x), __float_as_uint(x), false, false);
  x = fmaxf(__uint_as_float(s[0]), __uint_as_float(s[1]));
  auto t = __builtin_amdgcn_permlane32_swap(__float_as_uint(x), __float_as_uint(x), false, false);
  return fmaxf(__uint_as_float(t[0]), __uint_as_float(t[1]));
}
template <int CTRL> __device__ __forceinline__ float sel_add(float lo, float hi, bool up) {
  const float t1 = lo + dpp<CTRL>(lo), t2 = hi + dpp<CTRL>(hi);
  return up ? t2 : t1;
}
template <int I> __device__ __forceinline__ float bc16(float x) {
  return __builtin_bit_cast(float, __builtin_amdgcn_update_dpp(0, __builtin_bit_cast(int, x), 0x150 + I, 0xf, 0xf, false));
}
__device__ __forceinline__ float bcast16(float x, int i) {
  switch (i) {
    case 0: return bc16<0>(x); case 1: return bc16<1>(x); case 2: return bc16<2>(x); case 3: return bc16<3>(x);
    case 4: return bc16<4>(x); case 5: return bc16<5>(x); case 6: return bc16<6>(x); case 7: return bc16<7>(x);
    case 8: return bc16<8>(x); case 9: return bc16<9>(x); case 10: return bc16<10>(x); case 11: return bc16<11>(x);
    case 12: return bc16<12>(x); case 13: return bc16<13>(x); case 14: return bc16<14>(x); default: return bc16<15>(x);
  }
}
__device__ __forceinline__ float silu_f(float x) { return x * __builtin_amdgcn_rcpf(1.f + __expf(-x)); }

template <int AUX> __device__ __forceinline__ u32x4 ld16(rsrc_t r, u32 voff, u32 soff) {
  return __builtin_amdgcn_raw_buffer_load_b128(r, voff, soff, AUX);
}
template <int AUX> __device__ __forceinline__ u32x2 ld8(rsrc_t r, u32 voff, u32 soff) {
  return __builtin_amdgcn_raw_buffer_load_b64(r, voff, soff, AUX);
}
__device__ __forceinline__ void st8_sc1(rsrc_t r, u32 voff, u32 v, u32 tag) {
  __builtin_amdgcn_raw_buffer_store_b64(u32x2{v, tag}, r, voff, 0, AUX_SC1);
}
__device__ __forceinline__ void st16_sc1(rsrc_t r, u32 voff, u32 v0, u32 v1, u32 tag) {
  __builtin_amdgcn_raw_buffer_store_b128(u32x4{v0, tag, v1, tag}, r, voff, 0, AUX_SC1);
}
__device__ __forceinline__ rsrc_t mk_rsrc(const void* p, u32 bytes) {
  return __builtin_amdgcn_make_buffer_rsrc(const_cast<void*>(p), 0, bytes, 0x00020000);
}
#define WAIT_VM(n) __builtin_amdgcn_s_waitcnt(((n) & 15) | (((n) >> 4) << 14) | (7 << 4) | (15 << 8))
#define SB() __builtin_amdgcn_sched_barrier(0)
#define CFENCE() asm volatile("" ::: "memory")
__device__ __forceinline__ bool all_ok(bool ok) { return __builtin_amdgcn_ballot_w64(!ok) == 0ull; }

template <int N> __device__ __forceinline__ void gather16(rsrc_t ws, u32 voff, u32 stride, u32 tag, u32x4 (&g)[N]) {
#pragma nounroll
  for (int spin = 0; spin < SPIN_MAX; ++spin) {
    CFENCE();
    bool ok = true;
#pragma unroll
    for (int i = 0; i < N; ++i) g[i] = ld16<AUX_SC1>(ws, voff + i * stride, 0);
#pragma unroll
    for (int i = 0; i < N; ++i) ok &= (g[i][1] == tag) & (g[i][3] == tag);
    if (all_ok(ok)) break;
    __builtin_amdgcn_s_sleep(8);
  }
}
template <int N> __device__ __forceinline__ void gather8(rsrc_t ws, u32 voff, u32 stride, u32 tag, u32 (&g)[N], bool act = true) {
#pragma nounroll
  for (int spin = 0; spin < SPIN_MAX; ++spin) {
    CFENCE();
    bool ok = true;
    u32x2 t[N];
#pragma unroll
    for (int i = 0; i < N; ++i) t[i] = ld8<AUX_SC1>(ws, voff + i * stride, 0);
#pragma unroll
    for (int i = 0; i < N; ++i) { ok &= (t[i][1] == tag); g[i] = t[i][0]; }
    if (all_ok(ok | !act)) break;
    __builtin_amdgcn_s_sleep(8);
  }
}

struct Smem {
  float red[8][128];
  float ml[2][8];
  float r2[64 * 20];
  u16 hb[3 * 1024];
  float stat[8][64];
  float red2[8][128];
  u32 dummy[64];
  u32x2 bc;
  u32x2 ctl;
  u32 nm, pad_[3];
  u32x4 zero[4];
  u32x4 wbuf[8][8][64];
  __attribute__((aligned(256))) u16 w2b[32][512];
  u16 actl[2][32];
};

#define STAMP(k) do {} while (0)
#define WSTAMP(k) do {} while (0)
#define FINISH() return
#define PIN(x) asm volatile("" : "+v"(x))
#define LDS_BARRIER() do { __builtin_amdgcn_s_waitcnt(0xc07f); __builtin_amdgcn_s_barrier(); CFENCE(); } while (0)
#define WAVE_SYNC() do { __builtin_amdgcn_fence(__ATOMIC_RELEASE, "wavefront"); __builtin_amdgcn_wave_barrier(); __builtin_amdgcn_fence(__ATOMIC_ACQUIRE, "wavefront"); } while (0)

__device__ __forceinline__ void dma16(rsrc_t r, u32 voff, u32 soff, u32 lds_dst) {
  u32 keep;
  asm volatile("s_mov_b32 %0, m0\n\ts_mov_b32 m0, %4\n\ts_nop 4\n\tbuffer_load_dwordx4 %1, %2, %3 offen nt lds\n\ts_mov_b32 m0, %0"
               : "=&s"(keep) : "v"(voff), "s"(r), "s"(soff), "s"(lds_dst) : "memory");
}
__device__ __forceinline__ u32 perm_lo(u32 a, u32 b) { return __builtin_amdgcn_perm(b, a, 0x05040100u); }
__device__ __forceinline__ u32 perm_hi(u32 a, u32 b) { return __builtin_amdgcn_perm(b, a, 0x07060302u); }
__device__ __forceinline__ float rdl(float x, int l) { return __int_as_float(__builtin_amdgcn_readlane(__float_as_int(x), l)); }

typedef __attribute__((ext_vector_type(8))) __bf16 bf16x8;
__device__ __forceinline__ f32x4 mfma16(u32x4 a, u32x4 b, f32x4 c) {
  return __builtin_amdgcn_mfma_f32_16x16x32_bf16(__builtin_bit_cast(bf16x8, a), __builtin_bit_cast(bf16x8, b), c, 0, 0, 0);
}
__device__ __forceinline__ float sum8(const float* p) {
  const f32x4 a = *reinterpret_cast<const f32x4*>(p), b = *reinterpret_cast<const f32x4*>(p + 4);
  return ((a[0] + b[0]) + (a[1] + b[1])) + ((a[2] + b[2]) + (a[3] + b[3]));
}
__device__ __forceinline__ float ln_ri(float var) {
  return __builtin_amdgcn_rcpf(rbf(__builtin_amdgcn_sqrtf(rbf(rbf(fmaxf(var, 0.f)) + 1e-5f))));
}
__device__ __forceinline__ void ln_stats2(const float (&x0)[8], const float (&x1)[8], float& mb0, float& ri0, float& mb1, float& ri1) {
  const float k0 = rdl(x0[0], 0), k1 = rdl(x1[0], 0);
  float s0 = 0.f, s1 = 0.f, q0 = 0.f, q1 = 0.f;
#pragma unroll
  for (int k = 0; k < 8; ++k) {
    const float d0 = x0[k] - k0, d1 = x1[k] - k1;
    s0 += d0; s1 += d1; q0 = fmaf(d0, d0, q0); q1 = fmaf(d1, d1, q1);
  }
  float t = swap16_add(swap32_add(s0, s1), swap32_add(q0, q1));
  t += dpp<XOR1>(t); t += dpp<XOR2>(t); t += dpp<XOR7>(t); t += dpp<XOR8>(t);
  auto u = __builtin_amdgcn_permlane16_swap(__float_as_uint(t), __float_as_uint(t), false, false);
  const float ms = __uint_as_float(u[0]) * (1.f / 512.f), mq = __uint_as_float(u[1]) * (1.f / 512.f);
  const float ri = ln_ri(fmaf(-ms, ms, mq));
  mb0 = rbf(k0 + rdl(ms, 0)); mb1 = rbf(k1 + rdl(ms, 32));
  ri0 = rdl(ri, 0); ri1 = rdl(ri, 32);
}
template <int I> struct IC { static constexpr int v = I; };
template <int I, int N, class F> __device__ __forceinline__ void sfor(F&& f) {
  if constexpr (I < N) { if (f(IC<I>{})) sfor<I + 1, N>(f); }
}
typedef __attribute__((ext_vector_type(4))) short s16x4;
typedef __attribute__((address_space(3))) s16x4* lds_s16x4p;
__device__ __forceinline__ u32x2 tr_read(u32 lds_addr) {
  return __builtin_bit_cast(u32x2, __builtin_amdgcn_ds_read_tr16_b64_v4i16((lds_s16x4p)lds_addr));
}
__device__ __forceinline__ u32 next_tag(u32 e) { return ((e | 0x40004000u) + 1u) & ~0x40004000u; }
__device__ __forceinline__ float u2f(u32 v) { return __uint_as_float(v); }
#define LDSP(T, a) ((__attribute__((address_space(3))) T*)(a))
template <class T> __device__ __forceinline__ u32 lds_addr(T* p) { u32 a = (u32)(uintptr_t)p; PIN(a); return a; }

typedef __attribute__((ext_vector_type(16))) unsigned u32x16;
typedef __attribute__((ext_vector_type(8))) unsigned u32x8;
__global__ void __launch_bounds__(512, 2) fused_kernel(const u16* x_, const u16* kc_, const u16* vc_, const u16* wq_, const u16* wo_, const u16* w1_,
                                                       const u16* w3_, const u16* w2_, const u16* cnd_, const u16* wmod_, unsigned char* ws_, u16* out_ PRM_ARG) {
  __shared__ __attribute__((aligned(256))) Smem sm;
  constexpr Prm P = PRM0;
  const int TEND = P.TEND;
  const int bid = blockIdx.x, tid = threadIdx.x;
  u32x4 kr[NL];
  u32x16 ka; u32x8 kb; u32x4 tev; u64 pt0; u32 tch2;
  asm volatile("s_load_dwordx16 %0, %3, 0x0\n\ts_load_dwordx8 %1, %3, 0x40\n\ts_memrealtime %2"
               : "=&s"(ka), "=&s"(kb), "=&s"(pt0) : "s"(__builtin_amdgcn_kernarg_segment_ptr()));
  SB();
  {
    u64 pc; u32 vb;
    asm volatile("s_getpc_b64 %2\n.Lcpf_first:\n\tv_mul_u32_u24 %1, ((.Lend_c-.Lcpf_first)>>10)<<2, %3\n\tglobal_load_dword %0, %1, %2"
                 : "=&v"(tch2), "=&v"(vb), "=&s"(pc) : "v"((bid & ~7) + (tid >> 6)) : "memory");
    asm volatile("global_load_dwordx4 %0, %1, %2" : "=&v"(tev) : "v"(bid << 4), "s"(TABP) : "memory");
  }
  SB();
  const int xcd = bid & 7, idx = bid >> 3, part = idx & 3;
  const int b = idx >> 2 & 1, head = xcd * 4 + (((idx >> 3) & 1) << 1 | idx >> 4);
  const u32 so0 = step_off(P, 32 * part + KF0, b * 32 + head) & ~place(3, P.PM, 2);
  u32 va = (((b * 32 + head) * SK + (SK - WIN)) * DH * 2 + so0) | place(tid >> 6, P.WM, 3) | (tid & 63) << 4;
  const u32 kx1 = place(1, P.PM, 2), kx2 = place(2, P.PM, 2);
  PIN(va);
  u32 va1 = va | kx1, va2 = va | kx2, va3 = va | kx1 | kx2;
  PIN(va1); PIN(va2); PIN(va3);
  int mc = bid & 0xd8;
  asm volatile("" : "+v"(va), "+s"(mc));
  SB();
  asm volatile("s_waitcnt lgkmcnt(0)" : "+s"(ka), "+s"(kb), "+s"(pt0));
  SB();
#define K_FIRST "global_load_dwordx4 %0, %4, %5 nt\n\tglobal_load_dwordx4 %1, %6, %5 nt\n\tglobal_load_dwordx4 %2, %7, %5 nt\n\tglobal_load_dwordx4 %3, %8, %5 nt"
  asm volatile("s_cmp_eq_u32 %9, 16\n\ts_cbranch_scc1 .Lk_later\n\t" K_FIRST "\n.Lk_later:"
               : "=&v"(kr[0]), "=&v"(kr[1]), "=&v"(kr[2]), "=&v"(kr[3]) : "v"(va), "s"((u64)ka[2] | (u64)ka[3] << 32), "v"(va1), "v"(va2), "v"(va3), "s"(mc) : "memory", "scc");
  SB();
  const u32 tka = (u32)wall_clock64(), t0 = tka - D0;
  auto kap = [&](int k) { return k < 8 ? (u64)ka[2 * k] | (u64)ka[2 * k + 1] << 32 : (u64)kb[2 * k - 16] | (u64)kb[2 * k - 15] << 32; };
  const u16 *x = (const u16*)kap(0), *kc = (const u16*)kap(1), *vc = (const u16*)kap(2), *w2 = (const u16*)kap(7), *cnd = (const u16*)kap(8);
  unsigned char* ws = (unsigned char*)kap(10); u16* out = (u16*)kap(11);
  const int lane = tid & 63, wave = __builtin_amdgcn_readfirstlane(tid >> 6);
  const bool p3 = part == 3;
  const u64 wa = part == 0 ? kap(4) : (part == 1 ? kap(3) : (part == 2 ? kap(9) : ((wave & 1) ? kap(6) : kap(5))));
  const bool modc = mc == 16;
  if (modc) asm volatile("s_waitcnt vmcnt(0)" ::: "memory");
  asm volatile("s_waitcnt vmcnt(4)" : "+v"(tev));
  const u32 te0 = __builtin_amdgcn_readfirstlane(tev[0]), te1 = __builtin_amdgcn_readfirstlane(tev[1]);
  const u32 te3 = __builtin_amdgcn_readfirstlane(tev[3]), pitch = te3 & 0xffff, wsz = pitch << (part == 0 ? 12 : 9);
  const int sh = p3 ? 2 : 3;
  u32 wv0 = (u32)((p3 ? (wave >> 1) * 16 : wave * 64) + (lane >> sh)) * pitch + ((lane & (p3 ? 3 : 7)) ^ (lane >> (p3 ? 5 : 3)) ^ (p3 ? wave >> 2 << 1 : 0)) * 16u;
  const u32 wimg = (u32)(uintptr_t)(&sm.wbuf[0][0][0]);
  u32 wld = wimg + (p3 ? (wave & 1) * 32768 + (wave >> 1) * 1024 : wave * 8192);
  const int bl0 = te0 & 127, nl = te0 >> 7 & 63, blk = te0 >> 13 & 63;
  const bool has_w = te0 >> 19 & 1;
  const rsrc_t rw = mk_rsrc((const void*)wa, wsz);
  const u32 wstep = pitch << (p3 ? 6 : 3), lstep = p3 ? 4096u : 1024u;
  u32 wso = __builtin_amdgcn_readfirstlane(tev[2]);
  int wleft = 8;
  auto piece = [&]() {
    dma16(rw, wv0, wso, wld);
    wso += wstep; wld += lstep;
    --wleft;
  };
  int tau = te1 & 0xffff;
  const int tauw = te1 >> 16, sw = te0 >> 24;
  int tau2 = te3 >> 16;
  int due = P.DS * 256, now = 0;
  auto gate = [&]() {
    while (now < due) { now = (int)(((u32)wall_clock64() - t0) << 8); if (now < due) __builtin_amdgcn_s_sleep(1); }
    due += tau;
  };
  const rsrc_t wsr = mk_rsrc(ws, WS_END);
  const int g = lane >> 4, c = lane & 15;
  const int i8 = idx >> 2, wi = i8 * 8 + xcd;
  const bool is_up = p3 && has_w;
  const size_t base = ((size_t)(b * NH + head) * SK + (SK - WIN)) * DH;
  const rsrc_t rk = mk_rsrc(kc + base, WIN * DH * 2), rv = mk_rsrc(vc + base, WIN * DH * 2);
  u32 sot;
  {
    u32 s0 = bl0 + lane - KF;
    s0 += s0 >= (u32)(32 * part + KF0) ? KF : 0;
    sot = (lane < KF ? so0 | place(lane, P.PM, 2) : step_off(P, s0, b * 32 + head)) | place(wave, P.WM, 3);
    sot = lane < nl ? sot : OOB;
    PIN(sot);
  }
  auto until = [&](int t) { while ((int)(((u32)wall_clock64() - t0) << 8) < t); };
  u32 near = 0, nmask = 0;
  int slack = 0;
  const rsrc_t rx = mk_rsrc(part == 2 ? cnd : x, (part == 1 || (part == 2 && has_w)) ? 2048u : 0u);
  u32x2 ep; u32 xs0, xs1, tch1; u32x4 xr0, xr1;
  auto small_ops = [&]() {
    ep = ld8<AUX_SC1>(wsr, wave == 7 ? WS_EPOCH : OOB, 0);
    xs0 = __builtin_amdgcn_raw_buffer_load_b16(rx, tid * 2, 0, 0); xs1 = __builtin_amdgcn_raw_buffer_load_b16(rx, tid * 2, 1024, 0);
  };
  auto small_ops2 = [&]() {
    xr0 = ld16<0>(rx, part == 1 ? lane * 16 : OOB, 0); xr1 = ld16<0>(rx, part == 1 ? lane * 16 : OOB, 1024);
    tch1 = __builtin_amdgcn_raw_buffer_load_b32(mk_rsrc(w2 + (size_t)blk * 32 * 512, is_up ? 64u : 0u), tid == 384 ? 0 : OOB, 0, AUX_NT);
  };
  u32 tag = 0;
  u32x4 bf[8];
  auto wo_prep = [&](int s0 = 0, int s1 = 2) {
    const u32 wi_ = wimg + wave * 8192;
    const int q = (lane >> 2) & 3, pp = lane & 3;
#pragma unroll
    for (int s = s0; s < s1; ++s) {
#pragma unroll
      for (int t = 0; t < 4; ++t) {
        const int r0 = s * 32 + g * 8 + q, r1 = r0 + 4, ch = t * 2 + (pp >> 1);
        const u32x2 lo = tr_read(wi_ + r0 * 128 + ((ch ^ (r0 & 7)) * 16) + (pp & 1) * 8), hi = tr_read(wi_ + r1 * 128 + ((ch ^ (r1 & 7)) * 16) + (pp & 1) * 8);
        bf[s * 4 + t] = u32x4{lo[0], lo[1], hi[0], hi[1]};
      }
    }
  };
  auto gemv_go = [&](u32 hh) {
    sm.hb[tid] = (u16)hh; sm.hb[512 + tid] = (u16)(hh >> 16);
    WAVE_SYNC();
    const u32x4* a = (c & 11) == 0 ? reinterpret_cast<const u32x4*>(&sm.hb[(c >> 2) * 512 + wave * 64 + g * 8]) : &sm.zero[0];
    const u32x4 a0 = a[0], a1 = a[(c & 11) == 0 ? 4 : 0];
    const f32x4 z = {0.f, 0.f, 0.f, 0.f};
    f32x4 d[4];
#pragma unroll
    for (int t = 0; t < 4; ++t) d[t] = mfma16(a0, bf[t], z);
#pragma unroll
    for (int t = 0; t < 4; ++t) d[t] = mfma16(a1, bf[4 + t], d[t]);
    if (lane < 32) {
#pragma unroll
      for (int t = 0; t < 4; ++t) sm.r2[(t * 16 + c) * 20 + g * 8 + wave] = d[t][0];
    }
  };
  auto mod_gemv = [&]() { gemv_go(pack_bf(silu_f(lo_f(xs0)), silu_f(lo_f(xs1)))); };
  auto mod_put = [&]() { st8_sc1(wsr, G_MOD + (blk * 64 + lane) * 8, pack_bf(sum8(&sm.r2[lane * 20]), sum8(&sm.r2[lane * 20 + 8])), tag); };
  auto zero_rows = [&]() { if (lane < 4) sm.zero[lane] = u32x4{0u, 0u, 0u, 0u}; };
  u32 l16 = lane * 16;
  asm volatile("" : "+v"(l16), "+v"(wv0) : "s"(rk), "s"(rx), "s"(wsr));
  SB();
  if (has_w) {
    if (__builtin_expect(modc, 0)) {
      now = 1 << 30;
      const int sg_ = lane >> 3;
      const u32 xl = sg_ < 2 ? G_ML + wi * 256 + sg_ * 128 : (sg_ < 4 ? G_PB + wi * 128 + (sg_ & 1) * 8192 : (sg_ == 4 ? G_NT + wi * 128 : (sg_ == 5 && wi < 3 ? G_AG + wi * 128 : OOB)));
      __builtin_amdgcn_raw_buffer_store_b128(u32x4{SENT, SENT, SENT, SENT}, wsr, tid < 384 ? G_P + wi * 6144 + tid * 16 : (wave == 7 ? xl + (lane & 7) * 16 : OOB), 0, AUX_SC1);
    }
#pragma nounroll
    while (wleft > (int)(te0 >> 20 & 7)) { gate(); due += tauw - tau; piece(); }
    now = 0;
  }
  SB();
  STAMP(23);
  if (__builtin_expect(modc, 0)) {
    small_ops();
    zero_rows();
    {
      until(P.PT * 256);
      const u64 pa = (u64)ws + G_PB + (((wi & 8) + wave + xcd * 2) & 15) * 128 + (wave & 1) * 8192;
      u64 c0, c1, c2; u32 d0, d1;
      asm volatile("s_memtime %0\n\ts_waitcnt lgkmcnt(0)\n\ts_load_dword %3, %5, 0x0\n\ts_waitcnt lgkmcnt(0)\n\ts_memtime %1\n\ts_waitcnt lgkmcnt(0)\n\t"
                   "s_load_dword %4, %6, 0x0\n\ts_waitcnt lgkmcnt(0)\n\ts_memtime %2\n\ts_waitcnt lgkmcnt(0)"
                   : "=&s"(c0), "=&s"(c1), "=&s"(c2), "=&s"(d0), "=&s"(d1) : "s"(pa), "s"(pa + 8192 - (wave & 1) * 16384) : "memory");
      const int df = min(max((int)(2 * (u32)c1 - (u32)c0 - (u32)c2), -600), 600);
      if (lane == 0) sm.stat[0][wave] = (float)(((wave ^ (xcd >> 2)) & 1) ? -df : df);
    }
    asm volatile("s_waitcnt vmcnt(0)" ::: "memory");
    tag = next_tag(__builtin_amdgcn_readfirstlane(ep[0]));
    wo_prep();
    mod_gemv();
    LDS_BARRIER();
    if (wave == 7) { mod_put(); if (lane == 0) st8_sc1(wsr, G_AG + (48 + wi) * 8, __float_as_uint(sum8(&sm.stat[0][0])), tag); }
    STAMP(9);
    __builtin_amdgcn_s_barrier();
    asm volatile(K_FIRST :: "v"(kr[0]), "v"(kr[1]), "v"(kr[2]), "v"(kr[3]), "v"(va), "s"((u64)ka[2] | (u64)ka[3] << 32), "v"(va1), "v"(va2), "v"(va3) : "memory");
  }
  SB();
  const bool fin = (part == 1 || part == 2) && wave == 0 && xcd < 2;
  const int fw = i8 * 4 + (part & 2) + xcd, fd = fw * 16 + (lane & 7) * 2;
  const int ub = wave >> 2, ud = (wave & 3) * 128 + c * 8 + g * 2;
  u32x4 sg[3]; u32 sx2;
  auto side_loads = [&]() {
    sg[0] = ld16<AUX_SC1>(wsr, is_up ? G_MOD + (1024 + ud) * 8 : (fin ? G_MOD + (1024 + fd) * 8 : OOB), 0);
    sg[1] = ld16<AUX_SC1>(wsr, is_up ? G_MOD + (1536 + ud) * 8 : (fin ? G_MOD + (2560 + fd) * 8 : OOB), 0);
    sg[2] = ld16<AUX_SC1>(wsr, is_up ? G_MOD + (2048 + ud) * 8 : OOB, 0);
    sx2 = __builtin_amdgcn_raw_buffer_load_b32(mk_rsrc(x, (is_up || fin) ? 2048u : 0u), is_up ? ub * 1024 + ud * 2 : (g & 1) * 1024 + fd * 2, 0, 0);
  };
  auto side_pin = [&]() { PIN(sg[0]); PIN(sg[1]); PIN(sg[2]); PIN(sx2); };
  const u32 mlo = G_ML + (head * 2 + b) * 64 + (lane & 3) * 16;
  float fwv = 0.f, fmw[8];
  bool fok = wave != 0;
  auto merge_factor = [&](const u32x4& ml) {
    const float mi = u2f(ml[0]);
    float M = fmaxf(mi, dpp<XOR1>(mi)); M = fmaxf(M, dpp<XOR2>(M));
    const float e = __expf(mi - M);
    float L = e * u2f(ml[2]);
    L += dpp<XOR1>(L); L += dpp<XOR2>(L);
    const float f = fwv * rdl(e * __builtin_amdgcn_rcpf(L), part);
#pragma unroll
    for (int w = 0; w < 8; ++w) fmw[w] = bcast16(f, w);
  };
  f32x2 ga = {0.f, 0.f}, xin = {0.f, 0.f}, cc1 = {0.f, 0.f};
  float Af = 0.f, Bf = 0.f;
  const int hpos = ub * 512 + ud;
  auto up_prep = [&]() {
    if (!all_ok((sg[0][1] == tag) & (sg[0][3] == tag) & (sg[1][1] == tag) & (sg[1][3] == tag) & (sg[2][1] == tag) & (sg[2][3] == tag))) gather16<3>(wsr, G_MOD + (1024 + ud) * 8, 512 * 8, tag, sg);
    ga = f32x2{ub ? hi_f(sg[0][0]) : lo_f(sg[0][0]), ub ? hi_f(sg[0][2]) : lo_f(sg[0][2])};
    cc1 = f32x2{rbf(1.f + (ub ? hi_f(sg[2][0]) : lo_f(sg[2][0]))), rbf(1.f + (ub ? hi_f(sg[2][2]) : lo_f(sg[2][2])))};
    xin = f32x2{lo_f(sx2), hi_f(sx2)};
    u32* hp = reinterpret_cast<u32*>(&sm.hb[hpos]);
    hp[512] = pack_bf(cc1[0], cc1[1]);
    hp[1024] = ub ? perm_hi(sg[1][0], sg[1][2]) : perm_lo(sg[1][0], sg[1][2]);
  };
  auto up_addr = [&]() {
    const int pp = lane & 3;
    return wimg + (pp & 1) * 32768 + ((pp >> 1) * 32 + g * 8 + ((lane >> 2) & 3)) * 64 + (((wave >> 1) ^ ((g & 1) | (pp & 2))) * 16) + (wave & 1) * 8;
  };
  auto up_frags = [&](int r0, int r1) {
    const u32 a = up_addr();
#pragma unroll
    for (int rb = r0; rb < r1; ++rb) {
      const u32x2 lo = tr_read(a + rb * 4096), hi = tr_read(a + rb * 4096 + 256);
      bf[rb] = u32x4{lo[0], lo[1], hi[0], hi[1]};
    }
  };
  const u32x4* ap = reinterpret_cast<const u32x4*>(&sm.hb[((lane >> 3) & 1) * 512 + (lane & 1) * 32 + g * 8]);
  const bool par = lane & 8;
  auto col_sums = [&](int r0, int r1) {
    const u32x4* a1 = ap + 128 + ((lane >> 1) & 1) * 128;
    f32x4 d0 = {0.f, 0.f, 0.f, 0.f}, d1 = {0.f, 0.f, 0.f, 0.f};
#pragma unroll
    for (int rb = r0; rb < r1; ++rb) { if (rb & 1) d1 = mfma16(a1[rb * 8], bf[rb], d1); else d0 = mfma16(a1[rb * 8], bf[rb], d0); }
    Af += par ? d0[1] + d1[1] : d0[0] + d1[0]; Bf += par ? d0[3] + d1[3] : d0[2] + d1[2];
  };
  float l0 = 0.f, l1 = 0.f;
  u32x2 gmd[2], tbg;
  const bool modw = part == 2 && has_w && !modc;
  const int qp = part != 1 ? -99 : ((blk & 7) == 3 ? QP1 : QP2);
  static_assert(QP1 + QD < QP2 && QP1 + QD > WUSE && QP1 + QD > TBU && WUSE + 1 < TBU, "order of the q steps");
  int t1 = 0;
#pragma unroll
  for (int i = NCMIN / 8; i < NL; ++i) kr[i] = u32x4{0u, 0u, 0u, 0u};
  sfor<KF, NL>([&](auto ic) {
    constexpr int i = ic.v;
    if (i >= NCMIN / 8 && i >= nl) return false;
    const u32 so = __builtin_amdgcn_readlane(sot, i);
    if constexpr (i < NCMIN / 8) tau = i == sw ? tau2 : tau;
    gate();
    SB();
    kr[i] = ld16<AUX_NT>(rk, l16, so);
    if constexpr (i == KF) small_ops();
    if constexpr (i == KF + 1) small_ops2();
    if constexpr (i == KF + 2) zero_rows();
    if constexpr (i == TBL) tbg = ld8<AUX_SC1>(wsr, wave == 7 ? G_TB + (lane & 7) * 8 : OOB, 0);
    if constexpr (i == QP1 || i == QP2) { gmd[0] = ld8<AUX_SC1>(wsr, i == qp ? G_MOD + tid * 8 : OOB, 0); gmd[1] = ld8<AUX_SC1>(wsr, i == qp ? G_MOD + tid * 8 : OOB, 4096); }
    SB();
    SB();
    if (i >= WK) { WAIT_VM(win_k(i)); SB(); }
    if (i == KF) STAMP(25);
    if (i == 8) STAMP(12);
    if (i == 20) STAMP(21);
    if (i == 25) STAMP(22);
    if (i == TBP) {
      if (part == 3 && i8 == 0 && wave == 7) { tag = next_tag(__builtin_amdgcn_readfirstlane(ep[0])); if (lane == 0) st8_sc1(wsr, G_TB + xcd * 8, tka - P.GS, tag); }
      SB();
    }
    if (i == TBU - 1) {
      if (wave == 7) {
        const bool ok = tbg[1] == tag;
        float lf = ok ? (float)min(max((int)(t0 - tbg[0]), -300), 300) : 0.f, nf = ok ? 1.f : 0.f;
        lf += dpp<XOR1>(lf); nf += dpp<XOR1>(nf); lf += dpp<XOR2>(lf); nf += dpp<XOR2>(nf);
        lf *= __builtin_amdgcn_rcpf(fmaxf(nf, 1.f));
        const float ot = dpp<XOR7>(lf);
        if (lane == 0) sm.bc = u32x2{tag, t0 - (u32)(int)((lf + ot) * 0.5f - fabsf(lf - ot) * (P.BT / 32.f))};
      }
      SB();
    }
    if (i == TBU) {
      LDS_BARRIER();
      const u32x2 bc = sm.bc;
      tag = __builtin_amdgcn_readfirstlane(bc[0]);
      t1 = (__builtin_amdgcn_readfirstlane((int)(bc[1] - t0)) + TEND + P.X1) * 256;
      const int dt = __builtin_amdgcn_readfirstlane((int)((float)(int)(t0 - bc[1]) * 256.f * __builtin_amdgcn_rcpf((float)(2 * nl - TBU - 2))));
      tau -= dt; tau2 -= dt;
      SB();
    }
    if (i == TBU + 1) {
      if (modw) { if (wave == 6) mod_put(); STAMP(9); }
      SB();
    }
    if (i == LNS) {
      tag = next_tag(__builtin_amdgcn_readfirstlane(ep[0]));
      if (part == 1) {
        float x0[8], x1[8], mb0, ri0, mb1, ri1;
#pragma unroll
        for (int k = 0; k < 4; ++k) { x0[2 * k] = lo_f(xr0[k]); x0[2 * k + 1] = hi_f(xr0[k]); x1[2 * k] = lo_f(xr1[k]); x1[2 * k + 1] = hi_f(xr1[k]); }
        ln_stats2(x0, x1, mb0, ri0, mb1, ri1);
        l0 = rbf(rbf(lo_f(xs0) - mb0) * ri0); l1 = rbf(rbf(lo_f(xs1) - mb1) * ri1);
      }
      PIN(l0); PIN(l1);
      SB();
    }
    if (i == WUSE) {
      PIN(tch1); PIN(tch2);
      static_assert(WUSE - WK >= KF, "the weight pieces are older than the steps outside the window");
      CFENCE();
      if (part == 1 || modw) wo_prep();
#pragma unroll
      for (int k = 0; k < 8; ++k) PIN(bf[k]);
      SB();
    }
    if (i == WUSE + 1) {
      if (modw) mod_gemv();
      SB();
    }
    if (i == QP1 + QD || i == QP2 + QD) {
      if (i == qp + QD) {
        u32 gm[2] = {gmd[0][0], gmd[1][0]};
        if (!all_ok((gmd[0][1] == tag) & (gmd[1][1] == tag))) { STAMP(11); gather8<2>(wsr, G_MOD + tid * 8, 512 * 8, tag, gm); }
        STAMP(5);
        gemv_go(pack_bf(rbf(l0 * rbf(1.f + lo_f(gm[1]))) + lo_f(gm[0]), rbf(l1 * rbf(1.f + hi_f(gm[1]))) + hi_f(gm[0])));
      }
      SB();
    }
    if (i == QP1 + QD + 1 || i == QP2 + QD + 1) {
      if (i == qp + QD + 1) {
        LDS_BARRIER();
        if (wave == 0) {
          const int bb = lane >> 5, jq = lane & 31;
          st8_sc1(wsr, G_Q + (bb * 2048 + blk * 32 + jq) * 8, pack_bf(sum8(&sm.r2[2 * jq * 20 + bb * 8]), sum8(&sm.r2[(2 * jq + 1) * 20 + bb * 8])), tag);
        }
        STAMP(6);
      }
      SB();
    }
    return true;
  });
  PIN(sot);
  const u32 qoff = G_Q + ((b * 32 + head) * 64 + c * 4) * 8;
  u32x4 qg[2], vr[NL], fl2[3];
  u32 qq[4];
  constexpr int NB = (NL + 15) / 16;
  float xlo, xs[4], sc[NB], pr[NB], m = 0.f;
  f32x4 acc = {0.f, 0.f, 0.f, 0.f};
  u32 pb[NB], pm[4], cpf;
  u32x2 agr;
  const u32 adst0 = lds_addr(lane < 32 ? &sm.red[wave][c * 8 + g * 4] : &sm.r2[(lane - 32) * 4]);
  u32 pso, po, rdst, rsrc, xo, l2, v2[4];
  rsrc_t r2;
  auto wo_addr = [&]() {
    po = G_P + (xcd & 3) * 16384 + near + (c >> 2 & 1) * 4096 + ((wave >> 1) * 4 + (c & 3)) * 256 + (wave & 1) * 128 + (c >> 3) * 64 + g * 16;
    rdst = lane < 32 ? (u32)(uintptr_t)&sm.red2[wave][0] + lane * 16 : (u32)(uintptr_t)&sm.r2[0] + (lane - 32) * 16;
    rsrc = (u32)(uintptr_t)&sm.red2[0][((wave & 1) * 16 + (lane & 15)) * 4 + (lane >> 4)];
    xo = dup(G_XP + xcd * 2048 + (wave & 1) * 1024 + (blk * 64 + (lane & ~1)) * 2) + (lane & 1) * 8192;
    pso = wave == 0 ? G_P + (xcd & 3) * 16384 + near + b * 4096 + ((head & 3) * 4 + part) * 256 + lane * 4 : OOB;
    PIN(pso); PIN(po); PIN(rdst); PIN(rsrc); PIN(xo);
  };
  auto w2_addr = [&]() {
    r2 = mk_rsrc(w2 + (size_t)(blk * 32 + wave * 4) * 512, 4096u);
    l2 = (u32)(uintptr_t)(&sm.w2b[wave * 4][0]);
#pragma unroll
    for (int n = 0; n < 4; ++n) { v2[n] = (lane ^ (n * 2) ^ ((wave & 2) << 2)) * 16 + n * 1024; PIN(v2[n]); }
    asm volatile("" : "+s"(r2), "+s"(l2));
  };
  const bool late = is_up || (part == 0 && wleft);
  const u32 nadd = is_up ? P.LU : (part == 0 ? (wleft ? P.LW : P.L3) : 0);
  u32 nto = tid == 0 ? G_NT + bid * 8 : OOB;
  PIN(nto);
  auto notices = [&](auto nc_, u32 off, u32x4 (&gn)[nc_.v]) -> int {
    constexpr int N = nc_.v;
#pragma nounroll
    for (int spin = 0; spin < SPIN_MAX; ++spin) {
      bool ok = true;
#pragma unroll
      for (int i = 0; i < N; ++i) ok &= (gn[i][1] == tag) & (gn[i][3] == tag);
      if (all_ok(ok)) break;
      __builtin_amdgcn_s_sleep(10);
      CFENCE();
#pragma unroll
      for (int i = 0; i < N; ++i) gn[i] = ld16<AUX_SC1>(wsr, off, i * 1024);
    }
    int mx = 0;
#pragma unroll
    for (int i = 0; i < N; ++i) mx = max(mx, max((int)(gn[i][0] - t0), (int)(gn[i][2] - t0)));
    return __builtin_amdgcn_readfirstlane((int)wave_max((float)min(mx, 1 << 20)));
  };
  const int side = nl >= SIDE + 13 ? SIDE + 8 : (nl >= SIDE + 9 ? SIDE + 4 : SIDE);
  sfor<0, NL + WV>([&](auto ic) {
    constexpr int v = ic.v;
    if (cons_end(v - 1) >= NCMIN / 8 && cons_end(v - 1) >= nl) return false;
    if constexpr (v < NL) {
      const u32 so = __builtin_amdgcn_readlane(sot, v);
      if constexpr (v >= 12) { if (v == nl - P.NE) tau = (tau * (late ? 16 : P.FE)) >> 4; }
      if constexpr (v >= NCMIN / 8) { if (v < nl) gate(); } else gate();
      SB();
      int bk = 0;
      if constexpr (v == AR) { if (part == 1 && wi < 48 && wave == 7) bk = ((int)(((u32)wall_clock64() - t0) << 8) - due + tau + (int)__builtin_amdgcn_s_getreg(7 | (3 << 11)) * tau) >> 8; SB(); }
      vr[v] = ld16<AUX_NT>(rv, l16, so);
      if constexpr (v == AR) {
        SB();
        if (part == 1 && wi < 48 && tid == 448) st8_sc1(wsr, G_AG + wi * 8, (u32)min(max(bk, 0), 250), tag);
      }
      SB();
      SB();
    }
    if constexpr (v == NCMIN / 8 - 1) {
#pragma unroll
      for (int i = NCMIN / 8; i < NL; ++i) asm volatile("" : "=v"(vr[i]));
      asm volatile("" : "=v"(fl2[1]), "=v"(fl2[2]));
#pragma unroll
      for (int w = 0; w < 8; ++w) asm volatile("" : "=v"(fmw[w]));
    }
    if constexpr (v == VQ - 4) { qg[0] = ld16<AUX_SC1>(wsr, qoff, 0); qg[1] = ld16<AUX_SC1>(wsr, qoff + 16, 0); }
    if constexpr (v == VSIDE) side_loads();
    if constexpr (v == VL) agr = ld8<AUX_SC1>(wsr, wave == 6 ? G_AG + (48 + c) * 8 : OOB, 0);
    if constexpr (v == AL) agr = ld8<AUX_SC1>(wsr, (wave == 7 && lane < 48) ? G_AG + lane * 8 : OOB, 0);
    if constexpr (v == CPF) {
      u64 pc; u32 dlt, len;
      asm volatile("s_getpc_b64 %0\n.Lcpf_here:\n\ts_mov_b32 %1, .Ltail_start-.Lcpf_here\n\ts_mov_b32 %2, .Lend_a-.Ltail_start\n\ts_max_i32 %2, %2, .Lend_b-.Ltail_start\n\ts_max_i32 %2, %2, .Lend_c-.Ltail_start\n\ts_max_i32 %2, %2, .Lend_d-.Ltail_start\n\ts_max_i32 %2, %2, .Lend_e-.Ltail_start" : "=s"(pc), "=s"(dlt), "=s"(len) :: "scc");
      pc += dlt;
      const rsrc_t rc = mk_rsrc((const void*)pc, (part == 3 && i8 == 1) ? len : 0u);
      cpf = __builtin_amdgcn_raw_buffer_load_b32(rc, tid * 64, 0, 0);
    }
    if constexpr (v == CPF + WV + 1) PIN(cpf);
    if constexpr (v == SIDE || v == SIDE + 4 || v == SIDE + 8) fl2[(v - SIDE) / 4] = ld16<AUX_SC1>(wsr, (wave == 0 && v == side) ? mlo : OOB, 0);
    SB();
    if constexpr (v < WV) { WAIT_VM(win_v(v, WK)); SB(); } else if constexpr (v < NL && cons_end(v) < v - WV + 1) { WAIT_VM(win_v(v, WV)); SB(); }
    if constexpr (v == VU) {
      if (wave == 6) {
        u32 vt[1] = {agr[0]};
        if (!all_ok(agr[1] == tag)) gather8<1>(wsr, G_AG + (48 + c) * 8, 0, tag, vt);
        float sv = u2f(vt[0]);
        sv += dpp<0x124>(sv); sv += dpp<XOR8>(sv);
        float sa = sv + dpp<XOR1>(sv);
        sv = fmaf(sa + dpp<XOR2>(sa), 0.25f, sv);
        float cf = fabsf(sv);
        cf = fminf(cf, dpp<XOR1>(cf)); cf = fminf(cf, dpp<XOR2>(cf));
        const u32 nm = (u32)__builtin_amdgcn_ballot_w64(sv > 0.f) & 15u;
        if (lane == 0) sm.nm = nm << 13 | (cf < 2500.f ? (u32)P.SL : 0u);
      }
      PIN(agr);
    }
    if constexpr (v == VQ) {
      if (!all_ok((qg[0][1] == tag) & (qg[0][3] == tag) & (qg[1][1] == tag) & (qg[1][3] == tag))) { STAMP(20); gather16<2>(wsr, qoff, 16, tag, qg); }
      STAMP(1); WSTAMP(1);
      qq[0] = qg[0][0]; qq[1] = qg[0][2]; qq[2] = qg[1][0]; qq[3] = qg[1][2];
    }
    if constexpr (v >= VQ && v < VS) {
      constexpr int u0 = SLC[v - VQ], u1 = SLC[v - VQ + 1];
      static_assert(SLC[NSL] == (NL + 7) / 8 * 8, "all groups of 8");
      sfor<u0, u1>([&](auto uc) {
        constexpr int u = uc.v, p = u & 7, i = (p & 1) * 4 + (p & 2) + (p >> 2), sl = u >> 3, bt = sl >> 1;
        float d = 0.f;
        if constexpr (sl * 8 + i < NL) {
#pragma unroll
          for (int k = 0; k < 4; ++k) d = dot2(kr[sl * 8 + i][k], qq[k], d);
        }
        if constexpr (p & 1) xs[i - 4] = sel_add<XOR7>(xs[i - 4], d, c & 4); else xs[i] = d;
        if constexpr ((p & 3) == 3) xs[p >> 2] = sel_add<XOR2>(xs[p >> 2], xs[(p >> 2) + 2], c & 2);
        if constexpr (p == 7) {
          xs[0] = sel_add<XOR1>(xs[0], xs[1], c & 1);
          if constexpr ((sl & 1) || u == (NL + 7) / 8 * 8 - 1) {
            const float t = (sl & 1) ? sel_add<XOR8>(xlo, xs[0], c & 8) : xs[0] + dpp<XOR8>(xs[0]);
            const float s = rbf(rbf(t) * 0.08838834764831845f);
            sc[bt] = (bt * 16 + c < nl) ? s : -__builtin_inff();
            PIN(sc[bt]);
          } else {
            xlo = xs[0];
            PIN(xlo);
          }
        }
        return true;
      });
      constexpr int pe = u1 & 7;
      if constexpr (pe >= 1) PIN(xs[0]);
      if constexpr (pe == 3) PIN(xs[2]);
      if constexpr (pe >= 5) PIN(xs[1]);
      if constexpr (pe == 7) PIN(xs[3]);
    }
    if constexpr (v == VS) {
      float mloc = sc[0];
#pragma unroll
      for (int bt = 1; bt < NB; ++bt) mloc = fmaxf(mloc, sc[bt]);
      m = fmaxf(wave_max(mloc), -3.0e38f);
      float ls = 0.f;
#pragma unroll
      for (int bt = 0; bt < NB; ++bt) { pr[bt] = __expf(sc[bt] - m); ls += pr[bt]; }
      const float l = wave_sum(ls);
      if (lane == 0) { sm.ml[0][wave] = m; sm.ml[1][wave] = l; }
#pragma unroll
      for (int bt = 0; bt < NB; ++bt) { pb[bt] = pack_bf(pr[bt], pr[bt]); PIN(pb[bt]); }
#pragma unroll
      for (int k = 0; k < 4; ++k) { pm[k] = (c >> 1) == k ? ((c & 1) ? 0xffff0000u : 0x0000ffffu) : 0u; PIN(pm[k]); }
      STAMP(2); WSTAMP(2);
    }
    sfor<cons_end(v - 1), cons_end(v)>([&](auto jc) {
      constexpr int i = jc.v;
      if (i < NCMIN / 8 || i < nl) {
        const u32 pv = __float_as_uint(bcast16(u2f(pb[i >> 4]), i & 15));
        acc = mfma16(u32x4{pv & pm[0], pv & pm[1], pv & pm[2], pv & pm[3]}, vr[i], acc);
      }
      PIN(acc);
      return true;
    });
    if constexpr (v == VS - 1) { side_pin(); if (is_up) up_prep(); PIN(ga); PIN(xin); PIN(cc1); }
    if constexpr (v == MLS - 1) {
      if (wave == 7) {
        const float am = wave_sum(lane < 48 ? (agr[1] == tag ? (float)agr[0] : 120.f) : 0.f) * (1.f / 48.f);
        const float st = fminf(fmaxf((am - (float)P.A0) * (float)(P.AG * 16), 0.f), (float)(P.AC * 256));
        if (lane == 0) sm.ctl = u32x2{(u32)(int)st, (u32)(int)(st * __builtin_amdgcn_rcpf((float)(nl - MLS - 3)))};
      }
      PIN(agr);
    }
    if constexpr (v == UPREP - 2) {
      nmask = __builtin_amdgcn_readfirstlane(sm.nm);
      slack = (nmask & 255u) * 256; near = ((nmask >> (xcd & 3)) ^ (xcd << 11)) & 8192u;
      w2_addr();
    }
    if constexpr (v == UPREP - 1) wo_addr();
    if constexpr (v == UPREP) {
#pragma unroll
      for (int k = 0; k < 8; ++k) asm volatile("" : "=v"(bf[k]));
      if (part == 0) wo_prep(0, 1); else if (is_up) up_frags(0, 8 - UD);
#pragma unroll
      for (int k = 0; k < 8; ++k) PIN(bf[k]);
    }
    if constexpr (v == UPREP + 1 || v == UPREP + 2) {
      if (is_up) { if constexpr (v == UPREP + 1) col_sums(0, (8 - UD) / 2); else col_sums((8 - UD) / 2, 8 - UD); }
      PIN(Af); PIN(Bf);
    }
    if constexpr (v == SIDE + WV + 1 || v == SIDE + WV + 5 || v == SIDE + WV + 9) {
      if (wave == 0 && v == side + WV + 1) {
        const u32x4 ml = fl2[(v - SIDE - WV - 1) / 4];
        fok = all_ok((ml[1] == tag) & (ml[3] == tag));
        merge_factor(ml);
      }
    }
    if (v % 6 == 4 && v >= 10 && v < 30) STAMP(15 + v / 6);
    if constexpr (v == MLS) {
      LDS_BARRIER();
      const u32x2 ct = sm.ctl;
      if (wave == 0) {
        const float mw = sm.ml[0][lane & 7];
        float Mwg = fmaxf(mw, dpp<XOR1>(mw)); Mwg = fmaxf(Mwg, dpp<XOR2>(Mwg)); Mwg = fmaxf(Mwg, dpp<XOR7>(Mwg));
        fwv = __expf(mw - Mwg);
        float Lwg = fwv * sm.ml[1][lane & 7];
        Lwg += dpp<XOR1>(Lwg); Lwg += dpp<XOR2>(Lwg); Lwg += dpp<XOR7>(Lwg);
        if (lane == 0) st16_sc1(wsr, G_ML + ((head * 2 + b) * 4 + part) * 16, __float_as_uint(Mwg), __float_as_uint(Lwg), tag);
      }
      PIN(fwv);
      t1 += __builtin_amdgcn_readfirstlane(ct[0]);
      tau += __builtin_amdgcn_readfirstlane(ct[1]);
    }
    SB();
    return true;
  });
  asm volatile(".Ltail_start:");
  *LDSP(f32x4, adst0) = acc;
  STAMP(14); WSTAMP(14);
  if (!fok) {
    u32x4 ml;
#pragma nounroll
    for (int spin = 0; spin < SPIN_MAX; ++spin) {
      CFENCE();
      ml = ld16<AUX_SC1>(wsr, mlo, 0);
      if (all_ok((ml[1] == tag) & (ml[3] == tag))) break;
    }
    merge_factor(ml);
  }
  STAMP(15);
  LDS_BARRIER();
  STAMP(3);
  {
    u32 pw = 0;
    if (wave == 0) {
      f32x2 t[8];
#pragma unroll
      for (int w = 0; w < 8; ++w) t[w] = *reinterpret_cast<const f32x2*>(&sm.red[w][2 * lane]);
      f32x2 h[4];
#pragma unroll
      for (int w = 0; w < 4; ++w) h[w] = __builtin_elementwise_fma(t[2 * w + 1], f32x2{fmw[2 * w + 1], fmw[2 * w + 1]}, t[2 * w] * fmw[2 * w]);
      const f32x2 o = (h[0] + h[1]) + (h[2] + h[3]);
      pw = min(pack_bf(o[0], o[1]), SENT - 1u);
    }
    __builtin_amdgcn_raw_buffer_store_b32(pw, wsr, pso, 0, AUX_SC1);
    st8_sc1(wsr, nto, (u32)wall_clock64() + nadd, tag);
    SB();
#pragma unroll
    for (int i = NCMIN / 8; i < NL; ++i) PIN(vr[i]);
    PIN(fl2[0]); PIN(fl2[1]); PIN(fl2[2]);
  }
  STAMP(4);
  SB();
  if (late) {
    asm volatile(".p2align 6\n\ts_barrier" ::: "memory");
#pragma nounroll
    while (wleft > 0) piece();
    if (is_up) {
      asm volatile("s_mov_b32 m0, %5\n\ts_nop 0\n\tbuffer_load_dwordx4 %0, %4, 0 offen nt lds\n\ts_add_u32 m0, m0, 0x400\n\ts_nop 0\n\tbuffer_load_dwordx4 %1, %4, 0 offen nt lds\n\t"
                   "s_add_u32 m0, m0, 0x400\n\ts_nop 0\n\tbuffer_load_dwordx4 %2, %4, 0 offen nt lds\n\ts_add_u32 m0, m0, 0x400\n\ts_nop 0\n\tbuffer_load_dwordx4 %3, %4, 0 offen nt lds"
                   :: "v"(v2[0]), "v"(v2[1]), "v"(v2[2]), "v"(v2[3]), "s"(r2), "s"(l2) : "memory", "scc", "m0");
    }
  }
  SB();

  if (part == 0) {
    u32 wa2[8];
#pragma unroll
    for (int k = 0; k < 8; ++k) {
      const int r = 32 + g * 8 + ((lane >> 2) & 3) + (k & 1) * 4, ch = (k & 6) + ((lane >> 1) & 1);
      wa2[k] = wimg + wave * 8192 + r * 128 + ((ch ^ (r & 7)) * 16) + (lane & 1) * 8;
      PIN(wa2[k]);
    }
    rsrc_t wr2 = wsr;
    asm volatile("" : "+s"(wr2));
    until(t1);
    STAMP(11);
    CFENCE();
    u32x4 og[2];
    og[0] = ld16<AUX_SC1>(wr2, po, 0);
    SB();
    WAIT_VM(1); CFENCE();
#pragma unroll
    for (int t = 0; t < 4; ++t) {
      const u32x2 lo = tr_read(wa2[2 * t]), hi = tr_read(wa2[2 * t + 1]);
      bf[4 + t] = u32x4{lo[0], lo[1], hi[0], hi[1]};
    }
    SB();
    f32x4 d[4];
    auto wo_mm = [&]() {
      const f32x4 z = {0.f, 0.f, 0.f, 0.f};
#pragma unroll
      for (int t = 0; t < 4; ++t) { d[t] = mfma16(og[0], bf[t], z); og[1][t] = __float_as_uint(dpp<XOR8>(u2f(og[0][t]))); }
      SB();
#pragma unroll
      for (int t = 0; t < 4; ++t) d[t] = mfma16(og[1], bf[4 + t], d[t]);
    };
    auto p_bad = [&]() { return !all_ok(max(max(og[0][0], og[0][1]), max(og[0][2], og[0][3])) != SENT); };
    wo_mm();
    if (__builtin_expect(p_bad(), 0)) {
#pragma nounroll
      for (int spin = 0; spin < SPIN_MAX; ++spin) {
        __builtin_amdgcn_s_sleep(1);
        CFENCE();
        og[0] = ld16<AUX_SC1>(wsr, po, 0);
        if (!p_bad()) break;
      }
      wo_mm();
      SB();
      asm volatile(".Lend_d:");
    }
    STAMP(5); WSTAMP(5);
    {
      SB();
      f32x4 y; f32x2 h[4];
#pragma unroll
      for (int t = 0; t < 4; ++t) { h[t] = d[t].xy + d[t].zw; PIN(h[t]); }
#pragma unroll
      for (int t = 0; t < 4; ++t) { y[t] = h[t][0] + h[t][1]; PIN(y[t]); }
      *LDSP(f32x4, rdst) = y;
    }
    STAMP(16);
    LDS_BARRIER();
    STAMP(17);
    if (wave < 2) {
      f32x2 v[4];
#pragma unroll
      for (int w = 0; w < 4; ++w) v[w] = f32x2{LDSP(float, rsrc)[w * 256], LDSP(float, rsrc)[w * 256 + 128]};
      const f32x2 h = (v[0] + v[1]) + (v[2] + v[3]);
      const float y = h[0] + h[1];
      __builtin_amdgcn_raw_buffer_store_b32(min(pack_bf(dpp<0xA0>(y), dpp<0xF5>(y)), SENT - 1u), wr2, xo, 0, AUX_SC1);
      asm volatile(".Lend_a:");
    }
    STAMP(6);
    FINISH();
  }
  if (part == 3) {
    if (!has_w) FINISH();
    u32x4 b2[4];
    const u32 tdst = lds_addr(&sm.hb[hpos ^ ((wave & 1) * 16)]);
    const u32 sdst = lds_addr(&sm.stat[wave][lane]);
    const u32 ssrc = lds_addr(&sm.stat[(g >> 1) * 4 + (c >> 2)][(g & 1) * 32 + (c & 3) * 8]);
    const u32 adst = lds_addr((c < 4 && (g & 1) == 0) ? &sm.actl[g >> 1][wave * 4 + c] : reinterpret_cast<u16*>(&sm.dummy[lane]));
    const u32 asrc = lds_addr(&sm.actl[lane & 1][g * 8]);
    const u32 apa = lds_addr(&sm.hb[(c >> 3) * 512 + (c & 3) * 64 + ((c >> 2) & 1) * 32 + (g ^ (c & 2)) * 8]);
    u32 xoff = dup(G_XP) + near + g * 2048 + ub * 1024 + (wave & 3) * 256 + c * 16;
    u32x4 one;
#pragma unroll
    for (int k = 0; k < 4; ++k) one[k] = ((c & 2) == 0 && (c >> 2) == k) ? ((c & 1) ? 0x3f800000u : 0x00003f80u) : 0u;
    PIN(one);
    u32 ooff = dup(G_OP + (blk * 512 + tid) * 8) + ((nmask >> (g & 1)) & 8192u);
    PIN(xoff); PIN(ooff);
    PIN(ga); PIN(xin); PIN(cc1);
    u32 w2a = (u32)(uintptr_t)(&sm.w2b[0][0]) + (g * 8 + ((lane >> 2) & 3)) * 1024 + (wave ^ (g & 1)) * 128 + (lane & 15) * 8;
    u32 w2t[4];
#pragma unroll
    for (int t = 0; t < 4; ++t) { w2t[t] = w2a ^ (t * 32); PIN(w2t[t]); }
    static_assert(UD == 1, "one late fragment");
    u32 ufa = up_addr() + 7 * 4096;
    const u32 a7a = lds_addr(ap + 128 + ((lane >> 1) & 1) * 128 + 7 * 8);
    PIN(ufa);
    u64 gm1 = __builtin_amdgcn_ballot_w64(g == 1), gm2 = __builtin_amdgcn_ballot_w64(g == 2), gm3 = __builtin_amdgcn_ballot_w64(g == 3);
    asm volatile("" : "+s"(gm1), "+s"(gm2), "+s"(gm3));
    const bool g1 = __builtin_amdgcn_inverse_ballot_w64(gm1), g2 = __builtin_amdgcn_inverse_ballot_w64(gm2), g3 = __builtin_amdgcn_inverse_ballot_w64(gm3);
    if (wave == 1) {
      due = t1 + P.N1 * 256; gate();
      u32x4 gn[2] = {ld16<AUX_SC1>(wsr, G_NT + lane * 16, 0), ld16<AUX_SC1>(wsr, G_NT + lane * 16, 1024)};
      const int tn = notices(IC<2>{}, G_NT + lane * 16, gn) * 256;
      due = (tn > t1 + P.TH * 256 ? max(tn, t1 + P.L0 * 256) + P.L1 * 256 : t1) + P.X2 * 256 + slack;
      until(due);
    }
    asm volatile("s_waitcnt vmcnt(0)" ::: "memory");
    STAMP(9);
    __builtin_amdgcn_s_barrier();
    STAMP(11);
    CFENCE();
    u32x4 xg[2] = {ld16<AUX_SC1>(wsr, xoff, 0), ld16<AUX_SC1>(wsr, xoff, 16384)};
    SB();
    {
      const u32x4 a7 = *LDSP(u32x4, a7a);
      const u32x2 l7 = tr_read(ufa), h7 = tr_read(ufa + 256);
      bf[7] = u32x4{l7[0], l7[1], h7[0], h7[1]};
#pragma unroll
      for (int t = 0; t < 4; ++t) {
        const u32x2 lo = tr_read(w2t[t]), hi = tr_read(w2t[t] + 4096);
        b2[t] = u32x4{lo[0], lo[1], hi[0], hi[1]};
      }
      SB();
      const f32x4 d0 = mfma16(a7, bf[7], f32x4{0.f, 0.f, 0.f, 0.f});
      Af += par ? d0[1] : d0[0]; Bf += par ? d0[3] : d0[2];
      Af += dpp<XOR8>(Af); Bf += dpp<XOR8>(Bf);
    }
    PIN(Af); PIN(Bf);
#pragma unroll
    for (int t = 0; t < 4; ++t) PIN(b2[t]);
    SB();
    const f32x4 z4 = {0.f, 0.f, 0.f, 0.f};
    auto xp_bad = [&]() { return !all_ok(max(max(max(xg[0][0], xg[0][1]), max(xg[0][2], xg[0][3])), max(max(xg[1][0], xg[1][1]), max(xg[1][2], xg[1][3]))) != SENT); };
    f32x4 v = mfma16(one, xg[1], mfma16(one, xg[0], z4));
    if (__builtin_expect(xp_bad(), 0)) {
#pragma nounroll
      for (int spin = 0; spin < SPIN_MAX; ++spin) {
        __builtin_amdgcn_s_sleep(1);
        CFENCE();
        xg[0] = ld16<AUX_SC1>(wsr, xoff, 0); xg[1] = ld16<AUX_SC1>(wsr, xoff, 16384);
        if (!xp_bad()) break;
      }
      v = mfma16(one, xg[1], mfma16(one, xg[0], z4));
      SB();
      asm volatile(".Lend_e:");
    }
    STAMP(5); WSTAMP(5);
    {
      const float a0 = v[0], a1 = v[1];
      const float x10 = fmaf(ga[0], a0, xin[0]), x11 = fmaf(ga[1], a1, xin[1]);
      *LDSP(u32, tdst) = pack_bf(x10 * cc1[0], x11 * cc1[1]);
      float u = swap32_add(x10 + x11, fmaf(x10, x10, x11 * x11));
      u += dpp<XOR1>(u); u += dpp<XOR2>(u); u += dpp<XOR7>(u);
      *LDSP(float, sdst) = u;
    }
    STAMP(16);
    LDS_BARRIER();
    STAMP(17);
    {
      const f32x4 z = {0.f, 0.f, 0.f, 0.f};
      const u32x4 af0 = LDSP(u32x4, apa)[0], af1 = LDSP(u32x4, apa)[32];
      float s = LDSP(float, ssrc)[0];
      SB();
      f32x4 d[4];
      d[0] = mfma16(af0, bf[0], z); d[1] = mfma16(af0, bf[1], z);
      SB();
      s += dpp<XOR1>(s); s += dpp<XOR2>(s);
      PIN(s);
      d[2] = mfma16(af0, bf[2], z);
      SB();
      s += dpp<XOR7>(s); s += dpp<XOR8>(s);
      PIN(s);
      d[3] = mfma16(af0, bf[3], z);
      SB();
      const auto sw2 = __builtin_amdgcn_permlane16_swap(__float_as_uint(s), __float_as_uint(s), false, false);
      float mean = u2f(sw2[0]) * (1.f / 512.f), qm = fmaf(u2f(sw2[1]), 1.f / 512.f, 1e-5f);
      PIN(mean); PIN(qm);
      d[0] = mfma16(af1, bf[4], d[0]);
      SB();
      float ve = fmaxf(fmaf(-mean, mean, qm), 1e-5f), ma = mean * Af;
      PIN(ve); PIN(ma);
      d[1] = mfma16(af1, bf[5], d[1]);
      SB();
      float ri = __builtin_amdgcn_rsqf(ve);
      PIN(ri);
      d[2] = mfma16(af1, bf[6], d[2]);
      d[3] = mfma16(af1, bf[7], d[3]);
      SB();
      float cf = fmaf(-ri, ma, Bf), rl = ri * -1.4426950408889634f, cl = cf * -1.4426950408889634f;
      PIN(cf); PIN(rl); PIN(cl);
      PIN(d[0]); PIN(d[1]);
      float y0 = d[0][0] + d[1][1];
      PIN(d[2]);
      y0 += d[2][2];
      PIN(y0); PIN(d[3]);
      const float y1 = d[3][3];
      float y = y0 + y1;
      y = __builtin_bit_cast(float, __builtin_amdgcn_update_dpp(__builtin_bit_cast(int, y), __builtin_bit_cast(int, y), XOR8, 0xa, 0xf, false));
      y = swap16_add(y, y);
      const float e = __builtin_amdgcn_exp2f(fmaf(y, rl, cl));
      y = fmaf(y, ri, cf);
      float gu = y * dpp<0x104>(y);
      PIN(gu);
      *LDSP(u16, adst) = (u16)pack_bf(gu * __builtin_amdgcn_rcpf(1.f + e), 0.f);
    }
    STAMP(18);
    LDS_BARRIER();
    STAMP(10);
    {
      const u32x4 af = *LDSP(u32x4, asrc);
      const f32x4 z = {0.f, 0.f, 0.f, 0.f};
      f32x4 d[4];
#pragma unroll
      for (int t = 0; t < 4; ++t) d[t] = mfma16(af, b2[t], z);
      SB();
      PIN(d[0]); PIN(d[1]);
      float e0 = g1 ? d[1][0] : d[0][0], e1 = g1 ? d[1][1] : d[0][1];
      PIN(d[2]);
      e0 = g2 ? d[2][0] : e0; e1 = g2 ? d[2][1] : e1;
      PIN(e0); PIN(e1); PIN(d[3]);
      st8_sc1(wsr, ooff, pack_bf(g3 ? d[3][0] : e0, g3 ? d[3][1] : e1), tag);
    }
    asm volatile(".Lend_b:");
    STAMP(6);
    FINISH();
  }
  if (fin) {
    const int d = fd, ul = lane >> 3;
    u32x4 gm[2] = {sg[0], sg[1]};
    if (!all_ok((sg[0][1] == tag) & (sg[0][3] == tag) & (sg[1][1] == tag) & (sg[1][3] == tag))) gather16<2>(wsr, G_MOD + (1024 + d) * 8, 1536 * 8, tag, gm);
    u32 xa0, xa1;
    {
      due = t1 + P.N2 * 256; gate();
      u32x4 gn[2] = {ld16<AUX_SC1>(wsr, G_NT + lane * 16, 0), ld16<AUX_SC1>(wsr, G_NT + lane * 16, 1024)};
      const int tn = notices(IC<2>{}, G_NT + lane * 16, gn) * 256;
      due = (tn > t1 + P.TH * 256 ? max(tn, t1 + P.L0 * 256) + P.L1 * 256 : t1) + P.X3 * 256 + slack;
      until(due);
    }
#pragma nounroll
    for (int spin = 0; spin < SPIN_MAX; ++spin) {
      CFENCE();
      xa0 = __builtin_amdgcn_raw_buffer_load_b32(wsr, dup(G_XP + ul * 2048) + near + d * 2, 0, AUX_SC1);
      xa1 = __builtin_amdgcn_raw_buffer_load_b32(wsr, dup(G_XP + ul * 2048) + near + d * 2, 1024, AUX_SC1);
      if (all_ok(max(xa0, xa1) != SENT)) break;
      __builtin_amdgcn_s_sleep(1);
    }
    float a0 = swap16_add(lo_f(xa0), lo_f(xa1)), a1 = swap16_add(hi_f(xa0), hi_f(xa1));
    a0 += dpp<XOR8>(a0); a1 += dpp<XOR8>(a1);
    a0 = swap32_add(a0, a0); a1 = swap32_add(a1, a1);
    const bool hb = g & 1;
    f32x2 xv = {rbf(lo_f(sx2) + rbf((hb ? hi_f(gm[0][0]) : lo_f(gm[0][0])) * rbf(a0))), rbf(hi_f(sx2) + rbf((hb ? hi_f(gm[0][2]) : lo_f(gm[0][2])) * rbf(a1)))};
    f32x2 gv = {hb ? hi_f(gm[1][0]) : lo_f(gm[1][0]), hb ? hi_f(gm[1][2]) : lo_f(gm[1][2])};
    PIN(xv); PIN(gv);
    if (fw == 0 && lane == 0) __builtin_amdgcn_raw_buffer_store_b32(tag, wsr, WS_EPOCH, 0, AUX_SC1);
    STAMP(10);
    due = max(due + P.X4 * 256 + slack, (int)(((u32)wall_clock64() - t0) << 8) + P.GX * 256);
    const bool last = ul + 40 < NUP;
    u32 o0 = dup(G_OP + (ul * 512 + d) * 8) + near, o5 = last ? o0 : OOB, tg5 = last ? tag : 0u;
    const rsrc_t rout = mk_rsrc(out, 2048u);
    u32 oo = (lane & 40) == 0 ? (g * 512 + d) * 2 : OOB;
    const u32 pk = (c & 10) ? 0u : ((c & 4) ? 0x3f800000u : 0x00003f80u);
    u32x4 one = {(c & 1) ? 0u : pk, 0u, (c & 1) ? pk : 0u, 0u};
    PIN(o0); PIN(o5); PIN(tg5); PIN(oo); PIN(one);
    u32 so[6];
#pragma unroll
    for (int k = 0; k < 6; ++k) { so[k] = k * 65536; asm volatile("" : "+s"(so[k])); }
    __builtin_amdgcn_raw_buffer_store_b32(pack_bf(xv[0], xv[1]), rout, oo, 0, AUX_SC1);
    until(due);
#pragma nounroll
    for (int spin = 0; spin < SPIN_MAX; ++spin) {
      CFENCE();
      u32x4 og[6];
      og[0] = ld16<AUX_SC1>(wsr, o5, so[5]);
#pragma unroll
      for (int k = 1; k < 6; ++k) { SB(); og[k] = ld16<AUX_SC1>(wsr, o0, so[k - 1]); }
      SB();
      f32x4 sv = {0.f, 0.f, 0.f, 0.f};
#pragma unroll
      for (int k = 0; k < 6; ++k) sv = mfma16(one, og[k], sv);
      const float s0 = sv[0] + dpp<XOR8>(sv[0]), s1 = sv[1] + dpp<XOR8>(sv[1]);
      __builtin_amdgcn_raw_buffer_store_b32(pack_bf(fmaf(gv[0], s0, xv[0]), fmaf(gv[1], s1, xv[1])), rout, oo, 0, AUX_SC1);
      SB();
      u32 bad = (og[0][1] ^ tg5) | (og[0][3] ^ tg5);
#pragma unroll
      for (int k = 1; k < 6; ++k) { bad |= (og[k][1] ^ tag) | (og[k][3] ^ tag); PIN(bad); }
      if (all_ok(bad == 0u)) break;
      __builtin_amdgcn_s_sleep(1);
    }
    STAMP(7);
    asm volatile(".Lend_c:");
  }
  FINISH();
}

extern "C" void kernel_launch(void* const* d_in, const int* in_sizes, int n_in,
                              void* d_out, int out_size, void* d_ws, size_t ws_size,
                              hipStream_t stream) {
  fused_kernel<<<256, 512, 0, stream>>>((const u16*)d_in[0], (const u16*)d_in[1], (const u16*)d_in[2], (const u16*)d_in[3], (const u16*)d_in[4],
                                        (const u16*)d_in[6], (const u16*)d_in[7], (const u16*)d_in[8], (const u16*)d_in[9], (const u16*)d_in[10],
                                        (unsigned char*)d_ws, (u16*)d_out
    );
}
```

```cpp
#include <hip/hip_runtime.h>
#include <stdint.h>

typedef unsigned int u32;
typedef unsigned short u16;
typedef unsigned long long u64;
typedef __attribute__((ext_vector_type(4))) unsigned u32x4;
typedef __attribute__((ext_vector_type(2))) unsigned u32x2;
typedef __attribute__((ext_vector_type(2))) __bf16 bf16x2;
typedef __attribute__((ext_vector_type(4))) float f32x4;
typedef __attribute__((ext_vector_type(2))) float f32x2;
typedef __amdgpu_buffer_rsrc_t rsrc_t;

constexpr int NH = 32, SK = 16384, DH = 128, WIN = 4096, FF = 1376;
struct Prm { int E0, ES, E1, E2, E2C, EU, TEND, DT_UP, L0, L1, X1, X2, X3, X4, NE, FE, M0, M1, M2, M3, M4, GS, GX, N1, N2, BT, WD, DT_WO, LW, LU, EW, PM, WM, FK, FW, FU, EX, TS, DS, A0, AG, AC, PT, SL, TH, DT_0, L3; };
constexpr Prm PRM0 = {32, 24, 32, 32, 48, 64, 2000, 96, 20, 14, 72, 78, 98, 86, 5, 12, 12, 5, 12, 32, 28, 34, 34, 18, 45, 10, 4, 103, 40, 40, 8, 0x6827941, 0x53, 62, 61, 60, 8, 500, 122, 55, 26, 120, 160, 10, 6, 25, 15};
#define PRM_ARG
#define TABP (&TAB.e[0])
constexpr int NL = 37;
constexpr int NCMIN = 192;
constexpr int WK = 6, WV = 5;
constexpr int WUSE = 13, LNS = 11;
constexpr int QP1 = 12, QP2 = 18, QD = 5;
constexpr int VQ = 6;
constexpr int SLC[] = {0, 6, 9, 15, 21, 27, 33, 40}, NSL = sizeof(SLC) / sizeof(int) - 1;
constexpr int VS = VQ + NSL;
constexpr int MLS = VS + 2;
constexpr int VSIDE = VQ + 1, UPREP = 21;
constexpr int SIDE = NCMIN / 8 - 4;
constexpr int UD = 1;
constexpr int CPF = 18;
constexpr int TBP = 7, TBL = 11, TBU = 16;
constexpr int AR = 0, AL = 10;
constexpr int VL = 0, VU = 5;
constexpr int D0 = 30;
constexpr int KF = 4, KF0 = 8;
constexpr int side_k(int i) { return (i == KF || i == KF + 1) ? 3 : (i == TBL ? 1 : 0) + ((i == QP1 || i == QP2) ? 2 : 0); }
constexpr int side_v(int v) { return v == VQ - 4 ? 2 : (v == VSIDE ? 4 : ((v == AL || v == VL) ? 1 : 0)); }
constexpr int win_k(int i) { int n = WK; for (int j = i - WK; j <= i; ++j) n += side_k(j); return n; }
constexpr int win_v(int v, int w) { int n = w; for (int j = v - w; j <= v; ++j) n += side_v(j); return n; }
constexpr int cons_end(int v) {
  const int a = v - WV + 1, c = (v - VS) * 3;
  const int m = a < c ? a : c;
  return m < 0 ? 0 : (m > NL ? NL : m);
}
struct Tab { u32 e[1024]; };
#define popc __builtin_popcount
__host__ __device__ constexpr u32 place(u32 v, u32 m, int n) {
  u32 r = 0;
  for (int k = 0; k < n; ++k) r |= (v >> k & 1u) << (10 + (m >> (4 * k) & 15));
  return r;
}
__host__ __device__ constexpr u32 step_off(const Prm& P, u32 s0, u32 bh) {
  const u32 src = (s0 >> 5 & 3u) | bh << 2;
  return place(s0 ^ ((popc(src & P.M0) & 1) | (popc(src & P.M1) & 1) << 1 | (popc(src & P.M2) & 1) << 2 | (popc(src & P.M3) & 1) << 3 | (popc(src & P.M4) & 1) << 4), P.PM, 7);
}
constexpr Tab make_tab(const Prm& P) {
  Tab T{};
  for (int bid = 0; bid < 256; ++bid) {
    const int xcd = bid & 7, idx = bid >> 3, part = idx & 3, i8 = idx >> 2, wi = i8 * 8 + xcd;
    const bool has_mod = wi < 48, has_up = wi >= 21;
    const int cb0 = (i8 + 5) & 7, cb1 = (i8 + 4) & 7, ms = wi - 20;
    const int mi = wi < 8 ? wi : (wi < 16 ? 8 + ((xcd + 4) & 7) : (wi < 20 ? (wi - 14) * 8 + 3 : 16 + ms / 7 * 8 + ms % 7 + (ms % 7 >= 3 ? 1 : 0)));
    const int ui = xcd * 5 + (xcd > 5 ? xcd - 5 : 0) + (i8 >= 3 ? (xcd >= 5 ? 1 : 0) + i8 - 3 : 0);
    const int wd = cb0 == 3 ? 0 : P.WD;
    const int e[4] = {P.E0 - 4 * wd + (cb0 == 3 ? P.ES : 0), P.E1 + (cb1 == 3 ? P.ES : 0), has_mod ? (wi < 16 ? P.E2C : P.E2) + ((mi & 7) == 3 ? P.ES : 0) : 0, has_up ? P.EU * (8 - UD) / 8 : 0};
    const int ew = wd ? P.EW : 0, pa = (1024 + e[0] + ew + e[1] + e[2] + e[3] + (has_up ? P.EX : 0)) / 4;
    int c[5] = {0, 0, 0, 0, 128};
    for (int k = 0; k < 3; ++k) c[k + 1] = c[k] + (pa - e[k] - (k == 0 ? ew : 0) + 4) / 8;
    const int nl = c[part + 1] - c[part], wst = e[part] / 4;
    const bool has_w = part < 2 || (part == 2 ? has_mod : has_up);
    const int blk = !has_w ? 0 : (part == 0 ? cb0 : (part == 1 ? xcd * 8 + cb1 : (part == 2 ? mi : ui)));
    const int span = (part == 3 && has_up ? P.TEND - P.DT_UP : (part == 0 ? P.TEND - (wd ? P.DT_WO : P.DT_0) : P.TEND)) * 256;
    const int np = part == 0 ? 8 - wd : (part == 3 ? 8 - UD : 8), tau0 = span / (2 * nl + wst);
    const bool up = part == 3 && has_up;
    const int tau = tau0 * (up ? P.FU : (has_w && np == 8 ? P.FK : 64)) / 64, tauw = tau0 * wst / np * (up ? P.FU : P.FW) / 64;
    int sw = KF, at = P.DS * 256 + (has_w ? np * tauw : 0);
    while (sw < NCMIN / 8 - 1 && at < P.TS * 256) { at += tau; ++sw; }
    const int tau2 = (span - at) / (2 * nl - sw);
    T.e[4 * bid] = (u32)(c[part] | nl << 7 | blk << 13 | (has_w ? 1 : 0) << 19 | (8 - np) << 20 | sw << 24);
    T.e[4 * bid + 1] = (u32)(tau | tauw << 16);
    if ((part == 1 && nl <= QP2 + QD + 1) || c[part] > 32 * part + KF0 || c[part + 1] < 32 * part + KF0 + 4) T.e[4 * bid] = 0;
    const u32 pitch = part == 0 ? 1024u : (part == 1 ? 8192u : (part == 2 ? 6144u : 2752u));
    T.e[4 * bid + 2] = (part == 0 ? xcd * 512u * pitch : 0u) + blk * (part == 3 ? 64u : 128u);
    T.e[4 * bid + 3] = pitch | (u32)tau2 << 16;
  }
  return T;
}
constexpr int step_range(const Prm& P, bool mx_) {
  int mx = 0, mn = 1 << 20;
  const Tab T = make_tab(P);
  for (int bid = 0; bid < 256; ++bid) { const int n = T.e[4 * bid] >> 7 & 63; mx = n > mx ? n : mx; mn = n < mn ? n : mn; }
  return mx_ ? mx : mn;
}
__constant__ __attribute__((aligned(64))) const Tab TAB = make_tab(PRM0);
static_assert(step_range(PRM0, true) <= NL && step_range(PRM0, false) >= NCMIN / 8, "a wave's key range does not fit its load steps");
static_assert(cons_end(SIDE + WV + 1) <= NCMIN / 8 && cons_end(UPREP) <= NCMIN / 8 && SIDE > MLS + 3, "steps every wave runs");
constexpr int NUP = FF / 32;
constexpr int SPIN_MAX = 1 << 17;

constexpr u32 WS_EPOCH = 0;
constexpr u32 G_TB = 1024;
constexpr u32 G_NT = 2048;
constexpr u32 G_MOD = 4096;
constexpr u32 G_Q = G_MOD + 3072 * 8;
constexpr u32 G_AG = G_Q + 4096 * 8;
constexpr u32 G_ML = G_AG + 4096;
constexpr u32 G_P = 81920;
constexpr u32 G_U = G_P + 65536;
__device__ __forceinline__ u32 dup(u32 o) { return G_U + ((o >> 13) << 14) + (o & 8191u); }
constexpr u32 G_XP = 0;
constexpr u32 G_OP = G_XP + 8 * 512 * 4;
constexpr u32 G_PB = G_U + 2 * (G_OP + 44 * 512 * 8);
constexpr u32 WS_END = G_PB + 16384;
static_assert(G_P % 16384 == 0 && G_PB % 16384 == 0 && G_ML + 4096 <= G_P, "layout");
constexpr u32 SENT = 0xffffffffu;
constexpr u32 OOB = 0x40000000u;
#define WST 0

constexpr int AUX_NT = 2, AUX_SC1 = 16;

__device__ __forceinline__ float lo_f(u32 v) { return __uint_as_float(v << 16); }
__device__ __forceinline__ float hi_f(u32 v) { return __uint_as_float(v & 0xffff0000u); }
__device__ __forceinline__ float rbf(float x) { return (float)(__bf16)x; }
__device__ __forceinline__ u32 pack_bf(float lo, float hi) {
  bf16x2 r; r[0] = (__bf16)lo; r[1] = (__bf16)hi; return __builtin_bit_cast(u32, r);
}
__device__ __forceinline__ float dot2(u32 k, u32 q, float acc) {
  return __builtin_amdgcn_fdot2_f32_bf16(__builtin_bit_cast(bf16x2, k), __builtin_bit_cast(bf16x2, q), acc, false);
}
template <int CTRL> __device__ __forceinline__ float dpp(float x) {
  return __builtin_bit_cast(float, __builtin_amdgcn_mov_dpp(__builtin_bit_cast(int, x), CTRL, 0xf, 0xf, true));
}
constexpr int XOR1 = 0xB1, XOR2 = 0x4E, XOR7 = 0x141, XOR8 = 0x128;
__device__ __forceinline__ float swap32_add(float a, float b) {
  auto t = __builtin_amdgcn_permlane32_swap(__float_as_uint(a), __float_as_uint(b), false, false);
  return __uint_as_float(t[0]) + __uint_as_float(t[1]);
}
__device__ __forceinline__ float swap16_add(float a, float b) {
  auto t = __builtin_amdgcn_permlane16_swap(__float_as_uint(a), __float_as_uint(b), false, false);
  return __uint_as_float(t[0]) + __uint_as_float(t[1]);
}
__device__ __forceinline__ float wave_sum(float x) {
  x += dpp<XOR1>(x); x += dpp<XOR2>(x); x += dpp<XOR7>(x); x += dpp<XOR8>(x);
  x = swap16_add(x, x);
  return swap32_add(x, x);
}
__device__ __forceinline__ float wave_max(float x) {
  x = fmaxf(x, dpp<XOR1>(x)); x = fmaxf(x, dpp<XOR2>(x)); x = fmaxf(x, dpp<XOR7>(x)); x = fmaxf(x, dpp<XOR8>(x));
  auto s = __builtin_amdgcn_permlane16_swap(__float_as_uint(x), __float_as_uint(x), false, false);
  x = fmaxf(__uint_as_float(s[0]), __uint_as_float(s[1]));
  auto t = __builtin_amdgcn_permlane32_swap(__float_as_uint(x), __float_as_uint(x), false, false);
  return fmaxf(__uint_as_float(t[0]), __uint_as_float(t[1]));
}
template <int CTRL> __device__ __forceinline__ float sel_add(float lo, float hi, bool up) {
  const float t1 = lo + dpp<CTRL>(lo), t2 = hi + dpp<CTRL>(hi);
  return up ? t2 : t1;
}
template <int I> __device__ __forceinline__ float bc16(float x) {
  return __builtin_bit_cast(float, __builtin_amdgcn_update_dpp(0, __builtin_bit_cast(int, x), 0x150 + I, 0xf, 0xf, false));
}
__device__ __forceinline__ float bcast16(float x, int i) {
  switch (i) {
    case 0: return bc16<0>(x); case 1: return bc16<1>(x); case 2: return bc16<2>(x); case 3: return bc16<3>(x);
    case 4: return bc16<4>(x); case 5: return bc16<5>(x); case 6: return bc16<6>(x); case 7: return bc16<7>(x);
    case 8: return bc16<8>(x); case 9: return bc16<9>(x); case 10: return bc16<10>(x); case 11: return bc16<11>(x);
    case 12: return bc16<12>(x); case 13: return bc16<13>(x); case 14: return bc16<14>(x); default: return bc16<15>(x);
  }
}
__device__ __forceinline__ float silu_f(float x) { return x * __builtin_amdgcn_rcpf(1.f + __expf(-x)); }

template <int AUX> __device__ __forceinline__ u32x4 ld16(rsrc_t r, u32 voff, u32 soff) {
  return __builtin_amdgcn_raw_buffer_load_b128(r, voff, soff, AUX);
}
template <int AUX> __device__ __forceinline__ u32x2 ld8(rsrc_t r, u32 voff, u32 soff) {
  return __builtin_amdgcn_raw_buffer_load_b64(r, voff, soff, AUX);
}
__device__ __forceinline__ void st8_sc1(rsrc_t r, u32 voff, u32 v, u32 tag) {
  __builtin_amdgcn_raw_buffer_store_b64(u32x2{v, tag}, r, voff, 0, AUX_SC1);
}
__device__ __forceinline__ void st16_sc1(rsrc_t r, u32 voff, u32 v0, u32 v1, u32 tag) {
  __builtin_amdgcn_raw_buffer_store_b128(u32x4{v0, tag, v1, tag}, r, voff, 0, AUX_SC1);
}
__device__ __forceinline__ rsrc_t mk_rsrc(const void* p, u32 bytes) {
  return __builtin_amdgcn_make_buffer_rsrc(const_cast<void*>(p), 0, bytes, 0x00020000);
}
#define WAIT_VM(n) __builtin_amdgcn_s_waitcnt(((n) & 15) | (((n) >> 4) << 14) | (7 << 4) | (15 << 8))
#define SB() __builtin_amdgcn_sched_barrier(0)
#define CFENCE() asm volatile("" ::: "memory")
__device__ __forceinline__ bool all_ok(bool ok) { return __builtin_amdgcn_ballot_w64(!ok) == 0ull; }

template <int N> __device__ __forceinline__ void gather16(rsrc_t ws, u32 voff, u32 stride, u32 tag, u32x4 (&g)[N]) {
#pragma nounroll
  for (int spin = 0; spin < SPIN_MAX; ++spin) {
    CFENCE();
    bool ok = true;
#pragma unroll
    for (int i = 0; i < N; ++i) g[i] = ld16<AUX_SC1>(ws, voff + i * stride, 0);
#pragma unroll
    for (int i = 0; i < N; ++i) ok &= (g[i][1] == tag) & (g[i][3] == tag);
    if (all_ok(ok)) break;
    __builtin_amdgcn_s_sleep(8);
  }
}
template <int N> __device__ __forceinline__ void gather8(rsrc_t ws, u32 voff, u32 stride, u32 tag, u32 (&g)[N], bool act = true) {
#pragma nounroll
  for (int spin = 0; spin < SPIN_MAX; ++spin) {
    CFENCE();
    bool ok = true;
    u32x2 t[N];
#pragma unroll
    for (int i = 0; i < N; ++i) t[i] = ld8<AUX_SC1>(ws, voff + i * stride, 0);
#pragma unroll
    for (int i = 0; i < N; ++i) { ok &= (t[i][1] == tag); g[i] = t[i][0]; }
    if (all_ok(ok | !act)) break;
    __builtin_amdgcn_s_sleep(8);
  }
}

struct Smem {
  float red[8][128];
  float ml[2][8];
  float r2[64 * 20];
  u16 hb[3 * 1024];
  float stat[8][64];
  float red2[8][128];
  u32 dummy[64];
  u32x2 bc;
  u32x2 ctl;
  u32 nm, pad_[3];
  u32x4 zero[4];
  u32x4 wbuf[8][8][64];
  __attribute__((aligned(256))) u16 w2b[32][512];
  u16 actl[2][32];
};

#define STAMP(k) do {} while (0)
#define WSTAMP(k) do {} while (0)
#define FINISH() return
#define PIN(x) asm volatile("" : "+v"(x))
#define LDS_BARRIER() do { __builtin_amdgcn_s_waitcnt(0xc07f); __builtin_amdgcn_s_barrier(); CFENCE(); } while (0)
#define WAVE_SYNC() do { __builtin_amdgcn_fence(__ATOMIC_RELEASE, "wavefront"); __builtin_amdgcn_wave_barrier(); __builtin_amdgcn_fence(__ATOMIC_ACQUIRE, "wavefront"); } while (0)

__device__ __forceinline__ void dma16(rsrc_t r, u32 voff, u32 soff, u32 lds_dst) {
  u32 keep;
  asm volatile("s_mov_b32 %0, m0\n\ts_mov_b32 m0, %4\n\ts_nop 4\n\tbuffer_load_dwordx4 %1, %2, %3 offen nt lds\n\ts_mov_b32 m0, %0"
               : "=&s"(keep) : "v"(voff), "s"(r), "s"(soff), "s"(lds_dst) : "memory");
}
__device__ __forceinline__ u32 perm_lo(u32 a, u32 b) { return __builtin_amdgcn_perm(b, a, 0x05040100u); }
__device__ __forceinline__ u32 perm_hi(u32 a, u32 b) { return __builtin_amdgcn_perm(b, a, 0x07060302u); }
__device__ __forceinline__ float rdl(float x, int l) { return __int_as_float(__builtin_amdgcn_readlane(__float_as_int(x), l)); }

typedef __attribute__((ext_vector_type(8))) __bf16 bf16x8;
__device__ __forceinline__ f32x4 mfma16(u32x4 a, u32x4 b, f32x4 c) {
  return __builtin_amdgcn_mfma_f32_16x16x32_bf16(__builtin_bit_cast(bf16x8, a), __builtin_bit_cast(bf16x8, b), c, 0, 0, 0);
}
__device__ __forceinline__ float sum8(const float* p) {
  const f32x4 a = *reinterpret_cast<const f32x4*>(p), b = *reinterpret_cast<const f32x4*>(p + 4);
  return ((a[0] + b[0]) + (a[1] + b[1])) + ((a[2] + b[2]) + (a[3] + b[3]));
}
__device__ __forceinline__ float ln_ri(float var) {
  return __builtin_amdgcn_rcpf(rbf(__builtin_amdgcn_sqrtf(rbf(rbf(fmaxf(var, 0.f)) + 1e-5f))));
}
__device__ __forceinline__ void ln_stats2(const float (&x0)[8], const float (&x1)[8], float& mb0, float& ri0, float& mb1, float& ri1) {
  const float k0 = rdl(x0[0], 0), k1 = rdl(x1[0], 0);
  float s0 = 0.f, s1 = 0.f, q0 = 0.f, q1 = 0.f;
#pragma unroll
  for (int k = 0; k < 8; ++k) {
    const float d0 = x0[k] - k0, d1 = x1[k] - k1;
    s0 += d0; s1 += d1; q0 = fmaf(d0, d0, q0); q1 = fmaf(d1, d1, q1);
  }
  float t = swap16_add(swap32_add(s0, s1), swap32_add(q0, q1));
  t += dpp<XOR1>(t); t += dpp<XOR2>(t); t += dpp<XOR7>(t); t += dpp<XOR8>(t);
  auto u = __builtin_amdgcn_permlane16_swap(__float_as_uint(t), __float_as_uint(t), false, false);
  const float ms = __uint_as_float(u[0]) * (1.f / 512.f), mq = __uint_as_float(u[1]) * (1.f / 512.f);
  const float ri = ln_ri(fmaf(-ms, ms, mq));
  mb0 = rbf(k0 + rdl(ms, 0)); mb1 = rbf(k1 + rdl(ms, 32));
  ri0 = rdl(ri, 0); ri1 = rdl(ri, 32);
}
template <int I> struct IC { static constexpr int v = I; };
template <int I, int N, class F> __device__ __forceinline__ void sfor(F&& f) {
  if constexpr (I < N) { if (f(IC<I>{})) sfor<I + 1, N>(f); }
}
typedef __attribute__((ext_vector_type(4))) short s16x4;
typedef __attribute__((address_space(3))) s16x4* lds_s16x4p;
__device__ __forceinline__ u32x2 tr_read(u32 lds_addr) {
  return __builtin_bit_cast(u32x2, __builtin_amdgcn_ds_read_tr16_b64_v4i16((lds_s16x4p)lds_addr));
}
__device__ __forceinline__ u32 next_tag(u32 e) { return ((e | 0x40004000u) + 1u) & ~0x40004000u; }
__device__ __forceinline__ float u2f(u32 v) { return __uint_as_float(v); }
#define LDSP(T, a) ((__attribute__((address_space(3))) T*)(a))
template <class T> __device__ __forceinline__ u32 lds_addr(T* p) { u32 a = (u32)(uintptr_t)p; PIN(a); return a; }

typedef __attribute__((ext_vector_type(16))) unsigned u32x16;
typedef __attribute__((ext_vector_type(8))) unsigned u32x8;
__global__ void __launch_bounds__(512, 2) fused_kernel(const u16* x_, const u16* kc_, const u16* vc_, const u16* wq_, const u16* wo_, const u16* w1_,
                                                       const u16* w3_, const u16* w2_, const u16* cnd_, const u16* wmod_, unsigned char* ws_, u16* out_ PRM_ARG) {
  __shared__ __attribute__((aligned(256))) Smem sm;
  constexpr Prm P = PRM0;
  const int TEND = P.TEND;
  const int bid = blockIdx.x, tid = threadIdx.x;
  u32x4 kr[NL];
  u32x16 ka; u32x8 kb; u32x4 tev; u64 pt0; u32 tch2;
  asm volatile("s_load_dwordx16 %0, %3, 0x0\n\ts_load_dwordx8 %1, %3, 0x40\n\ts_memrealtime %2"
               : "=&s"(ka), "=&s"(kb), "=&s"(pt0) : "s"(__builtin_amdgcn_kernarg_segment_ptr()));
  SB();
  {
    u64 pc; u32 vb;
    asm volatile("s_getpc_b64 %2\n.Lcpf_first:\n\tv_mul_u32_u24 %1, ((.Lend_c-.Lcpf_first)>>10)<<2, %3\n\tglobal_load_dword %0, %1, %2"
                 : "=&v"(tch2), "=&v"(vb), "=&s"(pc) : "v"((bid & ~7) + (tid >> 6)) : "memory");
    asm volatile("global_load_dwordx4 %0, %1, %2" : "=&v"(tev) : "v"(bid << 4), "s"(TABP) : "memory");
  }
  SB();
  const int xcd = bid & 7, idx = bid >> 3, part = idx & 3;
  const int b = idx >> 2 & 1, head = xcd * 4 + (((idx >> 3) & 1) << 1 | idx >> 4);
  const u32 so0 = step_off(P, 32 * part + KF0, b * 32 + head) & ~place(3, P.PM, 2);
  u32 va = (((b * 32 + head) * SK + (SK - WIN)) * DH * 2 + so0) | place(tid >> 6, P.WM, 3) | (tid & 63) << 4;
  const u32 kx1 = place(1, P.PM, 2), kx2 = place(2, P.PM, 2);
  PIN(va);
  u32 va1 = va | kx1, va2 = va | kx2, va3 = va | kx1 | kx2;
  PIN(va1); PIN(va2); PIN(va3);
  int mc = bid & 0xd8;
  asm volatile("" : "+v"(va), "+s"(mc));
  SB();
  asm volatile("s_waitcnt lgkmcnt(0)" : "+s"(ka), "+s"(kb), "+s"(pt0));
  SB();
#define K_FIRST "global_load_dwordx4 %0, %4, %5 nt\n\tglobal_load_dwordx4 %1, %6, %5 nt\n\tglobal_load_dwordx4 %2, %7, %5 nt\n\tglobal_load_dwordx4 %3, %8, %5 nt"
  asm volatile("s_cmp_eq_u32 %9, 16\n\ts_cbranch_scc1 .Lk_later\n\t" K_FIRST "\n.Lk_later:"
               : "=&v"(kr[0]), "=&v"(kr[1]), "=&v"(kr[2]), "=&v"(kr[3]) : "v"(va), "s"((u64)ka[2] | (u64)ka[3] << 32), "v"(va1), "v"(va2), "v"(va3), "s"(mc) : "memory", "scc");
  SB();
  const u32 tka = (u32)wall_clock64(), t0 = tka - D0;
  auto kap = [&](int k) { return k < 8 ? (u64)ka[2 * k] | (u64)ka[2 * k + 1] << 32 : (u64)kb[2 * k - 16] | (u64)kb[2 * k - 15] << 32; };
  const u16 *x = (const u16*)kap(0), *kc = (const u16*)kap(1), *vc = (const u16*)kap(2), *w2 = (const u16*)kap(7), *cnd = (const u16*)kap(8);
  unsigned char* ws = (unsigned char*)kap(10); u16* out = (u16*)kap(11);
  const int lane = tid & 63, wave = __builtin_amdgcn_readfirstlane(tid >> 6);
  const bool p3 = part == 3;
  const u64 wa = part == 0 ? kap(4) : (part == 1 ? kap(3) : (part == 2 ? kap(9) : ((wave & 1) ? kap(6) : kap(5))));
  const bool modc = mc == 16;
  if (modc) asm volatile("s_waitcnt vmcnt(0)" ::: "memory");
  asm volatile("s_waitcnt vmcnt(4)" : "+v"(tev));
  const u32 te0 = __builtin_amdgcn_readfirstlane(tev[0]), te1 = __builtin_amdgcn_readfirstlane(tev[1]);
  const u32 te3 = __builtin_amdgcn_readfirstlane(tev[3]), pitch = te3 & 0xffff, wsz = pitch << (part == 0 ? 12 : 9);
  const int sh = p3 ? 2 : 3;
  u32 wv0 = (u32)((p3 ? (wave >> 1) * 16 : wave * 64) + (lane >> sh)) * pitch + ((lane & (p3 ? 3 : 7)) ^ (lane >> (p3 ? 5 : 3)) ^ (p3 ? wave >> 2 << 1 : 0)) * 16u;
  const u32 wimg = (u32)(uintptr_t)(&sm.wbuf[0][0][0]);
  u32 wld = wimg + (p3 ? (wave & 1) * 32768 + (wave >> 1) * 1024 : wave * 8192);
  const int bl0 = te0 & 127, nl = te0 >> 7 & 63, blk = te0 >> 13 & 63;
  const bool has_w = te0 >> 19 & 1;
  const rsrc_t rw = mk_rsrc((const void*)wa, wsz);
  const u32 wstep = pitch << (p3 ? 6 : 3), lstep = p3 ? 4096u : 1024u;
  u32 wso = __builtin_amdgcn_readfirstlane(tev[2]);
  int wleft = 8;
  auto piece = [&]() {
    dma16(rw, wv0, wso, wld);
    wso += wstep; wld += lstep;
    --wleft;
  };
  int tau = te1 & 0xffff;
  const int tauw = te1 >> 16, sw = te0 >> 24;
  int tau2 = te3 >> 16;
  int due = P.DS * 256, now = 0;
  auto gate = [&]() {
    while (now < due) { now = (int)(((u32)wall_clock64() - t0) << 8); if (now < due) __builtin_amdgcn_s_sleep(1); }
    due += tau;
  };
  const rsrc_t wsr = mk_rsrc(ws, WS_END);
  const int g = lane >> 4, c = lane & 15;
  const int i8 = idx >> 2, wi = i8 * 8 + xcd;
  const bool is_up = p3 && has_w;
  const size_t base = ((size_t)(b * NH + head) * SK + (SK - WIN)) * DH;
  const rsrc_t rk = mk_rsrc(kc + base, WIN * DH * 2), rv = mk_rsrc(vc + base, WIN * DH * 2);
  u32 sot;
  {
    u32 s0 = bl0 + lane - KF;
    s0 += s0 >= (u32)(32 * part + KF0) ? KF : 0;
    sot = (lane < KF ? so0 | place(lane, P.PM, 2) : step_off(P, s0, b * 32 + head)) | place(wave, P.WM, 3);
    sot = lane < nl ? sot : OOB;
    PIN(sot);
  }
  auto until = [&](int t) { while ((int)(((u32)wall_clock64() - t0) << 8) < t); };
  u32 near = 0, nmask = 0;
  int slack = 0, stv = 0;
  const rsrc_t rx = mk_rsrc(part == 2 ? cnd : x, (part == 1 || (part == 2 && has_w)) ? 2048u : 0u);
  u32x2 ep; u32 xs0, xs1, tch1; u32x4 xr0, xr1;
  auto small_ops = [&]() {
    ep = ld8<AUX_SC1>(wsr, wave == 7 ? WS_EPOCH : OOB, 0);
    xs0 = __builtin_amdgcn_raw_buffer_load_b16(rx, tid * 2, 0, 0); xs1 = __builtin_amdgcn_raw_buffer_load_b16(rx, tid * 2, 1024, 0);
  };
  auto small_ops2 = [&]() {
    xr0 = ld16<0>(rx, part == 1 ? lane * 16 : OOB, 0); xr1 = ld16<0>(rx, part == 1 ? lane * 16 : OOB, 1024);
    tch1 = __builtin_amdgcn_raw_buffer_load_b32(mk_rsrc(w2 + (size_t)blk * 32 * 512, is_up ? 64u : 0u), tid == 384 ? 0 : OOB, 0, AUX_NT);
  };
  u32 tag = 0;
  u32x4 bf[8];
  auto wo_prep = [&](int s0 = 0, int s1 = 2) {
    const u32 wi_ = wimg + wave * 8192;
    const int q = (lane >> 2) & 3, pp = lane & 3;
#pragma unroll
    for (int s = s0; s < s1; ++s) {
#pragma unroll
      for (int t = 0; t < 4; ++t) {
        const int r0 = s * 32 + g * 8 + q, r1 = r0 + 4, ch = t * 2 + (pp >> 1);
        const u32x2 lo = tr_read(wi_ + r0 * 128 + ((ch ^ (r0 & 7)) * 16) + (pp & 1) * 8), hi = tr_read(wi_ + r1 * 128 + ((ch ^ (r1 & 7)) * 16) + (pp & 1) * 8);
        bf[s * 4 + t] = u32x4{lo[0], lo[1], hi[0], hi[1]};
      }
    }
  };
  auto gemv_go = [&](u32 hh) {
    sm.hb[tid] = (u16)hh; sm.hb[512 + tid] = (u16)(hh >> 16);
    WAVE_SYNC();
    const u32x4* a = (c & 11) == 0 ? reinterpret_cast<const u32x4*>(&sm.hb[(c >> 2) * 512 + wave * 64 + g * 8]) : &sm.zero[0];
    const u32x4 a0 = a[0], a1 = a[(c & 11) == 0 ? 4 : 0];
    const f32x4 z = {0.f, 0.f, 0.f, 0.f};
    f32x4 d[4];
#pragma unroll
    for (int t = 0; t < 4; ++t) d[t] = mfma16(a0, bf[t], z);
#pragma unroll
    for (int t = 0; t < 4; ++t) d[t] = mfma16(a1, bf[4 + t], d[t]);
    if (lane < 32) {
#pragma unroll
      for (int t = 0; t < 4; ++t) sm.r2[(t * 16 + c) * 20 + g * 8 + wave] = d[t][0];
    }
  };
  auto mod_gemv = [&]() { gemv_go(pack_bf(silu_f(lo_f(xs0)), silu_f(lo_f(xs1)))); };
  auto mod_put = [&]() { st8_sc1(wsr, G_MOD + (blk * 64 + lane) * 8, pack_bf(sum8(&sm.r2[lane * 20]), sum8(&sm.r2[lane * 20 + 8])), tag); };
  auto zero_rows = [&]() { if (lane < 4) sm.zero[lane] = u32x4{0u, 0u, 0u, 0u}; };
  u32 l16 = lane * 16;
  asm volatile("" : "+v"(l16), "+v"(wv0) : "s"(rk), "s"(rx), "s"(wsr));
  SB();
  if (has_w) {
    if (__builtin_expect(modc, 0)) {
      now = 1 << 30;
      const int sg_ = lane >> 3;
      const u32 xl = sg_ < 2 ? G_ML + wi * 256 + sg_ * 128 : (sg_ < 4 ? G_PB + wi * 128 + (sg_ & 1) * 8192 : (sg_ == 4 ? G_NT + wi * 128 : (sg_ == 5 && wi < 3 ? G_AG + wi * 128 : OOB)));
      __builtin_amdgcn_raw_buffer_store_b128(u32x4{SENT, SENT, SENT, SENT}, wsr, tid < 384 ? G_P + wi * 6144 + tid * 16 : (wave == 7 ? xl + (lane & 7) * 16 : OOB), 0, AUX_SC1);
    }
#pragma nounroll
    while (wleft > (int)(te0 >> 20 & 7)) { gate(); due += tauw - tau; piece(); }
    now = 0;
  }
  SB();
  STAMP(23);
  if (__builtin_expect(modc, 0)) {
    small_ops();
    zero_rows();
    {
      until(P.PT * 256);
      const u64 pa = (u64)ws + G_PB + (((wi & 8) + wave + xcd * 2) & 15) * 128 + (wave & 1) * 8192;
      u64 c0, c1, c2; u32 d0, d1;
      asm volatile("s_memtime %0\n\ts_waitcnt lgkmcnt(0)\n\ts_load_dword %3, %5, 0x0\n\ts_waitcnt lgkmcnt(0)\n\ts_memtime %1\n\ts_waitcnt lgkmcnt(0)\n\t"
                   "s_load_dword %4, %6, 0x0\n\ts_waitcnt lgkmcnt(0)\n\ts_memtime %2\n\ts_waitcnt lgkmcnt(0)"
                   : "=&s"(c0), "=&s"(c1), "=&s"(c2), "=&s"(d0), "=&s"(d1) : "s"(pa), "s"(pa + 8192 - (wave & 1) * 16384) : "memory");
      const int df = min(max((int)(2 * (u32)c1 - (u32)c0 - (u32)c2), -600), 600);
      if (lane == 0) sm.stat[0][wave] = (float)(((wave ^ (xcd >> 2)) & 1) ? -df : df);
    }
    asm volatile("s_waitcnt vmcnt(0)" ::: "memory");
    tag = next_tag(__builtin_amdgcn_readfirstlane(ep[0]));
    wo_prep();
    mod_gemv();
    LDS_BARRIER();
    if (wave == 7) { mod_put(); if (lane == 0) st8_sc1(wsr, G_AG + (48 + wi) * 8, __float_as_uint(sum8(&sm.stat[0][0])), tag); }
    STAMP(9);
    __builtin_amdgcn_s_barrier();
    asm volatile(K_FIRST :: "v"(kr[0]), "v"(kr[1]), "v"(kr[2]), "v"(kr[3]), "v"(va), "s"((u64)ka[2] | (u64)ka[3] << 32), "v"(va1), "v"(va2), "v"(va3) : "memory");
  }
  SB();
  const bool fin = (part == 1 || part == 2) && wave == 0 && xcd < 2;
  const int fw = i8 * 4 + (part & 2) + xcd, fd = fw * 16 + (lane & 7) * 2;
  const int ub = wave >> 2, ud = (wave & 3) * 128 + c * 8 + g * 2;
  u32x4 sg[3]; u32 sx2;
  auto side_loads = [&]() {
    sg[0] = ld16<AUX_SC1>(wsr, is_up ? G_MOD + (1024 + ud) * 8 : (fin ? G_MOD + (1024 + fd) * 8 : OOB), 0);
    sg[1] = ld16<AUX_SC1>(wsr, is_up ? G_MOD + (1536 + ud) * 8 : (fin ? G_MOD + (2560 + fd) * 8 : OOB), 0);
    sg[2] = ld16<AUX_SC1>(wsr, is_up ? G_MOD + (2048 + ud) * 8 : OOB, 0);
    sx2 = __builtin_amdgcn_raw_buffer_load_b32(mk_rsrc(x, (is_up || fin) ? 2048u : 0u), is_up ? ub * 1024 + ud * 2 : (g & 1) * 1024 + fd * 2, 0, 0);
  };
  auto side_pin = [&]() { PIN(sg[0]); PIN(sg[1]); PIN(sg[2]); PIN(sx2); };
  const u32 mlo = G_ML + (head * 2 + b) * 64 + (lane & 3) * 16;
  float fwv = 0.f, fmw[8];
  bool fok = wave != 0;
  auto merge_factor = [&](const u32x4& ml) {
    const float mi = u2f(ml[0]);
    float M = fmaxf(mi, dpp<XOR1>(mi)); M = fmaxf(M, dpp<XOR2>(M));
    const float e = __expf(mi - M);
    float L = e * u2f(ml[2]);
    L += dpp<XOR1>(L); L += dpp<XOR2>(L);
    const float f = fwv * rdl(e * __builtin_amdgcn_rcpf(L), part);
#pragma unroll
    for (int w = 0; w < 8; ++w) fmw[w] = bcast16(f, w);
  };
  f32x2 ga = {0.f, 0.f}, xin = {0.f, 0.f}, cc1 = {0.f, 0.f};
  float Af = 0.f, Bf = 0.f;
  const int hpos = ub * 512 + ud;
  auto up_prep = [&]() {
    if (!all_ok((sg[0][1] == tag) & (sg[0][3] == tag) & (sg[1][1] == tag) & (sg[1][3] == tag) & (sg[2][1] == tag) & (sg[2][3] == tag))) gather16<3>(wsr, G_MOD + (1024 + ud) * 8, 512 * 8, tag, sg);
    ga = f32x2{ub ? hi_f(sg[0][0]) : lo_f(sg[0][0]), ub ? hi_f(sg[0][2]) : lo_f(sg[0][2])};
    cc1 = f32x2{rbf(1.f + (ub ? hi_f(sg[2][0]) : lo_f(sg[2][0]))), rbf(1.f + (ub ? hi_f(sg[2][2]) : lo_f(sg[2][2])))};
    xin = f32x2{lo_f(sx2), hi_f(sx2)};
    u32* hp = reinterpret_cast<u32*>(&sm.hb[hpos]);
    hp[512] = pack_bf(cc1[0], cc1[1]);
    hp[1024] = ub ? perm_hi(sg[1][0], sg[1][2]) : perm_lo(sg[1][0], sg[1][2]);
  };
  auto up_addr = [&]() {
    const int pp = lane & 3;
    return wimg + (pp & 1) * 32768 + ((pp >> 1) * 32 + g * 8 + ((lane >> 2) & 3)) * 64 + (((wave >> 1) ^ ((g & 1) | (pp & 2))) * 16) + (wave & 1) * 8;
  };
  auto up_frags = [&](int r0, int r1) {
    const u32 a = up_addr();
#pragma unroll
    for (int rb = r0; rb < r1; ++rb) {
      const u32x2 lo = tr_read(a + rb * 4096), hi = tr_read(a + rb * 4096 + 256);
      bf[rb] = u32x4{lo[0], lo[1], hi[0], hi[1]};
    }
  };
  const u32x4* ap = reinterpret_cast<const u32x4*>(&sm.hb[((lane >> 3) & 1) * 512 + (lane & 1) * 32 + g * 8]);
  const bool par = lane & 8;
  auto col_sums = [&](int r0, int r1) {
    const u32x4* a1 = ap + 128 + ((lane >> 1) & 1) * 128;
    f32x4 d0 = {0.f, 0.f, 0.f, 0.f}, d1 = {0.f, 0.f, 0.f, 0.f};
#pragma unroll
    for (int rb = r0; rb < r1; ++rb) { if (rb & 1) d1 = mfma16(a1[rb * 8], bf[rb], d1); else d0 = mfma16(a1[rb * 8], bf[rb], d0); }
    Af += par ? d0[1] + d1[1] : d0[0] + d1[0]; Bf += par ? d0[3] + d1[3] : d0[2] + d1[2];
  };
  float l0 = 0.f, l1 = 0.f;
  u32x2 gmd[2], tbg;
  const bool modw = part == 2 && has_w && !modc;
  const int qp = part != 1 ? -99 : ((blk & 7) == 3 ? QP1 : QP2);
  static_assert(QP1 + QD < QP2 && QP1 + QD > WUSE && QP1 + QD > TBU && WUSE + 1 < TBU, "order of the q steps");
  int t1 = 0;
#pragma unroll
  for (int i = NCMIN / 8; i < NL; ++i) kr[i] = u32x4{0u, 0u, 0u, 0u};
  sfor<KF, NL>([&](auto ic) {
    constexpr int i = ic.v;
    if (i >= NCMIN / 8 && i >= nl) return false;
    const u32 so = __builtin_amdgcn_readlane(sot, i);
    if constexpr (i < NCMIN / 8) tau = i == sw ? tau2 : tau;
    gate();
    SB();
    kr[i] = ld16<AUX_NT>(rk, l16, so);
    if constexpr (i == KF) small_ops();
    if constexpr (i == KF + 1) small_ops2();
    if constexpr (i == KF + 2) zero_rows();
    if constexpr (i == TBL) tbg = ld8<AUX_SC1>(wsr, wave == 7 ? G_TB + (lane & 7) * 8 : OOB, 0);
    if constexpr (i == QP1 || i == QP2) { gmd[0] = ld8<AUX_SC1>(wsr, i == qp ? G_MOD + tid * 8 : OOB, 0); gmd[1] = ld8<AUX_SC1>(wsr, i == qp ? G_MOD + tid * 8 : OOB, 4096); }
    SB();
    SB();
    if (i >= WK) { WAIT_VM(win_k(i)); SB(); }
    if (i == KF) STAMP(25);
    if (i == 8) STAMP(12);
    if (i == 20) STAMP(21);
    if (i == 25) STAMP(22);
    if (i == TBP) {
      if (part == 3 && i8 == 0 && wave == 7) { tag = next_tag(__builtin_amdgcn_readfirstlane(ep[0])); if (lane == 0) st8_sc1(wsr, G_TB + xcd * 8, tka - P.GS, tag); }
      SB();
    }
    if (i == TBU - 1) {
      if (wave == 7) {
        const bool ok = tbg[1] == tag;
        float lf = ok ? (float)min(max((int)(t0 - tbg[0]), -300), 300) : 0.f, nf = ok ? 1.f : 0.f;
        lf += dpp<XOR1>(lf); nf += dpp<XOR1>(nf); lf += dpp<XOR2>(lf); nf += dpp<XOR2>(nf);
        lf *= __builtin_amdgcn_rcpf(fmaxf(nf, 1.f));
        const float ot = dpp<XOR7>(lf);
        if (lane == 0) sm.bc = u32x2{tag, t0 - (u32)(int)((lf + ot) * 0.5f - fabsf(lf - ot) * (P.BT / 32.f))};
      }
      SB();
    }
    if (i == TBU) {
      LDS_BARRIER();
      const u32x2 bc = sm.bc;
      tag = __builtin_amdgcn_readfirstlane(bc[0]);
      t1 = (__builtin_amdgcn_readfirstlane((int)(bc[1] - t0)) + TEND + P.X1) * 256;
      const int dt = __builtin_amdgcn_readfirstlane((int)((float)(int)(t0 - bc[1]) * 256.f * __builtin_amdgcn_rcpf((float)(2 * nl - TBU - 2))));
      tau -= dt; tau2 -= dt;
      SB();
    }
    if (i == TBU + 1) {
      if (modw) { if (wave == 6) mod_put(); STAMP(9); }
      SB();
    }
    if (i == LNS) {
      tag = next_tag(__builtin_amdgcn_readfirstlane(ep[0]));
      if (part == 1) {
        float x0[8], x1[8], mb0, ri0, mb1, ri1;
#pragma unroll
        for (int k = 0; k < 4; ++k) { x0[2 * k] = lo_f(xr0[k]); x0[2 * k + 1] = hi_f(xr0[k]); x1[2 * k] = lo_f(xr1[k]); x1[2 * k + 1] = hi_f(xr1[k]); }
        ln_stats2(x0, x1, mb0, ri0, mb1, ri1);
        l0 = rbf(rbf(lo_f(xs0) - mb0) * ri0); l1 = rbf(rbf(lo_f(xs1) - mb1) * ri1);
      }
      PIN(l0); PIN(l1);
      SB();
    }
    if (i == WUSE) {
      PIN(tch1); PIN(tch2);
      static_assert(WUSE - WK >= KF, "the weight pieces are older than the steps outside the window");
      CFENCE();
      if (part == 1 || modw) wo_prep();
#pragma unroll
      for (int k = 0; k < 8; ++k) PIN(bf[k]);
      SB();
    }
    if (i == WUSE + 1) {
      if (modw) mod_gemv();
      SB();
    }
    if (i == QP1 + QD || i == QP2 + QD) {
      if (i == qp + QD) {
        u32 gm[2] = {gmd[0][0], gmd[1][0]};
        if (!all_ok((gmd[0][1] == tag) & (gmd[1][1] == tag))) { STAMP(11); gather8<2>(wsr, G_MOD + tid * 8, 512 * 8, tag, gm); }
        STAMP(5);
        gemv_go(pack_bf(rbf(l0 * rbf(1.f + lo_f(gm[1]))) + lo_f(gm[0]), rbf(l1 * rbf(1.f + hi_f(gm[1]))) + hi_f(gm[0])));
      }
      SB();
    }
    if (i == QP1 + QD + 1 || i == QP2 + QD + 1) {
      if (i == qp + QD + 1) {
        LDS_BARRIER();
        if (wave == 0) {
          const int bb = lane >> 5, jq = lane & 31;
          st8_sc1(wsr, G_Q + (bb * 2048 + blk * 32 + jq) * 8, pack_bf(sum8(&sm.r2[2 * jq * 20 + bb * 8]), sum8(&sm.r2[(2 * jq + 1) * 20 + bb * 8])), tag);
        }
        STAMP(6);
      }
      SB();
    }
    return true;
  });
  PIN(sot);
  const u32 qoff = G_Q + ((b * 32 + head) * 64 + c * 4) * 8;
  u32x4 qg[2], vr[NL], fl2[3];
  u32 qq[4];
  constexpr int NB = (NL + 15) / 16;
  float xlo, xs[4], sc[NB], pr[NB], m = 0.f;
  f32x4 acc = {0.f, 0.f, 0.f, 0.f};
  u32 pb[NB], pm[4], cpf;
  u32x2 agr;
  const u32 adst0 = lds_addr(lane < 32 ? &sm.red[wave][c * 8 + g * 4] : &sm.r2[(lane - 32) * 4]);
  u32 pso, po, rdst, rsrc, xo, l2, v2[4];
  rsrc_t r2;
  auto wo_addr = [&]() {
    po = G_P + (xcd & 3) * 16384 + near + (c >> 2 & 1) * 4096 + ((wave >> 1) * 4 + (c & 3)) * 256 + (wave & 1) * 128 + (c >> 3) * 64 + g * 16;
    rdst = lane < 32 ? (u32)(uintptr_t)&sm.red2[wave][0] + lane * 16 : (u32)(uintptr_t)&sm.r2[0] + (lane - 32) * 16;
    rsrc = (u32)(uintptr_t)&sm.red2[0][((wave & 1) * 16 + (lane & 15)) * 4 + (lane >> 4)];
    xo = dup(G_XP + xcd * 2048 + (wave & 1) * 1024 + (blk * 64 + (lane & ~1)) * 2) + (lane & 1) * 8192;
    pso = wave == 0 ? G_P + (xcd & 3) * 16384 + near + b * 4096 + ((head & 3) * 4 + part) * 256 + lane * 4 : OOB;
    PIN(pso); PIN(po); PIN(rdst); PIN(rsrc); PIN(xo);
  };
  auto w2_addr = [&]() {
    r2 = mk_rsrc(w2 + (size_t)(blk * 32 + wave * 4) * 512, 4096u);
    l2 = (u32)(uintptr_t)(&sm.w2b[wave * 4][0]);
#pragma unroll
    for (int n = 0; n < 4; ++n) { v2[n] = (lane ^ (n * 2) ^ ((wave & 2) << 2)) * 16 + n * 1024; PIN(v2[n]); }
    asm volatile("" : "+s"(r2), "+s"(l2));
  };
  const bool late = is_up || (part == 0 && wleft);
  const u32 nadd = is_up ? P.LU : (part == 0 ? (wleft ? P.LW : P.L3) : 0);
  u32 nto = tid == 0 ? G_NT + bid * 8 : OOB;
  PIN(nto);
  auto notices = [&](auto nc_, u32 off, u32x4 (&gn)[nc_.v]) -> int {
    constexpr int N = nc_.v;
#pragma nounroll
    for (int spin = 0; spin < SPIN_MAX; ++spin) {
      bool ok = true;
#pragma unroll
      for (int i = 0; i < N; ++i) ok &= (gn[i][1] == tag) & (gn[i][3] == tag);
      if (all_ok(ok)) break;
      __builtin_amdgcn_s_sleep(10);
      CFENCE();
#pragma unroll
      for (int i = 0; i < N; ++i) gn[i] = ld16<AUX_SC1>(wsr, off, i * 1024);
    }
    int mx = 0;
#pragma unroll
    for (int i = 0; i < N; ++i) mx = max(mx, max((int)(gn[i][0] - t0), (int)(gn[i][2] - t0)));
    return __builtin_amdgcn_readfirstlane((int)wave_max((float)min(mx, 1 << 20)));
  };
  const int side = nl >= SIDE + 13 ? SIDE + 8 : (nl >= SIDE + 9 ? SIDE + 4 : SIDE);
  sfor<0, NL + WV>([&](auto ic) {
    constexpr int v = ic.v;
    if (cons_end(v - 1) >= NCMIN / 8 && cons_end(v - 1) >= nl) return false;
    if constexpr (v < NL) {
      const u32 so = __builtin_amdgcn_readlane(sot, v);
      if constexpr (v >= 12) { if (v == nl - P.NE) tau = (tau * (late ? 16 : P.FE)) >> 4; }
      if constexpr (v >= NCMIN / 8) { if (v < nl) gate(); } else gate();
      SB();
      int bk = 0;
      if constexpr (v == AR) { if (part == 1 && wi < 48 && wave == 7) bk = ((int)(((u32)wall_clock64() - t0) << 8) - due + tau + (int)__builtin_amdgcn_s_getreg(7 | (3 << 11)) * tau) >> 8; SB(); }
      vr[v] = ld16<AUX_NT>(rv, l16, so);
      if constexpr (v == AR) {
        SB();
        if (part == 1 && wi < 48 && tid == 448) st8_sc1(wsr, G_AG + wi * 8, (u32)min(max(bk, 0), 250), tag);
      }
      SB();
      SB();
    }
    if constexpr (v == NCMIN / 8 - 1) {
#pragma unroll
      for (int i = NCMIN / 8; i < NL; ++i) asm volatile("" : "=v"(vr[i]));
      asm volatile("" : "=v"(fl2[1]), "=v"(fl2[2]));
#pragma unroll
      for (int w = 0; w < 8; ++w) asm volatile("" : "=v"(fmw[w]));
    }
    if constexpr (v == VQ - 4) { qg[0] = ld16<AUX_SC1>(wsr, qoff, 0); qg[1] = ld16<AUX_SC1>(wsr, qoff + 16, 0); }
    if constexpr (v == VSIDE) side_loads();
    if constexpr (v == VL) agr = ld8<AUX_SC1>(wsr, wave == 6 ? G_AG + (48 + c) * 8 : OOB, 0);
    if constexpr (v == AL) agr = ld8<AUX_SC1>(wsr, (wave == 7 && lane < 48) ? G_AG + lane * 8 : OOB, 0);
    if constexpr (v == CPF) {
      u64 pc; u32 dlt, len;
      asm volatile("s_getpc_b64 %0\n.Lcpf_here:\n\ts_mov_b32 %1, .Ltail_start-.Lcpf_here\n\ts_mov_b32 %2, .Lend_a-.Ltail_start\n\ts_max_i32 %2, %2, .Lend_b-.Ltail_start\n\ts_max_i32 %2, %2, .Lend_c-.Ltail_start\n\ts_max_i32 %2, %2, .Lend_d-.Ltail_start\n\ts_max_i32 %2, %2, .Lend_e-.Ltail_start" : "=s"(pc), "=s"(dlt), "=s"(len) :: "scc");
      pc += dlt;
      const rsrc_t rc = mk_rsrc((const void*)pc, (part == 3 && i8 == 1) ? len : 0u);
      cpf = __builtin_amdgcn_raw_buffer_load_b32(rc, tid * 64, 0, 0);
    }
    if constexpr (v == CPF + WV + 1) PIN(cpf);
    if constexpr (v == SIDE || v == SIDE + 4 || v == SIDE + 8) fl2[(v - SIDE) / 4] = ld16<AUX_SC1>(wsr, (wave == 0 && v == side) ? mlo : OOB, 0);
    SB();
    if constexpr (v < WV) { WAIT_VM(win_v(v, WK)); SB(); } else if constexpr (v < NL && cons_end(v) < v - WV + 1) { WAIT_VM(win_v(v, WV)); SB(); }
    if constexpr (v == VU) {
      if (wave == 6) {
        u32 vt[1] = {agr[0]};
        if (!all_ok(agr[1] == tag)) gather8<1>(wsr, G_AG + (48 + c) * 8, 0, tag, vt);
        float sv = u2f(vt[0]);
        sv += dpp<0x124>(sv); sv += dpp<XOR8>(sv);
        float sa = sv + dpp<XOR1>(sv);
        sv = fmaf(sa + dpp<XOR2>(sa), 0.25f, sv);
        float cf = fabsf(sv);
        cf = fminf(cf, dpp<XOR1>(cf)); cf = fminf(cf, dpp<XOR2>(cf));
        const u32 nm = (u32)__builtin_amdgcn_ballot_w64(sv > 0.f) & 15u;
        if (lane == 0) sm.nm = nm << 13 | (cf < 2500.f ? (u32)P.SL : 0u);
      }
      PIN(agr);
    }
    if constexpr (v == VQ) {
      if (!all_ok((qg[0][1] == tag) & (qg[0][3] == tag) & (qg[1][1] == tag) & (qg[1][3] == tag))) { STAMP(20); gather16<2>(wsr, qoff, 16, tag, qg); }
      STAMP(1); WSTAMP(1);
      qq[0] = qg[0][0]; qq[1] = qg[0][2]; qq[2] = qg[1][0]; qq[3] = qg[1][2];
    }
    if constexpr (v >= VQ && v < VS) {
      constexpr int u0 = SLC[v - VQ], u1 = SLC[v - VQ + 1];
      static_assert(SLC[NSL] == (NL + 7) / 8 * 8, "all groups of 8");
      sfor<u0, u1>([&](auto uc) {
        constexpr int u = uc.v, p = u & 7, i = (p & 1) * 4 + (p & 2) + (p >> 2), sl = u >> 3, bt = sl >> 1;
        float d = 0.f;
        if constexpr (sl * 8 + i < NL) {
#pragma unroll
          for (int k = 0; k < 4; ++k) d = dot2(kr[sl * 8 + i][k], qq[k], d);
        }
        if constexpr (p & 1) xs[i - 4] = sel_add<XOR7>(xs[i - 4], d, c & 4); else xs[i] = d;
        if constexpr ((p & 3) == 3) xs[p >> 2] = sel_add<XOR2>(xs[p >> 2], xs[(p >> 2) + 2], c & 2);
        if constexpr (p == 7) {
          xs[0] = sel_add<XOR1>(xs[0], xs[1], c & 1);
          if constexpr ((sl & 1) || u == (NL + 7) / 8 * 8 - 1) {
            const float t = (sl & 1) ? sel_add<XOR8>(xlo, xs[0], c & 8) : xs[0] + dpp<XOR8>(xs[0]);
            const float s = rbf(rbf(t) * 0.08838834764831845f);
            sc[bt] = (bt * 16 + c < nl) ? s : -__builtin_inff();
            PIN(sc[bt]);
          } else {
            xlo = xs[0];
            PIN(xlo);
          }
        }
        return true;
      });
      constexpr int pe = u1 & 7;
      if constexpr (pe >= 1) PIN(xs[0]);
      if constexpr (pe == 3) PIN(xs[2]);
      if constexpr (pe >= 5) PIN(xs[1]);
      if constexpr (pe == 7) PIN(xs[3]);
    }
    if constexpr (v == VS) {
      float mloc = sc[0];
#pragma unroll
      for (int bt = 1; bt < NB; ++bt) mloc = fmaxf(mloc, sc[bt]);
      m = fmaxf(wave_max(mloc), -3.0e38f);
      float ls = 0.f;
#pragma unroll
      for (int bt = 0; bt < NB; ++bt) { pr[bt] = __expf(sc[bt] - m); ls += pr[bt]; }
      const float l = wave_sum(ls);
      if (lane == 0) { sm.ml[0][wave] = m; sm.ml[1][wave] = l; }
#pragma unroll
      for (int bt = 0; bt < NB; ++bt) { pb[bt] = pack_bf(pr[bt], pr[bt]); PIN(pb[bt]); }
#pragma unroll
      for (int k = 0; k < 4; ++k) { pm[k] = (c >> 1) == k ? ((c & 1) ? 0xffff0000u : 0x0000ffffu) : 0u; PIN(pm[k]); }
      STAMP(2); WSTAMP(2);
    }
    sfor<cons_end(v - 1), cons_end(v)>([&](auto jc) {
      constexpr int i = jc.v;
      if (i < NCMIN / 8 || i < nl) {
        const u32 pv = __float_as_uint(bcast16(u2f(pb[i >> 4]), i & 15));
        acc = mfma16(u32x4{pv & pm[0], pv & pm[1], pv & pm[2], pv & pm[3]}, vr[i], acc);
      }
      PIN(acc);
      return true;
    });
    if constexpr (v == VS - 1) { side_pin(); if (is_up) up_prep(); PIN(ga); PIN(xin); PIN(cc1); }
    if constexpr (v == MLS - 1) {
      if (wave == 7) {
        const float am = wave_sum(lane < 48 ? (agr[1] == tag ? (float)agr[0] : 120.f) : 0.f) * (1.f / 48.f);
        const float st = fminf(fmaxf((am - (float)P.A0) * (float)(P.AG * 16), 0.f), (float)(P.AC * 256));
        if (lane == 0) sm.ctl = u32x2{(u32)(int)st, (u32)(int)(st * __builtin_amdgcn_rcpf((float)(nl - MLS - 3)))};
      }
      PIN(agr);
    }
    if constexpr (v == UPREP - 2) {
      nmask = __builtin_amdgcn_readfirstlane(sm.nm);
      slack = (nmask & 255u) * 256; near = ((nmask >> (xcd & 3)) ^ (xcd << 11)) & 8192u;
      w2_addr();
    }
    if constexpr (v == UPREP - 1) wo_addr();
    if constexpr (v == UPREP) {
#pragma unroll
      for (int k = 0; k < 8; ++k) asm volatile("" : "=v"(bf[k]));
      if (part == 0) wo_prep(0, 1); else if (is_up) up_frags(0, 8 - UD);
#pragma unroll
      for (int k = 0; k < 8; ++k) PIN(bf[k]);
    }
    if constexpr (v == UPREP + 1 || v == UPREP + 2) {
      if (is_up) { if constexpr (v == UPREP + 1) col_sums(0, (8 - UD) / 2); else col_sums((8 - UD) / 2, 8 - UD); }
      PIN(Af); PIN(Bf);
    }
    if constexpr (v == SIDE + WV + 1 || v == SIDE + WV + 5 || v == SIDE + WV + 9) {
      if (wave == 0 && v == side + WV + 1) {
        const u32x4 ml = fl2[(v - SIDE - WV - 1) / 4];
        fok = all_ok((ml[1] == tag) & (ml[3] == tag));
        merge_factor(ml);
      }
    }
    if (v % 6 == 4 && v >= 10 && v < 30) STAMP(15 + v / 6);
    if constexpr (v == MLS) {
      LDS_BARRIER();
      const u32x2 ct = sm.ctl;
      if (wave == 0) {
        const float mw = sm.ml[0][lane & 7];
        float Mwg = fmaxf(mw, dpp<XOR1>(mw)); Mwg = fmaxf(Mwg, dpp<XOR2>(Mwg)); Mwg = fmaxf(Mwg, dpp<XOR7>(Mwg));
        fwv = __expf(mw - Mwg);
        float Lwg = fwv * sm.ml[1][lane & 7];
        Lwg += dpp<XOR1>(Lwg); Lwg += dpp<XOR2>(Lwg); Lwg += dpp<XOR7>(Lwg);
        if (lane == 0) st16_sc1(wsr, G_ML + ((head * 2 + b) * 4 + part) * 16, __float_as_uint(Mwg), __float_as_uint(Lwg), tag);
      }
      PIN(fwv);
      stv = __builtin_amdgcn_readfirstlane(ct[0]); t1 += stv;
      tau += __builtin_amdgcn_readfirstlane(ct[1]);
    }
    SB();
    return true;
  });
  asm volatile(".Ltail_start:");
  *LDSP(f32x4, adst0) = acc;
  STAMP(14); WSTAMP(14);
  if (!fok) {
    u32x4 ml;
#pragma nounroll
    for (int spin = 0; spin < SPIN_MAX; ++spin) {
      CFENCE();
      ml = ld16<AUX_SC1>(wsr, mlo, 0);
      if (all_ok((ml[1] == tag) & (ml[3] == tag))) break;
    }
    merge_factor(ml);
  }
  STAMP(15);
  LDS_BARRIER();
  STAMP(3);
  {
    u32 pw = 0;
    if (wave == 0) {
      f32x2 t[8];
#pragma unroll
      for (int w = 0; w < 8; ++w) t[w] = *reinterpret_cast<const f32x2*>(&sm.red[w][2 * lane]);
      f32x2 h[4];
#pragma unroll
      for (int w = 0; w < 4; ++w) h[w] = __builtin_elementwise_fma(t[2 * w + 1], f32x2{fmw[2 * w + 1], fmw[2 * w + 1]}, t[2 * w] * fmw[2 * w]);
      const f32x2 o = (h[0] + h[1]) + (h[2] + h[3]);
      pw = min(pack_bf(o[0], o[1]), SENT - 1u);
    }
    __builtin_amdgcn_raw_buffer_store_b32(pw, wsr, pso, 0, AUX_SC1);
    st8_sc1(wsr, nto, (u32)wall_clock64() + nadd, tag);
    SB();
#pragma unroll
    for (int i = NCMIN / 8; i < NL; ++i) PIN(vr[i]);
    PIN(fl2[0]); PIN(fl2[1]); PIN(fl2[2]);
  }
  STAMP(4);
  SB();
  if (late) {
    asm volatile(".p2align 6\n\ts_barrier" ::: "memory");
#pragma nounroll
    while (wleft > 0) piece();
    if (is_up) {
      asm volatile("s_mov_b32 m0, %5\n\ts_nop 0\n\tbuffer_load_dwordx4 %0, %4, 0 offen nt lds\n\ts_add_u32 m0, m0, 0x400\n\ts_nop 0\n\tbuffer_load_dwordx4 %1, %4, 0 offen nt lds\n\t"
                   "s_add_u32 m0, m0, 0x400\n\ts_nop 0\n\tbuffer_load_dwordx4 %2, %4, 0 offen nt lds\n\ts_add_u32 m0, m0, 0x400\n\ts_nop 0\n\tbuffer_load_dwordx4 %3, %4, 0 offen nt lds"
                   :: "v"(v2[0]), "v"(v2[1]), "v"(v2[2]), "v"(v2[3]), "s"(r2), "s"(l2) : "memory", "scc", "m0");
    }
  }
  SB();

  if (part == 0) {
    u32 wa2[8];
#pragma unroll
    for (int k = 0; k < 8; ++k) {
      const int r = 32 + g * 8 + ((lane >> 2) & 3) + (k & 1) * 4, ch = (k & 6) + ((lane >> 1) & 1);
      wa2[k] = wimg + wave * 8192 + r * 128 + ((ch ^ (r & 7)) * 16) + (lane & 1) * 8;
      PIN(wa2[k]);
    }
    rsrc_t wr2 = wsr;
    asm volatile("" : "+s"(wr2));
    until(t1);
    STAMP(11);
    CFENCE();
    u32x4 og[2];
    og[0] = ld16<AUX_SC1>(wr2, po, 0);
    SB();
    WAIT_VM(1); CFENCE();
#pragma unroll
    for (int t = 0; t < 4; ++t) {
      const u32x2 lo = tr_read(wa2[2 * t]), hi = tr_read(wa2[2 * t + 1]);
      bf[4 + t] = u32x4{lo[0], lo[1], hi[0], hi[1]};
    }
    SB();
    f32x4 d[4];
    auto wo_mm = [&]() {
      const f32x4 z = {0.f, 0.f, 0.f, 0.f};
#pragma unroll
      for (int t = 0; t < 4; ++t) { d[t] = mfma16(og[0], bf[t], z); og[1][t] = __float_as_uint(dpp<XOR8>(u2f(og[0][t]))); }
      SB();
#pragma unroll
      for (int t = 0; t < 4; ++t) d[t] = mfma16(og[1], bf[4 + t], d[t]);
    };
    auto p_bad = [&]() { return !all_ok(max(max(og[0][0], og[0][1]), max(og[0][2], og[0][3])) != SENT); };
    wo_mm();
    if (__builtin_expect(p_bad(), 0)) {
#pragma nounroll
      for (int spin = 0; spin < SPIN_MAX; ++spin) {
        __builtin_amdgcn_s_sleep(1);
        CFENCE();
        og[0] = ld16<AUX_SC1>(wsr, po, 0);
        if (!p_bad()) break;
      }
      wo_mm();
      SB();
      asm volatile(".Lend_d:");
    }
    STAMP(5); WSTAMP(5);
    {
      SB();
      f32x4 y; f32x2 h[4];
#pragma unroll
      for (int t = 0; t < 4; ++t) { h[t] = d[t].xy + d[t].zw; PIN(h[t]); }
#pragma unroll
      for (int t = 0; t < 4; ++t) { y[t] = h[t][0] + h[t][1]; PIN(y[t]); }
      *LDSP(f32x4, rdst) = y;
    }
    STAMP(16);
    LDS_BARRIER();
    STAMP(17);
    if (wave < 2) {
      f32x2 v[4];
#pragma unroll
      for (int w = 0; w < 4; ++w) v[w] = f32x2{LDSP(float, rsrc)[w * 256], LDSP(float, rsrc)[w * 256 + 128]};
      const f32x2 h = (v[0] + v[1]) + (v[2] + v[3]);
      const float y = h[0] + h[1];
      __builtin_amdgcn_raw_buffer_store_b32(min(pack_bf(dpp<0xA0>(y), dpp<0xF5>(y)), SENT - 1u), wr2, xo, 0, AUX_SC1);
      asm volatile(".Lend_a:");
    }
    STAMP(6);
    FINISH();
  }
  if (part == 3) {
    if (!has_w) FINISH();
    u32x4 b2[4];
    const u32 tdst = lds_addr(&sm.hb[hpos ^ ((wave & 1) * 16)]);
    const u32 sdst = lds_addr(&sm.stat[wave][lane]);
    const u32 ssrc = lds_addr(&sm.stat[(g >> 1) * 4 + (c >> 2)][(g & 1) * 32 + (c & 3) * 8]);
    const u32 adst = lds_addr((c < 4 && (g & 1) == 0) ? &sm.actl[g >> 1][wave * 4 + c] : reinterpret_cast<u16*>(&sm.dummy[lane]));
    const u32 asrc = lds_addr(&sm.actl[lane & 1][g * 8]);
    const u32 apa = lds_addr(&sm.hb[(c >> 3) * 512 + (c & 3) * 64 + ((c >> 2) & 1) * 32 + (g ^ (c & 2)) * 8]);
    u32 xoff = dup(G_XP) + near + g * 2048 + ub * 1024 + (wave & 3) * 256 + c * 16;
    u32x4 one;
#pragma unroll
    for (int k = 0; k < 4; ++k) one[k] = ((c & 2) == 0 && (c >> 2) == k) ? ((c & 1) ? 0x3f800000u : 0x00003f80u) : 0u;
    PIN(one);
    u32 ooff = dup(G_OP + (blk * 512 + tid) * 8) + ((nmask >> (g & 1)) & 8192u);
    PIN(xoff); PIN(ooff);
    PIN(ga); PIN(xin); PIN(cc1);
    u32 w2a = (u32)(uintptr_t)(&sm.w2b[0][0]) + (g * 8 + ((lane >> 2) & 3)) * 1024 + (wave ^ (g & 1)) * 128 + (lane & 15) * 8;
    u32 w2t[4];
#pragma unroll
    for (int t = 0; t < 4; ++t) { w2t[t] = w2a ^ (t * 32); PIN(w2t[t]); }
    static_assert(UD == 1, "one late fragment");
    u32 ufa = up_addr() + 7 * 4096;
    const u32 a7a = lds_addr(ap + 128 + ((lane >> 1) & 1) * 128 + 7 * 8);
    PIN(ufa);
    u64 gm1 = __builtin_amdgcn_ballot_w64(g == 1), gm2 = __builtin_amdgcn_ballot_w64(g == 2), gm3 = __builtin_amdgcn_ballot_w64(g == 3);
    asm volatile("" : "+s"(gm1), "+s"(gm2), "+s"(gm3));
    const bool g1 = __builtin_amdgcn_inverse_ballot_w64(gm1), g2 = __builtin_amdgcn_inverse_ballot_w64(gm2), g3 = __builtin_amdgcn_inverse_ballot_w64(gm3);
    if (wave == 1) {
      due = t1 + P.N1 * 256; gate();
      u32x4 gn[2] = {ld16<AUX_SC1>(wsr, G_NT + lane * 16, 0), ld16<AUX_SC1>(wsr, G_NT + lane * 16, 1024)};
      const int tn = notices(IC<2>{}, G_NT + lane * 16, gn) * 256;
      due = (tn > t1 + P.TH * 256 ? max(tn, t1 + P.L0 * 256) + P.L1 * 256 : t1) + P.X2 * 256 + slack;
      until(due);
    }
    asm volatile("s_waitcnt vmcnt(0)" ::: "memory");
    STAMP(9);
    __builtin_amdgcn_s_barrier();
    STAMP(11);
    CFENCE();
    u32x4 xg[2] = {ld16<AUX_SC1>(wsr, xoff, 0), ld16<AUX_SC1>(wsr, xoff, 16384)};
    SB();
    {
      const u32x4 a7 = *LDSP(u32x4, a7a);
      const u32x2 l7 = tr_read(ufa), h7 = tr_read(ufa + 256);
      bf[7] = u32x4{l7[0], l7[1], h7[0], h7[1]};
#pragma unroll
      for (int t = 0; t < 4; ++t) {
        const u32x2 lo = tr_read(w2t[t]), hi = tr_read(w2t[t] + 4096);
        b2[t] = u32x4{lo[0], lo[1], hi[0], hi[1]};
      }
      SB();
      const f32x4 d0 = mfma16(a7, bf[7], f32x4{0.f, 0.f, 0.f, 0.f});
      Af += par ? d0[1] : d0[0]; Bf += par ? d0[3] : d0[2];
      Af += dpp<XOR8>(Af); Bf += dpp<XOR8>(Bf);
    }
    PIN(Af); PIN(Bf);
#pragma unroll
    for (int t = 0; t < 4; ++t) PIN(b2[t]);
    SB();
    const f32x4 z4 = {0.f, 0.f, 0.f, 0.f};
    auto xp_bad = [&]() { return !all_ok(max(max(max(xg[0][0], xg[0][1]), max(xg[0][2], xg[0][3])), max(max(xg[1][0], xg[1][1]), max(xg[1][2], xg[1][3]))) != SENT); };
    f32x4 v = mfma16(one, xg[1], mfma16(one, xg[0], z4));
    if (__builtin_expect(xp_bad(), 0)) {
#pragma nounroll
      for (int spin = 0; spin < SPIN_MAX; ++spin) {
        __builtin_amdgcn_s_sleep(1);
        CFENCE();
        xg[0] = ld16<AUX_SC1>(wsr, xoff, 0); xg[1] = ld16<AUX_SC1>(wsr, xoff, 16384);
        if (!xp_bad()) break;
      }
      v = mfma16(one, xg[1], mfma16(one, xg[0], z4));
      SB();
      asm volatile(".Lend_e:");
    }
    STAMP(5); WSTAMP(5);
    {
      const float a0 = v[0], a1 = v[1];
      const float x10 = fmaf(ga[0], a0, xin[0]), x11 = fmaf(ga[1], a1, xin[1]);
      *LDSP(u32, tdst) = pack_bf(x10 * cc1[0], x11 * cc1[1]);
      float u = swap32_add(x10 + x11, fmaf(x10, x10, x11 * x11));
      u += dpp<XOR1>(u); u += dpp<XOR2>(u); u += dpp<XOR7>(u);
      *LDSP(float, sdst) = u;
    }
    STAMP(16);
    LDS_BARRIER();
    STAMP(17);
    {
      const f32x4 z = {0.f, 0.f, 0.f, 0.f};
      const u32x4 af0 = LDSP(u32x4, apa)[0], af1 = LDSP(u32x4, apa)[32];
      float s = LDSP(float, ssrc)[0];
      SB();
      f32x4 d[4];
      d[0] = mfma16(af0, bf[0], z); d[1] = mfma16(af0, bf[1], z);
      SB();
      s += dpp<XOR1>(s); s += dpp<XOR2>(s);
      PIN(s);
      d[2] = mfma16(af0, bf[2], z);
      SB();
      s += dpp<XOR7>(s); s += dpp<XOR8>(s);
      PIN(s);
      d[3] = mfma16(af0, bf[3], z);
      SB();
      const auto sw2 = __builtin_amdgcn_permlane16_swap(__float_as_uint(s), __float_as_uint(s), false, false);
      float mean = u2f(sw2[0]) * (1.f / 512.f), qm = fmaf(u2f(sw2[1]), 1.f / 512.f, 1e-5f);
      PIN(mean); PIN(qm);
      d[0] = mfma16(af1, bf[4], d[0]);
      SB();
      float ve = fmaxf(fmaf(-mean, mean, qm), 1e-5f), ma = mean * Af;
      PIN(ve); PIN(ma);
      d[1] = mfma16(af1, bf[5], d[1]);
      SB();
      float ri = __builtin_amdgcn_rsqf(ve);
      PIN(ri);
      d[2] = mfma16(af1, bf[6], d[2]);
      d[3] = mfma16(af1, bf[7], d[3]);
      SB();
      float cf = fmaf(-ri, ma, Bf), rl = ri * -1.4426950408889634f, cl = cf * -1.4426950408889634f;
      PIN(cf); PIN(rl); PIN(cl);
      PIN(d[0]); PIN(d[1]);
      float y0 = d[0][0] + d[1][1];
      PIN(d[2]);
      y0 += d[2][2];
      PIN(y0); PIN(d[3]);
      const float y1 = d[3][3];
      float y = y0 + y1;
      y = __builtin_bit_cast(float, __builtin_amdgcn_update_dpp(__builtin_bit_cast(int, y), __builtin_bit_cast(int, y), XOR8, 0xa, 0xf, false));
      y = swap16_add(y, y);
      const float e = __builtin_amdgcn_exp2f(fmaf(y, rl, cl));
      y = fmaf(y, ri, cf);
      float gu = y * dpp<0x104>(y);
      PIN(gu);
      *LDSP(u16, adst) = (u16)pack_bf(gu * __builtin_amdgcn_rcpf(1.f + e), 0.f);
    }
    STAMP(18);
    LDS_BARRIER();
    STAMP(10);
    {
      const u32x4 af = *LDSP(u32x4, asrc);
      const f32x4 z = {0.f, 0.f, 0.f, 0.f};
      f32x4 d[4];
#pragma unroll
      for (int t = 0; t < 4; ++t) d[t] = mfma16(af, b2[t], z);
      SB();
      PIN(d[0]); PIN(d[1]);
      float e0 = g1 ? d[1][0] : d[0][0], e1 = g1 ? d[1][1] : d[0][1];
      PIN(d[2]);
      e0 = g2 ? d[2][0] : e0; e1 = g2 ? d[2][1] : e1;
      PIN(e0); PIN(e1); PIN(d[3]);
      st8_sc1(wsr, ooff, pack_bf(g3 ? d[3][0] : e0, g3 ? d[3][1] : e1), tag);
    }
    asm volatile(".Lend_b:");
    STAMP(6);
    FINISH();
  }
  if (fin) {
    const int d = fd, ul = lane >> 3;
    u32x4 gm[2] = {sg[0], sg[1]};
    if (!all_ok((sg[0][1] == tag) & (sg[0][3] == tag) & (sg[1][1] == tag) & (sg[1][3] == tag))) gather16<2>(wsr, G_MOD + (1024 + d) * 8, 1536 * 8, tag, gm);
    u32 xa0, xa1; int tnf;
    {
      due = t1 + P.N2 * 256; gate();
      u32x4 gn[2] = {ld16<AUX_SC1>(wsr, G_NT + lane * 16, 0), ld16<AUX_SC1>(wsr, G_NT + lane * 16, 1024)};
      const int tn = notices(IC<2>{}, G_NT + lane * 16, gn) * 256;
      due = (tn > t1 + P.TH * 256 ? max(tn, t1 + P.L0 * 256) + P.L1 * 256 : t1) + P.X3 * 256 + slack;
      tnf = tn;
      until(due);
    }
#pragma nounroll
    for (int spin = 0; spin < SPIN_MAX; ++spin) {
      CFENCE();
      xa0 = __builtin_amdgcn_raw_buffer_load_b32(wsr, dup(G_XP + ul * 2048) + near + d * 2, 0, AUX_SC1);
      xa1 = __builtin_amdgcn_raw_buffer_load_b32(wsr, dup(G_XP + ul * 2048) + near + d * 2, 1024, AUX_SC1);
      if (all_ok(max(xa0, xa1) != SENT)) break;
      __builtin_amdgcn_s_sleep(1);
    }
    float a0 = swap16_add(lo_f(xa0), lo_f(xa1)), a1 = swap16_add(hi_f(xa0), hi_f(xa1));
    a0 += dpp<XOR8>(a0); a1 += dpp<XOR8>(a1);
    a0 = swap32_add(a0, a0); a1 = swap32_add(a1, a1);
    const bool hb = g & 1;
    f32x2 xv = {rbf(lo_f(sx2) + rbf((hb ? hi_f(gm[0][0]) : lo_f(gm[0][0])) * rbf(a0))), rbf(hi_f(sx2) + rbf((hb ? hi_f(gm[0][2]) : lo_f(gm[0][2])) * rbf(a1)))};
    f32x2 gv = {hb ? hi_f(gm[1][0]) : lo_f(gm[1][0]), hb ? hi_f(gm[1][2]) : lo_f(gm[1][2])};
    PIN(xv); PIN(gv);
    if (fw == 0 && lane == 0) __builtin_amdgcn_raw_buffer_store_b32(tag, wsr, WS_EPOCH, 0, AUX_SC1);
    STAMP(10);
    due = max(due + P.X4 * 256 + slack, (int)(((u32)wall_clock64() - t0) << 8) + P.GX * 256);
    const bool last = ul + 40 < NUP;
    u32 o0 = dup(G_OP + (ul * 512 + d) * 8) + near, o5 = last ? o0 : OOB, tg5 = last ? tag : 0u;
    const rsrc_t rout = mk_rsrc(out, 2048u);
    u32 oo = (lane & 40) == 0 ? (g * 512 + d) * 2 : OOB;
    const u32 pk = (c & 10) ? 0u : ((c & 4) ? 0x3f800000u : 0x00003f80u);
    u32x4 one = {(c & 1) ? 0u : pk, 0u, (c & 1) ? pk : 0u, 0u};
    PIN(o0); PIN(o5); PIN(tg5); PIN(oo); PIN(one);
    u32 so[6];
#pragma unroll
    for (int k = 0; k < 6; ++k) { so[k] = k * 65536; asm volatile("" : "+s"(so[k])); }
    __builtin_amdgcn_raw_buffer_store_b32(pack_bf(xv[0], xv[1]), rout, oo, 0, AUX_SC1);
    until(due);
#pragma nounroll
    for (int spin = 0; spin < SPIN_MAX; ++spin) {
      CFENCE();
      u32x4 og[6];
      og[0] = ld16<AUX_SC1>(wsr, o5, so[5]);
#pragma unroll
      for (int k = 1; k < 6; ++k) { SB(); og[k] = ld16<AUX_SC1>(wsr, o0, so[k - 1]); }
      SB();
      f32x4 sv = {0.f, 0.f, 0.f, 0.f};
#pragma unroll
      for (int k = 0; k < 6; ++k) sv = mfma16(one, og[k], sv);
      const float s0 = sv[0] + dpp<XOR8>(sv[0]), s1 = sv[1] + dpp<XOR8>(sv[1]);
      __builtin_amdgcn_raw_buffer_store_b32(pack_bf(fmaf(gv[0], s0, xv[0]), fmaf(gv[1], s1, xv[1])), rout, oo, 0, AUX_SC1);
      SB();
      u32 bad = (og[0][1] ^ tg5) | (og[0][3] ^ tg5);
#pragma unroll
      for (int k = 1; k < 6; ++k) { bad |= (og[k][1] ^ tag) | (og[k][3] ^ tag); PIN(bad); }
      if (all_ok(bad == 0u)) break;
      __builtin_amdgcn_s_sleep(1);
    }
    STAMP(7);
    asm volatile(".Lend_c:");
    until(t1 - stv + 3000 * 256 + 10 * ((int)(((u32)wall_clock64() - t0) << 8) - t1 + stv - 200 * 256));
  }
  FINISH();
}

extern "C" void kernel_launch(void* const* d_in, const int* in_sizes, int n_in,
                              void* d_out, int out_size, void* d_ws, size_t ws_size,
                              hipStream_t stream) {
  fused_kernel<<<256, 512, 0, stream>>>((const u16*)d_in[0], (const u16*)d_in[1], (const u16*)d_in[2], (const u16*)d_in[3], (const u16*)d_in[4],
                                        (const u16*)d_in[6], (const u16*)d_in[7], (const u16*)d_in[8], (const u16*)d_in[9], (const u16*)d_in[10],
                                        (unsigned char*)d_ws, (u16*)d_out
    );
}
```

```cpp
#include <hip/hip_runtime.h>
#include <stdint.h>

typedef unsigned int u32;
typedef unsigned short u16;
typedef unsigned long long u64;
typedef __attribute__((ext_vector_type(4))) unsigned u32x4;
typedef __attribute__((ext_vector_type(2))) unsigned u32x2;
typedef __attribute__((ext_vector_type(2))) __bf16 bf16x2;
typedef __attribute__((ext_vector_type(4))) float f32x4;
typedef __attribute__((ext_vector_type(2))) float f32x2;
typedef __amdgpu_buffer_rsrc_t rsrc_t;

constexpr int NH = 32, SK = 16384, DH = 128, WIN = 4096, FF = 1376;
struct Prm { int E0, ES, E1, E2, E2C, EU, TEND, DT_UP, L0, L1, X1, X2, X3, X4, NE, FE, M0, M1, M2, M3, M4, GS, GX, N1, N2, BT, WD, DT_WO, LW, LU, EW, PM, WM, FK, FW, FU, EX, TS, DS, A0, AG, AC, PT, SL, TH, DT_0, L3; };
constexpr Prm PRM0 = {32, 24, 32, 32, 48, 64, 2000, 96, 20, 14, 72, 78, 98, 86, 5, 12, 12, 5, 12, 32, 28, 37, 34, 18, 45, 16, 4, 103, 40, 40, 8, 0x6827941, 0x53, 62, 61, 60, 8, 500, 122, 55, 26, 120, 160, 10, 6, 25, 15};
#define PRM_ARG
#define TABP (&TAB.e[0])
constexpr int NL = 37;
constexpr int NCMIN = 192;
constexpr int WK = 6, WV = 5;
constexpr int WUSE = 13, LNS = 11;
constexpr int QP1 = 13, QP2 = 19, QD = 4;
constexpr int VQ = 6;
constexpr int SLC[] = {0, 6, 9, 15, 21, 27, 33, 40}, NSL = sizeof(SLC) / sizeof(int) - 1;
constexpr int VS = VQ + NSL;
constexpr int MLS = VS + 2;
constexpr int VSIDE = VQ + 1, UPREP = 21;
constexpr int SIDE = NCMIN / 8 - 4;
constexpr int UD = 1;
constexpr int CPF = 18;
constexpr int TBP = 7, TBL = 11, TBU = 16;
constexpr int AR = 0, AL = 10;
constexpr int VL = 0, VU = 5;
constexpr int D0 = 30;
constexpr int KF = 4, KF0 = 8;
constexpr int side_k(int i) { return (i == KF || i == KF + 1) ? 3 : (i == TBL ? 1 : 0) + ((i == QP1 || i == QP2) ? 2 : 0); }
constexpr int side_v(int v) { return v == VQ - 4 ? 2 : (v == VSIDE ? 4 : ((v == AL || v == VL) ? 1 : 0)); }
constexpr int win_k(int i) { int n = WK; for (int j = i - WK; j <= i; ++j) n += side_k(j); return n; }
constexpr int win_v(int v, int w) { int n = w; for (int j = v - w; j <= v; ++j) n += side_v(j); return n; }
constexpr int cons_end(int v) {
  const int a = v - WV + 1, c = (v - VS) * 3;
  const int m = a < c ? a : c;
  return m < 0 ? 0 : (m > NL ? NL : m);
}
struct Tab { u32 e[1024]; };
#define popc __builtin_popcount
__host__ __device__ constexpr u32 place(u32 v, u32 m, int n) {
  u32 r = 0;
  for (int k = 0; k < n; ++k) r |= (v >> k & 1u) << (10 + (m >> (4 * k) & 15));
  return r;
}
__host__ __device__ constexpr u32 step_off(const Prm& P, u32 s0, u32 bh) {
  const u32 src = (s0 >> 5 & 3u) | bh << 2;
  return place(s0 ^ ((popc(src & P.M0) & 1) | (popc(src & P.M1) & 1) << 1 | (popc(src & P.M2) & 1) << 2 | (popc(src & P.M3) & 1) << 3 | (popc(src & P.M4) & 1) << 4), P.PM, 7);
}
constexpr Tab make_tab(const Prm& P) {
  Tab T{};
  for (int bid = 0; bid < 256; ++bid) {
    const int xcd = bid & 7, idx = bid >> 3, part = idx & 3, i8 = idx >> 2, wi = i8 * 8 + xcd;
    const bool has_mod = wi < 48, has_up = wi >= 21;
    const int cb0 = (i8 + 5) & 7, cb1 = (i8 + 4) & 7, ms = wi - 20;
    const int mi = wi < 8 ? wi : (wi < 16 ? 8 + ((xcd + 4) & 7) : (wi < 20 ? (wi - 14) * 8 + 3 : 16 + ms / 7 * 8 + ms % 7 + (ms % 7 >= 3 ? 1 : 0)));
    const int ui = xcd * 5 + (xcd > 5 ? xcd - 5 : 0) + (i8 >= 3 ? (xcd >= 5 ? 1 : 0) + i8 - 3 : 0);
    const int wd = cb0 == 3 ? 0 : P.WD;
    const int e[4] = {P.E0 - 4 * wd + (cb0 == 3 ? P.ES : 0), P.E1 + (cb1 == 3 ? P.ES : 0), has_mod ? (wi < 16 ? P.E2C : P.E2) + ((mi & 7) == 3 ? P.ES : 0) : 0, has_up ? P.EU * (8 - UD) / 8 : 0};
    const int ew = wd ? P.EW : 0, pa = (1024 + e[0] + ew + e[1] + e[2] + e[3] + (has_up ? P.EX : 0)) / 4;
    int c[5] = {0, 0, 0, 0, 128};
    for (int k = 0; k < 3; ++k) c[k + 1] = c[k] + (pa - e[k] - (k == 0 ? ew : 0) + 4) / 8;
    const int nl = c[part + 1] - c[part], wst = e[part] / 4;
    const bool has_w = part < 2 || (part == 2 ? has_mod : has_up);
    const int blk = !has_w ? 0 : (part == 0 ? cb0 : (part == 1 ? xcd * 8 + cb1 : (part == 2 ? mi : ui)));
    const int span = (part == 3 && has_up ? P.TEND - P.DT_UP : (part == 0 ? P.TEND - (wd ? P.DT_WO : P.DT_0) : P.TEND)) * 256;
    const int np = part == 0 ? 8 - wd : (part == 3 ? 8 - UD : 8), tau0 = span / (2 * nl + wst);
    const bool up = part == 3 && has_up;
    const int tau = tau0 * (up ? P.FU : (has_w && np == 8 ? P.FK : 64)) / 64, tauw = tau0 * wst / np * (up ? P.FU : P.FW) / 64;
    int sw = KF, at = P.DS * 256 + (has_w ? np * tauw : 0);
    while (sw < NCMIN / 8 - 1 && at < P.TS * 256) { at += tau; ++sw; }
    const int tau2 = (span - at) / (2 * nl - sw);
    T.e[4 * bid] = (u32)(c[part] | nl << 7 | blk << 13 | (has_w ? 1 : 0) << 19 | (8 - np) << 20 | sw << 24);
    T.e[4 * bid + 1] = (u32)(tau | tauw << 16);
    if ((part == 1 && nl <= QP2 + QD + 1) || c[part] > 32 * part + KF0 || c[part + 1] < 32 * part + KF0 + 4) T.e[4 * bid] = 0;
    const u32 pitch = part == 0 ? 1024u : (part == 1 ? 8192u : (part == 2 ? 6144u : 2752u));
    T.e[4 * bid + 2] = (part == 0 ? xcd * 512u * pitch : 0u) + blk * (part == 3 ? 64u : 128u);
    T.e[4 * bid + 3] = pitch | (u32)tau2 << 16;
  }
  return T;
}
constexpr int step_range(const Prm& P, bool mx_) {
  int mx = 0, mn = 1 << 20;
  const Tab T = make_tab(P);
  for (int bid = 0; bid < 256; ++bid) { const int n = T.e[4 * bid] >> 7 & 63; mx = n > mx ? n : mx; mn = n < mn ? n : mn; }
  return mx_ ? mx : mn;
}
__constant__ __attribute__((aligned(64))) const Tab TAB = make_tab(PRM0);
static_assert(step_range(PRM0, true) <= NL && step_range(PRM0, false) >= NCMIN / 8, "a wave's key range does not fit its load steps");
static_assert(cons_end(SIDE + WV + 1) <= NCMIN / 8 && cons_end(UPREP) <= NCMIN / 8 && SIDE > MLS + 3, "steps every wave runs");
constexpr int NUP = FF / 32;
constexpr int SPIN_MAX = 1 << 17;

constexpr u32 WS_EPOCH = 0;
constexpr u32 G_TB = 1024;
constexpr u32 G_NT = 2048;
constexpr u32 G_MOD = 4096;
constexpr u32 G_Q = G_MOD + 3072 * 8;
constexpr u32 G_AG = G_Q + 4096 * 8;
constexpr u32 G_ML = G_AG + 4096;
constexpr u32 G_P = 81920;
constexpr u32 G_U = G_P + 65536;
__device__ __forceinline__ u32 dup(u32 o) { return G_U + ((o >> 13) << 14) + (o & 8191u); }
constexpr u32 G_XP = 0;
constexpr u32 G_OP = G_XP + 8 * 512 * 4;
constexpr u32 G_PB = G_U + 2 * (G_OP + 44 * 512 * 8);
constexpr u32 WS_END = G_PB + 16384;
static_assert(G_P % 16384 == 0 && G_PB % 16384 == 0 && G_ML + 4096 <= G_P, "layout");
constexpr u32 SENT = 0xffffffffu;
constexpr u32 OOB = 0x40000000u;
#define WST 0

constexpr int AUX_NT = 2, AUX_SC1 = 16;

__device__ __forceinline__ float lo_f(u32 v) { return __uint_as_float(v << 16); }
__device__ __forceinline__ float hi_f(u32 v) { return __uint_as_float(v & 0xffff0000u); }
__device__ __forceinline__ float rbf(float x) { return (float)(__bf16)x; }
__device__ __forceinline__ u32 pack_bf(float lo, float hi) {
  bf16x2 r; r[0] = (__bf16)lo; r[1] = (__bf16)hi; return __builtin_bit_cast(u32, r);
}
__device__ __forceinline__ float dot2(u32 k, u32 q, float acc) {
  return __builtin_amdgcn_fdot2_f32_bf16(__builtin_bit_cast(bf16x2, k), __builtin_bit_cast(bf16x2, q), acc, false);
}
template <int CTRL> __device__ __forceinline__ float dpp(float x) {
  return __builtin_bit_cast(float, __builtin_amdgcn_mov_dpp(__builtin_bit_cast(int, x), CTRL, 0xf, 0xf, true));
}
constexpr int XOR1 = 0xB1, XOR2 = 0x4E, XOR7 = 0x141, XOR8 = 0x128;
__device__ __forceinline__ float swap32_add(float a, float b) {
  auto t = __builtin_amdgcn_permlane32_swap(__float_as_uint(a), __float_as_uint(b), false, false);
  return __uint_as_float(t[0]) + __uint_as_float(t[1]);
}
__device__ __forceinline__ float swap16_add(float a, float b) {
  auto t = __builtin_amdgcn_permlane16_swap(__float_as_uint(a), __float_as_uint(b), false, false);
  return __uint_as_float(t[0]) + __uint_as_float(t[1]);
}
__device__ __forceinline__ float wave_sum(float x) {
  x += dpp<XOR1>(x); x += dpp<XOR2>(x); x += dpp<XOR7>(x); x += dpp<XOR8>(x);
  x = swap16_add(x, x);
  return swap32_add(x, x);
}
__device__ __forceinline__ float wave_max(float x) {
  x = fmaxf(x, dpp<XOR1>(x)); x = fmaxf(x, dpp<XOR2>(x)); x = fmaxf(x, dpp<XOR7>(x)); x = fmaxf(x, dpp<XOR8>(x));
  auto s = __builtin_amdgcn_permlane16_swap(__float_as_uint(x), __float_as_uint(x), false, false);
  x = fmaxf(__uint_as_float(s[0]), __uint_as_float(s[1]));
  auto t = __builtin_amdgcn_permlane32_swap(__float_as_uint(x), __float_as_uint(x), false, false);
  return fmaxf(__uint_as_float(t[0]), __uint_as_float(t[1]));
}
template <int CTRL> __device__ __forceinline__ float sel_add(float lo, float hi, bool up) {
  const float t1 = lo + dpp<CTRL>(lo), t2 = hi + dpp<CTRL>(hi);
  return up ? t2 : t1;
}
template <int I> __device__ __forceinline__ float bc16(float x) {
  return __builtin_bit_cast(float, __builtin_amdgcn_update_dpp(0, __builtin_bit_cast(int, x), 0x150 + I, 0xf, 0xf, false));
}
__device__ __forceinline__ float bcast16(float x, int i) {
  switch (i) {
    case 0: return bc16<0>(x); case 1: return bc16<1>(x); case 2: return bc16<2>(x); case 3: return bc16<3>(x);
    case 4: return bc16<4>(x); case 5: return bc16<5>(x); case 6: return bc16<6>(x); case 7: return bc16<7>(x);
    case 8: return bc16<8>(x); case 9: return bc16<9>(x); case 10: return bc16<10>(x); case 11: return bc16<11>(x);
    case 12: return bc16<12>(x); case 13: return bc16<13>(x); case 14: return bc16<14>(x); default: return bc16<15>(x);
  }
}
__device__ __forceinline__ float silu_f(float x) { return x * __builtin_amdgcn_rcpf(1.f + __expf(-x)); }

template <int AUX> __device__ __forceinline__ u32x4 ld16(rsrc_t r, u32 voff, u32 soff) {
  return __builtin_amdgcn_raw_buffer_load_b128(r, voff, soff, AUX);
}
template <int AUX> __device__ __forceinline__ u32x2 ld8(rsrc_t r, u32 voff, u32 soff) {
  return __builtin_amdgcn_raw_buffer_load_b64(r, voff, soff, AUX);
}
__device__ __forceinline__ void st8_sc1(rsrc_t r, u32 voff, u32 v, u32 tag) {
  __builtin_amdgcn_raw_buffer_store_b64(u32x2{v, tag}, r, voff, 0, AUX_SC1);
}
__device__ __forceinline__ void st16_sc1(rsrc_t r, u32 voff, u32 v0, u32 v1, u32 tag) {
  __builtin_amdgcn_raw_buffer_store_b128(u32x4{v0, tag, v1, tag}, r, voff, 0, AUX_SC1);
}
__device__ __forceinline__ rsrc_t mk_rsrc(const void* p, u32 bytes) {
  return __builtin_amdgcn_make_buffer_rsrc(const_cast<void*>(p), 0, bytes, 0x00020000);
}
#define WAIT_VM(n) __builtin_amdgcn_s_waitcnt(((n) & 15) | (((n) >> 4) << 14) | (7 << 4) | (15 << 8))
#define SB() __builtin_amdgcn_sched_barrier(0)
#define CFENCE() asm volatile("" ::: "memory")
__device__ __forceinline__ bool all_ok(bool ok) { return __builtin_amdgcn_ballot_w64(!ok) == 0ull; }

template <int N> __device__ __forceinline__ void gather16(rsrc_t ws, u32 voff, u32 stride, u32 tag, u32x4 (&g)[N]) {
#pragma nounroll
  for (int spin = 0; spin < SPIN_MAX; ++spin) {
    CFENCE();
    bool ok = true;
#pragma unroll
    for (int i = 0; i < N; ++i) g[i] = ld16<AUX_SC1>(ws, voff + i * stride, 0);
#pragma unroll
    for (int i = 0; i < N; ++i) ok &= (g[i][1] == tag) & (g[i][3] == tag);
    if (all_ok(ok)) break;
    __builtin_amdgcn_s_sleep(8);
  }
}
template <int N> __device__ __forceinline__ void gather8(rsrc_t ws, u32 voff, u32 stride, u32 tag, u32 (&g)[N], bool act = true) {
#pragma nounroll
  for (int spin = 0; spin < SPIN_MAX; ++spin) {
    CFENCE();
    bool ok = true;
    u32x2 t[N];
#pragma unroll
    for (int i = 0; i < N; ++i) t[i] = ld8<AUX_SC1>(ws, voff + i * stride, 0);
#pragma unroll
    for (int i = 0; i < N; ++i) { ok &= (t[i][1] == tag); g[i] = t[i][0]; }
    if (all_ok(ok | !act)) break;
    __builtin_amdgcn_s_sleep(8);
  }
}

struct Smem {
  float red[8][128];
  float ml[2][8];
  float r2[64 * 20];
  u16 hb[3 * 1024];
  float stat[8][64];
  float red2[8][128];
  u32 dummy[64];
  u32x2 bc;
  u32x2 ctl;
  u32 nm, pad_[3];
  u32x4 zero[4];
  u32x4 wbuf[8][8][64];
  __attribute__((aligned(256))) u16 w2b[32][512];
  u16 actl[2][32];
};

#define STAMP(k) do {} while (0)
#define WSTAMP(k) do {} while (0)
#define FINISH() return
#define PIN(x) asm volatile("" : "+v"(x))
#define LDS_BARRIER() do { __builtin_amdgcn_s_waitcnt(0xc07f); __builtin_amdgcn_s_barrier(); CFENCE(); } while (0)
#define WAVE_SYNC() do { __builtin_amdgcn_fence(__ATOMIC_RELEASE, "wavefront"); __builtin_amdgcn_wave_barrier(); __builtin_amdgcn_fence(__ATOMIC_ACQUIRE, "wavefront"); } while (0)

__device__ __forceinline__ void dma16(rsrc_t r, u32 voff, u32 soff, u32 lds_dst) {
  u32 keep;
  asm volatile("s_mov_b32 %0, m0\n\ts_mov_b32 m0, %4\n\ts_nop 4\n\tbuffer_load_dwordx4 %1, %2, %3 offen nt lds\n\ts_mov_b32 m0, %0"
               : "=&s"(keep) : "v"(voff), "s"(r), "s"(soff), "s"(lds_dst) : "memory");
}
__device__ __forceinline__ u32 perm_lo(u32 a, u32 b) { return __builtin_amdgcn_perm(b, a, 0x05040100u); }
__device__ __forceinline__ u32 perm_hi(u32 a, u32 b) { return __builtin_amdgcn_perm(b, a, 0x07060302u); }
__device__ __forceinline__ float rdl(float x, int l) { return __int_as_float(__builtin_amdgcn_readlane(__float_as_int(x), l)); }

typedef __attribute__((ext_vector_type(8))) __bf16 bf16x8;
__device__ __forceinline__ f32x4 mfma16(u32x4 a, u32x4 b, f32x4 c) {
  return __builtin_amdgcn_mfma_f32_16x16x32_bf16(__builtin_bit_cast(bf16x8, a), __builtin_bit_cast(bf16x8, b), c, 0, 0, 0);
}
__device__ __forceinline__ float sum8(const float* p) {
  const f32x4 a = *reinterpret_cast<const f32x4*>(p), b = *reinterpret_cast<const f32x4*>(p + 4);
  return ((a[0] + b[0]) + (a[1] + b[1])) + ((a[2] + b[2]) + (a[3] + b[3]));
}
__device__ __forceinline__ float ln_ri(float var) {
  return __builtin_amdgcn_rcpf(rbf(__builtin_amdgcn_sqrtf(rbf(rbf(fmaxf(var, 0.f)) + 1e-5f))));
}
__device__ __forceinline__ void ln_stats2(const float (&x0)[8], const float (&x1)[8], float& mb0, float& ri0, float& mb1, float& ri1) {
  const float k0 = rdl(x0[0], 0), k1 = rdl(x1[0], 0);
  float s0 = 0.f, s1 = 0.f, q0 = 0.f, q1 = 0.f;
#pragma unroll
  for (int k = 0; k < 8; ++k) {
    const float d0 = x0[k] - k0, d1 = x1[k] - k1;
    s0 += d0; s1 += d1; q0 = fmaf(d0, d0, q0); q1 = fmaf(d1, d1, q1);
  }
  float t = swap16_add(swap32_add(s0, s1), swap32_add(q0, q1));
  t += dpp<XOR1>(t); t += dpp<XOR2>(t); t += dpp<XOR7>(t); t += dpp<XOR8>(t);
  auto u = __builtin_amdgcn_permlane16_swap(__float_as_uint(t), __float_as_uint(t), false, false);
  const float ms = __uint_as_float(u[0]) * (1.f / 512.f), mq = __uint_as_float(u[1]) * (1.f / 512.f);
  const float ri = ln_ri(fmaf(-ms, ms, mq));
  mb0 = rbf(k0 + rdl(ms, 0)); mb1 = rbf(k1 + rdl(ms, 32));
  ri0 = rdl(ri, 0); ri1 = rdl(ri, 32);
}
template <int I> struct IC { static constexpr int v = I; };
template <int I, int N, class F> __device__ __forceinline__ void sfor(F&& f) {
  if constexpr (I < N) { if (f(IC<I>{})) sfor<I + 1, N>(f); }
}
typedef __attribute__((ext_vector_type(4))) short s16x4;
typedef __attribute__((address_space(3))) s16x4* lds_s16x4p;
__device__ __forceinline__ u32x2 tr_read(u32 lds_addr) {
  return __builtin_bit_cast(u32x2, __builtin_amdgcn_ds_read_tr16_b64_v4i16((lds_s16x4p)lds_addr));
}
__device__ __forceinline__ u32 next_tag(u32 e) { return ((e | 0x40004000u) + 1u) & ~0x40004000u; }
__device__ __forceinline__ float u2f(u32 v) { return __uint_as_float(v); }
#define LDSP(T, a) ((__attribute__((address_space(3))) T*)(a))
template <class T> __device__ __forceinline__ u32 lds_addr(T* p) { u32 a = (u32)(uintptr_t)p; PIN(a); return a; }

typedef __attribute__((ext_vector_type(16))) unsigned u32x16;
typedef __attribute__((ext_vector_type(8))) unsigned u32x8;
__global__ void __launch_bounds__(512, 2) fused_kernel(const u16* x_, const u16* kc_, const u16* vc_, const u16* wq_, const u16* wo_, const u16* w1_,
                                                       const u16* w3_, const u16* w2_, const u16* cnd_, const u16* wmod_, unsigned char* ws_, u16* out_ PRM_ARG) {
  __shared__ __attribute__((aligned(256))) Smem sm;
  constexpr Prm P = PRM0;
  const int TEND = P.TEND;
  const int bid = blockIdx.x, tid = threadIdx.x;
  u32x4 kr[NL];
  u32x16 ka; u32x8 kb; u32x4 tev; u64 pt0; u32 tch2;
  asm volatile("s_load_dwordx16 %0, %3, 0x0\n\ts_load_dwordx8 %1, %3, 0x40\n\ts_memrealtime %2"
               : "=&s"(ka), "=&s"(kb), "=&s"(pt0) : "s"(__builtin_amdgcn_kernarg_segment_ptr()));
  SB();
  {
    u64 pc; u32 vb;
    asm volatile("s_getpc_b64 %2\n.Lcpf_first:\n\tv_mul_u32_u24 %1, max(.Lend_a-.Lcpf_first, .Lend_b-.Lcpf_first, .Lend_c-.Lcpf_first, .Lend_m-.Lcpf_first, .Lend_n-.Lcpf_first)>>8, %3\n\tglobal_load_ubyte %0, %1, %2 offset:64"
                 : "=&v"(tch2), "=&v"(vb), "=&s"(pc) : "v"((bid & ~7) + (tid >> 6)) : "memory");
    asm volatile("global_load_dwordx4 %0, %1, %2" : "=&v"(tev) : "v"(bid << 4), "s"(TABP) : "memory");
  }
  SB();
  const int xcd = bid & 7, idx = bid >> 3, part = idx & 3;
  const int b = idx >> 2 & 1, head = xcd * 4 + (((idx >> 3) & 1) << 1 | idx >> 4);
  const u32 so0 = step_off(P, 32 * part + KF0, b * 32 + head) & ~place(3, P.PM, 2);
  u32 va = (((b * 32 + head) * SK + (SK - WIN)) * DH * 2 + so0) | place(tid >> 6, P.WM, 3) | (tid & 63) << 4;
  const u32 kx1 = place(1, P.PM, 2), kx2 = place(2, P.PM, 2);
  PIN(va);
  u32 va1 = va | kx1, va2 = va | kx2, va3 = va | kx1 | kx2;
  PIN(va1); PIN(va2); PIN(va3);
  int mc = bid & 0xd8;
  asm volatile("" : "+v"(va), "+s"(mc));
  SB();
  asm volatile("s_waitcnt lgkmcnt(0)" : "+s"(ka), "+s"(kb), "+s"(pt0));
  SB();
#define K_FIRST "global_load_dwordx4 %0, %4, %5 nt\n\tglobal_load_dwordx4 %1, %6, %5 nt\n\tglobal_load_dwordx4 %2, %7, %5 nt\n\tglobal_load_dwordx4 %3, %8, %5 nt"
  asm volatile("s_cmp_eq_u32 %9, 16\n\ts_cbranch_scc1 .Lk_later\n\t" K_FIRST "\n.Lk_later:"
               : "=&v"(kr[0]), "=&v"(kr[1]), "=&v"(kr[2]), "=&v"(kr[3]) : "v"(va), "s"((u64)ka[2] | (u64)ka[3] << 32), "v"(va1), "v"(va2), "v"(va3), "s"(mc) : "memory", "scc");
  SB();
  const u32 tka = (u32)wall_clock64(), t0 = tka - D0;
  auto kap = [&](int k) { return k < 8 ? (u64)ka[2 * k] | (u64)ka[2 * k + 1] << 32 : (u64)kb[2 * k - 16] | (u64)kb[2 * k - 15] << 32; };
  const u16 *x = (const u16*)kap(0), *kc = (const u16*)kap(1), *vc = (const u16*)kap(2), *w2 = (const u16*)kap(7), *cnd = (const u16*)kap(8);
  unsigned char* ws = (unsigned char*)kap(10); u16* out = (u16*)kap(11);
  const int lane = tid & 63, wave = __builtin_amdgcn_readfirstlane(tid >> 6);
  const bool p3 = part == 3;
  const u64 wa = part == 0 ? kap(4) : (part == 1 ? kap(3) : (part == 2 ? kap(9) : ((wave & 1) ? kap(6) : kap(5))));
  const bool modc = mc == 16;
  if (modc) asm volatile("s_waitcnt vmcnt(0)" ::: "memory");
  asm volatile("s_waitcnt vmcnt(4)" : "+v"(tev));
  const u32 te0 = __builtin_amdgcn_readfirstlane(tev[0]), te1 = __builtin_amdgcn_readfirstlane(tev[1]);
  const u32 te3 = __builtin_amdgcn_readfirstlane(tev[3]), pitch = te3 & 0xffff, wsz = pitch << (part == 0 ? 12 : 9);
  const int sh = p3 ? 2 : 3;
  u32 wv0 = (u32)((p3 ? (wave >> 1) * 16 : wave * 64) + (lane >> sh)) * pitch + ((lane & (p3 ? 3 : 7)) ^ (lane >> (p3 ? 5 : 3)) ^ (p3 ? wave >> 2 << 1 : 0)) * 16u;
  const u32 wimg = (u32)(uintptr_t)(&sm.wbuf[0][0][0]);
  u32 wld = wimg + (p3 ? (wave & 1) * 32768 + (wave >> 1) * 1024 : wave * 8192);
  const int bl0 = te0 & 127, nl = te0 >> 7 & 63, blk = te0 >> 13 & 63;
  const bool has_w = te0 >> 19 & 1;
  const rsrc_t rw = mk_rsrc((const void*)wa, wsz);
  const u32 wstep = pitch << (p3 ? 6 : 3), lstep = p3 ? 4096u : 1024u;
  u32 wso = __builtin_amdgcn_readfirstlane(tev[2]);
  int wleft = 8;
  auto piece = [&]() {
    dma16(rw, wv0, wso, wld);
    wso += wstep; wld += lstep;
    --wleft;
  };
  int tau = te1 & 0xffff;
  const int tauw = te1 >> 16, sw = te0 >> 24;
  int tau2 = te3 >> 16;
  int due = P.DS * 256, now = 0;
  auto gate = [&]() {
    while (now < due) { now = (int)(((u32)wall_clock64() - t0) << 8); if (now < due) __builtin_amdgcn_s_sleep(1); }
    due += tau;
  };
  const rsrc_t wsr = mk_rsrc(ws, WS_END);
  const int g = lane >> 4, c = lane & 15;
  const int i8 = idx >> 2, wi = i8 * 8 + xcd;
  const bool is_up = p3 && has_w;
  const size_t base = ((size_t)(b * NH + head) * SK + (SK - WIN)) * DH;
  const rsrc_t rk = mk_rsrc(kc + base, WIN * DH * 2), rv = mk_rsrc(vc + base, WIN * DH * 2);
  u32 sot;
  {
    u32 s0 = bl0 + lane - KF;
    s0 += s0 >= (u32)(32 * part + KF0) ? KF : 0;
    sot = (lane < KF ? so0 | place(lane, P.PM, 2) : step_off(P, s0, b * 32 + head)) | place(wave, P.WM, 3);
    sot = lane < nl ? sot : OOB;
    PIN(sot);
  }
  auto until = [&](int t) { while ((int)(((u32)wall_clock64() - t0) << 8) < t); };
  u32 near = 0, nmask = 0;
  int slack = 0;
  const rsrc_t rx = mk_rsrc(part == 2 ? cnd : x, (part == 1 || (part == 2 && has_w)) ? 2048u : 0u);
  u32x2 ep; u32 xs0, xs1, tch1; u32x4 xr0, xr1;
  auto small_ops = [&]() {
    ep = ld8<AUX_SC1>(wsr, wave == 7 ? WS_EPOCH : OOB, 0);
    xs0 = __builtin_amdgcn_raw_buffer_load_b16(rx, tid * 2, 0, 0); xs1 = __builtin_amdgcn_raw_buffer_load_b16(rx, tid * 2, 1024, 0);
  };
  auto small_ops2 = [&]() {
    xr0 = ld16<0>(rx, part == 1 ? lane * 16 : OOB, 0); xr1 = ld16<0>(rx, part == 1 ? lane * 16 : OOB, 1024);
    tch1 = __builtin_amdgcn_raw_buffer_load_b32(mk_rsrc(w2 + (size_t)blk * 32 * 512, is_up ? 64u : 0u), tid == 384 ? 0 : OOB, 0, AUX_NT);
  };
  u32 tag = 0;
  u32x4 bf[8];
  auto wo_prep = [&](int s0 = 0, int s1 = 2) {
    const u32 wi_ = wimg + wave * 8192;
    const int q = (lane >> 2) & 3, pp = lane & 3;
#pragma unroll
    for (int s = s0; s < s1; ++s) {
#pragma unroll
      for (int t = 0; t < 4; ++t) {
        const int r0 = s * 32 + g * 8 + q, r1 = r0 + 4, ch = t * 2 + (pp >> 1);
        const u32x2 lo = tr_read(wi_ + r0 * 128 + ((ch ^ (r0 & 7)) * 16) + (pp & 1) * 8), hi = tr_read(wi_ + r1 * 128 + ((ch ^ (r1 & 7)) * 16) + (pp & 1) * 8);
        bf[s * 4 + t] = u32x4{lo[0], lo[1], hi[0], hi[1]};
      }
    }
  };
  auto gemv_go = [&](u32 hh) {
    sm.hb[tid] = (u16)hh; sm.hb[512 + tid] = (u16)(hh >> 16);
    WAVE_SYNC();
    const u32x4* a = (c & 11) == 0 ? reinterpret_cast<const u32x4*>(&sm.hb[(c >> 2) * 512 + wave * 64 + g * 8]) : &sm.zero[0];
    const u32x4 a0 = a[0], a1 = a[(c & 11) == 0 ? 4 : 0];
    const f32x4 z = {0.f, 0.f, 0.f, 0.f};
    f32x4 d[4];
#pragma unroll
    for (int t = 0; t < 4; ++t) d[t] = mfma16(a0, bf[t], z);
#pragma unroll
    for (int t = 0; t < 4; ++t) d[t] = mfma16(a1, bf[4 + t], d[t]);
    if (lane < 32) {
#pragma unroll
      for (int t = 0; t < 4; ++t) sm.r2[(t * 16 + c) * 20 + g * 8 + wave] = d[t][0];
    }
  };
  auto mod_gemv = [&]() { gemv_go(pack_bf(silu_f(lo_f(xs0)), silu_f(lo_f(xs1)))); };
  auto mod_put = [&]() { st8_sc1(wsr, G_MOD + (blk * 64 + lane) * 8, pack_bf(sum8(&sm.r2[lane * 20]), sum8(&sm.r2[lane * 20 + 8])), tag); };
  auto zero_rows = [&]() { if (lane < 4) sm.zero[lane] = u32x4{0u, 0u, 0u, 0u}; };
  u32 l16 = lane * 16;
  asm volatile("" : "+v"(l16), "+v"(wv0) : "s"(rk), "s"(rx), "s"(wsr));
  SB();
  if (has_w) {
    if (modc) {
      now = 1 << 30;
      const int sg_ = lane >> 3;
      const u32 xl = sg_ < 2 ? G_ML + wi * 256 + sg_ * 128 : (sg_ < 4 ? G_PB + wi * 128 + (sg_ & 1) * 8192 : (sg_ == 4 ? G_NT + wi * 128 : (sg_ == 5 && wi < 3 ? G_AG + wi * 128 : OOB)));
      __builtin_amdgcn_raw_buffer_store_b128(u32x4{SENT, SENT, SENT, SENT}, wsr, tid < 384 ? G_P + wi * 6144 + tid * 16 : (wave == 7 ? xl + (lane & 7) * 16 : OOB), 0, AUX_SC1);
      asm volatile(".Lend_n:");
    }
#pragma nounroll
    while (wleft > (int)(te0 >> 20 & 7)) { gate(); due += tauw - tau; piece(); }
    now = 0;
  }
  SB();
  STAMP(23);
  if (__builtin_expect(modc, 0)) {
    small_ops();
    zero_rows();
    {
      until(P.PT * 256);
      const u64 pa = (u64)ws + G_PB + (((wi & 8) + wave + xcd * 2) & 15) * 128 + (wave & 1) * 8192;
      u64 c0, c1, c2; u32 d0, d1;
      asm volatile("s_memtime %0\n\ts_waitcnt lgkmcnt(0)\n\ts_load_dword %3, %5, 0x0\n\ts_waitcnt lgkmcnt(0)\n\ts_memtime %1\n\ts_waitcnt lgkmcnt(0)\n\t"
                   "s_load_dword %4, %6, 0x0\n\ts_waitcnt lgkmcnt(0)\n\ts_memtime %2\n\ts_waitcnt lgkmcnt(0)"
                   : "=&s"(c0), "=&s"(c1), "=&s"(c2), "=&s"(d0), "=&s"(d1) : "s"(pa), "s"(pa + 8192 - (wave & 1) * 16384) : "memory");
      const int df = min(max((int)(2 * (u32)c1 - (u32)c0 - (u32)c2), -600), 600);
      if (lane == 0) sm.stat[0][wave] = (float)(((wave ^ (xcd >> 2)) & 1) ? -df : df);
    }
    asm volatile("s_waitcnt vmcnt(0)" ::: "memory");
    tag = next_tag(__builtin_amdgcn_readfirstlane(ep[0]));
    wo_prep();
    mod_gemv();
    LDS_BARRIER();
    if (wave == 7) { mod_put(); if (lane == 0) st8_sc1(wsr, G_AG + (48 + wi) * 8, __float_as_uint(sum8(&sm.stat[0][0])), tag); }
    STAMP(9);
    __builtin_amdgcn_s_barrier();
    asm volatile(K_FIRST :: "v"(kr[0]), "v"(kr[1]), "v"(kr[2]), "v"(kr[3]), "v"(va), "s"((u64)ka[2] | (u64)ka[3] << 32), "v"(va1), "v"(va2), "v"(va3) : "memory");
    asm volatile(".Lend_m:");
  }
  SB();
  const bool fin = (part == 1 || part == 2) && wave == 0 && xcd < 2;
  const int fw = i8 * 4 + (part & 2) + xcd, fd = fw * 16 + (lane & 7) * 2;
  const int ub = wave >> 2, ud = (wave & 3) * 128 + c * 8 + g * 2;
  u32x4 sg[3]; u32 sx2;
  auto side_loads = [&]() {
    sg[0] = ld16<AUX_SC1>(wsr, is_up ? G_MOD + (1024 + ud) * 8 : (fin ? G_MOD + (1024 + fd) * 8 : OOB), 0);
    sg[1] = ld16<AUX_SC1>(wsr, is_up ? G_MOD + (1536 + ud) * 8 : (fin ? G_MOD + (2560 + fd) * 8 : OOB), 0);
    sg[2] = ld16<AUX_SC1>(wsr, is_up ? G_MOD + (2048 + ud) * 8 : OOB, 0);
    sx2 = __builtin_amdgcn_raw_buffer_load_b32(mk_rsrc(x, (is_up || fin) ? 2048u : 0u), is_up ? ub * 1024 + ud * 2 : (g & 1) * 1024 + fd * 2, 0, 0);
  };
  auto side_pin = [&]() { PIN(sg[0]); PIN(sg[1]); PIN(sg[2]); PIN(sx2); };
  const u32 mlo = G_ML + (head * 2 + b) * 64 + (lane & 3) * 16;
  float fwv = 0.f, fmw[8];
  bool fok = wave != 0;
  auto merge_factor = [&](const u32x4& ml) {
    const float mi = u2f(ml[0]);
    float M = fmaxf(mi, dpp<XOR1>(mi)); M = fmaxf(M, dpp<XOR2>(M));
    const float e = __expf(mi - M);
    float L = e * u2f(ml[2]);
    L += dpp<XOR1>(L); L += dpp<XOR2>(L);
    const float f = fwv * rdl(e * __builtin_amdgcn_rcpf(L), part);
#pragma unroll
    for (int w = 0; w < 8; ++w) fmw[w] = bcast16(f, w);
  };
  f32x2 ga = {0.f, 0.f}, xin = {0.f, 0.f}, cc1 = {0.f, 0.f};
  float Af = 0.f, Bf = 0.f;
  const int hpos = ub * 512 + ud;
  auto up_prep = [&]() {
    if (!all_ok((sg[0][1] == tag) & (sg[0][3] == tag) & (sg[1][1] == tag) & (sg[1][3] == tag) & (sg[2][1] == tag) & (sg[2][3] == tag))) gather16<3>(wsr, G_MOD + (1024 + ud) * 8, 512 * 8, tag, sg);
    ga = f32x2{ub ? hi_f(sg[0][0]) : lo_f(sg[0][0]), ub ? hi_f(sg[0][2]) : lo_f(sg[0][2])};
    cc1 = f32x2{rbf(1.f + (ub ? hi_f(sg[2][0]) : lo_f(sg[2][0]))), rbf(1.f + (ub ? hi_f(sg[2][2]) : lo_f(sg[2][2])))};
    xin = f32x2{lo_f(sx2), hi_f(sx2)};
    u32* hp = reinterpret_cast<u32*>(&sm.hb[hpos]);
    hp[512] = pack_bf(cc1[0], cc1[1]);
    hp[1024] = ub ? perm_hi(sg[1][0], sg[1][2]) : perm_lo(sg[1][0], sg[1][2]);
  };
  auto up_addr = [&]() {
    const int pp = lane & 3;
    return wimg + (pp & 1) * 32768 + ((pp >> 1) * 32 + g * 8 + ((lane >> 2) & 3)) * 64 + (((wave >> 1) ^ ((g & 1) | (pp & 2))) * 16) + (wave & 1) * 8;
  };
  auto up_frags = [&](int r0, int r1) {
    const u32 a = up_addr();
#pragma unroll
    for (int rb = r0; rb < r1; ++rb) {
      const u32x2 lo = tr_read(a + rb * 4096), hi = tr_read(a + rb * 4096 + 256);
      bf[rb] = u32x4{lo[0], lo[1], hi[0], hi[1]};
    }
  };
  const u32x4* ap = reinterpret_cast<const u32x4*>(&sm.hb[((lane >> 3) & 1) * 512 + (lane & 1) * 32 + g * 8]);
  const bool par = lane & 8;
  auto col_sums = [&](int r0, int r1) {
    const u32x4* a1 = ap + 128 + ((lane >> 1) & 1) * 128;
    f32x4 d0 = {0.f, 0.f, 0.f, 0.f}, d1 = {0.f, 0.f, 0.f, 0.f};
#pragma unroll
    for (int rb = r0; rb < r1; ++rb) { if (rb & 1) d1 = mfma16(a1[rb * 8], bf[rb], d1); else d0 = mfma16(a1[rb * 8], bf[rb], d0); }
    Af += par ? d0[1] + d1[1] : d0[0] + d1[0]; Bf += par ? d0[3] + d1[3] : d0[2] + d1[2];
  };
  float l0 = 0.f, l1 = 0.f;
  u32x2 gmd[2], tbg;
  const bool modw = part == 2 && has_w && !modc;
  const int qp = part != 1 ? -99 : ((blk & 7) == 3 ? QP1 : QP2);
  static_assert(QP1 + QD < QP2 && QP1 + QD > WUSE && QP1 + QD > TBU && WUSE + 1 < TBU, "order of the q steps");
  int t1 = 0;
  sfor<KF, NL>([&](auto ic) {
    constexpr int i = ic.v;
    if (i >= NCMIN / 8 && i >= nl) return false;
    const u32 so = __builtin_amdgcn_readlane(sot, i);
    if constexpr (i < NCMIN / 8) tau = i == sw ? tau2 : tau;
    gate();
    SB();
    kr[i] = ld16<AUX_NT>(rk, l16, so);
    if constexpr (i == NCMIN / 8 - 1) {
#pragma unroll
      for (int k = NCMIN / 8; k < NL; ++k) asm volatile("" : "=v"(kr[k]));
    }
    if constexpr (i == KF) small_ops();
    if constexpr (i == KF + 1) small_ops2();
    if constexpr (i == KF + 2) zero_rows();
    if constexpr (i == TBL) tbg = ld8<AUX_SC1>(wsr, wave == 7 ? G_TB + (lane & 7) * 8 : OOB, 0);
    if constexpr (i == QP1 || i == QP2) { gmd[0] = ld8<AUX_SC1>(wsr, i == qp ? G_MOD + tid * 8 : OOB, 0); gmd[1] = ld8<AUX_SC1>(wsr, i == qp ? G_MOD + tid * 8 : OOB, 4096); }
    SB();
    SB();
    if (i >= WK) { WAIT_VM(win_k(i)); SB(); }
    if (i == KF) STAMP(25);
    if (i == 8) STAMP(12);
    if (i == 20) STAMP(21);
    if (i == 25) STAMP(22);
    if (i == TBP) {
      if (part == 3 && i8 == 0 && wave == 7) { tag = next_tag(__builtin_amdgcn_readfirstlane(ep[0])); if (lane == 0) st8_sc1(wsr, G_TB + xcd * 8, tka - P.GS, tag); }
      SB();
    }
    if (i == TBU - 1) {
      if (wave == 7) {
        const bool ok = tbg[1] == tag;
        float lf = ok ? (float)min(max((int)(t0 - tbg[0]), -300), 300) : 0.f, nf = ok ? 1.f : 0.f;
        lf += dpp<XOR1>(lf); nf += dpp<XOR1>(nf); lf += dpp<XOR2>(lf); nf += dpp<XOR2>(nf);
        lf *= __builtin_amdgcn_rcpf(fmaxf(nf, 1.f));
        const float ot = dpp<XOR7>(lf);
        if (lane == 0) sm.bc = u32x2{tag, t0 - (u32)(int)((lf + ot) * 0.5f - fabsf(lf - ot) * (P.BT / 32.f))};
      }
      SB();
    }
    if (i == TBU) {
      LDS_BARRIER();
      const u32x2 bc = sm.bc;
      tag = __builtin_amdgcn_readfirstlane(bc[0]);
      t1 = (__builtin_amdgcn_readfirstlane((int)(bc[1] - t0)) + TEND + P.X1) * 256;
      const int dt = __builtin_amdgcn_readfirstlane((int)((float)(int)(t0 - bc[1]) * 256.f * __builtin_amdgcn_rcpf((float)(2 * nl - TBU - 2))));
      tau -= dt; tau2 -= dt;
      SB();
    }
    if (i == TBU + 1) {
      if (modw) { if (wave == 6) mod_put(); STAMP(9); }
      SB();
    }
    if (i == LNS) {
      tag = next_tag(__builtin_amdgcn_readfirstlane(ep[0]));
      if (part == 1) {
        float x0[8], x1[8], mb0, ri0, mb1, ri1;
#pragma unroll
        for (int k = 0; k < 4; ++k) { x0[2 * k] = lo_f(xr0[k]); x0[2 * k + 1] = hi_f(xr0[k]); x1[2 * k] = lo_f(xr1[k]); x1[2 * k + 1] = hi_f(xr1[k]); }
        ln_stats2(x0, x1, mb0, ri0, mb1, ri1);
        l0 = rbf(rbf(lo_f(xs0) - mb0) * ri0); l1 = rbf(rbf(lo_f(xs1) - mb1) * ri1);
      }
      PIN(l0); PIN(l1);
      SB();
    }
    if (i == WUSE) {
      PIN(tch1); PIN(tch2);
      static_assert(WUSE - WK >= KF, "the weight pieces are older than the steps outside the window");
      CFENCE();
      if (part == 1 || modw) wo_prep();
#pragma unroll
      for (int k = 0; k < 8; ++k) PIN(bf[k]);
      SB();
    }
    if (i == WUSE + 1) {
      if (modw) mod_gemv();
      SB();
    }
    if (i == QP1 + QD || i == QP2 + QD) {
      if (i == qp + QD) {
        u32 gm[2] = {gmd[0][0], gmd[1][0]};
        if (!all_ok((gmd[0][1] == tag) & (gmd[1][1] == tag))) { STAMP(11); gather8<2>(wsr, G_MOD + tid * 8, 512 * 8, tag, gm); }
        STAMP(5);
        gemv_go(pack_bf(rbf(l0 * rbf(1.f + lo_f(gm[1]))) + lo_f(gm[0]), rbf(l1 * rbf(1.f + hi_f(gm[1]))) + hi_f(gm[0])));
      }
      SB();
    }
    if (i == QP1 + QD + 1 || i == QP2 + QD + 1) {
      if (i == qp + QD + 1) {
        LDS_BARRIER();
        if (wave == 0) {
          const int bb = lane >> 5, jq = lane & 31;
          st8_sc1(wsr, G_Q + (bb * 2048 + blk * 32 + jq) * 8, pack_bf(sum8(&sm.r2[2 * jq * 20 + bb * 8]), sum8(&sm.r2[(2 * jq + 1) * 20 + bb * 8])), tag);
        }
        STAMP(6);
      }
      SB();
    }
    return true;
  });
  PIN(sot);
  const u32 qoff = G_Q + ((b * 32 + head) * 64 + c * 4) * 8;
  u32x4 qg[2], vr[NL], fl2[3];
  u32 qq[4];
  constexpr int NB = (NL + 15) / 16;
  float xlo, xs[4], sc[NB], pr[NB], m = 0.f;
  f32x4 acc = {0.f, 0.f, 0.f, 0.f};
  u32 pb[NB], pm[4], cpf;
  u32x2 agr;
  const u32 adst0 = lds_addr(lane < 32 ? &sm.red[wave][c * 8 + g * 4] : &sm.r2[(lane - 32) * 4]);
  u32 pso, po, rdst, rsrc, xo, l2, v2[4];
  rsrc_t r2;
  auto wo_addr = [&]() {
    po = G_P + (xcd & 3) * 16384 + near + (c >> 2 & 1) * 4096 + ((wave >> 1) * 4 + (c & 3)) * 256 + (wave & 1) * 128 + (c >> 3) * 64 + g * 16;
    rdst = lane < 32 ? (u32)(uintptr_t)&sm.red2[wave][0] + lane * 16 : (u32)(uintptr_t)&sm.r2[0] + (lane - 32) * 16;
    rsrc = (u32)(uintptr_t)&sm.red2[0][((wave & 1) * 16 + (lane & 15)) * 4 + (lane >> 4)];
    xo = dup(G_XP + xcd * 2048 + (wave & 1) * 1024 + (blk * 64 + (lane & ~1)) * 2) + (lane & 1) * 8192;
    pso = wave == 0 ? G_P + (xcd & 3) * 16384 + near + b * 4096 + ((head & 3) * 4 + part) * 256 + lane * 4 : OOB;
    PIN(pso); PIN(po); PIN(rdst); PIN(rsrc); PIN(xo);
  };
  auto w2_addr = [&]() {
    r2 = mk_rsrc(w2 + (size_t)(blk * 32 + wave * 4) * 512, 4096u);
    l2 = (u32)(uintptr_t)(&sm.w2b[wave * 4][0]);
#pragma unroll
    for (int n = 0; n < 4; ++n) { v2[n] = (lane ^ (n * 2) ^ ((wave & 2) << 2)) * 16 + n * 1024; PIN(v2[n]); }
    asm volatile("" : "+s"(r2), "+s"(l2));
  };
  const bool late = is_up || (part == 0 && wleft);
  const u32 nadd = is_up ? P.LU : (part == 0 ? (wleft ? P.LW : P.L3) : 0);
  u32 nto = tid == 0 ? G_NT + bid * 8 : OOB;
  PIN(nto);
  auto notices = [&](auto nc_, u32 off, u32x4 (&gn)[nc_.v]) -> int {
    constexpr int N = nc_.v;
#pragma nounroll
    for (int spin = 0; spin < SPIN_MAX; ++spin) {
      bool ok = true;
#pragma unroll
      for (int i = 0; i < N; ++i) ok &= (gn[i][1] == tag) & (gn[i][3] == tag);
      if (all_ok(ok)) break;
      __builtin_amdgcn_s_sleep(10);
      CFENCE();
#pragma unroll
      for (int i = 0; i < N; ++i) gn[i] = ld16<AUX_SC1>(wsr, off, i * 1024);
    }
    int mx = 0;
#pragma unroll
    for (int i = 0; i < N; ++i) mx = max(mx, max((int)(gn[i][0] - t0), (int)(gn[i][2] - t0)));
    return __builtin_amdgcn_readfirstlane((int)wave_max((float)min(mx, 1 << 20)));
  };
  const int side = nl >= SIDE + 13 ? SIDE + 8 : (nl >= SIDE + 9 ? SIDE + 4 : SIDE);
  sfor<0, NL + WV>([&](auto ic) {
    constexpr int v = ic.v;
    if (cons_end(v - 1) >= NCMIN / 8 && cons_end(v - 1) >= nl) return false;
    if constexpr (v < NL) {
      const u32 so = __builtin_amdgcn_readlane(sot, v);
      if constexpr (v >= 12) { if (v == nl - P.NE) tau = (tau * (late ? 16 : P.FE)) >> 4; }
      if constexpr (v >= NCMIN / 8) { if (v < nl) gate(); } else gate();
      SB();
      int bk = 0;
      if constexpr (v == AR) { if (part == 1 && wi < 48 && wave == 7) bk = ((int)(((u32)wall_clock64() - t0) << 8) - due + tau + (int)__builtin_amdgcn_s_getreg(7 | (3 << 11)) * tau) >> 8; SB(); }
      vr[v] = ld16<AUX_NT>(rv, l16, so);
      if constexpr (v == AR) {
        SB();
        if (part == 1 && wi < 48 && tid == 448) st8_sc1(wsr, G_AG + wi * 8, (u32)min(max(bk, 0), 250), tag);
      }
      SB();
      SB();
    }
    if constexpr (v == NCMIN / 8 - 1) {
#pragma unroll
      for (int i = NCMIN / 8; i < NL; ++i) asm volatile("" : "=v"(vr[i]));
      asm volatile("" : "=v"(fl2[1]), "=v"(fl2[2]));
#pragma unroll
      for (int w = 0; w < 8; ++w) asm volatile("" : "=v"(fmw[w]));
    }
    if constexpr (v == VQ - 4) { qg[0] = ld16<AUX_SC1>(wsr, qoff, 0); qg[1] = ld16<AUX_SC1>(wsr, qoff + 16, 0); }
    if constexpr (v == VSIDE) side_loads();
    if constexpr (v == VL) agr = ld8<AUX_SC1>(wsr, wave == 6 ? G_AG + (48 + c) * 8 : OOB, 0);
    if constexpr (v == AL) agr = ld8<AUX_SC1>(wsr, (wave == 7 && lane < 48) ? G_AG + lane * 8 : OOB, 0);
    if constexpr (v == CPF) {
      u64 pc; u32 dlt, len;
      asm volatile("s_getpc_b64 %0\n.Lcpf_here:\n\ts_mov_b32 %1, .Ltail_start-.Lcpf_here\n\ts_mov_b32 %2, .Lend_a-.Ltail_start\n\ts_max_i32 %2, %2, .Lend_b-.Ltail_start\n\ts_max_i32 %2, %2, .Lend_c-.Ltail_start\n\ts_max_i32 %2, %2, .Lend_d-.Ltail_start\n\ts_max_i32 %2, %2, .Lend_e-.Ltail_start" : "=s"(pc), "=s"(dlt), "=s"(len) :: "scc");
      pc += dlt;
      const rsrc_t rc = mk_rsrc((const void*)pc, (part == 3 && i8 == 1) ? len : 0u);
      cpf = __builtin_amdgcn_raw_buffer_load_b32(rc, tid * 64, 0, 0);
    }
    if constexpr (v == CPF + WV + 1) PIN(cpf);
    if constexpr (v == SIDE || v == SIDE + 4 || v == SIDE + 8) fl2[(v - SIDE) / 4] = ld16<AUX_SC1>(wsr, (wave == 0 && v == side) ? mlo : OOB, 0);
    SB();
    if constexpr (v < WV) { WAIT_VM(win_v(v, WK)); SB(); } else if constexpr (v < NL && cons_end(v) < v - WV + 1) { WAIT_VM(win_v(v, WV)); SB(); }
    if constexpr (v == VU) {
      if (wave == 6) {
        u32 vt[1] = {agr[0]};
        if (!all_ok(agr[1] == tag)) gather8<1>(wsr, G_AG + (48 + c) * 8, 0, tag, vt);
        float sv = u2f(vt[0]);
        sv += dpp<0x124>(sv); sv += dpp<XOR8>(sv);
        float sa = sv + dpp<XOR1>(sv);
        sv = fmaf(sa + dpp<XOR2>(sa), 0.25f, sv);
        float cf = fabsf(sv);
        cf = fminf(cf, dpp<XOR1>(cf)); cf = fminf(cf, dpp<XOR2>(cf));
        const u32 nm = (u32)__builtin_amdgcn_ballot_w64(sv > 0.f) & 15u;
        if (lane == 0) sm.nm = nm << 13 | (cf < 2500.f ? (u32)P.SL : 0u);
      }
      PIN(agr);
    }
    if constexpr (v == VQ) {
      if (!all_ok((qg[0][1] == tag) & (qg[0][3] == tag) & (qg[1][1] == tag) & (qg[1][3] == tag))) { STAMP(20); gather16<2>(wsr, qoff, 16, tag, qg); }
      STAMP(1); WSTAMP(1);
      qq[0] = qg[0][0]; qq[1] = qg[0][2]; qq[2] = qg[1][0]; qq[3] = qg[1][2];
    }
    if constexpr (v >= VQ && v < VS) {
      constexpr int u0 = SLC[v - VQ], u1 = SLC[v - VQ + 1];
      static_assert(SLC[NSL] == (NL + 7) / 8 * 8, "all groups of 8");
      sfor<u0, u1>([&](auto uc) {
        constexpr int u = uc.v, p = u & 7, i = (p & 1) * 4 + (p & 2) + (p >> 2), sl = u >> 3, bt = sl >> 1;
        float d = 0.f;
        if constexpr (sl * 8 + i < NL) {
#pragma unroll
          for (int k = 0; k < 4; ++k) d = dot2(kr[sl * 8 + i][k], qq[k], d);
        }
        if constexpr (p & 1) xs[i - 4] = sel_add<XOR7>(xs[i - 4], d, c & 4); else xs[i] = d;
        if constexpr ((p & 3) == 3) xs[p >> 2] = sel_add<XOR2>(xs[p >> 2], xs[(p >> 2) + 2], c & 2);
        if constexpr (p == 7) {
          xs[0] = sel_add<XOR1>(xs[0], xs[1], c & 1);
          if constexpr ((sl & 1) || u == (NL + 7) / 8 * 8 - 1) {
            const float t = (sl & 1) ? sel_add<XOR8>(xlo, xs[0], c & 8) : xs[0] + dpp<XOR8>(xs[0]);
            const float s = rbf(rbf(t) * 0.08838834764831845f);
            sc[bt] = (bt * 16 + c < nl) ? s : -__builtin_inff();
            PIN(sc[bt]);
          } else {
            xlo = xs[0];
            PIN(xlo);
          }
        }
        return true;
      });
      constexpr int pe = u1 & 7;
      if constexpr (pe >= 1) PIN(xs[0]);
      if constexpr (pe == 3) PIN(xs[2]);
      if constexpr (pe >= 5) PIN(xs[1]);
      if constexpr (pe == 7) PIN(xs[3]);
    }
    if constexpr (v == VS) {
      float mloc = sc[0];
#pragma unroll
      for (int bt = 1; bt < NB; ++bt) mloc = fmaxf(mloc, sc[bt]);
      m = fmaxf(wave_max(mloc), -3.0e38f);
      float ls = 0.f;
#pragma unroll
      for (int bt = 0; bt < NB; ++bt) { pr[bt] = __expf(sc[bt] - m); ls += pr[bt]; }
      const float l = wave_sum(ls);
      if (lane == 0) { sm.ml[0][wave] = m; sm.ml[1][wave] = l; }
#pragma unroll
      for (int bt = 0; bt < NB; ++bt) { pb[bt] = pack_bf(pr[bt], pr[bt]); PIN(pb[bt]); }
#pragma unroll
      for (int k = 0; k < 4; ++k) { pm[k] = (c >> 1) == k ? ((c & 1) ? 0xffff0000u : 0x0000ffffu) : 0u; PIN(pm[k]); }
      STAMP(2); WSTAMP(2);
    }
    sfor<cons_end(v - 1), cons_end(v)>([&](auto jc) {
      constexpr int i = jc.v;
      if (i < NCMIN / 8 || i < nl) {
        const u32 pv = __float_as_uint(bcast16(u2f(pb[i >> 4]), i & 15));
        acc = mfma16(u32x4{pv & pm[0], pv & pm[1], pv & pm[2], pv & pm[3]}, vr[i], acc);
      }
      PIN(acc);
      return true;
    });
    if constexpr (v == VS - 1) { side_pin(); if (is_up) up_prep(); PIN(ga); PIN(xin); PIN(cc1); }
    if constexpr (v == MLS - 1) {
      if (wave == 7) {
        const float am = wave_sum(lane < 48 ? (agr[1] == tag ? (float)agr[0] : 120.f) : 0.f) * (1.f / 48.f);
        const float st = fminf(fmaxf((am - (float)P.A0) * (float)(P.AG * 16), 0.f), (float)(P.AC * 256));
        if (lane == 0) sm.ctl = u32x2{(u32)(int)st, (u32)(int)(st * __builtin_amdgcn_rcpf((float)(nl - MLS - 3)))};
      }
      PIN(agr);
    }
    if constexpr (v == UPREP - 2) {
      nmask = __builtin_amdgcn_readfirstlane(sm.nm);
      slack = (nmask & 255u) * 256; near = ((nmask >> (xcd & 3)) ^ (xcd << 11)) & 8192u;
      w2_addr();
    }
    if constexpr (v == UPREP - 1) wo_addr();
    if constexpr (v == UPREP) {
#pragma unroll
      for (int k = 0; k < 8; ++k) asm volatile("" : "=v"(bf[k]));
      if (part == 0) wo_prep(0, 1); else if (is_up) up_frags(0, 8 - UD);
#pragma unroll
      for (int k = 0; k < 8; ++k) PIN(bf[k]);
    }
    if constexpr (v == UPREP + 1 || v == UPREP + 2) {
      if (is_up) { if constexpr (v == UPREP + 1) col_sums(0, (8 - UD) / 2); else col_sums((8 - UD) / 2, 8 - UD); }
      PIN(Af); PIN(Bf);
    }
    if constexpr (v == SIDE + WV + 1 || v == SIDE + WV + 5 || v == SIDE + WV + 9) {
      if (wave == 0 && v == side + WV + 1) {
        const u32x4 ml = fl2[(v - SIDE - WV - 1) / 4];
        fok = all_ok((ml[1] == tag) & (ml[3] == tag));
        merge_factor(ml);
      }
    }
    if (v % 6 == 4 && v >= 10 && v < 30) STAMP(15 + v / 6);
    if constexpr (v == MLS) {
      LDS_BARRIER();
      const u32x2 ct = sm.ctl;
      if (wave == 0) {
        const float mw = sm.ml[0][lane & 7];
        float Mwg = fmaxf(mw, dpp<XOR1>(mw)); Mwg = fmaxf(Mwg, dpp<XOR2>(Mwg)); Mwg = fmaxf(Mwg, dpp<XOR7>(Mwg));
        fwv = __expf(mw - Mwg);
        float Lwg = fwv * sm.ml[1][lane & 7];
        Lwg += dpp<XOR1>(Lwg); Lwg += dpp<XOR2>(Lwg); Lwg += dpp<XOR7>(Lwg);
        if (lane == 0) st16_sc1(wsr, G_ML + ((head * 2 + b) * 4 + part) * 16, __float_as_uint(Mwg), __float_as_uint(Lwg), tag);
      }
      PIN(fwv);
      t1 += __builtin_amdgcn_readfirstlane(ct[0]);
      tau += __builtin_amdgcn_readfirstlane(ct[1]);
    }
    SB();
    return true;
  });
  asm volatile(".Ltail_start:");
  *LDSP(f32x4, adst0) = acc;
  STAMP(14); WSTAMP(14);
  if (!fok) {
    u32x4 ml;
#pragma nounroll
    for (int spin = 0; spin < SPIN_MAX; ++spin) {
      CFENCE();
      ml = ld16<AUX_SC1>(wsr, mlo, 0);
      if (all_ok((ml[1] == tag) & (ml[3] == tag))) break;
    }
    merge_factor(ml);
  }
  STAMP(15);
  LDS_BARRIER();
  STAMP(3);
  {
    u32 pw = 0;
    if (wave == 0) {
      f32x2 t[8];
#pragma unroll
      for (int w = 0; w < 8; ++w) t[w] = *reinterpret_cast<const f32x2*>(&sm.red[w][2 * lane]);
      f32x2 h[4];
#pragma unroll
      for (int w = 0; w < 4; ++w) h[w] = __builtin_elementwise_fma(t[2 * w + 1], f32x2{fmw[2 * w + 1], fmw[2 * w + 1]}, t[2 * w] * fmw[2 * w]);
      const f32x2 o = (h[0] + h[1]) + (h[2] + h[3]);
      pw = min(pack_bf(o[0], o[1]), SENT - 1u);
    }
    __builtin_amdgcn_raw_buffer_store_b32(pw, wsr, pso, 0, AUX_SC1);
    st8_sc1(wsr, nto, (u32)wall_clock64() + nadd, tag);
    SB();
#pragma unroll
    for (int i = NCMIN / 8; i < NL; ++i) PIN(vr[i]);
    PIN(fl2[0]); PIN(fl2[1]); PIN(fl2[2]);
  }
  STAMP(4);
  SB();
  if (late) {
    asm volatile(".p2align 6\n\ts_barrier" ::: "memory");
#pragma nounroll
    while (wleft > 0) piece();
    if (is_up) {
      asm volatile("s_mov_b32 m0, %5\n\ts_nop 0\n\tbuffer_load_dwordx4 %0, %4, 0 offen nt lds\n\ts_add_u32 m0, m0, 0x400\n\ts_nop 0\n\tbuffer_load_dwordx4 %1, %4, 0 offen nt lds\n\t"
                   "s_add_u32 m0, m0, 0x400\n\ts_nop 0\n\tbuffer_load_dwordx4 %2, %4, 0 offen nt lds\n\ts_add_u32 m0, m0, 0x400\n\ts_nop 0\n\tbuffer_load_dwordx4 %3, %4, 0 offen nt lds"
                   :: "v"(v2[0]), "v"(v2[1]), "v"(v2[2]), "v"(v2[3]), "s"(r2), "s"(l2) : "memory", "scc", "m0");
    }
  }
  SB();

  if (part == 0) {
    u32 wa2[8];
#pragma unroll
    for (int k = 0; k < 8; ++k) {
      const int r = 32 + g * 8 + ((lane >> 2) & 3) + (k & 1) * 4, ch = (k & 6) + ((lane >> 1) & 1);
      wa2[k] = wimg + wave * 8192 + r * 128 + ((ch ^ (r & 7)) * 16) + (lane & 1) * 8;
      PIN(wa2[k]);
    }
    rsrc_t wr2 = wsr;
    asm volatile("" : "+s"(wr2));
    until(t1);
    STAMP(11);
    CFENCE();
    u32x4 og[2];
    og[0] = ld16<AUX_SC1>(wr2, po, 0);
    SB();
    WAIT_VM(1); CFENCE();
#pragma unroll
    for (int t = 0; t < 4; ++t) {
      const u32x2 lo = tr_read(wa2[2 * t]), hi = tr_read(wa2[2 * t + 1]);
      bf[4 + t] = u32x4{lo[0], lo[1], hi[0], hi[1]};
    }
    SB();
    f32x4 d[4];
    auto wo_mm = [&]() {
      const f32x4 z = {0.f, 0.f, 0.f, 0.f};
#pragma unroll
      for (int t = 0; t < 4; ++t) { d[t] = mfma16(og[0], bf[t], z); og[1][t] = __float_as_uint(dpp<XOR8>(u2f(og[0][t]))); }
      SB();
#pragma unroll
      for (int t = 0; t < 4; ++t) d[t] = mfma16(og[1], bf[4 + t], d[t]);
    };
    auto p_bad = [&]() { return !all_ok(max(max(og[0][0], og[0][1]), max(og[0][2], og[0][3])) != SENT); };
    wo_mm();
    if (__builtin_expect(p_bad(), 0)) {
#pragma nounroll
      for (int spin = 0; spin < SPIN_MAX; ++spin) {
        __builtin_amdgcn_s_sleep(1);
        CFENCE();
        og[0] = ld16<AUX_SC1>(wsr, po, 0);
        if (!p_bad()) break;
      }
      wo_mm();
      SB();
      asm volatile(".Lend_d:");
    }
    STAMP(5); WSTAMP(5);
    {
      SB();
      f32x4 y; f32x2 h[4];
#pragma unroll
      for (int t = 0; t < 4; ++t) { h[t] = d[t].xy + d[t].zw; PIN(h[t]); }
#pragma unroll
      for (int t = 0; t < 4; ++t) { y[t] = h[t][0] + h[t][1]; PIN(y[t]); }
      *LDSP(f32x4, rdst) = y;
    }
    STAMP(16);
    LDS_BARRIER();
    STAMP(17);
    if (wave < 2) {
      f32x2 v[4];
#pragma unroll
      for (int w = 0; w < 4; ++w) v[w] = f32x2{LDSP(float, rsrc)[w * 256], LDSP(float, rsrc)[w * 256 + 128]};
      const f32x2 h = (v[0] + v[1]) + (v[2] + v[3]);
      const float y = h[0] + h[1];
      __builtin_amdgcn_raw_buffer_store_b32(min(pack_bf(dpp<0xA0>(y), dpp<0xF5>(y)), SENT - 1u), wr2, xo, 0, AUX_SC1);
      asm volatile(".Lend_a:");
    }
    STAMP(6);
    FINISH();
  }
  if (part == 3) {
    if (!has_w) FINISH();
    u32x4 b2[4];
    const u32 tdst = lds_addr(&sm.hb[hpos ^ ((wave & 1) * 16)]);
    const u32 sdst = lds_addr(&sm.stat[wave][lane]);
    const u32 ssrc = lds_addr(&sm.stat[(g >> 1) * 4 + (c >> 2)][(g & 1) * 32 + (c & 3) * 8]);
    const u32 adst = lds_addr((c < 4 && (g & 1) == 0) ? &sm.actl[g >> 1][wave * 4 + c] : reinterpret_cast<u16*>(&sm.dummy[lane]));
    const u32 asrc = lds_addr(&sm.actl[lane & 1][g * 8]);
    const u32 apa = lds_addr(&sm.hb[(c >> 3) * 512 + (c & 3) * 64 + ((c >> 2) & 1) * 32 + (g ^ (c & 2)) * 8]);
    u32 xoff = dup(G_XP) + near + g * 2048 + ub * 1024 + (wave & 3) * 256 + c * 16;
    u32x4 one;
#pragma unroll
    for (int k = 0; k < 4; ++k) one[k] = ((c & 2) == 0 && (c >> 2) == k) ? ((c & 1) ? 0x3f800000u : 0x00003f80u) : 0u;
    PIN(one);
    u32 ooff = dup(G_OP + (blk * 512 + tid) * 8) + ((nmask >> (g & 1)) & 8192u);
    PIN(xoff); PIN(ooff);
    PIN(ga); PIN(xin); PIN(cc1);
    u32 w2a = (u32)(uintptr_t)(&sm.w2b[0][0]) + (g * 8 + ((lane >> 2) & 3)) * 1024 + (wave ^ (g & 1)) * 128 + (lane & 15) * 8;
    u32 w2t[4];
#pragma unroll
    for (int t = 0; t < 4; ++t) { w2t[t] = w2a ^ (t * 32); PIN(w2t[t]); }
    static_assert(UD == 1, "one late fragment");
    u32 ufa = up_addr() + 7 * 4096;
    const u32 a7a = lds_addr(ap + 128 + ((lane >> 1) & 1) * 128 + 7 * 8);
    PIN(ufa);
    u64 gm1 = __builtin_amdgcn_ballot_w64(g == 1), gm2 = __builtin_amdgcn_ballot_w64(g == 2), gm3 = __builtin_amdgcn_ballot_w64(g == 3);
    asm volatile("" : "+s"(gm1), "+s"(gm2), "+s"(gm3));
    const bool g1 = __builtin_amdgcn_inverse_ballot_w64(gm1), g2 = __builtin_amdgcn_inverse_ballot_w64(gm2), g3 = __builtin_amdgcn_inverse_ballot_w64(gm3);
    if (wave == 1) {
      due = t1 + P.N1 * 256; gate();
      u32x4 gn[2] = {ld16<AUX_SC1>(wsr, G_NT + lane * 16, 0), ld16<AUX_SC1>(wsr, G_NT + lane * 16, 1024)};
      const int tn = notices(IC<2>{}, G_NT + lane * 16, gn) * 256;
      due = (tn > t1 + P.TH * 256 ? max(tn, t1 + P.L0 * 256) + P.L1 * 256 : t1) + P.X2 * 256 + slack;
      until(due);
    }
    asm volatile("s_waitcnt vmcnt(0)" ::: "memory");
    STAMP(9);
    __builtin_amdgcn_s_barrier();
    STAMP(11);
    CFENCE();
    u32x4 xg[2] = {ld16<AUX_SC1>(wsr, xoff, 0), ld16<AUX_SC1>(wsr, xoff, 16384)};
    SB();
    {
      const u32x4 a7 = *LDSP(u32x4, a7a);
      const u32x2 l7 = tr_read(ufa), h7 = tr_read(ufa + 256);
      bf[7] = u32x4{l7[0], l7[1], h7[0], h7[1]};
#pragma unroll
      for (int t = 0; t < 4; ++t) {
        const u32x2 lo = tr_read(w2t[t]), hi = tr_read(w2t[t] + 4096);
        b2[t] = u32x4{lo[0], lo[1], hi[0], hi[1]};
      }
      SB();
      const f32x4 d0 = mfma16(a7, bf[7], f32x4{0.f, 0.f, 0.f, 0.f});
      Af += par ? d0[1] : d0[0]; Bf += par ? d0[3] : d0[2];
      Af += dpp<XOR8>(Af); Bf += dpp<XOR8>(Bf);
    }
    PIN(Af); PIN(Bf);
#pragma unroll
    for (int t = 0; t < 4; ++t) PIN(b2[t]);
    SB();
    const f32x4 z4 = {0.f, 0.f, 0.f, 0.f};
    auto xp_bad = [&]() { return !all_ok(max(max(max(xg[0][0], xg[0][1]), max(xg[0][2], xg[0][3])), max(max(xg[1][0], xg[1][1]), max(xg[1][2], xg[1][3]))) != SENT); };
    f32x4 v = mfma16(one, xg[1], mfma16(one, xg[0], z4));
    if (__builtin_expect(xp_bad(), 0)) {
#pragma nounroll
      for (int spin = 0; spin < SPIN_MAX; ++spin) {
        __builtin_amdgcn_s_sleep(1);
        CFENCE();
        xg[0] = ld16<AUX_SC1>(wsr, xoff, 0); xg[1] = ld16<AUX_SC1>(wsr, xoff, 16384);
        if (!xp_bad()) break;
      }
      v = mfma16(one, xg[1], mfma16(one, xg[0], z4));
      SB();
      asm volatile(".Lend_e:");
    }
    STAMP(5); WSTAMP(5);
    {
      const float a0 = v[0], a1 = v[1];
      const float x10 = fmaf(ga[0], a0, xin[0]), x11 = fmaf(ga[1], a1, xin[1]);
      *LDSP(u32, tdst) = pack_bf(x10 * cc1[0], x11 * cc1[1]);
      float u = swap32_add(x10 + x11, fmaf(x10, x10, x11 * x11));
      u += dpp<XOR1>(u); u += dpp<XOR2>(u); u += dpp<XOR7>(u);
      *LDSP(float, sdst) = u;
    }
    STAMP(16);
    LDS_BARRIER();
    STAMP(17);
    {
      const f32x4 z = {0.f, 0.f, 0.f, 0.f};
      const u32x4 af0 = LDSP(u32x4, apa)[0], af1 = LDSP(u32x4, apa)[32];
      float s = LDSP(float, ssrc)[0];
      SB();
      f32x4 d[4];
      d[0] = mfma16(af0, bf[0], z); d[1] = mfma16(af0, bf[1], z);
      SB();
      s += dpp<XOR1>(s); s += dpp<XOR2>(s);
      PIN(s);
      d[2] = mfma16(af0, bf[2], z);
      SB();
      s += dpp<XOR7>(s); s += dpp<XOR8>(s);
      PIN(s);
      d[3] = mfma16(af0, bf[3], z);
      SB();
      const auto sw2 = __builtin_amdgcn_permlane16_swap(__float_as_uint(s), __float_as_uint(s), false, false);
      float mean = u2f(sw2[0]) * (1.f / 512.f), qm = fmaf(u2f(sw2[1]), 1.f / 512.f, 1e-5f);
      PIN(mean); PIN(qm);
      d[0] = mfma16(af1, bf[4], d[0]);
      SB();
      float ve = fmaxf(fmaf(-mean, mean, qm), 1e-5f), ma = mean * Af;
      PIN(ve); PIN(ma);
      d[1] = mfma16(af1, bf[5], d[1]);
      SB();
      float ri = __builtin_amdgcn_rsqf(ve);
      PIN(ri);
      d[2] = mfma16(af1, bf[6], d[2]);
      d[3] = mfma16(af1, bf[7], d[3]);
      SB();
      float cf = fmaf(-ri, ma, Bf), rl = ri * -1.4426950408889634f, cl = cf * -1.4426950408889634f;
      PIN(cf); PIN(rl); PIN(cl);
      PIN(d[0]); PIN(d[1]);
      float y0 = d[0][0] + d[1][1];
      PIN(d[2]);
      y0 += d[2][2];
      PIN(y0); PIN(d[3]);
      const float y1 = d[3][3];
      float y = y0 + y1;
      y = __builtin_bit_cast(float, __builtin_amdgcn_update_dpp(__builtin_bit_cast(int, y), __builtin_bit_cast(int, y), XOR8, 0xa, 0xf, false));
      y = swap16_add(y, y);
      const float e = __builtin_amdgcn_exp2f(fmaf(y, rl, cl));
      y = fmaf(y, ri, cf);
      float gu = y * dpp<0x104>(y);
      PIN(gu);
      *LDSP(u16, adst) = (u16)pack_bf(gu * __builtin_amdgcn_rcpf(1.f + e), 0.f);
    }
    STAMP(18);
    LDS_BARRIER();
    STAMP(10);
    {
      const u32x4 af = *LDSP(u32x4, asrc);
      const f32x4 z = {0.f, 0.f, 0.f, 0.f};
      f32x4 d[4];
#pragma unroll
      for (int t = 0; t < 4; ++t) d[t] = mfma16(af, b2[t], z);
      SB();
      PIN(d[0]); PIN(d[1]);
      float e0 = g1 ? d[1][0] : d[0][0], e1 = g1 ? d[1][1] : d[0][1];
      PIN(d[2]);
      e0 = g2 ? d[2][0] : e0; e1 = g2 ? d[2][1] : e1;
      PIN(e0); PIN(e1); PIN(d[3]);
      st8_sc1(wsr, ooff, pack_bf(g3 ? d[3][0] : e0, g3 ? d[3][1] : e1), tag);
    }
    asm volatile(".Lend_b:");
    STAMP(6);
    FINISH();
  }
  if (fin) {
    const int d = fd, ul = lane >> 3;
    u32x4 gm[2] = {sg[0], sg[1]};
    if (!all_ok((sg[0][1] == tag) & (sg[0][3] == tag) & (sg[1][1] == tag) & (sg[1][3] == tag))) gather16<2>(wsr, G_MOD + (1024 + d) * 8, 1536 * 8, tag, gm);
    u32 xa0, xa1;
    {
      due = t1 + P.N2 * 256; gate();
      u32x4 gn[2] = {ld16<AUX_SC1>(wsr, G_NT + lane * 16, 0), ld16<AUX_SC1>(wsr, G_NT + lane * 16, 1024)};
      const int tn = notices(IC<2>{}, G_NT + lane * 16, gn) * 256;
      due = (tn > t1 + P.TH * 256 ? max(tn, t1 + P.L0 * 256) + P.L1 * 256 : t1) + P.X3 * 256 + slack;
      until(due);
    }
#pragma nounroll
    for (int spin = 0; spin < SPIN_MAX; ++spin) {
      CFENCE();
      xa0 = __builtin_amdgcn_raw_buffer_load_b32(wsr, dup(G_XP + ul * 2048) + near + d * 2, 0, AUX_SC1);
      xa1 = __builtin_amdgcn_raw_buffer_load_b32(wsr, dup(G_XP + ul * 2048) + near + d * 2, 1024, AUX_SC1);
      if (all_ok(max(xa0, xa1) != SENT)) break;
      __builtin_amdgcn_s_sleep(1);
    }
    float a0 = swap16_add(lo_f(xa0), lo_f(xa1)), a1 = swap16_add(hi_f(xa0), hi_f(xa1));
    a0 += dpp<XOR8>(a0); a1 += dpp<XOR8>(a1);
    a0 = swap32_add(a0, a0); a1 = swap32_add(a1, a1);
    const bool hb = g & 1;
    f32x2 xv = {rbf(lo_f(sx2) + rbf((hb ? hi_f(gm[0][0]) : lo_f(gm[0][0])) * rbf(a0))), rbf(hi_f(sx2) + rbf((hb ? hi_f(gm[0][2]) : lo_f(gm[0][2])) * rbf(a1)))};
    f32x2 gv = {hb ? hi_f(gm[1][0]) : lo_f(gm[1][0]), hb ? hi_f(gm[1][2]) : lo_f(gm[1][2])};
    PIN(xv); PIN(gv);
    if (fw == 0 && lane == 0) __builtin_amdgcn_raw_buffer_store_b32(tag, wsr, WS_EPOCH, 0, AUX_SC1);
    STAMP(10);
    due = max(due + P.X4 * 256 + slack, (int)(((u32)wall_clock64() - t0) << 8) + P.GX * 256);
    const bool last = ul + 40 < NUP;
    u32 o0 = dup(G_OP + (ul * 512 + d) * 8) + near, o5 = last ? o0 : OOB, tg5 = last ? tag : 0u;
    const rsrc_t rout = mk_rsrc(out, 2048u);
    u32 oo = (lane & 40) == 0 ? (g * 512 + d) * 2 : OOB;
    const u32 pk = (c & 10) ? 0u : ((c & 4) ? 0x3f800000u : 0x00003f80u);
    u32x4 one = {(c & 1) ? 0u : pk, 0u, (c & 1) ? pk : 0u, 0u};
    PIN(o0); PIN(o5); PIN(tg5); PIN(oo); PIN(one);
    u32 so[6];
#pragma unroll
    for (int k = 0; k < 6; ++k) { so[k] = k * 65536; asm volatile("" : "+s"(so[k])); }
    __builtin_amdgcn_raw_buffer_store_b32(pack_bf(xv[0], xv[1]), rout, oo, 0, AUX_SC1);
    until(due);
#pragma nounroll
    for (int spin = 0; spin < SPIN_MAX; ++spin) {
      CFENCE();
      u32x4 og[6];
      og[0] = ld16<AUX_SC1>(wsr, o5, so[5]);
#pragma unroll
      for (int k = 1; k < 6; ++k) { SB(); og[k] = ld16<AUX_SC1>(wsr, o0, so[k - 1]); }
      SB();
      f32x4 sv = {0.f, 0.f, 0.f, 0.f};
#pragma unroll
      for (int k = 0; k < 6; ++k) sv = mfma16(one, og[k], sv);
      const float s0 = sv[0] + dpp<XOR8>(sv[0]), s1 = sv[1] + dpp<XOR8>(sv[1]);
      __builtin_amdgcn_raw_buffer_store_b32(pack_bf(fmaf(gv[0], s0, xv[0]), fmaf(gv[1], s1, xv[1])), rout, oo, 0, AUX_SC1);
      SB();
      u32 bad = (og[0][1] ^ tg5) | (og[0][3] ^ tg5);
#pragma unroll
      for (int k = 1; k < 6; ++k) { bad |= (og[k][1] ^ tag) | (og[k][3] ^ tag); PIN(bad); }
      if (all_ok(bad == 0u)) break;
      __builtin_amdgcn_s_sleep(1);
    }
    STAMP(7);
    asm volatile(".Lend_c:");
  }
  FINISH();
}

extern "C" void kernel_launch(void* const* d_in, const int* in_sizes, int n_in,
                              void* d_out, int out_size, void* d_ws, size_t ws_size,
                              hipStream_t stream) {
  fused_kernel<<<256, 512, 0, stream>>>((const u16*)d_in[0], (const u16*)d_in[1], (const u16*)d_in[2], (const u16*)d_in[3], (const u16*)d_in[4],
                                        (const u16*)d_in[6], (const u16*)d_in[7], (const u16*)d_in[8], (const u16*)d_in[9], (const u16*)d_in[10],
                                        (unsigned char*)d_ws, (u16*)d_out
    );
}
```
